```python
import torch

def _softmax(x, axis=-1):
    return torch.softmax(x, dim=axis)
def _gelu(x):
    return torch.nn.functional.gelu(x, approximate="tanh")
def _silu(x):
    return torch.nn.functional.silu(x)
def _sigmoid(x):
    return torch.sigmoid(x)
def _layernorm(x, axis=-1, eps=1e-5):
    m = x.mean(dim=axis, keepdim=True)
    v = x.var(dim=axis, keepdim=True, unbiased=False)
    return (x - m) / torch.sqrt(v + eps)
def _scatter_add(tgt, idx, src, axis=0):
    return tgt.clone().index_add_(axis, idx, src)
def _segment_sum(src, seg, n):
    out = torch.zeros((n,) + src.shape[1:], dtype=src.dtype)
    return out.index_add_(0, seg, src)
def _pad(x, pw):
    flat = tuple(v for pair in reversed(pw) for v in pair)
    return torch.nn.functional.pad(x, flat)

kernel_name = 'gated_mix__compressed_kv_cla__geglu_ffn__prompt'

def setup_inputs():

    _w = (lambda a: (lambda t: t.to(torch.bfloat16) if t.is_floating_point() else t)(torch.as_tensor(a)))
    def _A_setup():
        B, H, Sq, Sk, D, M = 1, 32, 2048, 2048, 128, 1024
        Dc = 4 * D
        return {
            "x": _w((lambda *s: torch.randn(*s, dtype=torch.bfloat16) * 0.05)(B, Sq, M)),
            "c_kv": _w((lambda *s: torch.randn(*s, dtype=torch.bfloat16) * 0.4)(B, Sk, Dc)),
            "w_q": _w((lambda *s: torch.randn(*s, dtype=torch.bfloat16) * 6.4)(M, H, D) * (M ** -0.5)),
            "w_uk": _w((lambda *s: torch.randn(*s, dtype=torch.bfloat16) * 6.4)(Dc, H, D) * (Dc ** -0.5)),
            "w_uv": _w((lambda *s: torch.randn(*s, dtype=torch.bfloat16) * 0.4)(Dc, H, D) * (Dc ** -0.5)),
            "w_o": _w((lambda *s: torch.randn(*s, dtype=torch.bfloat16) * 0.4)(H, D, M) * ((H * D) ** -0.5)),
        }
    _A_in = _A_setup()

    def _B_setup():
        B, S, D, F = 1, 2048, 1024, 3584
        return {
            "x": _w((lambda *s: torch.randn(*s, dtype=torch.bfloat16) * 0.05)(B, S, D)),
            "w_gate": _w((lambda *s: torch.randn(*s, dtype=torch.bfloat16) * 0.05)(D, F)),
            "w_up": _w((lambda *s: torch.randn(*s, dtype=torch.bfloat16) * 0.05)(D, F)),
            "w_down": _w((lambda *s: torch.randn(*s, dtype=torch.bfloat16) * 0.05)(F, D)),
        }
    _B_in = _B_setup()

    return {
        "A_x": _A_in["x"],
        "A_c_kv": _A_in["c_kv"],
        "A_w_q": _A_in["w_q"],
        "A_w_uk": _A_in["w_uk"],
        "A_w_uv": _A_in["w_uv"],
        "A_w_o": _A_in["w_o"],
        "B_x": _B_in["x"],
        "B_w_gate": _B_in["w_gate"],
        "B_w_up": _B_in["w_up"],
        "B_w_down": _B_in["w_down"],
        "gate_w": _w((lambda *s: torch.randn(*s, dtype=torch.bfloat16) * 0.05)(1024) * 16.0),
    }

def reference(A_x, A_c_kv, A_w_q, A_w_uk, A_w_uv, A_w_o, B_x, B_w_gate, B_w_up, B_w_down, gate_w):
    def _A_ref(x, c_kv, w_q, w_uk, w_uv, w_o):
        B, Sq, M = x.shape[0], x.shape[1], x.shape[2]
        Sk = c_kv.shape[1]
        H, D = w_q.shape[1], w_q.shape[2]
        q = torch.einsum("bqm,mhd->bhqd", x, w_q)
        k = torch.einsum("bkc,chd->bhkd", c_kv, w_uk)
        v = torch.einsum("bkc,chd->bhkd", c_kv, w_uv)
        scores = torch.einsum("bhqd,bhkd->bhqk", q, k) * (D ** -0.5)
        off = Sk - Sq
        qi = torch.arange(Sq)[:, None]
        kj = torch.arange(Sk)[None, :]
        cmask = kj <= (qi + off)
        scores = torch.where(cmask[None, None], scores, -1e9)
        p = _softmax(scores, axis=-1)
        ctx = torch.einsum("bhqk,bhkd->bhqd", p, v)
        return torch.einsum("bhqd,hdm->bqm", ctx, w_o)

    def _B_ref(x, w_gate, w_up, w_down):
        return (_gelu(x @ w_gate) * (x @ w_up)) @ w_down

    x = A_x
    g = _sigmoid((x * gate_w).sum(-1, keepdims=True))
    a = _A_ref(x, A_c_kv, A_w_q, A_w_uk, A_w_uv, A_w_o)
    b = _B_ref(x, B_w_gate, B_w_up, B_w_down)
    return g * a + (1 - g) * b
```

```cpp
#include <hip/hip_runtime.h>
#include <hip/hip_bf16.h>
typedef unsigned short u16;
__device__ __forceinline__ float bf2f(u16 b){ return __uint_as_float(((unsigned)b)<<16); }
__device__ __forceinline__ u16 f2bf(float f){ __hip_bfloat16 h = __float2bfloat16(f); return *(u16*)&h; }
__global__ __launch_bounds__(256) void gemm_naive(const u16* A, const u16* B, u16* C, int M, int N, int K){
  __shared__ float As[16][65], Bs[16][65];
  int tx = threadIdx.x & 15, ty = threadIdx.x >> 4;
  int m0 = blockIdx.y*64, n0 = blockIdx.x*64;
  float acc[4][4] = {};
  for(int k0=0;k0<K;k0+=16){
    for(int i=threadIdx.x;i<1024;i+=256){ int r=i>>4,c=i&15; As[c][r]=bf2f(A[(size_t)(m0+r)*K+k0+c]); }
    for(int i=threadIdx.x;i<1024;i+=256){ int r=i>>6,c=i&63; Bs[r][c]=bf2f(B[(size_t)(k0+r)*N+n0+c]); }
    __syncthreads();
    for(int k=0;k<16;k++){ float a[4],b[4]; for(int i=0;i<4;i++){a[i]=As[k][ty*4+i]; b[i]=Bs[k][tx*4+i];}
      for(int i=0;i<4;i++)for(int j=0;j<4;j++)acc[i][j]+=a[i]*b[j]; }
    __syncthreads();
  }
  for(int i=0;i<4;i++)for(int j=0;j<4;j++)C[(size_t)(m0+ty*4+i)*N+n0+tx*4+j]=f2bf(acc[i][j]);
}
__global__ __launch_bounds__(64) void attn_naive(const u16* Q,const u16* Kt,const u16* V,u16* O){
  int q=blockIdx.x,h=blockIdx.y,l=threadIdx.x;
  __shared__ float qs[128]; __shared__ float p[2048];
  qs[l]=bf2f(Q[(size_t)q*4096+h*128+l]); qs[l+64]=bf2f(Q[(size_t)q*4096+h*128+l+64]);
  __syncthreads();
  float mx=-1e30f;
  for(int k=l;k<=q;k+=64){ float s=0; const u16* kr=Kt+(size_t)k*4096+h*128; for(int d=0;d<128;d++) s+=qs[d]*bf2f(kr[d]); s*=0.08838834764831845f; p[k]=s; mx=fmaxf(mx,s);}
  for(int o=32;o;o>>=1) mx=fmaxf(mx,__shfl_xor(mx,o));
  float sum=0; for(int k=l;k<=q;k+=64){ float e=__expf(p[k]-mx); p[k]=e; sum+=e; }
  for(int o=32;o;o>>=1) sum+=__shfl_xor(sum,o);
  __syncthreads();
  float a0=0,a1=0; for(int k=0;k<=q;k++){ const u16* vr=V+(size_t)k*4096+h*128; a0+=p[k]*bf2f(vr[l]); a1+=p[k]*bf2f(vr[l+64]); }
  O[(size_t)q*4096+h*128+l]=f2bf(a0/sum); O[(size_t)q*4096+h*128+l+64]=f2bf(a1/sum);
}
__global__ void geglu(const u16* g,const u16* u,u16* h,int n){ int i=blockIdx.x*256+threadIdx.x; if(i<n){ float x=bf2f(g[i]); float t=0.5f*x*(1.f+tanhf(0.7978845608f*(x+0.044715f*x*x*x))); h[i]=f2bf(bf2f(f2bf(t))*bf2f(u[i])); } }
__global__ __launch_bounds__(64) void mix(const u16* x,const u16* gw,const u16* a,const u16* b,u16* out){ int r=blockIdx.x,l=threadIdx.x; float s=0; for(int i=l;i<1024;i+=64) s+=bf2f(x[r*1024+i])*bf2f(gw[i]); for(int o=32;o;o>>=1) s+=__shfl_xor(s,o); float g=1.f/(1.f+__expf(-s)); for(int i=l;i<1024;i+=64) out[r*1024+i]=f2bf(g*bf2f(a[r*1024+i])+(1.f-g)*bf2f(b[r*1024+i])); }
extern "C" void kernel_launch(void* const* d_in, const int* in_sizes, int n_in, void* d_out, int out_size, void* d_ws, size_t ws_size, hipStream_t stream){
  const u16 *x=(const u16*)d_in[0],*c=(const u16*)d_in[1],*wq=(const u16*)d_in[2],*wuk=(const u16*)d_in[3],*wuv=(const u16*)d_in[4],*wo=(const u16*)d_in[5],*wg=(const u16*)d_in[7],*wu=(const u16*)d_in[8],*wd=(const u16*)d_in[9],*gw=(const u16*)d_in[10];
  u16* ws=(u16*)d_ws; size_t S=2048*4096;
  u16 *q=ws,*k=ws+S,*v=ws+2*S,*ctx=ws+3*S,*g=ws+4*S,*u=ws+5*S,*h=ws+6*S,*a=ws+7*S,*b=ws+8*S;
  gemm_naive<<<dim3(64,32),256,0,stream>>>(x,wq,q,2048,4096,1024);
  gemm_naive<<<dim3(64,32),256,0,stream>>>(c,wuk,k,2048,4096,512);
  gemm_naive<<<dim3(64,32),256,0,stream>>>(c,wuv,v,2048,4096,512);
  gemm_naive<<<dim3(56,32),256,0,stream>>>(x,wg,g,2048,3584,1024);
  gemm_naive<<<dim3(56,32),256,0,stream>>>(x,wu,u,2048,3584,1024);
  attn_naive<<<dim3(2048,32),64,0,stream>>>(q,k,v,ctx);
  geglu<<<2048*3584/256,256,0,stream>>>(g,u,h,2048*3584);
  gemm_naive<<<dim3(16,32),256,0,stream>>>(ctx,wo,a,2048,1024,4096);
  gemm_naive<<<dim3(16,32),256,0,stream>>>(h,wd,b,2048,1024,3584);
  mix<<<2048,64,0,stream>>>(x,gw,a,b,(u16*)d_out);
}
```

```cpp
#include <hip/hip_runtime.h>
__global__ __launch_bounds__(256, 1) __attribute__((aligned(4096))) void gm_k(const void* x, const void* c, const void* wq, const void* wuk, const void* wuv, const void* wg, const void* wu, void* ws, const void* gw, const void* wo, const void* wd, void* out) {
  const int wid = __builtin_amdgcn_readfirstlane(threadIdx.x >> 6);
  asm volatile(
R"(gm_KB_start:
s_load_dwordx16 s[16:31],%0,0
s_load_dwordx4 s[8:11],%0,72
s_load_dwordx2 s[96:97],%0,88
s_lshr_b32 s64,%1,3
s_lshl_b32 s64,s64,2
s_add_u32 s64,s64,%2
s_and_b32 s64,s64,15
s_lshl_b32 s64,s64,13
s_getpc_b64 s[92:93]
gm_CPF5:
s_add_u32 s92,s92,gm_KB_start-gm_CPF5
s_addc_u32 s93,s93,(gm_KB_start-gm_CPF5)>>32
s_and_b32 s93,s93,0xffff
s_mov_b32 s94,gm_KB_end-gm_KB_start
s_mov_b32 s95,0x20000
v_mbcnt_lo_u32_b32 v60,-1,0
v_mbcnt_hi_u32_b32 v60,-1,v60
v_lshlrev_b32 v60,7,v60
buffer_load_dword v61,v60,s[92:95],s64 offen
s_load_dwordx2 s[98:99],%0,64
s_mov_b32 s70,%2
s_and_b32 s73,%1,7
s_lshr_b32 s69,%1,3
)"
R"(s_and_b32 s71,s69,7
s_lshr_b32 s61,s69,3
s_lshl_b32 s86,s73,2
s_add_u32 s86,s86,s61
s_and_b32 s61,s61,1
s_lshl_b32 s74,s73,1
s_add_u32 s74,s74,s61
v_mbcnt_lo_u32_b32 v19,-1,0
v_mbcnt_hi_u32_b32 v19,-1,v19
s_lshl_b32 s56,s70,13
s_add_u32 s57,s56,0x10000
v_lshrrev_b32 v20,4,v19
v_add_u32 v20,0,v20
v_and_b32 v21,7,v19
v_xor_b32 v20,v20,v21
v_lshlrev_b32 v20,4,v20
v_lshrrev_b32 v21,3,v19
v_mul_u32_u24 v21,1024,v21
v_add_u32 v12,v20,v21
v_lshrrev_b32 v20,4,v19
v_or_b32 v20,0,v20
v_bfe_u32 v21,v19,1,3
v_xor_b32 v20,v20,v21
v_lshlrev_b32 v20,5,v20
)"
R"(v_and_b32 v21,1,v19
v_lshl_add_u32 v20,v21,4,v20
v_lshrrev_b32 v21,4,v19
v_mul_u32_u24 v21,8192,v21
v_add_u32 v16,v20,v21
v_lshrrev_b32 v20,4,v19
v_add_u32 v20,4,v20
v_and_b32 v21,7,v19
v_xor_b32 v20,v20,v21
v_lshlrev_b32 v20,4,v20
v_lshrrev_b32 v21,3,v19
v_mul_u32_u24 v21,1024,v21
v_add_u32 v13,v20,v21
v_lshrrev_b32 v20,4,v19
v_or_b32 v20,4,v20
v_bfe_u32 v21,v19,1,3
v_xor_b32 v20,v20,v21
v_lshlrev_b32 v20,5,v20
v_and_b32 v21,1,v19
v_lshl_add_u32 v20,v21,4,v20
v_lshrrev_b32 v21,4,v19
v_mul_u32_u24 v21,8192,v21
v_add_u32 v17,v20,v21
s_lshl_b32 s67,s71,2
)"
R"(s_add_u32 s67,s67,s70
s_mul_i32 s67,s67,65536
s_and_b32 s68,s70,1
s_mul_i32 s68,s68,262144
s_lshl_b32 s62,s74,9
s_add_u32 s68,s68,s62
s_lshr_b32 s62,s70,1
s_lshl_b32 s62,s62,8
s_add_u32 s68,s68,s62
s_mul_i32 s53,s73,128
s_mul_i32 s55,s73,524288
s_waitcnt lgkmcnt(0)
s_cmp_lt_u32 s69,16
s_cselect_b64 s[64:65],s[22:23],s[24:25]
s_add_u32 s44,s64,s68
s_addc_u32 s45,s65,0
s_and_b32 s45,s45,0xffff
s_sub_u32 s46,4194304,s68
s_mov_b32 s47,0x20000
s_add_u32 s36,s18,s67
s_addc_u32 s37,s19,0
s_and_b32 s37,s37,0xffff
s_sub_u32 s38,2097152,s67
s_mov_b32 s39,0x20000
)"
R"(s_add_u32 m0,s57,0
s_add_u32 s63,s55,0
buffer_load_dwordx4 v16,s[44:47],s63 offen lds
s_add_u32 m0,s57,1024
s_add_u32 s64,s55,32768
buffer_load_dwordx4 v16,s[44:47],s64 offen lds
s_add_u32 m0,s57,2048
s_add_u32 s63,s55,65536
buffer_load_dwordx4 v17,s[44:47],s63 offen lds
s_add_u32 m0,s57,3072
s_add_u32 s64,s55,98304
buffer_load_dwordx4 v17,s[44:47],s64 offen lds
s_add_u32 m0,s57,4096
s_add_u32 s63,s55,131072
buffer_load_dwordx4 v16,s[44:47],s63 offen lds
s_add_u32 m0,s57,5120
s_add_u32 s64,s55,163840
buffer_load_dwordx4 v16,s[44:47],s64 offen lds
s_add_u32 m0,s57,6144
s_add_u32 s63,s55,196608
buffer_load_dwordx4 v17,s[44:47],s63 offen lds
s_add_u32 m0,s57,7168
s_add_u32 s64,s55,229376
buffer_load_dwordx4 v17,s[44:47],s64 offen lds
)"
R"(s_add_u32 s57,s57,0x8000
s_cmp_ge_u32 s57,0x28000
s_cselect_b32 s65,0x18000,0
s_sub_u32 s57,s57,s65
s_add_u32 s55,s55,524288
s_cmp_eq_u32 s55,4194304
s_cselect_b32 s55,0,s55
s_add_u32 m0,s56,0
s_add_u32 s61,s53,0
buffer_load_dwordx4 v12,s[36:39],s61 offen lds
s_add_u32 m0,s56,1024
s_add_u32 s62,s53,8192
buffer_load_dwordx4 v13,s[36:39],s62 offen lds
s_add_u32 m0,s56,2048
s_add_u32 s61,s53,16384
buffer_load_dwordx4 v12,s[36:39],s61 offen lds
s_add_u32 m0,s56,3072
s_add_u32 s62,s53,24576
buffer_load_dwordx4 v13,s[36:39],s62 offen lds
s_add_u32 m0,s56,4096
s_add_u32 s61,s53,32768
buffer_load_dwordx4 v12,s[36:39],s61 offen lds
s_add_u32 m0,s56,5120
s_add_u32 s62,s53,40960
)"
R"(buffer_load_dwordx4 v13,s[36:39],s62 offen lds
s_add_u32 m0,s56,6144
s_add_u32 s61,s53,49152
buffer_load_dwordx4 v12,s[36:39],s61 offen lds
s_add_u32 m0,s56,7168
s_add_u32 s62,s53,57344
buffer_load_dwordx4 v13,s[36:39],s62 offen lds
s_xor_b32 s56,s56,0x8000
s_add_u32 s53,s53,128
s_cmp_eq_u32 s53,1024
s_cselect_b32 s53,0,s53
s_cmp_ge_u32 s69,16
s_cbranch_scc1 gm_KB_wkv
s_cmp_eq_u32 %2,1
s_cbranch_scc1 gm_KB_wkk_1
s_cmp_eq_u32 %2,2
s_cbranch_scc1 gm_KB_wkk_2
s_cmp_eq_u32 %2,3
s_cbranch_scc1 gm_KB_wkk_3
s_getpc_b64 s[100:101]
s_add_u32 s100,s100,12
s_addc_u32 s101,s101,0
s_branch gm_KB_pg1
s_getpc_b64 s[100:101]
)"
R"(s_add_u32 s100,s100,12
s_addc_u32 s101,s101,0
s_branch gm_KB_tp_done
s_getpc_b64 s[100:101]
s_add_u32 s100,s100,12
s_addc_u32 s101,s101,0
s_branch gm_KB_pg12
s_getpc_b64 s[100:101]
s_add_u32 s100,s100,12
s_addc_u32 s101,s101,0
s_branch gm_KB_pg16
s_branch gm_KB_wkk_e
gm_KB_wkk_1:
s_getpc_b64 s[100:101]
s_add_u32 s100,s100,12
s_addc_u32 s101,s101,0
s_branch gm_KB_pg2
s_getpc_b64 s[100:101]
s_add_u32 s100,s100,12
s_addc_u32 s101,s101,0
s_branch gm_KB_pg9
s_getpc_b64 s[100:101]
s_add_u32 s100,s100,12
s_addc_u32 s101,s101,0
)"
R"(s_branch gm_KB_pg13
s_getpc_b64 s[100:101]
s_add_u32 s100,s100,12
s_addc_u32 s101,s101,0
s_branch gm_KB_pg17
s_branch gm_KB_wkk_e
gm_KB_wkk_2:
s_getpc_b64 s[100:101]
s_add_u32 s100,s100,12
s_addc_u32 s101,s101,0
s_branch gm_KB_pg3
s_getpc_b64 s[100:101]
s_add_u32 s100,s100,12
s_addc_u32 s101,s101,0
s_branch gm_KB_pg10
s_getpc_b64 s[100:101]
s_add_u32 s100,s100,12
s_addc_u32 s101,s101,0
s_branch gm_KB_pg14
s_getpc_b64 s[100:101]
s_add_u32 s100,s100,12
s_addc_u32 s101,s101,0
s_branch gm_KB_pg18
s_branch gm_KB_wkk_e
)"
R"(gm_KB_wkk_3:
s_getpc_b64 s[100:101]
s_add_u32 s100,s100,12
s_addc_u32 s101,s101,0
s_branch gm_KB_pg4
s_getpc_b64 s[100:101]
s_add_u32 s100,s100,12
s_addc_u32 s101,s101,0
s_branch gm_KB_pg11
s_getpc_b64 s[100:101]
s_add_u32 s100,s100,12
s_addc_u32 s101,s101,0
s_branch gm_KB_pg15
s_getpc_b64 s[100:101]
s_add_u32 s100,s100,12
s_addc_u32 s101,s101,0
s_branch gm_KB_tp_last
gm_KB_wkk_e:
s_branch gm_KB_wkv_e
gm_KB_wkv:
s_cmp_eq_u32 %2,1
s_cbranch_scc1 gm_KB_wkv_1
s_cmp_eq_u32 %2,2
s_cbranch_scc1 gm_KB_wkv_2
)"
R"(s_cmp_eq_u32 %2,3
s_cbranch_scc1 gm_KB_wkv_3
s_getpc_b64 s[100:101]
s_add_u32 s100,s100,12
s_addc_u32 s101,s101,0
s_branch gm_KB_tp_v
s_getpc_b64 s[100:101]
s_add_u32 s100,s100,12
s_addc_u32 s101,s101,0
s_branch gm_KB_pg8
s_getpc_b64 s[100:101]
s_add_u32 s100,s100,12
s_addc_u32 s101,s101,0
s_branch gm_KB_pg12
s_getpc_b64 s[100:101]
s_add_u32 s100,s100,12
s_addc_u32 s101,s101,0
s_branch gm_KB_pg16
s_branch gm_KB_wkv_e
gm_KB_wkv_1:
s_getpc_b64 s[100:101]
s_add_u32 s100,s100,12
s_addc_u32 s101,s101,0
s_branch gm_KB_pg5
)"
R"(s_getpc_b64 s[100:101]
s_add_u32 s100,s100,12
s_addc_u32 s101,s101,0
s_branch gm_KB_pg9
s_getpc_b64 s[100:101]
s_add_u32 s100,s100,12
s_addc_u32 s101,s101,0
s_branch gm_KB_pg13
s_getpc_b64 s[100:101]
s_add_u32 s100,s100,12
s_addc_u32 s101,s101,0
s_branch gm_KB_pg17
s_branch gm_KB_wkv_e
gm_KB_wkv_2:
s_getpc_b64 s[100:101]
s_add_u32 s100,s100,12
s_addc_u32 s101,s101,0
s_branch gm_KB_pg6
s_getpc_b64 s[100:101]
s_add_u32 s100,s100,12
s_addc_u32 s101,s101,0
s_branch gm_KB_pg10
s_getpc_b64 s[100:101]
s_add_u32 s100,s100,12
)"
R"(s_addc_u32 s101,s101,0
s_branch gm_KB_pg14
s_getpc_b64 s[100:101]
s_add_u32 s100,s100,12
s_addc_u32 s101,s101,0
s_branch gm_KB_pg18
s_branch gm_KB_wkv_e
gm_KB_wkv_3:
s_getpc_b64 s[100:101]
s_add_u32 s100,s100,12
s_addc_u32 s101,s101,0
s_branch gm_KB_pg7
s_getpc_b64 s[100:101]
s_add_u32 s100,s100,12
s_addc_u32 s101,s101,0
s_branch gm_KB_pg11
s_getpc_b64 s[100:101]
s_add_u32 s100,s100,12
s_addc_u32 s101,s101,0
s_branch gm_KB_pg15
s_getpc_b64 s[100:101]
s_add_u32 s100,s100,12
s_addc_u32 s101,s101,0
s_branch gm_KB_tp_last
)"
R"(gm_KB_wkv_e:
s_mov_b32 s76,0xc0135761
s_mov_b32 s77,0xc0135761
s_mov_b32 s78,0xbdd2d3e8
s_mov_b32 s79,0xbdd2d3e8
s_mov_b32 s80,0x3e0293ee
s_mov_b32 s81,0x3e0293ee
v_lshlrev_b32 v18,4,v19
s_mov_b32 s59,0
s_and_b32 s61,s70,1
s_lshl_b32 s61,s61,14
s_add_u32 s61,s61,0x10000
s_lshr_b32 s62,s70,1
s_lshl_b32 s62,s62,14
v_and_b32 v22,15,v19
v_lshrrev_b32 v23,4,v19
v_lshrrev_b32 v20,1,v22
v_add_u32 v21,0,v23
v_xor_b32 v20,v20,v21
v_lshlrev_b32 v20,4,v20
v_lshl_add_u32 v20,v22,7,v20
v_add_u32 v0,s62,v20
v_lshrrev_b32 v20,1,v22
v_add_u32 v21,4,v23
)"
R"(v_xor_b32 v20,v20,v21
v_lshlrev_b32 v20,4,v20
v_lshl_add_u32 v20,v22,7,v20
v_add_u32 v1,s62,v20
v_bfe_u32 v20,v19,2,2
v_and_b32 v21,1,v23
v_lshl_or_b32 v21,v21,2,v20
v_lshl_add_u32 v20,v23,3,v20
v_lshlrev_b32 v20,8,v20
v_and_b32 v22,3,v19
v_lshl_add_u32 v20,v22,3,v20
v_add_u32 v20,s61,v20
v_xor_b32 v22,0,v21
v_lshl_add_u32 v2,v22,5,v20
v_xor_b32 v22,1,v21
v_lshl_add_u32 v3,v22,5,v20
v_xor_b32 v22,2,v21
v_lshl_add_u32 v4,v22,5,v20
v_xor_b32 v22,3,v21
v_lshl_add_u32 v5,v22,5,v20
v_xor_b32 v22,4,v21
v_lshl_add_u32 v6,v22,5,v20
v_xor_b32 v22,5,v21
v_lshl_add_u32 v7,v22,5,v20
)"
R"(v_xor_b32 v22,6,v21
v_lshl_add_u32 v8,v22,5,v20
v_xor_b32 v22,7,v21
v_lshl_add_u32 v9,v22,5,v20
s_add_u32 m0,s57,0
s_add_u32 s63,s55,0
buffer_load_dwordx4 v16,s[44:47],s63 offen lds
s_add_u32 m0,s57,1024
s_add_u32 s64,s55,32768
buffer_load_dwordx4 v16,s[44:47],s64 offen lds
s_add_u32 m0,s57,2048
s_add_u32 s63,s55,65536
buffer_load_dwordx4 v17,s[44:47],s63 offen lds
s_add_u32 m0,s57,3072
s_add_u32 s64,s55,98304
buffer_load_dwordx4 v17,s[44:47],s64 offen lds
s_add_u32 m0,s57,4096
s_add_u32 s63,s55,131072
buffer_load_dwordx4 v16,s[44:47],s63 offen lds
s_add_u32 m0,s57,5120
s_add_u32 s64,s55,163840
buffer_load_dwordx4 v16,s[44:47],s64 offen lds
s_add_u32 m0,s57,6144
s_add_u32 s63,s55,196608
)"
R"(buffer_load_dwordx4 v17,s[44:47],s63 offen lds
s_add_u32 m0,s57,7168
s_add_u32 s64,s55,229376
buffer_load_dwordx4 v17,s[44:47],s64 offen lds
s_add_u32 s57,s57,0x8000
s_cmp_ge_u32 s57,0x28000
s_cselect_b32 s65,0x18000,0
s_sub_u32 s57,s57,s65
s_add_u32 s55,s55,524288
s_cmp_eq_u32 s55,4194304
s_cselect_b32 s55,0,s55
s_add_u32 m0,s56,0
s_add_u32 s61,s53,0
buffer_load_dwordx4 v12,s[36:39],s61 offen lds
s_add_u32 m0,s56,1024
s_add_u32 s62,s53,8192
buffer_load_dwordx4 v13,s[36:39],s62 offen lds
s_add_u32 m0,s56,2048
s_add_u32 s61,s53,16384
buffer_load_dwordx4 v12,s[36:39],s61 offen lds
s_add_u32 m0,s56,3072
s_add_u32 s62,s53,24576
buffer_load_dwordx4 v13,s[36:39],s62 offen lds
s_add_u32 m0,s56,4096
)"
R"(s_add_u32 s61,s53,32768
buffer_load_dwordx4 v12,s[36:39],s61 offen lds
s_add_u32 m0,s56,5120
s_add_u32 s62,s53,40960
buffer_load_dwordx4 v13,s[36:39],s62 offen lds
s_add_u32 m0,s56,6144
s_add_u32 s61,s53,49152
buffer_load_dwordx4 v12,s[36:39],s61 offen lds
s_add_u32 m0,s56,7168
s_add_u32 s62,s53,57344
buffer_load_dwordx4 v13,s[36:39],s62 offen lds
s_xor_b32 s56,s56,0x8000
s_add_u32 s53,s53,128
s_cmp_eq_u32 s53,1024
s_cselect_b32 s53,0,s53
s_add_u32 s67,s73,s71
s_and_b32 s67,s67,7
s_lshl_b32 s67,s67,4
s_lshr_b32 s68,s69,3
s_lshl_b32 s68,s68,2
s_add_u32 s67,s67,s68
s_add_u32 s67,s67,s70
s_lshl_b32 s67,s67,6
s_add_u32 s67,s67,s73
)"
R"(s_lshl_b32 s67,s67,10
s_add_u32 s12,s20,s67
s_addc_u32 s13,s21,0
s_and_b32 s13,s13,0xffff
s_mov_b32 s14,58368
s_mov_b32 s15,0x20000
v_lshrrev_b32 v20,3,v19
v_lshlrev_b32 v20,13,v20
v_and_b32 v60,7,v19
v_lshl_add_u32 v60,v60,7,v20
s_mul_i32 s67,s71,8
s_lshr_b32 s67,s67,3
s_add_u32 s67,s67,0
s_lshl_b32 s67,s67,26
s_sub_u32 s87,0,s67
s_waitcnt vmcnt(16)
s_barrier
ds_read_b64_tr_b16 a[0:1],v2 offset:0
ds_read_b64_tr_b16 a[2:3],v2 offset:1024
ds_read_b64_tr_b16 a[32:33],v2 offset:8192
ds_read_b64_tr_b16 a[34:35],v2 offset:9216
ds_read_b128 a[128:131],v0 offset:8192
ds_read_b64_tr_b16 a[4:5],v3 offset:0
ds_read_b64_tr_b16 a[6:7],v3 offset:1024
)"
R"(ds_read_b64_tr_b16 a[36:37],v3 offset:8192
ds_read_b64_tr_b16 a[38:39],v3 offset:9216
ds_read_b128 a[144:147],v1 offset:8192
ds_read_b64_tr_b16 a[8:9],v4 offset:0
ds_read_b64_tr_b16 a[10:11],v4 offset:1024
ds_read_b64_tr_b16 a[40:41],v4 offset:8192
ds_read_b64_tr_b16 a[42:43],v4 offset:9216
ds_read_b128 a[132:135],v0 offset:10240
ds_read_b64_tr_b16 a[12:13],v5 offset:0
ds_read_b64_tr_b16 a[14:15],v5 offset:1024
ds_read_b64_tr_b16 a[44:45],v5 offset:8192
ds_read_b64_tr_b16 a[46:47],v5 offset:9216
ds_read_b128 a[148:151],v1 offset:10240
ds_read_b64_tr_b16 a[16:17],v6 offset:0
ds_read_b64_tr_b16 a[18:19],v6 offset:1024
ds_read_b64_tr_b16 a[48:49],v6 offset:8192
ds_read_b64_tr_b16 a[50:51],v6 offset:9216
ds_read_b128 a[136:139],v0 offset:12288
ds_read_b64_tr_b16 a[20:21],v7 offset:0
ds_read_b64_tr_b16 a[22:23],v7 offset:1024
ds_read_b64_tr_b16 a[52:53],v7 offset:8192
ds_read_b64_tr_b16 a[54:55],v7 offset:9216
ds_read_b128 a[152:155],v1 offset:12288
ds_read_b64_tr_b16 a[24:25],v8 offset:0
)"
R"(ds_read_b64_tr_b16 a[26:27],v8 offset:1024
ds_read_b64_tr_b16 a[56:57],v8 offset:8192
ds_read_b64_tr_b16 a[58:59],v8 offset:9216
ds_read_b128 a[140:143],v0 offset:14336
ds_read_b64_tr_b16 a[28:29],v9 offset:0
ds_read_b64_tr_b16 a[30:31],v9 offset:1024
ds_read_b64_tr_b16 a[60:61],v9 offset:8192
ds_read_b64_tr_b16 a[62:63],v9 offset:9216
ds_read_b128 a[156:159],v1 offset:14336
s_waitcnt lgkmcnt(0)
s_cmp_ge_u32 s69,16
s_cbranch_scc1 gm_K1_role_v
v_mfma_f32_16x16x32_bf16 v[192:195],a[0:3],a[128:131],0
s_add_u32 m0,s57,0
s_add_u32 s63,s55,0
ds_read_b128 a[160:163],v0 offset:0
v_mfma_f32_16x16x32_bf16 v[192:195],a[32:35],a[144:147],v[192:195]
buffer_load_dwordx4 v16,s[44:47],s63 offen lds
ds_read_b128 a[176:179],v1 offset:0
v_mfma_f32_16x16x32_bf16 v[196:199],a[4:7],a[128:131],0
s_add_u32 s66,s59,0x8000
s_cmp_eq_u32 s66,0x18000
ds_read_b128 a[164:167],v0 offset:2048
v_mfma_f32_16x16x32_bf16 v[196:199],a[36:39],a[144:147],v[196:199]
)"
R"(s_cselect_b32 s66,0,s66
ds_read_b128 a[180:183],v1 offset:2048
v_mfma_f32_16x16x32_bf16 v[200:203],a[8:11],a[128:131],0
s_sub_u32 s58,s66,s59
s_mov_b32 s59,s66
ds_read_b128 a[168:171],v0 offset:4096
v_mfma_f32_16x16x32_bf16 v[200:203],a[40:43],a[144:147],v[200:203]
s_add_u32 m0,s57,1024
v_mfma_f32_16x16x32_bf16 v[204:207],a[12:15],a[128:131],0
s_add_u32 s64,s55,32768
buffer_load_dwordx4 v16,s[44:47],s64 offen lds
ds_read_b128 a[184:187],v1 offset:4096
v_mfma_f32_16x16x32_bf16 v[204:207],a[44:47],a[144:147],v[204:207]
s_add_u32 m0,s57,2048
ds_read_b128 a[172:175],v0 offset:6144
v_mfma_f32_16x16x32_bf16 v[208:211],a[16:19],a[128:131],0
s_add_u32 s63,s55,65536
buffer_load_dwordx4 v17,s[44:47],s63 offen lds
ds_read_b128 a[188:191],v1 offset:6144
v_mfma_f32_16x16x32_bf16 v[208:211],a[48:51],a[144:147],v[208:211]
s_add_u32 m0,s57,3072
v_mfma_f32_16x16x32_bf16 v[212:215],a[20:23],a[128:131],0
s_add_u32 s64,s55,98304
buffer_load_dwordx4 v17,s[44:47],s64 offen lds
)"
R"(v_mfma_f32_16x16x32_bf16 v[212:215],a[52:55],a[144:147],v[212:215]
s_add_u32 m0,s57,4096
v_mfma_f32_16x16x32_bf16 v[216:219],a[24:27],a[128:131],0
s_add_u32 s63,s55,131072
buffer_load_dwordx4 v16,s[44:47],s63 offen lds
v_mfma_f32_16x16x32_bf16 v[216:219],a[56:59],a[144:147],v[216:219]
s_add_u32 m0,s57,5120
v_mfma_f32_16x16x32_bf16 v[220:223],a[28:31],a[128:131],0
s_add_u32 s64,s55,163840
buffer_load_dwordx4 v16,s[44:47],s64 offen lds
v_mfma_f32_16x16x32_bf16 v[220:223],a[60:63],a[144:147],v[220:223]
s_add_u32 m0,s57,6144
v_mfma_f32_16x16x32_bf16 v[252:255],a[28:31],a[132:135],0
s_add_u32 s63,s55,196608
buffer_load_dwordx4 v17,s[44:47],s63 offen lds
v_mfma_f32_16x16x32_bf16 v[252:255],a[60:63],a[148:151],v[252:255]
s_add_u32 m0,s57,7168
v_mfma_f32_16x16x32_bf16 v[248:251],a[24:27],a[132:135],0
s_add_u32 s64,s55,229376
buffer_load_dwordx4 v17,s[44:47],s64 offen lds
v_mfma_f32_16x16x32_bf16 v[248:251],a[56:59],a[148:151],v[248:251]
s_add_u32 s57,s57,0x8000
v_mfma_f32_16x16x32_bf16 v[244:247],a[20:23],a[132:135],0
s_cmp_ge_u32 s57,0x28000
)"
R"(s_cselect_b32 s65,0x18000,0
v_mfma_f32_16x16x32_bf16 v[244:247],a[52:55],a[148:151],v[244:247]
s_sub_u32 s57,s57,s65
v_mfma_f32_16x16x32_bf16 v[240:243],a[16:19],a[132:135],0
s_add_u32 s55,s55,524288
s_cmp_eq_u32 s55,4194304
v_mfma_f32_16x16x32_bf16 v[240:243],a[48:51],a[148:151],v[240:243]
s_cselect_b32 s55,0,s55
v_mfma_f32_16x16x32_bf16 v[236:239],a[12:15],a[132:135],0
s_lshl_b32 s61,%1,2
s_add_u32 s61,s61,s70
v_mfma_f32_16x16x32_bf16 v[236:239],a[44:47],a[148:151],v[236:239]
s_lshl_b32 s84,s61,3
v_mfma_f32_16x16x32_bf16 v[232:235],a[8:11],a[132:135],0
s_lshl_b32 s61,s61,12
s_add_u32 s88,s16,s61
v_mfma_f32_16x16x32_bf16 v[232:235],a[40:43],a[148:151],v[232:235]
s_addc_u32 s89,s17,0
v_mfma_f32_16x16x32_bf16 v[228:231],a[4:7],a[132:135],0
s_and_b32 s89,s89,0xffff
v_mfma_f32_16x16x32_bf16 v[228:231],a[36:39],a[148:151],v[228:231]
s_sub_u32 s90,4194304,s61
s_mov_b32 s91,0x20000
v_mfma_f32_16x16x32_bf16 v[224:227],a[0:3],a[132:135],0
)"
R"(buffer_load_dwordx4 v[32:35],v18,s[88:91],0 offen offset:0
v_mfma_f32_16x16x32_bf16 v[224:227],a[32:35],a[148:151],v[224:227]
buffer_load_dwordx4 v[36:39],v18,s[88:91],0 offen offset:1024
buffer_load_dwordx4 v[40:43],v18,s[88:91],0 offen offset:2048
v_mfma_f32_16x16x32_bf16 a[192:195],a[0:3],a[136:139],0
buffer_load_dwordx4 v[44:47],v18,s[88:91],0 offen offset:3072
v_mfma_f32_16x16x32_bf16 a[192:195],a[32:35],a[152:155],a[192:195]
s_mov_b32 s61,0
s_add_u32 s92,s98,s61
v_mfma_f32_16x16x32_bf16 a[196:199],a[4:7],a[136:139],0
s_addc_u32 s93,s99,0
v_mfma_f32_16x16x32_bf16 a[196:199],a[36:39],a[152:155],a[196:199]
s_and_b32 s93,s93,0xffff
s_sub_u32 s94,2048,s61
v_mfma_f32_16x16x32_bf16 a[200:203],a[8:11],a[136:139],0
s_mov_b32 s95,0x20000
v_mfma_f32_16x16x32_bf16 a[200:203],a[40:43],a[152:155],a[200:203]
buffer_load_dwordx4 v[48:51],v18,s[92:95],0 offen offset:0
buffer_load_dwordx4 v[52:55],v18,s[92:95],0 offen offset:1024
v_mfma_f32_16x16x32_bf16 a[204:207],a[12:15],a[136:139],0
s_mov_b32 s61,100663296
v_mfma_f32_16x16x32_bf16 a[204:207],a[44:47],a[152:155],a[204:207]
s_add_u32 s4,s30,s61
s_addc_u32 s5,s31,0
)"
R"(v_mfma_f32_16x16x32_bf16 a[208:211],a[16:19],a[136:139],0
s_and_b32 s5,s5,0xffff
v_mfma_f32_16x16x32_bf16 a[208:211],a[48:51],a[152:155],a[208:211]
s_mov_b32 s6,65536
s_mov_b32 s7,0x20000
v_mfma_f32_16x16x32_bf16 a[212:215],a[20:23],a[136:139],0
v_mov_b32 v28,0
v_mfma_f32_16x16x32_bf16 a[212:215],a[52:55],a[152:155],a[212:215]
s_mov_b32 s61,16384
buffer_load_dword v28,v28,s[4:7],s61 offen sc1
v_mfma_f32_16x16x32_bf16 a[216:219],a[24:27],a[136:139],0
s_and_b32 s67,s70,1
v_xor_b32 v0,0x8000,v0
v_mfma_f32_16x16x32_bf16 a[216:219],a[56:59],a[152:155],a[216:219]
s_lshl_b32 s68,s74,1
s_add_u32 s67,s67,s68
v_xor_b32 v1,0x8000,v1
v_mfma_f32_16x16x32_bf16 a[220:223],a[28:31],a[136:139],0
s_lshl_b32 s67,s67,4
v_add_u32 v2,s58,v2
v_mfma_f32_16x16x32_bf16 a[220:223],a[60:63],a[152:155],a[220:223]
s_lshr_b32 s68,s70,1
s_add_u32 s67,s67,s68
v_add_u32 v3,s58,v3
)"
R"(v_mfma_f32_16x16x32_bf16 a[252:255],a[28:31],a[140:143],0
s_lshl_b32 s68,s71,1
v_add_u32 v4,s58,v4
v_mfma_f32_16x16x32_bf16 a[252:255],a[60:63],a[156:159],a[252:255]
s_add_u32 s67,s67,s68
s_lshl_b32 s67,s67,15
v_add_u32 v5,s58,v5
v_mfma_f32_16x16x32_bf16 a[248:251],a[24:27],a[140:143],0
s_add_u32 s67,s67,16777216
v_add_u32 v6,s58,v6
v_mfma_f32_16x16x32_bf16 a[248:251],a[56:59],a[156:159],a[248:251]
s_add_u32 s48,s30,s67
s_addc_u32 s49,s31,0
v_add_u32 v7,s58,v7
v_mfma_f32_16x16x32_bf16 a[244:247],a[20:23],a[140:143],0
s_and_b32 s49,s49,0xffff
v_add_u32 v8,s58,v8
v_mfma_f32_16x16x32_bf16 a[244:247],a[52:55],a[156:159],a[244:247]
s_sub_u32 s50,33554432,s67
s_mov_b32 s51,0x20000
v_add_u32 v9,s58,v9
v_mfma_f32_16x16x32_bf16 a[240:243],a[16:19],a[140:143],0
s_cmp_lt_u32 s69,16
v_mfma_f32_16x16x32_bf16 a[240:243],a[48:51],a[156:159],a[240:243]
)"
R"(s_cselect_b32 s61,0,16777216
s_add_u32 s48,s48,s61
v_mfma_f32_16x16x32_bf16 a[236:239],a[12:15],a[140:143],0
s_addc_u32 s49,s49,0
v_mfma_f32_16x16x32_bf16 a[236:239],a[44:47],a[156:159],a[236:239]
s_mov_b32 s50,32768
v_mfma_f32_16x16x32_bf16 a[232:235],a[8:11],a[140:143],0
v_mfma_f32_16x16x32_bf16 a[232:235],a[40:43],a[156:159],a[232:235]
v_mfma_f32_16x16x32_bf16 a[228:231],a[4:7],a[140:143],0
v_mfma_f32_16x16x32_bf16 a[228:231],a[36:39],a[156:159],a[228:231]
v_mfma_f32_16x16x32_bf16 a[224:227],a[0:3],a[140:143],0
s_waitcnt vmcnt(15) lgkmcnt(0)
v_mfma_f32_16x16x32_bf16 a[224:227],a[32:35],a[156:159],a[224:227]
s_barrier
v_mfma_f32_16x16x32_bf16 v[64:67],a[0:3],a[160:163],0
s_add_u32 m0,s56,0
s_add_u32 s61,s53,0
ds_read_b64_tr_b16 a[64:65],v2 offset:0
v_mfma_f32_16x16x32_bf16 v[64:67],a[32:35],a[176:179],v[64:67]
buffer_load_dwordx4 v12,s[36:39],s61 offen lds
ds_read_b64_tr_b16 a[66:67],v2 offset:1024
v_mfma_f32_16x16x32_bf16 v[68:71],a[4:7],a[160:163],0
s_add_u32 m0,s56,1024
ds_read_b64_tr_b16 a[96:97],v2 offset:8192
)"
R"(v_mfma_f32_16x16x32_bf16 v[68:71],a[36:39],a[176:179],v[68:71]
s_add_u32 s62,s53,8192
buffer_load_dwordx4 v13,s[36:39],s62 offen lds
ds_read_b64_tr_b16 a[98:99],v2 offset:9216
v_mfma_f32_16x16x32_bf16 v[72:75],a[8:11],a[160:163],0
s_add_u32 m0,s56,2048
ds_read_b128 a[128:131],v0 offset:8192
v_mfma_f32_16x16x32_bf16 v[72:75],a[40:43],a[176:179],v[72:75]
s_add_u32 s61,s53,16384
ds_read_b64_tr_b16 a[68:69],v3 offset:0
s_branch gm_KB_sk1
.p2align 12
gm_KB_pg1:
s_setpc_b64 s[100:101]
gm_KB_sk1:
v_mfma_f32_16x16x32_bf16 v[76:79],a[12:15],a[160:163],0
buffer_load_dwordx4 v12,s[36:39],s61 offen lds
ds_read_b64_tr_b16 a[70:71],v3 offset:1024
v_mfma_f32_16x16x32_bf16 v[76:79],a[44:47],a[176:179],v[76:79]
s_add_u32 m0,s56,3072
s_add_u32 s62,s53,24576
ds_read_b64_tr_b16 a[100:101],v3 offset:8192
v_mfma_f32_16x16x32_bf16 v[80:83],a[16:19],a[160:163],0
buffer_load_dwordx4 v13,s[36:39],s62 offen lds
)"
R"(ds_read_b64_tr_b16 a[102:103],v3 offset:9216
v_mfma_f32_16x16x32_bf16 v[80:83],a[48:51],a[176:179],v[80:83]
s_add_u32 m0,s56,4096
s_add_u32 s61,s53,32768
ds_read_b128 a[144:147],v1 offset:8192
v_mfma_f32_16x16x32_bf16 v[84:87],a[20:23],a[160:163],0
buffer_load_dwordx4 v12,s[36:39],s61 offen lds
ds_read_b64_tr_b16 a[72:73],v4 offset:0
v_mfma_f32_16x16x32_bf16 v[84:87],a[52:55],a[176:179],v[84:87]
s_add_u32 m0,s56,5120
ds_read_b64_tr_b16 a[74:75],v4 offset:1024
v_mfma_f32_16x16x32_bf16 v[88:91],a[24:27],a[160:163],0
s_add_u32 s62,s53,40960
buffer_load_dwordx4 v13,s[36:39],s62 offen lds
ds_read_b64_tr_b16 a[104:105],v4 offset:8192
v_mfma_f32_16x16x32_bf16 v[88:91],a[56:59],a[176:179],v[88:91]
s_add_u32 m0,s56,6144
ds_read_b64_tr_b16 a[106:107],v4 offset:9216
v_mfma_f32_16x16x32_bf16 v[92:95],a[28:31],a[160:163],0
s_add_u32 s61,s53,49152
ds_read_b128 a[132:135],v0 offset:10240
v_mfma_f32_16x16x32_bf16 v[92:95],a[60:63],a[176:179],v[92:95]
buffer_load_dwordx4 v12,s[36:39],s61 offen lds
ds_read_b64_tr_b16 a[76:77],v5 offset:0
)"
R"(v_mfma_f32_16x16x32_bf16 v[124:127],a[28:31],a[164:167],0
s_add_u32 m0,s56,7168
s_add_u32 s62,s53,57344
ds_read_b64_tr_b16 a[78:79],v5 offset:1024
v_mfma_f32_16x16x32_bf16 v[124:127],a[60:63],a[180:183],v[124:127]
buffer_load_dwordx4 v13,s[36:39],s62 offen lds
ds_read_b64_tr_b16 a[108:109],v5 offset:8192
v_mfma_f32_16x16x32_bf16 v[120:123],a[24:27],a[164:167],0
buffer_load_dword v61,v60,s[12:15],s87 offen
ds_read_b64_tr_b16 a[110:111],v5 offset:9216
v_mfma_f32_16x16x32_bf16 v[120:123],a[56:59],a[180:183],v[120:123]
s_add_u32 s87,s87,67108864
v_lshrrev_b32 v20,4,v19
ds_read_b128 a[148:151],v1 offset:10240
v_mfma_f32_16x16x32_bf16 v[116:119],a[20:23],a[164:167],0
v_add_u32 v20,0,v20
ds_read_b64_tr_b16 a[80:81],v6 offset:0
v_mfma_f32_16x16x32_bf16 v[116:119],a[52:55],a[180:183],v[116:119]
v_and_b32 v21,7,v19
ds_read_b64_tr_b16 a[82:83],v6 offset:1024
v_mfma_f32_16x16x32_bf16 v[112:115],a[16:19],a[164:167],0
v_xor_b32 v20,v20,v21
ds_read_b64_tr_b16 a[112:113],v6 offset:8192
v_mfma_f32_16x16x32_bf16 v[112:115],a[48:51],a[180:183],v[112:115]
)"
R"(v_lshlrev_b32 v20,4,v20
ds_read_b64_tr_b16 a[114:115],v6 offset:9216
v_mfma_f32_16x16x32_bf16 v[108:111],a[12:15],a[164:167],0
v_lshrrev_b32 v21,3,v19
ds_read_b128 a[136:139],v0 offset:12288
v_mfma_f32_16x16x32_bf16 v[108:111],a[44:47],a[180:183],v[108:111]
v_mul_u32_u24 v21,2048,v21
ds_read_b64_tr_b16 a[84:85],v7 offset:0
v_mfma_f32_16x16x32_bf16 v[104:107],a[8:11],a[164:167],0
v_add_u32 v10,v20,v21
ds_read_b64_tr_b16 a[86:87],v7 offset:1024
v_mfma_f32_16x16x32_bf16 v[104:107],a[40:43],a[180:183],v[104:107]
v_lshrrev_b32 v20,4,v19
ds_read_b64_tr_b16 a[116:117],v7 offset:8192
v_mfma_f32_16x16x32_bf16 v[100:103],a[4:7],a[164:167],0
v_or_b32 v20,0,v20
ds_read_b64_tr_b16 a[118:119],v7 offset:9216
v_mfma_f32_16x16x32_bf16 v[100:103],a[36:39],a[180:183],v[100:103]
v_bfe_u32 v21,v19,1,3
ds_read_b128 a[152:155],v1 offset:12288
v_mfma_f32_16x16x32_bf16 v[96:99],a[0:3],a[164:167],0
v_xor_b32 v20,v20,v21
ds_read_b64_tr_b16 a[88:89],v8 offset:0
v_mfma_f32_16x16x32_bf16 v[96:99],a[32:35],a[180:183],v[96:99]
)"
R"(v_add_u32 v22,1,v20
ds_read_b64_tr_b16 a[90:91],v8 offset:1024
v_mfma_f32_16x16x32_bf16 v[128:131],a[0:3],a[168:171],0
v_lshrrev_b32 v22,3,v22
ds_read_b64_tr_b16 a[120:121],v8 offset:8192
v_mfma_f32_16x16x32_bf16 v[128:131],a[32:35],a[184:187],v[128:131]
s_and_b32 s63,s86,1
v_add_u32 v20,s63,v20
ds_read_b64_tr_b16 a[122:123],v8 offset:9216
v_mfma_f32_16x16x32_bf16 v[132:135],a[4:7],a[168:171],0
v_cmp_ne_u32 vcc,7,v20
ds_read_b128 a[140:143],v0 offset:14336
v_mfma_f32_16x16x32_bf16 v[132:135],a[36:39],a[184:187],v[132:135]
v_cndmask_b32 v20,0,v20,vcc
ds_read_b64_tr_b16 a[92:93],v9 offset:0
v_mfma_f32_16x16x32_bf16 v[136:139],a[8:11],a[168:171],0
v_lshl_add_u32 v20,v22,25,v20
ds_read_b64_tr_b16 a[94:95],v9 offset:1024
v_mfma_f32_16x16x32_bf16 v[136:139],a[40:43],a[184:187],v[136:139]
v_lshlrev_b32 v20,5,v20
ds_read_b64_tr_b16 a[124:125],v9 offset:8192
v_mfma_f32_16x16x32_bf16 v[140:143],a[12:15],a[168:171],0
v_and_b32 v21,1,v19
ds_read_b64_tr_b16 a[126:127],v9 offset:9216
)"
R"(v_mfma_f32_16x16x32_bf16 v[140:143],a[44:47],a[184:187],v[140:143]
v_lshl_add_u32 v20,v21,4,v20
ds_read_b128 a[156:159],v1 offset:14336
v_mfma_f32_16x16x32_bf16 v[144:147],a[16:19],a[168:171],0
v_lshrrev_b32 v21,4,v19
v_mul_u32_u24 v21,7168,v21
v_mfma_f32_16x16x32_bf16 v[144:147],a[48:51],a[184:187],v[144:147]
v_add_u32 v14,v20,v21
v_lshrrev_b32 v20,4,v19
v_mfma_f32_16x16x32_bf16 v[148:151],a[20:23],a[168:171],0
v_add_u32 v20,4,v20
v_and_b32 v21,7,v19
v_mfma_f32_16x16x32_bf16 v[148:151],a[52:55],a[184:187],v[148:151]
v_xor_b32 v20,v20,v21
v_lshlrev_b32 v20,4,v20
v_mfma_f32_16x16x32_bf16 v[152:155],a[24:27],a[168:171],0
v_lshrrev_b32 v21,3,v19
v_mul_u32_u24 v21,2048,v21
v_mfma_f32_16x16x32_bf16 v[152:155],a[56:59],a[184:187],v[152:155]
v_add_u32 v11,v20,v21
v_lshrrev_b32 v20,4,v19
v_mfma_f32_16x16x32_bf16 v[156:159],a[28:31],a[168:171],0
v_or_b32 v20,4,v20
v_bfe_u32 v21,v19,1,3
)"
R"(v_mfma_f32_16x16x32_bf16 v[156:159],a[60:63],a[184:187],v[156:159]
v_xor_b32 v20,v20,v21
v_add_u32 v22,1,v20
v_mfma_f32_16x16x32_bf16 v[188:191],a[28:31],a[172:175],0
v_lshrrev_b32 v22,3,v22
s_and_b32 s63,s86,1
v_mfma_f32_16x16x32_bf16 v[188:191],a[60:63],a[188:191],v[188:191]
v_add_u32 v20,s63,v20
v_mfma_f32_16x16x32_bf16 v[184:187],a[24:27],a[172:175],0
v_cmp_ne_u32 vcc,7,v20
v_mfma_f32_16x16x32_bf16 v[184:187],a[56:59],a[188:191],v[184:187]
s_nop 0
v_cndmask_b32 v20,0,v20,vcc
v_lshl_add_u32 v20,v22,25,v20
v_mfma_f32_16x16x32_bf16 v[180:183],a[20:23],a[172:175],0
v_lshlrev_b32 v20,5,v20
v_mfma_f32_16x16x32_bf16 v[180:183],a[52:55],a[188:191],v[180:183]
v_and_b32 v21,1,v19
v_mfma_f32_16x16x32_bf16 v[176:179],a[16:19],a[172:175],0
v_lshl_add_u32 v20,v21,4,v20
v_lshrrev_b32 v21,4,v19
v_mfma_f32_16x16x32_bf16 v[176:179],a[48:51],a[188:191],v[176:179]
v_mul_u32_u24 v21,7168,v21
v_mfma_f32_16x16x32_bf16 v[172:175],a[12:15],a[172:175],0
)"
R"(v_add_u32 v15,v20,v21
v_mfma_f32_16x16x32_bf16 v[172:175],a[44:47],a[188:191],v[172:175]
s_xor_b32 s56,s56,0x8000
s_add_u32 s53,s53,128
v_mfma_f32_16x16x32_bf16 v[168:171],a[8:11],a[172:175],0
s_cmp_eq_u32 s53,1024
v_mfma_f32_16x16x32_bf16 v[168:171],a[40:43],a[188:191],v[168:171]
s_cselect_b32 s53,0,s53
v_mfma_f32_16x16x32_bf16 v[164:167],a[4:7],a[172:175],0
v_mfma_f32_16x16x32_bf16 v[164:167],a[36:39],a[188:191],v[164:167]
v_mfma_f32_16x16x32_bf16 v[160:163],a[0:3],a[172:175],0
s_waitcnt lgkmcnt(0)
v_mfma_f32_16x16x32_bf16 v[160:163],a[32:35],a[188:191],v[160:163]
v_mfma_f32_16x16x32_bf16 v[192:195],a[64:67],a[128:131],v[192:195]
s_add_u32 m0,s57,0
s_add_u32 s63,s55,0
ds_read_b128 a[160:163],v0 offset:0
v_mfma_f32_16x16x32_bf16 v[192:195],a[96:99],a[144:147],v[192:195]
buffer_load_dwordx4 v16,s[44:47],s63 offen lds
ds_read_b128 a[176:179],v1 offset:0
v_mfma_f32_16x16x32_bf16 v[196:199],a[68:71],a[128:131],v[196:199]
s_add_u32 s66,s59,0x8000
ds_read_b128 a[164:167],v0 offset:2048
v_mfma_f32_16x16x32_bf16 v[196:199],a[100:103],a[144:147],v[196:199]
)"
R"(s_cmp_eq_u32 s66,0x18000
s_cselect_b32 s66,0,s66
ds_read_b128 a[180:183],v1 offset:2048
v_mfma_f32_16x16x32_bf16 v[200:203],a[72:75],a[128:131],v[200:203]
s_sub_u32 s58,s66,s59
ds_read_b128 a[168:171],v0 offset:4096
v_mfma_f32_16x16x32_bf16 v[200:203],a[104:107],a[144:147],v[200:203]
s_mov_b32 s59,s66
v_mfma_f32_16x16x32_bf16 v[204:207],a[76:79],a[128:131],v[204:207]
s_add_u32 m0,s57,1024
s_add_u32 s64,s55,32768
ds_read_b128 a[184:187],v1 offset:4096
v_mfma_f32_16x16x32_bf16 v[204:207],a[108:111],a[144:147],v[204:207]
buffer_load_dwordx4 v16,s[44:47],s64 offen lds
ds_read_b128 a[172:175],v0 offset:6144
v_mfma_f32_16x16x32_bf16 v[208:211],a[80:83],a[128:131],v[208:211]
s_add_u32 m0,s57,2048
ds_read_b128 a[188:191],v1 offset:6144
v_mfma_f32_16x16x32_bf16 v[208:211],a[112:115],a[144:147],v[208:211]
s_add_u32 s63,s55,65536
buffer_load_dwordx4 v17,s[44:47],s63 offen lds
v_mfma_f32_16x16x32_bf16 v[212:215],a[84:87],a[128:131],v[212:215]
s_add_u32 m0,s57,3072
v_mfma_f32_16x16x32_bf16 v[212:215],a[116:119],a[144:147],v[212:215]
)"
R"(s_add_u32 s64,s55,98304
v_mfma_f32_16x16x32_bf16 v[216:219],a[88:91],a[128:131],v[216:219]
buffer_load_dwordx4 v17,s[44:47],s64 offen lds
s_add_u32 m0,s57,4096
v_mfma_f32_16x16x32_bf16 v[216:219],a[120:123],a[144:147],v[216:219]
s_add_u32 s63,s55,131072
v_mfma_f32_16x16x32_bf16 v[220:223],a[92:95],a[128:131],v[220:223]
buffer_load_dwordx4 v16,s[44:47],s63 offen lds
v_mfma_f32_16x16x32_bf16 v[220:223],a[124:127],a[144:147],v[220:223]
s_add_u32 m0,s57,5120
s_add_u32 s64,s55,163840
v_mfma_f32_16x16x32_bf16 v[252:255],a[92:95],a[132:135],v[252:255]
buffer_load_dwordx4 v16,s[44:47],s64 offen lds
v_mfma_f32_16x16x32_bf16 v[252:255],a[124:127],a[148:151],v[252:255]
s_add_u32 m0,s57,6144
v_mfma_f32_16x16x32_bf16 v[248:251],a[88:91],a[132:135],v[248:251]
s_add_u32 s63,s55,196608
buffer_load_dwordx4 v17,s[44:47],s63 offen lds
v_mfma_f32_16x16x32_bf16 v[248:251],a[120:123],a[148:151],v[248:251]
s_add_u32 m0,s57,7168
v_mfma_f32_16x16x32_bf16 v[244:247],a[84:87],a[132:135],v[244:247]
s_add_u32 s64,s55,229376
v_mfma_f32_16x16x32_bf16 v[244:247],a[116:119],a[148:151],v[244:247]
buffer_load_dwordx4 v17,s[44:47],s64 offen lds
)"
R"(s_add_u32 s57,s57,0x8000
v_mfma_f32_16x16x32_bf16 v[240:243],a[80:83],a[132:135],v[240:243]
s_cmp_ge_u32 s57,0x28000
v_mfma_f32_16x16x32_bf16 v[240:243],a[112:115],a[148:151],v[240:243]
s_cselect_b32 s65,0x18000,0
v_mfma_f32_16x16x32_bf16 v[236:239],a[76:79],a[132:135],v[236:239]
s_sub_u32 s57,s57,s65
s_add_u32 s55,s55,524288
v_mfma_f32_16x16x32_bf16 v[236:239],a[108:111],a[148:151],v[236:239]
s_cmp_eq_u32 s55,4194304
v_mfma_f32_16x16x32_bf16 v[232:235],a[72:75],a[132:135],v[232:235]
s_cselect_b32 s55,0,s55
v_mfma_f32_16x16x32_bf16 v[232:235],a[104:107],a[148:151],v[232:235]
s_lshl_b32 s67,s71,2
s_add_u32 s67,s67,s70
v_mfma_f32_16x16x32_bf16 v[228:231],a[68:71],a[132:135],v[228:231]
s_mul_i32 s67,s67,131072
v_mfma_f32_16x16x32_bf16 v[228:231],a[100:103],a[148:151],v[228:231]
s_and_b32 s68,s70,1
v_mfma_f32_16x16x32_bf16 v[224:227],a[64:67],a[132:135],v[224:227]
s_mul_i32 s68,s68,229376
s_mul_i32 s62,s86,224
v_mfma_f32_16x16x32_bf16 v[224:227],a[96:99],a[148:151],v[224:227]
s_add_u32 s68,s68,s62
)"
R"(v_mfma_f32_16x16x32_bf16 a[192:195],a[64:67],a[136:139],a[192:195]
s_mul_i32 s52,s73,256
v_mfma_f32_16x16x32_bf16 a[192:195],a[96:99],a[152:155],a[192:195]
s_mul_i32 s54,s73,917504
s_cmp_lt_u32 s70,2
v_mfma_f32_16x16x32_bf16 a[196:199],a[68:71],a[136:139],a[196:199]
s_cselect_b64 s[64:65],s[26:27],s[28:29]
v_mfma_f32_16x16x32_bf16 a[196:199],a[100:103],a[152:155],a[196:199]
s_add_u32 s40,s64,s68
v_mfma_f32_16x16x32_bf16 a[200:203],a[72:75],a[136:139],a[200:203]
s_addc_u32 s41,s65,0
s_and_b32 s41,s41,0xffff
v_mfma_f32_16x16x32_bf16 a[200:203],a[104:107],a[152:155],a[200:203]
s_sub_u32 s42,7340032,s68
v_mfma_f32_16x16x32_bf16 a[204:207],a[76:79],a[136:139],a[204:207]
s_mov_b32 s43,0x20000
v_mfma_f32_16x16x32_bf16 a[204:207],a[108:111],a[152:155],a[204:207]
s_add_u32 s32,s16,s67
s_addc_u32 s33,s17,0
v_mfma_f32_16x16x32_bf16 a[208:211],a[80:83],a[136:139],a[208:211]
s_and_b32 s33,s33,0xffff
v_mfma_f32_16x16x32_bf16 a[208:211],a[112:115],a[152:155],a[208:211]
s_sub_u32 s34,4194304,s67
v_mfma_f32_16x16x32_bf16 a[212:215],a[84:87],a[136:139],a[212:215]
)"
R"(s_mov_b32 s35,0x20000
s_lshl_b32 s67,s71,3
v_mfma_f32_16x16x32_bf16 a[212:215],a[116:119],a[152:155],a[212:215]
s_add_u32 s67,s67,s70
v_mfma_f32_16x16x32_bf16 a[216:219],a[88:91],a[136:139],a[216:219]
s_lshl_b32 s67,s67,15
v_xor_b32 v0,0x8000,v0
v_mfma_f32_16x16x32_bf16 a[216:219],a[120:123],a[152:155],a[216:219]
s_add_u32 s88,s16,s67
s_addc_u32 s89,s17,0
v_xor_b32 v1,0x8000,v1
v_mfma_f32_16x16x32_bf16 a[220:223],a[92:95],a[136:139],a[220:223]
s_and_b32 s89,s89,0xffff
v_add_u32 v2,s58,v2
v_mfma_f32_16x16x32_bf16 a[220:223],a[124:127],a[152:155],a[220:223]
s_sub_u32 s90,4194304,s67
v_add_u32 v3,s58,v3
v_mfma_f32_16x16x32_bf16 a[252:255],a[92:95],a[140:143],a[252:255]
s_mov_b32 s91,0x20000
s_lshl_b32 s67,s70,17
v_add_u32 v4,s58,v4
v_mfma_f32_16x16x32_bf16 a[252:255],a[124:127],a[156:159],a[252:255]
s_lshl_b32 s68,s86,8
v_add_u32 v5,s58,v5
)"
R"(v_mfma_f32_16x16x32_bf16 a[248:251],a[88:91],a[140:143],a[248:251]
s_add_u32 s67,s67,s68
v_add_u32 v6,s58,v6
v_mfma_f32_16x16x32_bf16 a[248:251],a[120:123],a[156:159],a[248:251]
s_add_u32 s92,s20,s67
s_addc_u32 s93,s21,0
v_add_u32 v7,s58,v7
v_mfma_f32_16x16x32_bf16 a[244:247],a[84:87],a[140:143],a[244:247]
s_and_b32 s93,s93,0xffff
v_add_u32 v8,s58,v8
v_mfma_f32_16x16x32_bf16 a[244:247],a[116:119],a[156:159],a[244:247]
s_sub_u32 s94,8388608,s67
v_add_u32 v9,s58,v9
v_mfma_f32_16x16x32_bf16 a[240:243],a[80:83],a[140:143],a[240:243]
s_mov_b32 s95,0x20000
s_and_b32 s61,s70,1
v_mfma_f32_16x16x32_bf16 a[240:243],a[112:115],a[156:159],a[240:243]
s_lshl_b32 s98,s61,13
v_mfma_f32_16x16x32_bf16 a[236:239],a[76:79],a[140:143],a[236:239]
s_lshl_b32 s61,s61,14
v_mfma_f32_16x16x32_bf16 a[236:239],a[108:111],a[156:159],a[236:239]
s_sub_u32 s75,0,s61
v_mfma_f32_16x16x32_bf16 a[232:235],a[72:75],a[140:143],a[232:235]
v_mfma_f32_16x16x32_bf16 a[232:235],a[104:107],a[156:159],a[232:235]
)"
R"(v_mfma_f32_16x16x32_bf16 a[228:231],a[68:71],a[140:143],a[228:231]
v_mfma_f32_16x16x32_bf16 a[228:231],a[100:103],a[156:159],a[228:231]
v_mfma_f32_16x16x32_bf16 a[224:227],a[64:67],a[140:143],a[224:227]
s_waitcnt vmcnt(9) lgkmcnt(0)
v_mfma_f32_16x16x32_bf16 a[224:227],a[96:99],a[156:159],a[224:227]
s_barrier
v_mfma_f32_16x16x32_bf16 v[64:67],a[64:67],a[160:163],v[64:67]
s_add_u32 m0,s56,0
ds_read_b64_tr_b16 a[0:1],v2 offset:0
v_mov_b32 v56,0
v_mfma_f32_16x16x32_bf16 v[64:67],a[96:99],a[176:179],v[64:67]
s_add_u32 s61,s53,0
ds_read_b64_tr_b16 a[2:3],v2 offset:1024
v_mov_b32 v57,0
v_mfma_f32_16x16x32_bf16 v[68:71],a[68:71],a[160:163],v[68:71]
buffer_load_dwordx4 v12,s[36:39],s61 offen lds
ds_read_b64_tr_b16 a[32:33],v2 offset:8192
v_mfma_f32_16x16x32_bf16 v[68:71],a[100:103],a[176:179],v[68:71]
s_add_u32 m0,s56,1024
ds_read_b64_tr_b16 a[34:35],v2 offset:9216
v_dot2c_f32_bf16 v56,v32,v48
v_mfma_f32_16x16x32_bf16 v[72:75],a[72:75],a[160:163],v[72:75]
ds_read_b128 a[128:131],v0 offset:8192
v_dot2c_f32_bf16 v57,v40,v48
)"
R"(v_mfma_f32_16x16x32_bf16 v[72:75],a[104:107],a[176:179],v[72:75]
s_add_u32 s62,s53,8192
ds_read_b64_tr_b16 a[4:5],v3 offset:0
v_dot2c_f32_bf16 v56,v33,v49
v_mfma_f32_16x16x32_bf16 v[76:79],a[76:79],a[160:163],v[76:79]
buffer_load_dwordx4 v13,s[36:39],s62 offen lds
ds_read_b64_tr_b16 a[6:7],v3 offset:1024
v_mfma_f32_16x16x32_bf16 v[76:79],a[108:111],a[176:179],v[76:79]
s_add_u32 m0,s56,2048
ds_read_b64_tr_b16 a[36:37],v3 offset:8192
v_dot2c_f32_bf16 v57,v41,v49
v_mfma_f32_16x16x32_bf16 v[80:83],a[80:83],a[160:163],v[80:83]
ds_read_b64_tr_b16 a[38:39],v3 offset:9216
v_dot2c_f32_bf16 v56,v34,v50
v_mfma_f32_16x16x32_bf16 v[80:83],a[112:115],a[176:179],v[80:83]
s_add_u32 s61,s53,16384
ds_read_b128 a[144:147],v1 offset:8192
v_dot2c_f32_bf16 v57,v42,v50
v_mfma_f32_16x16x32_bf16 v[84:87],a[84:87],a[160:163],v[84:87]
buffer_load_dwordx4 v12,s[36:39],s61 offen lds
ds_read_b64_tr_b16 a[8:9],v4 offset:0
v_mfma_f32_16x16x32_bf16 v[84:87],a[116:119],a[176:179],v[84:87]
s_add_u32 m0,s56,3072
ds_read_b64_tr_b16 a[10:11],v4 offset:1024
)"
R"(v_dot2c_f32_bf16 v56,v35,v51
v_mfma_f32_16x16x32_bf16 v[88:91],a[88:91],a[160:163],v[88:91]
ds_read_b64_tr_b16 a[40:41],v4 offset:8192
v_dot2c_f32_bf16 v57,v43,v51
v_mfma_f32_16x16x32_bf16 v[88:91],a[120:123],a[176:179],v[88:91]
s_add_u32 s62,s53,24576
ds_read_b64_tr_b16 a[42:43],v4 offset:9216
v_dot2c_f32_bf16 v56,v36,v52
v_mfma_f32_16x16x32_bf16 v[92:95],a[92:95],a[160:163],v[92:95]
buffer_load_dwordx4 v13,s[36:39],s62 offen lds
ds_read_b128 a[132:135],v0 offset:10240
v_mfma_f32_16x16x32_bf16 v[92:95],a[124:127],a[176:179],v[92:95]
s_add_u32 m0,s56,4096
ds_read_b64_tr_b16 a[12:13],v5 offset:0
v_dot2c_f32_bf16 v57,v44,v52
v_mfma_f32_16x16x32_bf16 v[124:127],a[92:95],a[164:167],v[124:127]
s_add_u32 s61,s53,32768
ds_read_b64_tr_b16 a[14:15],v5 offset:1024
v_dot2c_f32_bf16 v56,v37,v53
v_mfma_f32_16x16x32_bf16 v[124:127],a[124:127],a[180:183],v[124:127]
ds_read_b64_tr_b16 a[44:45],v5 offset:8192
v_dot2c_f32_bf16 v57,v45,v53
v_mfma_f32_16x16x32_bf16 v[120:123],a[88:91],a[164:167],v[120:123]
buffer_load_dwordx4 v12,s[36:39],s61 offen lds
)"
R"(ds_read_b64_tr_b16 a[46:47],v5 offset:9216
v_mfma_f32_16x16x32_bf16 v[120:123],a[120:123],a[180:183],v[120:123]
s_add_u32 m0,s56,5120
ds_read_b128 a[148:151],v1 offset:10240
v_dot2c_f32_bf16 v56,v38,v54
v_mfma_f32_16x16x32_bf16 v[116:119],a[84:87],a[164:167],v[116:119]
s_add_u32 s62,s53,40960
ds_read_b64_tr_b16 a[16:17],v6 offset:0
v_dot2c_f32_bf16 v57,v46,v54
v_mfma_f32_16x16x32_bf16 v[116:119],a[116:119],a[180:183],v[116:119]
ds_read_b64_tr_b16 a[18:19],v6 offset:1024
v_dot2c_f32_bf16 v56,v39,v55
v_mfma_f32_16x16x32_bf16 v[112:115],a[80:83],a[164:167],v[112:115]
buffer_load_dwordx4 v13,s[36:39],s62 offen lds
ds_read_b64_tr_b16 a[48:49],v6 offset:8192
v_mfma_f32_16x16x32_bf16 v[112:115],a[112:115],a[180:183],v[112:115]
s_add_u32 m0,s56,6144
ds_read_b64_tr_b16 a[50:51],v6 offset:9216
v_dot2c_f32_bf16 v57,v47,v55
v_mfma_f32_16x16x32_bf16 v[108:111],a[76:79],a[164:167],v[108:111]
s_add_u32 s61,s53,49152
ds_read_b128 a[136:139],v0 offset:12288
v_lshrrev_b32 v58,5,v19
v_mfma_f32_16x16x32_bf16 v[108:111],a[108:111],a[180:183],v[108:111]
)"
R"(ds_read_b64_tr_b16 a[20:21],v7 offset:0
v_lshlrev_b32 v58,2,v58
v_mfma_f32_16x16x32_bf16 v[104:107],a[72:75],a[164:167],v[104:107]
buffer_load_dwordx4 v12,s[36:39],s61 offen lds
ds_read_b64_tr_b16 a[22:23],v7 offset:1024
v_mfma_f32_16x16x32_bf16 v[104:107],a[104:107],a[180:183],v[104:107]
s_add_u32 m0,s56,7168
ds_read_b64_tr_b16 a[52:53],v7 offset:8192
v_mov_b32 v59,0
v_mfma_f32_16x16x32_bf16 v[100:103],a[68:71],a[164:167],v[100:103]
s_add_u32 s62,s53,57344
ds_read_b64_tr_b16 a[54:55],v7 offset:9216
v_nop
v_mfma_f32_16x16x32_bf16 v[100:103],a[100:103],a[180:183],v[100:103]
buffer_load_dwordx4 v13,s[36:39],s62 offen lds
ds_read_b128 a[152:155],v1 offset:12288
v_mfma_f32_16x16x32_bf16 v[96:99],a[64:67],a[164:167],v[96:99]
ds_read_b64_tr_b16 a[24:25],v8 offset:0
v_permlane32_swap_b32 v56,v57
v_mfma_f32_16x16x32_bf16 v[96:99],a[96:99],a[180:183],v[96:99]
buffer_load_dword v61,v60,s[12:15],s87 offen
ds_read_b64_tr_b16 a[26:27],v8 offset:1024
v_mfma_f32_16x16x32_bf16 v[128:131],a[64:67],a[168:171],v[128:131]
s_add_u32 s87,s87,67108864
)"
R"(ds_read_b64_tr_b16 a[56:57],v8 offset:8192
v_add_f32 v56,v56,v57
v_mfma_f32_16x16x32_bf16 v[128:131],a[96:99],a[184:187],v[128:131]
s_xor_b32 s56,s56,0x8000
ds_read_b64_tr_b16 a[58:59],v8 offset:9216
v_mov_b32 v57,v56
v_mfma_f32_16x16x32_bf16 v[132:135],a[68:71],a[168:171],v[132:135]
ds_read_b128 a[140:143],v0 offset:14336
v_permlane16_swap_b32 v56,v57
v_mfma_f32_16x16x32_bf16 v[132:135],a[100:103],a[184:187],v[132:135]
s_add_u32 s53,s53,128
ds_read_b64_tr_b16 a[28:29],v9 offset:0
v_add_f32 v56,v56,v57
v_mfma_f32_16x16x32_bf16 v[136:139],a[72:75],a[168:171],v[136:139]
s_cmp_eq_u32 s53,1024
ds_read_b64_tr_b16 a[30:31],v9 offset:1024
v_add_f32_dpp v56,v56,v56 row_ror:8 row_mask:0xf bank_mask:0xf
v_mfma_f32_16x16x32_bf16 v[136:139],a[104:107],a[184:187],v[136:139]
s_cselect_b32 s53,0,s53
ds_read_b64_tr_b16 a[60:61],v9 offset:8192
v_add_f32_dpp v56,v56,v56 row_ror:4 row_mask:0xf bank_mask:0xf
v_mfma_f32_16x16x32_bf16 v[140:143],a[76:79],a[168:171],v[140:143]
ds_read_b64_tr_b16 a[62:63],v9 offset:9216
v_add_f32_dpp v56,v56,v56 row_ror:2 row_mask:0xf bank_mask:0xf
)"
R"(v_mfma_f32_16x16x32_bf16 v[140:143],a[108:111],a[184:187],v[140:143]
ds_read_b128 a[156:159],v1 offset:14336
v_add_f32_dpp v56,v56,v56 row_ror:1 row_mask:0xf bank_mask:0xf
v_mfma_f32_16x16x32_bf16 v[144:147],a[80:83],a[168:171],v[144:147]
v_mul_f32 v56,0xbfb8aa3b,v56
v_mfma_f32_16x16x32_bf16 v[144:147],a[112:115],a[184:187],v[144:147]
v_exp_f32 v57,-v56
v_mfma_f32_16x16x32_bf16 v[148:151],a[84:87],a[168:171],v[148:151]
v_exp_f32 v56,v56
v_mfma_f32_16x16x32_bf16 v[148:151],a[116:119],a[184:187],v[148:151]
v_add_f32 v57,1.0,v57
v_add_f32 v56,1.0,v56
v_mfma_f32_16x16x32_bf16 v[152:155],a[88:91],a[168:171],v[152:155]
v_min_f32 v57,0x7149f2ca,v57
v_mfma_f32_16x16x32_bf16 v[152:155],a[120:123],a[184:187],v[152:155]
v_rcp_f32 v56,v56
v_mfma_f32_16x16x32_bf16 v[156:159],a[92:95],a[168:171],v[156:159]
v_add_u32 v32,0x2000,v58
buffer_store_dword v56,v58,s[4:7],s84 offen sc1
v_mfma_f32_16x16x32_bf16 v[156:159],a[124:127],a[184:187],v[156:159]
buffer_store_dword v57,v32,s[4:7],s84 offen sc1
v_xor_b32 v28,0x9e3779b9,v28
v_mfma_f32_16x16x32_bf16 v[188:191],a[92:95],a[172:175],v[188:191]
v_nop
)"
R"(v_readfirstlane_b32 s72,v28
v_mfma_f32_16x16x32_bf16 v[188:191],a[124:127],a[188:191],v[188:191]
v_mfma_f32_16x16x32_bf16 v[184:187],a[88:91],a[172:175],v[184:187]
v_mfma_f32_16x16x32_bf16 v[184:187],a[120:123],a[188:191],v[184:187]
v_mfma_f32_16x16x32_bf16 v[180:183],a[84:87],a[172:175],v[180:183]
v_mfma_f32_16x16x32_bf16 v[180:183],a[116:119],a[188:191],v[180:183]
v_mfma_f32_16x16x32_bf16 v[176:179],a[80:83],a[172:175],v[176:179]
v_mfma_f32_16x16x32_bf16 v[176:179],a[112:115],a[188:191],v[176:179]
v_mfma_f32_16x16x32_bf16 v[172:175],a[76:79],a[172:175],v[172:175]
v_mfma_f32_16x16x32_bf16 v[172:175],a[108:111],a[188:191],v[172:175]
v_mfma_f32_16x16x32_bf16 v[168:171],a[72:75],a[172:175],v[168:171]
v_mfma_f32_16x16x32_bf16 v[168:171],a[104:107],a[188:191],v[168:171]
v_mfma_f32_16x16x32_bf16 v[164:167],a[68:71],a[172:175],v[164:167]
v_mfma_f32_16x16x32_bf16 v[164:167],a[100:103],a[188:191],v[164:167]
v_mfma_f32_16x16x32_bf16 v[160:163],a[64:67],a[172:175],v[160:163]
s_waitcnt lgkmcnt(0)
v_mfma_f32_16x16x32_bf16 v[160:163],a[96:99],a[188:191],v[160:163]
s_mov_b32 s60,2
s_branch gm_KB_sk2
.p2align 12
gm_KB_pg2:
s_setpc_b64 s[100:101]
s_nop 0
gm_KB_sk2:
)"
R"(gm_K1_K_loop:
v_mfma_f32_16x16x32_bf16 v[192:195],a[0:3],a[128:131],v[192:195]
s_add_u32 m0,s57,0
ds_read_b128 a[160:163],v0 offset:0
v_mfma_f32_16x16x32_bf16 v[192:195],a[32:35],a[144:147],v[192:195]
s_add_u32 s63,s55,0
ds_read_b128 a[176:179],v1 offset:0
v_mfma_f32_16x16x32_bf16 v[196:199],a[4:7],a[128:131],v[196:199]
ds_read_b128 a[164:167],v0 offset:2048
v_mfma_f32_16x16x32_bf16 v[196:199],a[36:39],a[144:147],v[196:199]
buffer_load_dwordx4 v16,s[44:47],s63 offen lds
v_mfma_f32_16x16x32_bf16 v[200:203],a[8:11],a[128:131],v[200:203]
s_add_u32 s66,s59,0x8000
ds_read_b128 a[180:183],v1 offset:2048
v_mfma_f32_16x16x32_bf16 v[200:203],a[40:43],a[144:147],v[200:203]
ds_read_b128 a[168:171],v0 offset:4096
v_mfma_f32_16x16x32_bf16 v[204:207],a[12:15],a[128:131],v[204:207]
s_cmp_eq_u32 s66,0x18000
ds_read_b128 a[184:187],v1 offset:4096
v_mfma_f32_16x16x32_bf16 v[204:207],a[44:47],a[144:147],v[204:207]
ds_read_b128 a[172:175],v0 offset:6144
v_mfma_f32_16x16x32_bf16 v[208:211],a[16:19],a[128:131],v[208:211]
s_cselect_b32 s66,0,s66
ds_read_b128 a[188:191],v1 offset:6144
)"
R"(v_mfma_f32_16x16x32_bf16 v[208:211],a[48:51],a[144:147],v[208:211]
s_sub_u32 s58,s66,s59
v_mfma_f32_16x16x32_bf16 v[212:215],a[20:23],a[128:131],v[212:215]
v_mfma_f32_16x16x32_bf16 v[212:215],a[52:55],a[144:147],v[212:215]
s_mov_b32 s59,s66
v_mfma_f32_16x16x32_bf16 v[216:219],a[24:27],a[128:131],v[216:219]
s_add_u32 m0,s57,1024
v_mfma_f32_16x16x32_bf16 v[216:219],a[56:59],a[144:147],v[216:219]
v_mfma_f32_16x16x32_bf16 v[220:223],a[28:31],a[128:131],v[220:223]
s_add_u32 s64,s55,32768
v_mfma_f32_16x16x32_bf16 v[220:223],a[60:63],a[144:147],v[220:223]
v_mfma_f32_16x16x32_bf16 v[252:255],a[28:31],a[132:135],v[252:255]
buffer_load_dwordx4 v16,s[44:47],s64 offen lds
v_mfma_f32_16x16x32_bf16 v[252:255],a[60:63],a[148:151],v[252:255]
s_add_u32 m0,s57,2048
v_mfma_f32_16x16x32_bf16 v[248:251],a[24:27],a[132:135],v[248:251]
v_mfma_f32_16x16x32_bf16 v[248:251],a[56:59],a[148:151],v[248:251]
s_add_u32 s63,s55,65536
v_mfma_f32_16x16x32_bf16 v[244:247],a[20:23],a[132:135],v[244:247]
buffer_load_dwordx4 v17,s[44:47],s63 offen lds
v_mfma_f32_16x16x32_bf16 v[244:247],a[52:55],a[148:151],v[244:247]
v_mfma_f32_16x16x32_bf16 v[240:243],a[16:19],a[132:135],v[240:243]
s_add_u32 m0,s57,3072
v_mfma_f32_16x16x32_bf16 v[240:243],a[48:51],a[148:151],v[240:243]
)"
R"(v_mfma_f32_16x16x32_bf16 v[236:239],a[12:15],a[132:135],v[236:239]
s_add_u32 s64,s55,98304
v_mfma_f32_16x16x32_bf16 v[236:239],a[44:47],a[148:151],v[236:239]
buffer_load_dwordx4 v17,s[44:47],s64 offen lds
v_mfma_f32_16x16x32_bf16 v[232:235],a[8:11],a[132:135],v[232:235]
v_mfma_f32_16x16x32_bf16 v[232:235],a[40:43],a[148:151],v[232:235]
s_add_u32 m0,s57,4096
v_mfma_f32_16x16x32_bf16 v[228:231],a[4:7],a[132:135],v[228:231]
s_add_u32 s63,s55,131072
v_mfma_f32_16x16x32_bf16 v[228:231],a[36:39],a[148:151],v[228:231]
v_mfma_f32_16x16x32_bf16 v[224:227],a[0:3],a[132:135],v[224:227]
buffer_load_dwordx4 v16,s[44:47],s63 offen lds
v_mfma_f32_16x16x32_bf16 v[224:227],a[32:35],a[148:151],v[224:227]
v_mfma_f32_16x16x32_bf16 a[192:195],a[0:3],a[136:139],a[192:195]
s_add_u32 m0,s57,5120
v_mfma_f32_16x16x32_bf16 a[192:195],a[32:35],a[152:155],a[192:195]
s_add_u32 s64,s55,163840
v_mfma_f32_16x16x32_bf16 a[196:199],a[4:7],a[136:139],a[196:199]
v_mfma_f32_16x16x32_bf16 a[196:199],a[36:39],a[152:155],a[196:199]
buffer_load_dwordx4 v16,s[44:47],s64 offen lds
v_mfma_f32_16x16x32_bf16 a[200:203],a[8:11],a[136:139],a[200:203]
s_add_u32 m0,s57,6144
v_mfma_f32_16x16x32_bf16 a[200:203],a[40:43],a[152:155],a[200:203]
v_mfma_f32_16x16x32_bf16 a[204:207],a[12:15],a[136:139],a[204:207]
)"
R"(s_add_u32 s63,s55,196608
v_mfma_f32_16x16x32_bf16 a[204:207],a[44:47],a[152:155],a[204:207]
v_mfma_f32_16x16x32_bf16 a[208:211],a[16:19],a[136:139],a[208:211]
buffer_load_dwordx4 v17,s[44:47],s63 offen lds
v_mfma_f32_16x16x32_bf16 a[208:211],a[48:51],a[152:155],a[208:211]
s_add_u32 m0,s57,7168
v_mfma_f32_16x16x32_bf16 a[212:215],a[20:23],a[136:139],a[212:215]
v_mfma_f32_16x16x32_bf16 a[212:215],a[52:55],a[152:155],a[212:215]
s_add_u32 s64,s55,229376
v_mfma_f32_16x16x32_bf16 a[216:219],a[24:27],a[136:139],a[216:219]
buffer_load_dwordx4 v17,s[44:47],s64 offen lds
v_mfma_f32_16x16x32_bf16 a[216:219],a[56:59],a[152:155],a[216:219]
v_xor_b32 v0,0x8000,v0
v_mfma_f32_16x16x32_bf16 a[220:223],a[28:31],a[136:139],a[220:223]
s_add_u32 s57,s57,0x8000
v_xor_b32 v1,0x8000,v1
v_mfma_f32_16x16x32_bf16 a[220:223],a[60:63],a[152:155],a[220:223]
v_add_u32 v2,s58,v2
v_mfma_f32_16x16x32_bf16 a[252:255],a[28:31],a[140:143],a[252:255]
s_cmp_ge_u32 s57,0x28000
v_add_u32 v3,s58,v3
v_mfma_f32_16x16x32_bf16 a[252:255],a[60:63],a[156:159],a[252:255]
s_cselect_b32 s65,0x18000,0
v_add_u32 v4,s58,v4
)"
R"(v_mfma_f32_16x16x32_bf16 a[248:251],a[24:27],a[140:143],a[248:251]
v_add_u32 v5,s58,v5
v_mfma_f32_16x16x32_bf16 a[248:251],a[56:59],a[156:159],a[248:251]
s_sub_u32 s57,s57,s65
v_add_u32 v6,s58,v6
v_mfma_f32_16x16x32_bf16 a[244:247],a[20:23],a[140:143],a[244:247]
s_add_u32 s55,s55,524288
v_add_u32 v7,s58,v7
v_mfma_f32_16x16x32_bf16 a[244:247],a[52:55],a[156:159],a[244:247]
v_add_u32 v8,s58,v8
v_mfma_f32_16x16x32_bf16 a[240:243],a[16:19],a[140:143],a[240:243]
s_cmp_eq_u32 s55,4194304
v_add_u32 v9,s58,v9
v_mfma_f32_16x16x32_bf16 a[240:243],a[48:51],a[156:159],a[240:243]
v_mfma_f32_16x16x32_bf16 a[236:239],a[12:15],a[140:143],a[236:239]
s_cselect_b32 s55,0,s55
v_mfma_f32_16x16x32_bf16 a[236:239],a[44:47],a[156:159],a[236:239]
v_mfma_f32_16x16x32_bf16 a[232:235],a[8:11],a[140:143],a[232:235]
v_mfma_f32_16x16x32_bf16 a[232:235],a[40:43],a[156:159],a[232:235]
v_mfma_f32_16x16x32_bf16 a[228:231],a[4:7],a[140:143],a[228:231]
v_mfma_f32_16x16x32_bf16 a[228:231],a[36:39],a[156:159],a[228:231]
v_mfma_f32_16x16x32_bf16 a[224:227],a[0:3],a[140:143],a[224:227]
s_waitcnt vmcnt(9) lgkmcnt(0)
v_mfma_f32_16x16x32_bf16 a[224:227],a[32:35],a[156:159],a[224:227]
)"
R"(s_barrier
v_mfma_f32_16x16x32_bf16 v[64:67],a[0:3],a[160:163],v[64:67]
s_add_u32 m0,s56,0
ds_read_b64_tr_b16 a[64:65],v2 offset:0
v_mfma_f32_16x16x32_bf16 v[64:67],a[32:35],a[176:179],v[64:67]
s_add_u32 s61,s53,0
ds_read_b64_tr_b16 a[66:67],v2 offset:1024
v_mfma_f32_16x16x32_bf16 v[68:71],a[4:7],a[160:163],v[68:71]
buffer_load_dwordx4 v12,s[36:39],s61 offen lds
v_mfma_f32_16x16x32_bf16 v[68:71],a[36:39],a[176:179],v[68:71]
s_add_u32 m0,s56,1024
ds_read_b64_tr_b16 a[96:97],v2 offset:8192
v_mfma_f32_16x16x32_bf16 v[72:75],a[8:11],a[160:163],v[72:75]
ds_read_b64_tr_b16 a[98:99],v2 offset:9216
v_mfma_f32_16x16x32_bf16 v[72:75],a[40:43],a[176:179],v[72:75]
s_add_u32 s62,s53,8192
ds_read_b128 a[128:131],v0 offset:8192
v_mfma_f32_16x16x32_bf16 v[76:79],a[12:15],a[160:163],v[76:79]
buffer_load_dwordx4 v13,s[36:39],s62 offen lds
v_mfma_f32_16x16x32_bf16 v[76:79],a[44:47],a[176:179],v[76:79]
s_add_u32 m0,s56,2048
ds_read_b64_tr_b16 a[68:69],v3 offset:0
v_mfma_f32_16x16x32_bf16 v[80:83],a[16:19],a[160:163],v[80:83]
ds_read_b64_tr_b16 a[70:71],v3 offset:1024
)"
R"(v_mfma_f32_16x16x32_bf16 v[80:83],a[48:51],a[176:179],v[80:83]
s_add_u32 s61,s53,16384
ds_read_b64_tr_b16 a[100:101],v3 offset:8192
v_mfma_f32_16x16x32_bf16 v[84:87],a[20:23],a[160:163],v[84:87]
buffer_load_dwordx4 v12,s[36:39],s61 offen lds
v_mfma_f32_16x16x32_bf16 v[84:87],a[52:55],a[176:179],v[84:87]
s_add_u32 m0,s56,3072
ds_read_b64_tr_b16 a[102:103],v3 offset:9216
v_mfma_f32_16x16x32_bf16 v[88:91],a[24:27],a[160:163],v[88:91]
ds_read_b128 a[144:147],v1 offset:8192
v_mfma_f32_16x16x32_bf16 v[88:91],a[56:59],a[176:179],v[88:91]
s_add_u32 s62,s53,24576
ds_read_b64_tr_b16 a[72:73],v4 offset:0
v_mfma_f32_16x16x32_bf16 v[92:95],a[28:31],a[160:163],v[92:95]
buffer_load_dwordx4 v13,s[36:39],s62 offen lds
v_mfma_f32_16x16x32_bf16 v[92:95],a[60:63],a[176:179],v[92:95]
s_add_u32 m0,s56,4096
ds_read_b64_tr_b16 a[74:75],v4 offset:1024
v_mfma_f32_16x16x32_bf16 v[124:127],a[28:31],a[164:167],v[124:127]
s_add_u32 s61,s53,32768
ds_read_b64_tr_b16 a[104:105],v4 offset:8192
v_mfma_f32_16x16x32_bf16 v[124:127],a[60:63],a[180:183],v[124:127]
ds_read_b64_tr_b16 a[106:107],v4 offset:9216
v_mfma_f32_16x16x32_bf16 v[120:123],a[24:27],a[164:167],v[120:123]
)"
R"(buffer_load_dwordx4 v12,s[36:39],s61 offen lds
v_mfma_f32_16x16x32_bf16 v[120:123],a[56:59],a[180:183],v[120:123]
s_add_u32 m0,s56,5120
ds_read_b128 a[132:135],v0 offset:10240
v_mfma_f32_16x16x32_bf16 v[116:119],a[20:23],a[164:167],v[116:119]
s_add_u32 s62,s53,40960
ds_read_b64_tr_b16 a[76:77],v5 offset:0
v_mfma_f32_16x16x32_bf16 v[116:119],a[52:55],a[180:183],v[116:119]
ds_read_b64_tr_b16 a[78:79],v5 offset:1024
v_mfma_f32_16x16x32_bf16 v[112:115],a[16:19],a[164:167],v[112:115]
buffer_load_dwordx4 v13,s[36:39],s62 offen lds
v_mfma_f32_16x16x32_bf16 v[112:115],a[48:51],a[180:183],v[112:115]
s_add_u32 m0,s56,6144
ds_read_b64_tr_b16 a[108:109],v5 offset:8192
v_mfma_f32_16x16x32_bf16 v[108:111],a[12:15],a[164:167],v[108:111]
s_add_u32 s61,s53,49152
ds_read_b64_tr_b16 a[110:111],v5 offset:9216
v_mfma_f32_16x16x32_bf16 v[108:111],a[44:47],a[180:183],v[108:111]
ds_read_b128 a[148:151],v1 offset:10240
v_mfma_f32_16x16x32_bf16 v[104:107],a[8:11],a[164:167],v[104:107]
buffer_load_dwordx4 v12,s[36:39],s61 offen lds
v_mfma_f32_16x16x32_bf16 v[104:107],a[40:43],a[180:183],v[104:107]
s_add_u32 m0,s56,7168
ds_read_b64_tr_b16 a[80:81],v6 offset:0
)"
R"(v_mfma_f32_16x16x32_bf16 v[100:103],a[4:7],a[164:167],v[100:103]
s_add_u32 s62,s53,57344
ds_read_b64_tr_b16 a[82:83],v6 offset:1024
v_mfma_f32_16x16x32_bf16 v[100:103],a[36:39],a[180:183],v[100:103]
buffer_load_dwordx4 v13,s[36:39],s62 offen lds
v_mfma_f32_16x16x32_bf16 v[96:99],a[0:3],a[164:167],v[96:99]
ds_read_b64_tr_b16 a[112:113],v6 offset:8192
v_mfma_f32_16x16x32_bf16 v[96:99],a[32:35],a[180:183],v[96:99]
buffer_load_dword v61,v60,s[12:15],s87 offen
v_mfma_f32_16x16x32_bf16 v[128:131],a[0:3],a[168:171],v[128:131]
s_add_u32 s87,s87,67108864
ds_read_b64_tr_b16 a[114:115],v6 offset:9216
v_mfma_f32_16x16x32_bf16 v[128:131],a[32:35],a[184:187],v[128:131]
s_xor_b32 s56,s56,0x8000
ds_read_b128 a[136:139],v0 offset:12288
v_mfma_f32_16x16x32_bf16 v[132:135],a[4:7],a[168:171],v[132:135]
ds_read_b64_tr_b16 a[84:85],v7 offset:0
v_mfma_f32_16x16x32_bf16 v[132:135],a[36:39],a[184:187],v[132:135]
s_add_u32 s53,s53,128
ds_read_b64_tr_b16 a[86:87],v7 offset:1024
v_mfma_f32_16x16x32_bf16 v[136:139],a[8:11],a[168:171],v[136:139]
s_cmp_eq_u32 s53,1024
ds_read_b64_tr_b16 a[116:117],v7 offset:8192
v_mfma_f32_16x16x32_bf16 v[136:139],a[40:43],a[184:187],v[136:139]
)"
R"(s_cselect_b32 s53,0,s53
ds_read_b64_tr_b16 a[118:119],v7 offset:9216
v_mfma_f32_16x16x32_bf16 v[140:143],a[12:15],a[168:171],v[140:143]
ds_read_b128 a[152:155],v1 offset:12288
v_mfma_f32_16x16x32_bf16 v[140:143],a[44:47],a[184:187],v[140:143]
ds_read_b64_tr_b16 a[88:89],v8 offset:0
v_mfma_f32_16x16x32_bf16 v[144:147],a[16:19],a[168:171],v[144:147]
ds_read_b64_tr_b16 a[90:91],v8 offset:1024
v_mfma_f32_16x16x32_bf16 v[144:147],a[48:51],a[184:187],v[144:147]
ds_read_b64_tr_b16 a[120:121],v8 offset:8192
v_mfma_f32_16x16x32_bf16 v[148:151],a[20:23],a[168:171],v[148:151]
ds_read_b64_tr_b16 a[122:123],v8 offset:9216
v_mfma_f32_16x16x32_bf16 v[148:151],a[52:55],a[184:187],v[148:151]
ds_read_b128 a[140:143],v0 offset:14336
v_mfma_f32_16x16x32_bf16 v[152:155],a[24:27],a[168:171],v[152:155]
ds_read_b64_tr_b16 a[92:93],v9 offset:0
v_mfma_f32_16x16x32_bf16 v[152:155],a[56:59],a[184:187],v[152:155]
ds_read_b64_tr_b16 a[94:95],v9 offset:1024
v_mfma_f32_16x16x32_bf16 v[156:159],a[28:31],a[168:171],v[156:159]
ds_read_b64_tr_b16 a[124:125],v9 offset:8192
v_mfma_f32_16x16x32_bf16 v[156:159],a[60:63],a[184:187],v[156:159]
ds_read_b64_tr_b16 a[126:127],v9 offset:9216
v_mfma_f32_16x16x32_bf16 v[188:191],a[28:31],a[172:175],v[188:191]
ds_read_b128 a[156:159],v1 offset:14336
)"
R"(v_mfma_f32_16x16x32_bf16 v[188:191],a[60:63],a[188:191],v[188:191]
v_mfma_f32_16x16x32_bf16 v[184:187],a[24:27],a[172:175],v[184:187]
v_mfma_f32_16x16x32_bf16 v[184:187],a[56:59],a[188:191],v[184:187]
v_mfma_f32_16x16x32_bf16 v[180:183],a[20:23],a[172:175],v[180:183]
v_mfma_f32_16x16x32_bf16 v[180:183],a[52:55],a[188:191],v[180:183]
v_mfma_f32_16x16x32_bf16 v[176:179],a[16:19],a[172:175],v[176:179]
v_mfma_f32_16x16x32_bf16 v[176:179],a[48:51],a[188:191],v[176:179]
v_mfma_f32_16x16x32_bf16 v[172:175],a[12:15],a[172:175],v[172:175]
v_mfma_f32_16x16x32_bf16 v[172:175],a[44:47],a[188:191],v[172:175]
v_mfma_f32_16x16x32_bf16 v[168:171],a[8:11],a[172:175],v[168:171]
v_mfma_f32_16x16x32_bf16 v[168:171],a[40:43],a[188:191],v[168:171]
v_mfma_f32_16x16x32_bf16 v[164:167],a[4:7],a[172:175],v[164:167]
v_mfma_f32_16x16x32_bf16 v[164:167],a[36:39],a[188:191],v[164:167]
v_mfma_f32_16x16x32_bf16 v[160:163],a[0:3],a[172:175],v[160:163]
s_waitcnt lgkmcnt(0)
v_mfma_f32_16x16x32_bf16 v[160:163],a[32:35],a[188:191],v[160:163]
v_mfma_f32_16x16x32_bf16 v[192:195],a[64:67],a[128:131],v[192:195]
s_add_u32 m0,s57,0
ds_read_b128 a[160:163],v0 offset:0
v_mfma_f32_16x16x32_bf16 v[192:195],a[96:99],a[144:147],v[192:195]
s_add_u32 s63,s55,0
ds_read_b128 a[176:179],v1 offset:0
v_mfma_f32_16x16x32_bf16 v[196:199],a[68:71],a[128:131],v[196:199]
ds_read_b128 a[164:167],v0 offset:2048
)"
R"(v_mfma_f32_16x16x32_bf16 v[196:199],a[100:103],a[144:147],v[196:199]
buffer_load_dwordx4 v16,s[44:47],s63 offen lds
v_mfma_f32_16x16x32_bf16 v[200:203],a[72:75],a[128:131],v[200:203]
s_add_u32 s66,s59,0x8000
ds_read_b128 a[180:183],v1 offset:2048
v_mfma_f32_16x16x32_bf16 v[200:203],a[104:107],a[144:147],v[200:203]
ds_read_b128 a[168:171],v0 offset:4096
v_mfma_f32_16x16x32_bf16 v[204:207],a[76:79],a[128:131],v[204:207]
s_cmp_eq_u32 s66,0x18000
ds_read_b128 a[184:187],v1 offset:4096
v_mfma_f32_16x16x32_bf16 v[204:207],a[108:111],a[144:147],v[204:207]
ds_read_b128 a[172:175],v0 offset:6144
v_mfma_f32_16x16x32_bf16 v[208:211],a[80:83],a[128:131],v[208:211]
s_cselect_b32 s66,0,s66
ds_read_b128 a[188:191],v1 offset:6144
v_mfma_f32_16x16x32_bf16 v[208:211],a[112:115],a[144:147],v[208:211]
s_sub_u32 s58,s66,s59
v_mfma_f32_16x16x32_bf16 v[212:215],a[84:87],a[128:131],v[212:215]
v_mfma_f32_16x16x32_bf16 v[212:215],a[116:119],a[144:147],v[212:215]
s_mov_b32 s59,s66
v_mfma_f32_16x16x32_bf16 v[216:219],a[88:91],a[128:131],v[216:219]
s_add_u32 m0,s57,1024
v_mfma_f32_16x16x32_bf16 v[216:219],a[120:123],a[144:147],v[216:219]
v_mfma_f32_16x16x32_bf16 v[220:223],a[92:95],a[128:131],v[220:223]
)"
R"(s_add_u32 s64,s55,32768
v_mfma_f32_16x16x32_bf16 v[220:223],a[124:127],a[144:147],v[220:223]
v_mfma_f32_16x16x32_bf16 v[252:255],a[92:95],a[132:135],v[252:255]
buffer_load_dwordx4 v16,s[44:47],s64 offen lds
v_mfma_f32_16x16x32_bf16 v[252:255],a[124:127],a[148:151],v[252:255]
s_add_u32 m0,s57,2048
v_mfma_f32_16x16x32_bf16 v[248:251],a[88:91],a[132:135],v[248:251]
v_mfma_f32_16x16x32_bf16 v[248:251],a[120:123],a[148:151],v[248:251]
s_add_u32 s63,s55,65536
v_mfma_f32_16x16x32_bf16 v[244:247],a[84:87],a[132:135],v[244:247]
buffer_load_dwordx4 v17,s[44:47],s63 offen lds
v_mfma_f32_16x16x32_bf16 v[244:247],a[116:119],a[148:151],v[244:247]
v_mfma_f32_16x16x32_bf16 v[240:243],a[80:83],a[132:135],v[240:243]
s_add_u32 m0,s57,3072
v_mfma_f32_16x16x32_bf16 v[240:243],a[112:115],a[148:151],v[240:243]
v_mfma_f32_16x16x32_bf16 v[236:239],a[76:79],a[132:135],v[236:239]
s_add_u32 s64,s55,98304
v_mfma_f32_16x16x32_bf16 v[236:239],a[108:111],a[148:151],v[236:239]
buffer_load_dwordx4 v17,s[44:47],s64 offen lds
v_mfma_f32_16x16x32_bf16 v[232:235],a[72:75],a[132:135],v[232:235]
v_mfma_f32_16x16x32_bf16 v[232:235],a[104:107],a[148:151],v[232:235]
s_add_u32 m0,s57,4096
v_mfma_f32_16x16x32_bf16 v[228:231],a[68:71],a[132:135],v[228:231]
s_add_u32 s63,s55,131072
)"
R"(v_mfma_f32_16x16x32_bf16 v[228:231],a[100:103],a[148:151],v[228:231]
v_mfma_f32_16x16x32_bf16 v[224:227],a[64:67],a[132:135],v[224:227]
buffer_load_dwordx4 v16,s[44:47],s63 offen lds
v_mfma_f32_16x16x32_bf16 v[224:227],a[96:99],a[148:151],v[224:227]
v_mfma_f32_16x16x32_bf16 a[192:195],a[64:67],a[136:139],a[192:195]
s_add_u32 m0,s57,5120
v_mfma_f32_16x16x32_bf16 a[192:195],a[96:99],a[152:155],a[192:195]
s_add_u32 s64,s55,163840
v_mfma_f32_16x16x32_bf16 a[196:199],a[68:71],a[136:139],a[196:199]
v_mfma_f32_16x16x32_bf16 a[196:199],a[100:103],a[152:155],a[196:199]
buffer_load_dwordx4 v16,s[44:47],s64 offen lds
v_mfma_f32_16x16x32_bf16 a[200:203],a[72:75],a[136:139],a[200:203]
s_add_u32 m0,s57,6144
v_mfma_f32_16x16x32_bf16 a[200:203],a[104:107],a[152:155],a[200:203]
v_mfma_f32_16x16x32_bf16 a[204:207],a[76:79],a[136:139],a[204:207]
s_add_u32 s63,s55,196608
v_mfma_f32_16x16x32_bf16 a[204:207],a[108:111],a[152:155],a[204:207]
v_mfma_f32_16x16x32_bf16 a[208:211],a[80:83],a[136:139],a[208:211]
buffer_load_dwordx4 v17,s[44:47],s63 offen lds
v_mfma_f32_16x16x32_bf16 a[208:211],a[112:115],a[152:155],a[208:211]
s_add_u32 m0,s57,7168
v_mfma_f32_16x16x32_bf16 a[212:215],a[84:87],a[136:139],a[212:215]
v_mfma_f32_16x16x32_bf16 a[212:215],a[116:119],a[152:155],a[212:215]
s_add_u32 s64,s55,229376
)"
R"(v_mfma_f32_16x16x32_bf16 a[216:219],a[88:91],a[136:139],a[216:219]
buffer_load_dwordx4 v17,s[44:47],s64 offen lds
v_mfma_f32_16x16x32_bf16 a[216:219],a[120:123],a[152:155],a[216:219]
v_xor_b32 v0,0x8000,v0
v_mfma_f32_16x16x32_bf16 a[220:223],a[92:95],a[136:139],a[220:223]
s_add_u32 s57,s57,0x8000
v_xor_b32 v1,0x8000,v1
v_mfma_f32_16x16x32_bf16 a[220:223],a[124:127],a[152:155],a[220:223]
v_add_u32 v2,s58,v2
v_mfma_f32_16x16x32_bf16 a[252:255],a[92:95],a[140:143],a[252:255]
s_cmp_ge_u32 s57,0x28000
v_add_u32 v3,s58,v3
v_mfma_f32_16x16x32_bf16 a[252:255],a[124:127],a[156:159],a[252:255]
s_cselect_b32 s65,0x18000,0
v_add_u32 v4,s58,v4
v_mfma_f32_16x16x32_bf16 a[248:251],a[88:91],a[140:143],a[248:251]
v_add_u32 v5,s58,v5
v_mfma_f32_16x16x32_bf16 a[248:251],a[120:123],a[156:159],a[248:251]
s_sub_u32 s57,s57,s65
v_add_u32 v6,s58,v6
v_mfma_f32_16x16x32_bf16 a[244:247],a[84:87],a[140:143],a[244:247]
s_add_u32 s55,s55,524288
v_add_u32 v7,s58,v7
v_mfma_f32_16x16x32_bf16 a[244:247],a[116:119],a[156:159],a[244:247]
)"
R"(v_add_u32 v8,s58,v8
v_mfma_f32_16x16x32_bf16 a[240:243],a[80:83],a[140:143],a[240:243]
s_cmp_eq_u32 s55,4194304
v_add_u32 v9,s58,v9
v_mfma_f32_16x16x32_bf16 a[240:243],a[112:115],a[156:159],a[240:243]
v_mfma_f32_16x16x32_bf16 a[236:239],a[76:79],a[140:143],a[236:239]
s_cselect_b32 s55,0,s55
v_mfma_f32_16x16x32_bf16 a[236:239],a[108:111],a[156:159],a[236:239]
v_mfma_f32_16x16x32_bf16 a[232:235],a[72:75],a[140:143],a[232:235]
v_mfma_f32_16x16x32_bf16 a[232:235],a[104:107],a[156:159],a[232:235]
v_mfma_f32_16x16x32_bf16 a[228:231],a[68:71],a[140:143],a[228:231]
v_mfma_f32_16x16x32_bf16 a[228:231],a[100:103],a[156:159],a[228:231]
v_mfma_f32_16x16x32_bf16 a[224:227],a[64:67],a[140:143],a[224:227]
s_waitcnt vmcnt(9) lgkmcnt(0)
v_mfma_f32_16x16x32_bf16 a[224:227],a[96:99],a[156:159],a[224:227]
s_barrier
v_mfma_f32_16x16x32_bf16 v[64:67],a[64:67],a[160:163],v[64:67]
s_add_u32 m0,s56,0
ds_read_b64_tr_b16 a[0:1],v2 offset:0
v_mfma_f32_16x16x32_bf16 v[64:67],a[96:99],a[176:179],v[64:67]
s_add_u32 s61,s53,0
ds_read_b64_tr_b16 a[2:3],v2 offset:1024
v_mfma_f32_16x16x32_bf16 v[68:71],a[68:71],a[160:163],v[68:71]
buffer_load_dwordx4 v12,s[36:39],s61 offen lds
)"
R"(v_mfma_f32_16x16x32_bf16 v[68:71],a[100:103],a[176:179],v[68:71]
s_add_u32 m0,s56,1024
ds_read_b64_tr_b16 a[32:33],v2 offset:8192
v_mfma_f32_16x16x32_bf16 v[72:75],a[72:75],a[160:163],v[72:75]
ds_read_b64_tr_b16 a[34:35],v2 offset:9216
v_mfma_f32_16x16x32_bf16 v[72:75],a[104:107],a[176:179],v[72:75]
s_add_u32 s62,s53,8192
ds_read_b128 a[128:131],v0 offset:8192
v_mfma_f32_16x16x32_bf16 v[76:79],a[76:79],a[160:163],v[76:79]
buffer_load_dwordx4 v13,s[36:39],s62 offen lds
v_mfma_f32_16x16x32_bf16 v[76:79],a[108:111],a[176:179],v[76:79]
s_add_u32 m0,s56,2048
ds_read_b64_tr_b16 a[4:5],v3 offset:0
v_mfma_f32_16x16x32_bf16 v[80:83],a[80:83],a[160:163],v[80:83]
ds_read_b64_tr_b16 a[6:7],v3 offset:1024
v_mfma_f32_16x16x32_bf16 v[80:83],a[112:115],a[176:179],v[80:83]
s_add_u32 s61,s53,16384
ds_read_b64_tr_b16 a[36:37],v3 offset:8192
v_mfma_f32_16x16x32_bf16 v[84:87],a[84:87],a[160:163],v[84:87]
buffer_load_dwordx4 v12,s[36:39],s61 offen lds
v_mfma_f32_16x16x32_bf16 v[84:87],a[116:119],a[176:179],v[84:87]
s_add_u32 m0,s56,3072
ds_read_b64_tr_b16 a[38:39],v3 offset:9216
v_mfma_f32_16x16x32_bf16 v[88:91],a[88:91],a[160:163],v[88:91]
)"
R"(ds_read_b128 a[144:147],v1 offset:8192
v_mfma_f32_16x16x32_bf16 v[88:91],a[120:123],a[176:179],v[88:91]
s_add_u32 s62,s53,24576
ds_read_b64_tr_b16 a[8:9],v4 offset:0
v_mfma_f32_16x16x32_bf16 v[92:95],a[92:95],a[160:163],v[92:95]
buffer_load_dwordx4 v13,s[36:39],s62 offen lds
v_mfma_f32_16x16x32_bf16 v[92:95],a[124:127],a[176:179],v[92:95]
s_add_u32 m0,s56,4096
ds_read_b64_tr_b16 a[10:11],v4 offset:1024
v_mfma_f32_16x16x32_bf16 v[124:127],a[92:95],a[164:167],v[124:127]
s_add_u32 s61,s53,32768
ds_read_b64_tr_b16 a[40:41],v4 offset:8192
v_mfma_f32_16x16x32_bf16 v[124:127],a[124:127],a[180:183],v[124:127]
ds_read_b64_tr_b16 a[42:43],v4 offset:9216
v_mfma_f32_16x16x32_bf16 v[120:123],a[88:91],a[164:167],v[120:123]
buffer_load_dwordx4 v12,s[36:39],s61 offen lds
v_mfma_f32_16x16x32_bf16 v[120:123],a[120:123],a[180:183],v[120:123]
s_add_u32 m0,s56,5120
ds_read_b128 a[132:135],v0 offset:10240
v_mfma_f32_16x16x32_bf16 v[116:119],a[84:87],a[164:167],v[116:119]
s_add_u32 s62,s53,40960
ds_read_b64_tr_b16 a[12:13],v5 offset:0
v_mfma_f32_16x16x32_bf16 v[116:119],a[116:119],a[180:183],v[116:119]
ds_read_b64_tr_b16 a[14:15],v5 offset:1024
)"
R"(v_mfma_f32_16x16x32_bf16 v[112:115],a[80:83],a[164:167],v[112:115]
buffer_load_dwordx4 v13,s[36:39],s62 offen lds
v_mfma_f32_16x16x32_bf16 v[112:115],a[112:115],a[180:183],v[112:115]
s_add_u32 m0,s56,6144
ds_read_b64_tr_b16 a[44:45],v5 offset:8192
v_mfma_f32_16x16x32_bf16 v[108:111],a[76:79],a[164:167],v[108:111]
s_add_u32 s61,s53,49152
ds_read_b64_tr_b16 a[46:47],v5 offset:9216
v_mfma_f32_16x16x32_bf16 v[108:111],a[108:111],a[180:183],v[108:111]
ds_read_b128 a[148:151],v1 offset:10240
v_mfma_f32_16x16x32_bf16 v[104:107],a[72:75],a[164:167],v[104:107]
buffer_load_dwordx4 v12,s[36:39],s61 offen lds
v_mfma_f32_16x16x32_bf16 v[104:107],a[104:107],a[180:183],v[104:107]
s_add_u32 m0,s56,7168
ds_read_b64_tr_b16 a[16:17],v6 offset:0
v_mfma_f32_16x16x32_bf16 v[100:103],a[68:71],a[164:167],v[100:103]
s_add_u32 s62,s53,57344
ds_read_b64_tr_b16 a[18:19],v6 offset:1024
v_mfma_f32_16x16x32_bf16 v[100:103],a[100:103],a[180:183],v[100:103]
buffer_load_dwordx4 v13,s[36:39],s62 offen lds
v_mfma_f32_16x16x32_bf16 v[96:99],a[64:67],a[164:167],v[96:99]
ds_read_b64_tr_b16 a[48:49],v6 offset:8192
v_mfma_f32_16x16x32_bf16 v[96:99],a[96:99],a[180:183],v[96:99]
buffer_load_dword v61,v60,s[12:15],s87 offen
)"
R"(v_mfma_f32_16x16x32_bf16 v[128:131],a[64:67],a[168:171],v[128:131]
s_add_u32 s87,s87,67108864
ds_read_b64_tr_b16 a[50:51],v6 offset:9216
v_mfma_f32_16x16x32_bf16 v[128:131],a[96:99],a[184:187],v[128:131]
s_xor_b32 s56,s56,0x8000
ds_read_b128 a[136:139],v0 offset:12288
v_mfma_f32_16x16x32_bf16 v[132:135],a[68:71],a[168:171],v[132:135]
ds_read_b64_tr_b16 a[20:21],v7 offset:0
v_mfma_f32_16x16x32_bf16 v[132:135],a[100:103],a[184:187],v[132:135]
s_add_u32 s53,s53,128
ds_read_b64_tr_b16 a[22:23],v7 offset:1024
v_mfma_f32_16x16x32_bf16 v[136:139],a[72:75],a[168:171],v[136:139]
s_cmp_eq_u32 s53,1024
ds_read_b64_tr_b16 a[52:53],v7 offset:8192
v_mfma_f32_16x16x32_bf16 v[136:139],a[104:107],a[184:187],v[136:139]
s_cselect_b32 s53,0,s53
ds_read_b64_tr_b16 a[54:55],v7 offset:9216
v_mfma_f32_16x16x32_bf16 v[140:143],a[76:79],a[168:171],v[140:143]
ds_read_b128 a[152:155],v1 offset:12288
v_mfma_f32_16x16x32_bf16 v[140:143],a[108:111],a[184:187],v[140:143]
s_sub_u32 s60,s60,1
ds_read_b64_tr_b16 a[24:25],v8 offset:0
v_mfma_f32_16x16x32_bf16 v[144:147],a[80:83],a[168:171],v[144:147]
s_cmp_lg_u32 s60,0
)"
R"(ds_read_b64_tr_b16 a[26:27],v8 offset:1024
v_mfma_f32_16x16x32_bf16 v[144:147],a[112:115],a[184:187],v[144:147]
ds_read_b64_tr_b16 a[56:57],v8 offset:8192
v_mfma_f32_16x16x32_bf16 v[148:151],a[84:87],a[168:171],v[148:151]
ds_read_b64_tr_b16 a[58:59],v8 offset:9216
v_mfma_f32_16x16x32_bf16 v[148:151],a[116:119],a[184:187],v[148:151]
ds_read_b128 a[140:143],v0 offset:14336
v_mfma_f32_16x16x32_bf16 v[152:155],a[88:91],a[168:171],v[152:155]
ds_read_b64_tr_b16 a[28:29],v9 offset:0
v_mfma_f32_16x16x32_bf16 v[152:155],a[120:123],a[184:187],v[152:155]
ds_read_b64_tr_b16 a[30:31],v9 offset:1024
v_mfma_f32_16x16x32_bf16 v[156:159],a[92:95],a[168:171],v[156:159]
ds_read_b64_tr_b16 a[60:61],v9 offset:8192
v_mfma_f32_16x16x32_bf16 v[156:159],a[124:127],a[184:187],v[156:159]
ds_read_b64_tr_b16 a[62:63],v9 offset:9216
v_mfma_f32_16x16x32_bf16 v[188:191],a[92:95],a[172:175],v[188:191]
ds_read_b128 a[156:159],v1 offset:14336
v_mfma_f32_16x16x32_bf16 v[188:191],a[124:127],a[188:191],v[188:191]
v_mfma_f32_16x16x32_bf16 v[184:187],a[88:91],a[172:175],v[184:187]
v_mfma_f32_16x16x32_bf16 v[184:187],a[120:123],a[188:191],v[184:187]
v_mfma_f32_16x16x32_bf16 v[180:183],a[84:87],a[172:175],v[180:183]
v_mfma_f32_16x16x32_bf16 v[180:183],a[116:119],a[188:191],v[180:183]
v_mfma_f32_16x16x32_bf16 v[176:179],a[80:83],a[172:175],v[176:179]
v_mfma_f32_16x16x32_bf16 v[176:179],a[112:115],a[188:191],v[176:179]
)"
R"(v_mfma_f32_16x16x32_bf16 v[172:175],a[76:79],a[172:175],v[172:175]
v_mfma_f32_16x16x32_bf16 v[172:175],a[108:111],a[188:191],v[172:175]
v_mfma_f32_16x16x32_bf16 v[168:171],a[72:75],a[172:175],v[168:171]
v_mfma_f32_16x16x32_bf16 v[168:171],a[104:107],a[188:191],v[168:171]
v_mfma_f32_16x16x32_bf16 v[164:167],a[68:71],a[172:175],v[164:167]
v_mfma_f32_16x16x32_bf16 v[164:167],a[100:103],a[188:191],v[164:167]
v_mfma_f32_16x16x32_bf16 v[160:163],a[64:67],a[172:175],v[160:163]
s_waitcnt lgkmcnt(0)
v_mfma_f32_16x16x32_bf16 v[160:163],a[96:99],a[188:191],v[160:163]
s_cbranch_scc1 gm_K1_K_loop
v_mfma_f32_16x16x32_bf16 v[192:195],a[0:3],a[128:131],v[192:195]
s_add_u32 s66,s59,0x8000
s_cmp_eq_u32 s66,0x18000
ds_read_b128 a[160:163],v0 offset:0
v_mfma_f32_16x16x32_bf16 v[192:195],a[32:35],a[144:147],v[192:195]
s_cselect_b32 s66,0,s66
s_sub_u32 s58,s66,s59
ds_read_b128 a[176:179],v1 offset:0
v_mfma_f32_16x16x32_bf16 v[196:199],a[4:7],a[128:131],v[196:199]
s_mov_b32 s59,s66
s_mul_i32 s67,s73,256
ds_read_b128 a[164:167],v0 offset:2048
v_mfma_f32_16x16x32_bf16 v[196:199],a[36:39],a[144:147],v[196:199]
s_mul_i32 s74,s73,1048576
)"
R"(s_lshl_b32 s68,s71,1
ds_read_b128 a[180:183],v1 offset:2048
v_mfma_f32_16x16x32_bf16 v[200:203],a[8:11],a[128:131],v[200:203]
s_sub_u32 s68,15,s68
s_lshl_b32 s68,s68,18
ds_read_b128 a[168:171],v0 offset:4096
v_mfma_f32_16x16x32_bf16 v[200:203],a[40:43],a[144:147],v[200:203]
s_lshl_b32 s56,s70,16
s_branch gm_KB_sk3
.p2align 12
gm_KB_pg3:
s_setpc_b64 s[100:101]
gm_KB_sk3:
s_sub_u32 s56,229376,s56
v_add_u32 v12,s56,v10
v_mfma_f32_16x16x32_bf16 v[204:207],a[12:15],a[128:131],v[204:207]
v_add_u32 v13,s56,v11
ds_read_b128 a[184:187],v1 offset:4096
v_mfma_f32_16x16x32_bf16 v[204:207],a[44:47],a[144:147],v[204:207]
s_lshl_b32 s56,s70,13
s_lshl_b32 s57,s70,12
ds_read_b128 a[172:175],v0 offset:6144
v_mfma_f32_16x16x32_bf16 v[208:211],a[16:19],a[128:131],v[208:211]
s_add_u32 m0,s57,131072
)"
R"(s_add_u32 s82,s74,0
ds_read_b128 a[188:191],v1 offset:6144
v_mfma_f32_16x16x32_bf16 v[208:211],a[48:51],a[144:147],v[208:211]
buffer_load_dwordx4 v16,s[92:95],s82 offen lds
v_mfma_f32_16x16x32_bf16 v[212:215],a[20:23],a[128:131],v[212:215]
v_mfma_f32_16x16x32_bf16 v[212:215],a[52:55],a[144:147],v[212:215]
v_mfma_f32_16x16x32_bf16 v[216:219],a[24:27],a[128:131],v[216:219]
v_mfma_f32_16x16x32_bf16 v[216:219],a[56:59],a[144:147],v[216:219]
v_mfma_f32_16x16x32_bf16 v[220:223],a[28:31],a[128:131],v[220:223]
v_mfma_f32_16x16x32_bf16 v[220:223],a[60:63],a[144:147],v[220:223]
v_mfma_f32_16x16x32_bf16 v[252:255],a[28:31],a[132:135],v[252:255]
v_mfma_f32_16x16x32_bf16 v[252:255],a[60:63],a[148:151],v[252:255]
s_add_u32 m0,s57,132096
s_add_u32 s83,s74,32768
v_mfma_f32_16x16x32_bf16 v[248:251],a[24:27],a[132:135],v[248:251]
v_mfma_f32_16x16x32_bf16 v[248:251],a[56:59],a[148:151],v[248:251]
v_mfma_f32_16x16x32_bf16 v[244:247],a[20:23],a[132:135],v[244:247]
v_mfma_f32_16x16x32_bf16 v[244:247],a[52:55],a[148:151],v[244:247]
v_mfma_f32_16x16x32_bf16 v[240:243],a[16:19],a[132:135],v[240:243]
v_mfma_f32_16x16x32_bf16 v[240:243],a[48:51],a[148:151],v[240:243]
buffer_load_dwordx4 v16,s[92:95],s83 offen lds
v_mfma_f32_16x16x32_bf16 v[236:239],a[12:15],a[132:135],v[236:239]
v_mfma_f32_16x16x32_bf16 v[236:239],a[44:47],a[148:151],v[236:239]
v_mfma_f32_16x16x32_bf16 v[232:235],a[8:11],a[132:135],v[232:235]
)"
R"(v_mfma_f32_16x16x32_bf16 v[232:235],a[40:43],a[148:151],v[232:235]
v_mfma_f32_16x16x32_bf16 v[228:231],a[4:7],a[132:135],v[228:231]
v_mfma_f32_16x16x32_bf16 v[228:231],a[36:39],a[148:151],v[228:231]
v_mfma_f32_16x16x32_bf16 v[224:227],a[0:3],a[132:135],v[224:227]
v_mfma_f32_16x16x32_bf16 v[224:227],a[32:35],a[148:151],v[224:227]
v_mfma_f32_16x16x32_bf16 a[192:195],a[0:3],a[136:139],a[192:195]
v_mfma_f32_16x16x32_bf16 a[192:195],a[32:35],a[152:155],a[192:195]
v_mfma_f32_16x16x32_bf16 a[196:199],a[4:7],a[136:139],a[196:199]
s_add_u32 m0,s57,133120
s_add_u32 s82,s74,65536
v_mfma_f32_16x16x32_bf16 a[196:199],a[36:39],a[152:155],a[196:199]
v_mfma_f32_16x16x32_bf16 a[200:203],a[8:11],a[136:139],a[200:203]
v_mfma_f32_16x16x32_bf16 a[200:203],a[40:43],a[152:155],a[200:203]
v_mfma_f32_16x16x32_bf16 a[204:207],a[12:15],a[136:139],a[204:207]
v_mfma_f32_16x16x32_bf16 a[204:207],a[44:47],a[152:155],a[204:207]
v_mfma_f32_16x16x32_bf16 a[208:211],a[16:19],a[136:139],a[208:211]
buffer_load_dwordx4 v17,s[92:95],s82 offen lds
v_mfma_f32_16x16x32_bf16 a[208:211],a[48:51],a[152:155],a[208:211]
v_mfma_f32_16x16x32_bf16 a[212:215],a[20:23],a[136:139],a[212:215]
v_mfma_f32_16x16x32_bf16 a[212:215],a[52:55],a[152:155],a[212:215]
v_mfma_f32_16x16x32_bf16 a[216:219],a[24:27],a[136:139],a[216:219]
v_xor_b32 v0,0x8000,v0
v_mfma_f32_16x16x32_bf16 a[216:219],a[56:59],a[152:155],a[216:219]
v_xor_b32 v1,0x8000,v1
)"
R"(v_mfma_f32_16x16x32_bf16 a[220:223],a[28:31],a[136:139],a[220:223]
v_add_u32 v2,s58,v2
v_mfma_f32_16x16x32_bf16 a[220:223],a[60:63],a[152:155],a[220:223]
v_add_u32 v3,s58,v3
v_mfma_f32_16x16x32_bf16 a[252:255],a[28:31],a[140:143],a[252:255]
v_add_u32 v4,s58,v4
v_mfma_f32_16x16x32_bf16 a[252:255],a[60:63],a[156:159],a[252:255]
v_add_u32 v5,s58,v5
v_mfma_f32_16x16x32_bf16 a[248:251],a[24:27],a[140:143],a[248:251]
v_add_u32 v6,s58,v6
v_mfma_f32_16x16x32_bf16 a[248:251],a[56:59],a[156:159],a[248:251]
v_add_u32 v7,s58,v7
v_mfma_f32_16x16x32_bf16 a[244:247],a[20:23],a[140:143],a[244:247]
s_add_u32 m0,s57,134144
s_add_u32 s83,s74,98304
v_add_u32 v8,s58,v8
v_mfma_f32_16x16x32_bf16 a[244:247],a[52:55],a[156:159],a[244:247]
v_add_u32 v9,s58,v9
v_mfma_f32_16x16x32_bf16 a[240:243],a[16:19],a[140:143],a[240:243]
v_mfma_f32_16x16x32_bf16 a[240:243],a[48:51],a[156:159],a[240:243]
v_mfma_f32_16x16x32_bf16 a[236:239],a[12:15],a[140:143],a[236:239]
v_mfma_f32_16x16x32_bf16 a[236:239],a[44:47],a[156:159],a[236:239]
v_mfma_f32_16x16x32_bf16 a[232:235],a[8:11],a[140:143],a[232:235]
buffer_load_dwordx4 v17,s[92:95],s83 offen lds
)"
R"(s_add_u32 s74,s74,524288
v_mfma_f32_16x16x32_bf16 a[232:235],a[40:43],a[156:159],a[232:235]
s_and_b32 s74,s74,0x7fffff
v_mfma_f32_16x16x32_bf16 a[228:231],a[4:7],a[140:143],a[228:231]
v_mfma_f32_16x16x32_bf16 a[228:231],a[36:39],a[156:159],a[228:231]
v_mfma_f32_16x16x32_bf16 a[224:227],a[0:3],a[140:143],a[224:227]
s_waitcnt vmcnt(5) lgkmcnt(0)
v_mfma_f32_16x16x32_bf16 a[224:227],a[32:35],a[156:159],a[224:227]
s_barrier
v_mfma_f32_16x16x32_bf16 v[64:67],a[0:3],a[160:163],v[64:67]
ds_read_b64_tr_b16 a[64:65],v2 offset:0
v_mfma_f32_16x16x32_bf16 v[64:67],a[32:35],a[176:179],v[64:67]
buffer_load_dword v61,v60,s[12:15],s87 offen
v_mfma_f32_16x16x32_bf16 v[68:71],a[4:7],a[160:163],v[68:71]
ds_read_b64_tr_b16 a[66:67],v2 offset:1024
v_mfma_f32_16x16x32_bf16 v[68:71],a[36:39],a[176:179],v[68:71]
ds_read_b64_tr_b16 a[96:97],v2 offset:8192
v_mfma_f32_16x16x32_bf16 v[72:75],a[8:11],a[160:163],v[72:75]
ds_read_b64_tr_b16 a[98:99],v2 offset:9216
v_mfma_f32_16x16x32_bf16 v[72:75],a[40:43],a[176:179],v[72:75]
s_add_u32 s87,s87,67108864
s_lshl_b32 s99,%1,2
v_mfma_f32_16x16x32_bf16 v[76:79],a[12:15],a[160:163],v[76:79]
s_add_u32 s99,s99,32768
)"
R"(s_cmp_lg_u32 %2,0
s_cbranch_scc1 gm_KB_w28
v_mov_b32 v22,s72
v_mov_b32 v23,0
s_mov_b64 exec,1
buffer_store_dword v22,v23,s[4:7],s99 offen sc1
s_mov_b64 exec,-1
gm_KB_w28:
v_mfma_f32_16x16x32_bf16 v[76:79],a[44:47],a[176:179],v[76:79]
s_add_u32 s84,s67,s68
s_add_u32 m0,s56,0
v_mfma_f32_16x16x32_bf16 v[80:83],a[16:19],a[160:163],v[80:83]
s_add_u32 s82,s67,0
buffer_load_dwordx4 v10,s[88:91],s82 offen lds
v_mfma_f32_16x16x32_bf16 v[80:83],a[48:51],a[176:179],v[80:83]
ds_read_b128 a[128:131],v0 offset:8192
v_mfma_f32_16x16x32_bf16 v[84:87],a[20:23],a[160:163],v[84:87]
ds_read_b64_tr_b16 a[68:69],v3 offset:0
v_mfma_f32_16x16x32_bf16 v[84:87],a[52:55],a[176:179],v[84:87]
ds_read_b64_tr_b16 a[70:71],v3 offset:1024
v_mfma_f32_16x16x32_bf16 v[88:91],a[24:27],a[160:163],v[88:91]
s_add_u32 m0,s56,1024
s_add_u32 s83,s67,16384
v_mfma_f32_16x16x32_bf16 v[88:91],a[56:59],a[176:179],v[88:91]
)"
R"(ds_read_b64_tr_b16 a[100:101],v3 offset:8192
v_mfma_f32_16x16x32_bf16 v[92:95],a[28:31],a[160:163],v[92:95]
ds_read_b64_tr_b16 a[102:103],v3 offset:9216
v_mfma_f32_16x16x32_bf16 v[92:95],a[60:63],a[176:179],v[92:95]
buffer_load_dwordx4 v11,s[88:91],s83 offen lds
v_mfma_f32_16x16x32_bf16 v[124:127],a[28:31],a[164:167],v[124:127]
ds_read_b128 a[144:147],v1 offset:8192
v_mfma_f32_16x16x32_bf16 v[124:127],a[60:63],a[180:183],v[124:127]
ds_read_b64_tr_b16 a[72:73],v4 offset:0
v_mfma_f32_16x16x32_bf16 v[120:123],a[24:27],a[164:167],v[120:123]
ds_read_b64_tr_b16 a[74:75],v4 offset:1024
v_mfma_f32_16x16x32_bf16 v[120:123],a[56:59],a[180:183],v[120:123]
s_add_u32 m0,s56,2048
s_add_u32 s82,s67,0
v_mfma_f32_16x16x32_bf16 v[116:119],a[20:23],a[164:167],v[116:119]
ds_read_b64_tr_b16 a[104:105],v4 offset:8192
v_mfma_f32_16x16x32_bf16 v[116:119],a[52:55],a[180:183],v[116:119]
ds_read_b64_tr_b16 a[106:107],v4 offset:9216
v_mfma_f32_16x16x32_bf16 v[112:115],a[16:19],a[164:167],v[112:115]
buffer_load_dwordx4 v12,s[88:91],s82 offen lds
v_mfma_f32_16x16x32_bf16 v[112:115],a[48:51],a[180:183],v[112:115]
ds_read_b128 a[132:135],v0 offset:10240
v_mfma_f32_16x16x32_bf16 v[108:111],a[12:15],a[164:167],v[108:111]
ds_read_b64_tr_b16 a[76:77],v5 offset:0
)"
R"(v_mfma_f32_16x16x32_bf16 v[108:111],a[44:47],a[180:183],v[108:111]
ds_read_b64_tr_b16 a[78:79],v5 offset:1024
v_mfma_f32_16x16x32_bf16 v[104:107],a[8:11],a[164:167],v[104:107]
s_add_u32 m0,s56,3072
s_add_u32 s83,s67,16384
v_mfma_f32_16x16x32_bf16 v[104:107],a[40:43],a[180:183],v[104:107]
ds_read_b64_tr_b16 a[108:109],v5 offset:8192
v_mfma_f32_16x16x32_bf16 v[100:103],a[4:7],a[164:167],v[100:103]
ds_read_b64_tr_b16 a[110:111],v5 offset:9216
v_mfma_f32_16x16x32_bf16 v[100:103],a[36:39],a[180:183],v[100:103]
buffer_load_dwordx4 v13,s[88:91],s83 offen lds
v_mfma_f32_16x16x32_bf16 v[96:99],a[0:3],a[164:167],v[96:99]
ds_read_b128 a[148:151],v1 offset:10240
v_mfma_f32_16x16x32_bf16 v[96:99],a[32:35],a[180:183],v[96:99]
ds_read_b64_tr_b16 a[80:81],v6 offset:0
v_mfma_f32_16x16x32_bf16 v[128:131],a[0:3],a[168:171],v[128:131]
ds_read_b64_tr_b16 a[82:83],v6 offset:1024
v_mfma_f32_16x16x32_bf16 v[128:131],a[32:35],a[184:187],v[128:131]
s_add_u32 m0,s56,4096
s_add_u32 s82,s84,0
v_mfma_f32_16x16x32_bf16 v[132:135],a[4:7],a[168:171],v[132:135]
ds_read_b64_tr_b16 a[112:113],v6 offset:8192
v_mfma_f32_16x16x32_bf16 v[132:135],a[36:39],a[184:187],v[132:135]
ds_read_b64_tr_b16 a[114:115],v6 offset:9216
)"
R"(v_mfma_f32_16x16x32_bf16 v[136:139],a[8:11],a[168:171],v[136:139]
buffer_load_dwordx4 v10,s[88:91],s82 offen lds
v_mfma_f32_16x16x32_bf16 v[136:139],a[40:43],a[184:187],v[136:139]
ds_read_b128 a[136:139],v0 offset:12288
v_mfma_f32_16x16x32_bf16 v[140:143],a[12:15],a[168:171],v[140:143]
ds_read_b64_tr_b16 a[84:85],v7 offset:0
v_mfma_f32_16x16x32_bf16 v[140:143],a[44:47],a[184:187],v[140:143]
ds_read_b64_tr_b16 a[86:87],v7 offset:1024
v_mfma_f32_16x16x32_bf16 v[144:147],a[16:19],a[168:171],v[144:147]
s_add_u32 m0,s56,5120
s_add_u32 s83,s84,16384
v_mfma_f32_16x16x32_bf16 v[144:147],a[48:51],a[184:187],v[144:147]
ds_read_b64_tr_b16 a[116:117],v7 offset:8192
v_mfma_f32_16x16x32_bf16 v[148:151],a[20:23],a[168:171],v[148:151]
ds_read_b64_tr_b16 a[118:119],v7 offset:9216
v_mfma_f32_16x16x32_bf16 v[148:151],a[52:55],a[184:187],v[148:151]
buffer_load_dwordx4 v11,s[88:91],s83 offen lds
v_mfma_f32_16x16x32_bf16 v[152:155],a[24:27],a[168:171],v[152:155]
ds_read_b128 a[152:155],v1 offset:12288
v_mfma_f32_16x16x32_bf16 v[152:155],a[56:59],a[184:187],v[152:155]
ds_read_b64_tr_b16 a[88:89],v8 offset:0
v_mfma_f32_16x16x32_bf16 v[156:159],a[28:31],a[168:171],v[156:159]
ds_read_b64_tr_b16 a[90:91],v8 offset:1024
v_mfma_f32_16x16x32_bf16 v[156:159],a[60:63],a[184:187],v[156:159]
)"
R"(s_add_u32 m0,s56,6144
s_add_u32 s82,s84,0
v_mfma_f32_16x16x32_bf16 v[188:191],a[28:31],a[172:175],v[188:191]
ds_read_b64_tr_b16 a[120:121],v8 offset:8192
v_mfma_f32_16x16x32_bf16 v[188:191],a[60:63],a[188:191],v[188:191]
ds_read_b64_tr_b16 a[122:123],v8 offset:9216
v_mfma_f32_16x16x32_bf16 v[184:187],a[24:27],a[172:175],v[184:187]
buffer_load_dwordx4 v12,s[88:91],s82 offen lds
v_mfma_f32_16x16x32_bf16 v[184:187],a[56:59],a[188:191],v[184:187]
ds_read_b128 a[140:143],v0 offset:14336
v_mfma_f32_16x16x32_bf16 v[180:183],a[20:23],a[172:175],v[180:183]
ds_read_b64_tr_b16 a[92:93],v9 offset:0
v_mfma_f32_16x16x32_bf16 v[180:183],a[52:55],a[188:191],v[180:183]
ds_read_b64_tr_b16 a[94:95],v9 offset:1024
v_mfma_f32_16x16x32_bf16 v[176:179],a[16:19],a[172:175],v[176:179]
ds_read_b64_tr_b16 a[124:125],v9 offset:8192
v_mfma_f32_16x16x32_bf16 v[176:179],a[48:51],a[188:191],v[176:179]
s_add_u32 m0,s56,7168
s_add_u32 s83,s84,16384
v_mfma_f32_16x16x32_bf16 v[172:175],a[12:15],a[172:175],v[172:175]
ds_read_b64_tr_b16 a[126:127],v9 offset:9216
v_mfma_f32_16x16x32_bf16 v[172:175],a[44:47],a[188:191],v[172:175]
ds_read_b128 a[156:159],v1 offset:14336
v_mfma_f32_16x16x32_bf16 v[168:171],a[8:11],a[172:175],v[168:171]
)"
R"(buffer_load_dwordx4 v13,s[88:91],s83 offen lds
v_mfma_f32_16x16x32_bf16 v[168:171],a[40:43],a[188:191],v[168:171]
s_add_u32 s67,s67,128
s_and_b32 s67,s67,2047
v_mfma_f32_16x16x32_bf16 v[164:167],a[4:7],a[172:175],v[164:167]
v_mfma_f32_16x16x32_bf16 v[164:167],a[36:39],a[188:191],v[164:167]
v_mfma_f32_16x16x32_bf16 v[160:163],a[0:3],a[172:175],v[160:163]
s_waitcnt lgkmcnt(0)
v_mfma_f32_16x16x32_bf16 v[160:163],a[32:35],a[188:191],v[160:163]
v_mfma_f32_16x16x32_bf16 v[192:195],a[64:67],a[128:131],v[192:195]
s_add_u32 s66,s59,0x8000
s_cmp_eq_u32 s66,0x18000
ds_read_b128 a[160:163],v0 offset:0
v_mfma_f32_16x16x32_bf16 v[192:195],a[96:99],a[144:147],v[192:195]
s_cselect_b32 s66,0,s66
s_sub_u32 s58,s66,s59
ds_read_b128 a[176:179],v1 offset:0
v_mfma_f32_16x16x32_bf16 v[196:199],a[68:71],a[128:131],v[196:199]
s_mov_b32 s59,s66
s_add_u32 m0,s57,147456
ds_read_b128 a[164:167],v0 offset:2048
v_mfma_f32_16x16x32_bf16 v[196:199],a[100:103],a[144:147],v[196:199]
s_add_u32 s82,s74,0
ds_read_b128 a[180:183],v1 offset:2048
)"
R"(v_mfma_f32_16x16x32_bf16 v[200:203],a[72:75],a[128:131],v[200:203]
ds_read_b128 a[168:171],v0 offset:4096
v_mfma_f32_16x16x32_bf16 v[200:203],a[104:107],a[144:147],v[200:203]
v_mfma_f32_16x16x32_bf16 v[204:207],a[76:79],a[128:131],v[204:207]
buffer_load_dwordx4 v16,s[92:95],s82 offen lds
ds_read_b128 a[184:187],v1 offset:4096
v_mfma_f32_16x16x32_bf16 v[204:207],a[108:111],a[144:147],v[204:207]
s_add_u32 m0,s57,148480
s_add_u32 s83,s74,32768
ds_read_b128 a[172:175],v0 offset:6144
v_mfma_f32_16x16x32_bf16 v[208:211],a[80:83],a[128:131],v[208:211]
ds_read_b128 a[188:191],v1 offset:6144
v_mfma_f32_16x16x32_bf16 v[208:211],a[112:115],a[144:147],v[208:211]
v_mfma_f32_16x16x32_bf16 v[212:215],a[84:87],a[128:131],v[212:215]
buffer_load_dwordx4 v16,s[92:95],s83 offen lds
s_add_u32 m0,s57,149504
v_mfma_f32_16x16x32_bf16 v[212:215],a[116:119],a[144:147],v[212:215]
s_add_u32 s82,s74,65536
v_mfma_f32_16x16x32_bf16 v[216:219],a[88:91],a[128:131],v[216:219]
v_mfma_f32_16x16x32_bf16 v[216:219],a[120:123],a[144:147],v[216:219]
v_mfma_f32_16x16x32_bf16 v[220:223],a[92:95],a[128:131],v[220:223]
v_mfma_f32_16x16x32_bf16 v[220:223],a[124:127],a[144:147],v[220:223]
buffer_load_dwordx4 v17,s[92:95],s82 offen lds
s_add_u32 m0,s57,150528
)"
R"(v_mfma_f32_16x16x32_bf16 v[252:255],a[92:95],a[132:135],v[252:255]
s_add_u32 s83,s74,98304
v_mfma_f32_16x16x32_bf16 v[252:255],a[124:127],a[148:151],v[252:255]
v_mfma_f32_16x16x32_bf16 v[248:251],a[88:91],a[132:135],v[248:251]
v_mfma_f32_16x16x32_bf16 v[248:251],a[120:123],a[148:151],v[248:251]
v_mfma_f32_16x16x32_bf16 v[244:247],a[84:87],a[132:135],v[244:247]
buffer_load_dwordx4 v17,s[92:95],s83 offen lds
s_add_u32 s74,s74,524288
v_mfma_f32_16x16x32_bf16 v[244:247],a[116:119],a[148:151],v[244:247]
s_and_b32 s74,s74,0x7fffff
s_add_u32 s84,s67,s68
s_add_u32 m0,s56,65536
v_mfma_f32_16x16x32_bf16 v[240:243],a[80:83],a[132:135],v[240:243]
s_add_u32 s82,s67,0
v_mfma_f32_16x16x32_bf16 v[240:243],a[112:115],a[148:151],v[240:243]
v_mfma_f32_16x16x32_bf16 v[236:239],a[76:79],a[132:135],v[236:239]
buffer_load_dwordx4 v10,s[88:91],s82 offen lds
s_add_u32 m0,s56,66560
v_mfma_f32_16x16x32_bf16 v[236:239],a[108:111],a[148:151],v[236:239]
s_add_u32 s83,s67,16384
v_mfma_f32_16x16x32_bf16 v[232:235],a[72:75],a[132:135],v[232:235]
v_mfma_f32_16x16x32_bf16 v[232:235],a[104:107],a[148:151],v[232:235]
v_mfma_f32_16x16x32_bf16 v[228:231],a[68:71],a[132:135],v[228:231]
v_mfma_f32_16x16x32_bf16 v[228:231],a[100:103],a[148:151],v[228:231]
)"
R"(buffer_load_dwordx4 v11,s[88:91],s83 offen lds
s_add_u32 m0,s56,67584
v_mfma_f32_16x16x32_bf16 v[224:227],a[64:67],a[132:135],v[224:227]
s_add_u32 s82,s67,0
v_mfma_f32_16x16x32_bf16 v[224:227],a[96:99],a[148:151],v[224:227]
v_mfma_f32_16x16x32_bf16 a[192:195],a[64:67],a[136:139],a[192:195]
v_mfma_f32_16x16x32_bf16 a[192:195],a[96:99],a[152:155],a[192:195]
v_mfma_f32_16x16x32_bf16 a[196:199],a[68:71],a[136:139],a[196:199]
buffer_load_dwordx4 v12,s[88:91],s82 offen lds
s_add_u32 m0,s56,68608
v_mfma_f32_16x16x32_bf16 a[196:199],a[100:103],a[152:155],a[196:199]
s_add_u32 s83,s67,16384
v_mfma_f32_16x16x32_bf16 a[200:203],a[72:75],a[136:139],a[200:203]
v_mfma_f32_16x16x32_bf16 a[200:203],a[104:107],a[152:155],a[200:203]
v_mfma_f32_16x16x32_bf16 a[204:207],a[76:79],a[136:139],a[204:207]
v_mfma_f32_16x16x32_bf16 a[204:207],a[108:111],a[152:155],a[204:207]
buffer_load_dwordx4 v13,s[88:91],s83 offen lds
s_add_u32 m0,s56,69632
v_mfma_f32_16x16x32_bf16 a[208:211],a[80:83],a[136:139],a[208:211]
s_add_u32 s82,s84,0
v_mfma_f32_16x16x32_bf16 a[208:211],a[112:115],a[152:155],a[208:211]
v_mfma_f32_16x16x32_bf16 a[212:215],a[84:87],a[136:139],a[212:215]
v_mfma_f32_16x16x32_bf16 a[212:215],a[116:119],a[152:155],a[212:215]
buffer_load_dwordx4 v10,s[88:91],s82 offen lds
)"
R"(s_add_u32 m0,s56,70656
v_mfma_f32_16x16x32_bf16 a[216:219],a[88:91],a[136:139],a[216:219]
s_add_u32 s83,s84,16384
v_xor_b32 v0,0x8000,v0
v_mfma_f32_16x16x32_bf16 a[216:219],a[120:123],a[152:155],a[216:219]
v_xor_b32 v1,0x8000,v1
v_mfma_f32_16x16x32_bf16 a[220:223],a[92:95],a[136:139],a[220:223]
v_add_u32 v2,s58,v2
v_mfma_f32_16x16x32_bf16 a[220:223],a[124:127],a[152:155],a[220:223]
v_add_u32 v3,s58,v3
v_mfma_f32_16x16x32_bf16 a[252:255],a[92:95],a[140:143],a[252:255]
buffer_load_dwordx4 v11,s[88:91],s83 offen lds
v_add_u32 v4,s58,v4
v_mfma_f32_16x16x32_bf16 a[252:255],a[124:127],a[156:159],a[252:255]
s_add_u32 m0,s56,71680
s_add_u32 s82,s84,0
v_add_u32 v5,s58,v5
v_mfma_f32_16x16x32_bf16 a[248:251],a[88:91],a[140:143],a[248:251]
v_add_u32 v6,s58,v6
v_mfma_f32_16x16x32_bf16 a[248:251],a[120:123],a[156:159],a[248:251]
v_add_u32 v7,s58,v7
v_mfma_f32_16x16x32_bf16 a[244:247],a[84:87],a[140:143],a[244:247]
v_add_u32 v8,s58,v8
v_mfma_f32_16x16x32_bf16 a[244:247],a[116:119],a[156:159],a[244:247]
)"
R"(buffer_load_dwordx4 v12,s[88:91],s82 offen lds
v_add_u32 v9,s58,v9
v_mfma_f32_16x16x32_bf16 a[240:243],a[80:83],a[140:143],a[240:243]
s_add_u32 m0,s56,72704
s_add_u32 s83,s84,16384
v_mfma_f32_16x16x32_bf16 a[240:243],a[112:115],a[156:159],a[240:243]
v_mfma_f32_16x16x32_bf16 a[236:239],a[76:79],a[140:143],a[236:239]
v_mfma_f32_16x16x32_bf16 a[236:239],a[108:111],a[156:159],a[236:239]
v_mfma_f32_16x16x32_bf16 a[232:235],a[72:75],a[140:143],a[232:235]
buffer_load_dwordx4 v13,s[88:91],s83 offen lds
s_add_u32 s67,s67,128
v_mfma_f32_16x16x32_bf16 a[232:235],a[104:107],a[156:159],a[232:235]
s_and_b32 s67,s67,2047
v_mfma_f32_16x16x32_bf16 a[228:231],a[68:71],a[140:143],a[228:231]
v_mfma_f32_16x16x32_bf16 a[228:231],a[100:103],a[156:159],a[228:231]
v_mfma_f32_16x16x32_bf16 a[224:227],a[64:67],a[140:143],a[224:227]
s_waitcnt lgkmcnt(0)
v_mfma_f32_16x16x32_bf16 a[224:227],a[96:99],a[156:159],a[224:227]
s_barrier
v_mfma_f32_16x16x32_bf16 v[64:67],a[64:67],a[160:163],v[64:67]
v_mfma_f32_16x16x32_bf16 v[64:67],a[96:99],a[176:179],v[64:67]
buffer_load_dword v61,v60,s[12:15],s87 offen
v_mfma_f32_16x16x32_bf16 v[68:71],a[68:71],a[160:163],v[68:71]
s_add_u32 s87,s87,67108864
)"
R"(s_add_u32 m0,s57,98304
s_add_u32 s82,s74,0
v_mfma_f32_16x16x32_bf16 v[68:71],a[100:103],a[176:179],v[68:71]
v_mfma_f32_16x16x32_bf16 v[72:75],a[72:75],a[160:163],v[72:75]
v_cvt_pk_bf16_f32 v255,v254,v255
v_cvt_pk_bf16_f32 v254,v252,v253
v_mfma_f32_16x16x32_bf16 v[72:75],a[104:107],a[176:179],v[72:75]
buffer_load_dwordx4 v16,s[92:95],s82 offen lds
v_cvt_pk_bf16_f32 v253,v250,v251
v_mfma_f32_16x16x32_bf16 v[76:79],a[76:79],a[160:163],v[76:79]
v_cvt_pk_bf16_f32 v252,v248,v249
v_cvt_pk_bf16_f32 v251,v246,v247
v_mfma_f32_16x16x32_bf16 v[76:79],a[108:111],a[176:179],v[76:79]
s_add_u32 m0,s57,99328
s_add_u32 s83,s74,32768
v_cvt_pk_bf16_f32 v250,v244,v245
v_mfma_f32_16x16x32_bf16 v[80:83],a[80:83],a[160:163],v[80:83]
v_cvt_pk_bf16_f32 v249,v242,v243
v_cvt_pk_bf16_f32 v248,v240,v241
v_mfma_f32_16x16x32_bf16 v[80:83],a[112:115],a[176:179],v[80:83]
v_cvt_pk_bf16_f32 v247,v238,v239
v_cvt_pk_bf16_f32 v246,v236,v237
v_mfma_f32_16x16x32_bf16 v[84:87],a[84:87],a[160:163],v[84:87]
buffer_load_dwordx4 v16,s[92:95],s83 offen lds
)"
R"(v_cvt_pk_bf16_f32 v245,v234,v235
v_mfma_f32_16x16x32_bf16 v[84:87],a[116:119],a[176:179],v[84:87]
v_cvt_pk_bf16_f32 v244,v232,v233
v_cvt_pk_bf16_f32 v243,v230,v231
v_mfma_f32_16x16x32_bf16 v[88:91],a[88:91],a[160:163],v[88:91]
s_add_u32 m0,s57,100352
s_add_u32 s82,s74,65536
v_cvt_pk_bf16_f32 v242,v228,v229
v_mfma_f32_16x16x32_bf16 v[88:91],a[120:123],a[176:179],v[88:91]
v_cvt_pk_bf16_f32 v241,v226,v227
v_cvt_pk_bf16_f32 v240,v224,v225
v_mfma_f32_16x16x32_bf16 v[92:95],a[92:95],a[160:163],v[92:95]
v_cvt_pk_bf16_f32 v239,v222,v223
v_cvt_pk_bf16_f32 v238,v220,v221
v_mfma_f32_16x16x32_bf16 v[92:95],a[124:127],a[176:179],v[92:95]
buffer_load_dwordx4 v17,s[92:95],s82 offen lds
v_cvt_pk_bf16_f32 v237,v218,v219
v_mfma_f32_16x16x32_bf16 v[124:127],a[92:95],a[164:167],v[124:127]
v_cvt_pk_bf16_f32 v236,v216,v217
v_cvt_pk_bf16_f32 v235,v214,v215
v_mfma_f32_16x16x32_bf16 v[124:127],a[124:127],a[180:183],v[124:127]
s_add_u32 m0,s57,101376
s_add_u32 s83,s74,98304
v_cvt_pk_bf16_f32 v234,v212,v213
)"
R"(v_mfma_f32_16x16x32_bf16 v[120:123],a[88:91],a[164:167],v[120:123]
v_cvt_pk_bf16_f32 v233,v210,v211
v_cvt_pk_bf16_f32 v232,v208,v209
v_mfma_f32_16x16x32_bf16 v[120:123],a[120:123],a[180:183],v[120:123]
v_cvt_pk_bf16_f32 v231,v206,v207
v_cvt_pk_bf16_f32 v230,v204,v205
v_mfma_f32_16x16x32_bf16 v[116:119],a[84:87],a[164:167],v[116:119]
buffer_load_dwordx4 v17,s[92:95],s83 offen lds
v_cvt_pk_bf16_f32 v229,v202,v203
v_mfma_f32_16x16x32_bf16 v[116:119],a[116:119],a[180:183],v[116:119]
s_add_u32 s74,s74,524288
s_and_b32 s74,s74,0x7fffff
v_cvt_pk_bf16_f32 v228,v200,v201
v_mfma_f32_16x16x32_bf16 v[112:115],a[80:83],a[164:167],v[112:115]
s_add_u32 s84,s67,s68
s_add_u32 m0,s56,32768
v_cvt_pk_bf16_f32 v227,v198,v199
v_mfma_f32_16x16x32_bf16 v[112:115],a[112:115],a[180:183],v[112:115]
s_add_u32 s82,s67,0
v_cvt_pk_bf16_f32 v226,v196,v197
v_cvt_pk_bf16_f32 v225,v194,v195
v_mfma_f32_16x16x32_bf16 v[108:111],a[76:79],a[164:167],v[108:111]
buffer_load_dwordx4 v10,s[88:91],s82 offen lds
v_cvt_pk_bf16_f32 v224,v192,v193
)"
R"(v_mfma_f32_16x16x32_bf16 v[108:111],a[108:111],a[180:183],v[108:111]
v_accvgpr_read_b32 v24,a192
v_accvgpr_read_b32 v25,a193
v_mfma_f32_16x16x32_bf16 v[104:107],a[72:75],a[164:167],v[104:107]
s_add_u32 m0,s56,33792
s_add_u32 s83,s67,16384
v_accvgpr_read_b32 v26,a194
v_mfma_f32_16x16x32_bf16 v[104:107],a[104:107],a[180:183],v[104:107]
v_accvgpr_read_b32 v27,a195
v_accvgpr_read_b32 v28,a196
v_mfma_f32_16x16x32_bf16 v[100:103],a[68:71],a[164:167],v[100:103]
v_accvgpr_read_b32 v29,a197
v_accvgpr_read_b32 v30,a198
v_mfma_f32_16x16x32_bf16 v[100:103],a[100:103],a[180:183],v[100:103]
buffer_load_dwordx4 v11,s[88:91],s83 offen lds
v_accvgpr_read_b32 v31,a199
v_mfma_f32_16x16x32_bf16 v[96:99],a[64:67],a[164:167],v[96:99]
v_cvt_pk_bf16_f32 v192,v24,v25
v_cvt_pk_bf16_f32 v193,v26,v27
v_mfma_f32_16x16x32_bf16 v[96:99],a[96:99],a[180:183],v[96:99]
s_add_u32 m0,s56,34816
s_add_u32 s82,s67,0
v_cvt_pk_bf16_f32 v194,v28,v29
v_mfma_f32_16x16x32_bf16 v[128:131],a[64:67],a[168:171],v[128:131]
)"
R"(v_cvt_pk_bf16_f32 v195,v30,v31
v_accvgpr_read_b32 v32,a200
v_mfma_f32_16x16x32_bf16 v[128:131],a[96:99],a[184:187],v[128:131]
v_accvgpr_read_b32 v33,a201
v_accvgpr_read_b32 v34,a202
v_mfma_f32_16x16x32_bf16 v[132:135],a[68:71],a[168:171],v[132:135]
buffer_load_dwordx4 v12,s[88:91],s82 offen lds
v_accvgpr_read_b32 v35,a203
v_mfma_f32_16x16x32_bf16 v[132:135],a[100:103],a[184:187],v[132:135]
v_accvgpr_read_b32 v36,a204
v_accvgpr_read_b32 v37,a205
v_mfma_f32_16x16x32_bf16 v[136:139],a[72:75],a[168:171],v[136:139]
s_add_u32 m0,s56,35840
s_add_u32 s83,s67,16384
v_accvgpr_read_b32 v38,a206
v_mfma_f32_16x16x32_bf16 v[136:139],a[104:107],a[184:187],v[136:139]
v_accvgpr_read_b32 v39,a207
v_cvt_pk_bf16_f32 v196,v32,v33
v_mfma_f32_16x16x32_bf16 v[140:143],a[76:79],a[168:171],v[140:143]
v_cvt_pk_bf16_f32 v197,v34,v35
v_cvt_pk_bf16_f32 v198,v36,v37
v_mfma_f32_16x16x32_bf16 v[140:143],a[108:111],a[184:187],v[140:143]
buffer_load_dwordx4 v13,s[88:91],s83 offen lds
v_cvt_pk_bf16_f32 v199,v38,v39
)"
R"(v_mfma_f32_16x16x32_bf16 v[144:147],a[80:83],a[168:171],v[144:147]
s_add_u32 m0,s56,36864
s_add_u32 s82,s84,0
v_accvgpr_read_b32 v24,a208
v_mfma_f32_16x16x32_bf16 v[144:147],a[112:115],a[184:187],v[144:147]
v_accvgpr_read_b32 v25,a209
v_accvgpr_read_b32 v26,a210
v_mfma_f32_16x16x32_bf16 v[148:151],a[84:87],a[168:171],v[148:151]
v_accvgpr_read_b32 v27,a211
v_accvgpr_read_b32 v28,a212
v_mfma_f32_16x16x32_bf16 v[148:151],a[116:119],a[184:187],v[148:151]
buffer_load_dwordx4 v10,s[88:91],s82 offen lds
v_accvgpr_read_b32 v29,a213
v_mfma_f32_16x16x32_bf16 v[152:155],a[88:91],a[168:171],v[152:155]
v_accvgpr_read_b32 v30,a214
v_accvgpr_read_b32 v31,a215
v_mfma_f32_16x16x32_bf16 v[152:155],a[120:123],a[184:187],v[152:155]
s_add_u32 m0,s56,37888
s_add_u32 s83,s84,16384
v_cvt_pk_bf16_f32 v200,v24,v25
v_mfma_f32_16x16x32_bf16 v[156:159],a[92:95],a[168:171],v[156:159]
v_cvt_pk_bf16_f32 v201,v26,v27
v_cvt_pk_bf16_f32 v202,v28,v29
v_mfma_f32_16x16x32_bf16 v[156:159],a[124:127],a[184:187],v[156:159]
)"
R"(v_cvt_pk_bf16_f32 v203,v30,v31
v_accvgpr_read_b32 v32,a216
v_mfma_f32_16x16x32_bf16 v[188:191],a[92:95],a[172:175],v[188:191]
buffer_load_dwordx4 v11,s[88:91],s83 offen lds
v_accvgpr_read_b32 v33,a217
v_mfma_f32_16x16x32_bf16 v[188:191],a[124:127],a[188:191],v[188:191]
v_accvgpr_read_b32 v34,a218
v_accvgpr_read_b32 v35,a219
v_mfma_f32_16x16x32_bf16 v[184:187],a[88:91],a[172:175],v[184:187]
s_add_u32 m0,s56,38912
s_add_u32 s82,s84,0
v_accvgpr_read_b32 v36,a220
v_mfma_f32_16x16x32_bf16 v[184:187],a[120:123],a[188:191],v[184:187]
v_accvgpr_read_b32 v37,a221
v_accvgpr_read_b32 v38,a222
v_mfma_f32_16x16x32_bf16 v[180:183],a[84:87],a[172:175],v[180:183]
v_accvgpr_read_b32 v39,a223
v_cvt_pk_bf16_f32 v204,v32,v33
v_mfma_f32_16x16x32_bf16 v[180:183],a[116:119],a[188:191],v[180:183]
buffer_load_dwordx4 v12,s[88:91],s82 offen lds
v_cvt_pk_bf16_f32 v205,v34,v35
v_mfma_f32_16x16x32_bf16 v[176:179],a[80:83],a[172:175],v[176:179]
v_cvt_pk_bf16_f32 v206,v36,v37
s_branch gm_KB_sk4
)"
R"(.p2align 12
gm_KB_pg4:
s_setpc_b64 s[100:101]
gm_KB_sk4:
v_cvt_pk_bf16_f32 v207,v38,v39
v_mfma_f32_16x16x32_bf16 v[176:179],a[112:115],a[188:191],v[176:179]
s_add_u32 m0,s56,39936
s_add_u32 s83,s84,16384
v_accvgpr_read_b32 v24,a224
v_mfma_f32_16x16x32_bf16 v[172:175],a[76:79],a[172:175],v[172:175]
v_accvgpr_read_b32 v25,a225
v_accvgpr_read_b32 v26,a226
v_mfma_f32_16x16x32_bf16 v[172:175],a[108:111],a[188:191],v[172:175]
v_accvgpr_read_b32 v27,a227
v_accvgpr_read_b32 v28,a228
v_mfma_f32_16x16x32_bf16 v[168:171],a[72:75],a[172:175],v[168:171]
buffer_load_dwordx4 v13,s[88:91],s83 offen lds
v_accvgpr_read_b32 v29,a229
v_mfma_f32_16x16x32_bf16 v[168:171],a[104:107],a[188:191],v[168:171]
s_add_u32 s67,s67,128
s_and_b32 s67,s67,2047
v_accvgpr_read_b32 v30,a230
v_mfma_f32_16x16x32_bf16 v[164:167],a[68:71],a[172:175],v[164:167]
v_accvgpr_read_b32 v31,a231
)"
R"(v_cvt_pk_bf16_f32 v208,v24,v25
v_mfma_f32_16x16x32_bf16 v[164:167],a[100:103],a[188:191],v[164:167]
v_cvt_pk_bf16_f32 v209,v26,v27
v_cvt_pk_bf16_f32 v210,v28,v29
v_mfma_f32_16x16x32_bf16 v[160:163],a[64:67],a[172:175],v[160:163]
v_cvt_pk_bf16_f32 v211,v30,v31
s_waitcnt lgkmcnt(0)
v_mfma_f32_16x16x32_bf16 v[160:163],a[96:99],a[188:191],v[160:163]
s_branch gm_K1_kvdone
gm_KB_tp_v:
s_setpc_b64 s[100:101]
gm_K1_role_v:
v_mfma_f32_16x16x32_bf16 v[192:195],a[128:131],a[0:3],0
s_add_u32 m0,s57,0
s_add_u32 s63,s55,0
ds_read_b128 a[160:163],v0 offset:0
v_mfma_f32_16x16x32_bf16 v[192:195],a[144:147],a[32:35],v[192:195]
buffer_load_dwordx4 v16,s[44:47],s63 offen lds
ds_read_b128 a[176:179],v1 offset:0
v_mfma_f32_16x16x32_bf16 v[200:203],a[128:131],a[4:7],0
s_add_u32 s66,s59,0x8000
s_cmp_eq_u32 s66,0x18000
ds_read_b128 a[164:167],v0 offset:2048
v_mfma_f32_16x16x32_bf16 v[200:203],a[144:147],a[36:39],v[200:203]
)"
R"(s_cselect_b32 s66,0,s66
ds_read_b128 a[180:183],v1 offset:2048
v_mfma_f32_16x16x32_bf16 v[208:211],a[128:131],a[8:11],0
s_sub_u32 s58,s66,s59
s_mov_b32 s59,s66
ds_read_b128 a[168:171],v0 offset:4096
v_mfma_f32_16x16x32_bf16 v[208:211],a[144:147],a[40:43],v[208:211]
s_add_u32 m0,s57,1024
v_mfma_f32_16x16x32_bf16 v[216:219],a[128:131],a[12:15],0
s_add_u32 s64,s55,32768
buffer_load_dwordx4 v16,s[44:47],s64 offen lds
ds_read_b128 a[184:187],v1 offset:4096
v_mfma_f32_16x16x32_bf16 v[216:219],a[144:147],a[44:47],v[216:219]
s_add_u32 m0,s57,2048
ds_read_b128 a[172:175],v0 offset:6144
v_mfma_f32_16x16x32_bf16 v[224:227],a[128:131],a[16:19],0
s_add_u32 s63,s55,65536
buffer_load_dwordx4 v17,s[44:47],s63 offen lds
ds_read_b128 a[188:191],v1 offset:6144
v_mfma_f32_16x16x32_bf16 v[224:227],a[144:147],a[48:51],v[224:227]
s_add_u32 m0,s57,3072
v_mfma_f32_16x16x32_bf16 v[232:235],a[128:131],a[20:23],0
s_add_u32 s64,s55,98304
buffer_load_dwordx4 v17,s[44:47],s64 offen lds
)"
R"(v_mfma_f32_16x16x32_bf16 v[232:235],a[144:147],a[52:55],v[232:235]
s_add_u32 m0,s57,4096
v_mfma_f32_16x16x32_bf16 v[240:243],a[128:131],a[24:27],0
s_add_u32 s63,s55,131072
buffer_load_dwordx4 v16,s[44:47],s63 offen lds
v_mfma_f32_16x16x32_bf16 v[240:243],a[144:147],a[56:59],v[240:243]
s_add_u32 m0,s57,5120
v_mfma_f32_16x16x32_bf16 v[248:251],a[128:131],a[28:31],0
s_add_u32 s64,s55,163840
buffer_load_dwordx4 v16,s[44:47],s64 offen lds
v_mfma_f32_16x16x32_bf16 v[248:251],a[144:147],a[60:63],v[248:251]
s_add_u32 m0,s57,6144
v_mfma_f32_16x16x32_bf16 v[252:255],a[132:135],a[28:31],0
s_add_u32 s63,s55,196608
buffer_load_dwordx4 v17,s[44:47],s63 offen lds
v_mfma_f32_16x16x32_bf16 v[252:255],a[148:151],a[60:63],v[252:255]
s_add_u32 m0,s57,7168
v_mfma_f32_16x16x32_bf16 v[244:247],a[132:135],a[24:27],0
s_add_u32 s64,s55,229376
buffer_load_dwordx4 v17,s[44:47],s64 offen lds
v_mfma_f32_16x16x32_bf16 v[244:247],a[148:151],a[56:59],v[244:247]
s_add_u32 s57,s57,0x8000
v_mfma_f32_16x16x32_bf16 v[236:239],a[132:135],a[20:23],0
s_cmp_ge_u32 s57,0x28000
)"
R"(s_cselect_b32 s65,0x18000,0
v_mfma_f32_16x16x32_bf16 v[236:239],a[148:151],a[52:55],v[236:239]
s_sub_u32 s57,s57,s65
v_mfma_f32_16x16x32_bf16 v[228:231],a[132:135],a[16:19],0
s_add_u32 s55,s55,524288
s_cmp_eq_u32 s55,4194304
v_mfma_f32_16x16x32_bf16 v[228:231],a[148:151],a[48:51],v[228:231]
s_cselect_b32 s55,0,s55
v_mfma_f32_16x16x32_bf16 v[220:223],a[132:135],a[12:15],0
s_lshl_b32 s61,%1,2
s_add_u32 s61,s61,s70
v_mfma_f32_16x16x32_bf16 v[220:223],a[148:151],a[44:47],v[220:223]
s_lshl_b32 s84,s61,3
v_mfma_f32_16x16x32_bf16 v[212:215],a[132:135],a[8:11],0
s_lshl_b32 s61,s61,12
s_add_u32 s88,s16,s61
v_mfma_f32_16x16x32_bf16 v[212:215],a[148:151],a[40:43],v[212:215]
s_addc_u32 s89,s17,0
v_mfma_f32_16x16x32_bf16 v[204:207],a[132:135],a[4:7],0
s_and_b32 s89,s89,0xffff
v_mfma_f32_16x16x32_bf16 v[204:207],a[148:151],a[36:39],v[204:207]
s_sub_u32 s90,4194304,s61
s_mov_b32 s91,0x20000
v_mfma_f32_16x16x32_bf16 v[196:199],a[132:135],a[0:3],0
)"
R"(buffer_load_dwordx4 v[32:35],v18,s[88:91],0 offen offset:0
v_mfma_f32_16x16x32_bf16 v[196:199],a[148:151],a[32:35],v[196:199]
buffer_load_dwordx4 v[36:39],v18,s[88:91],0 offen offset:1024
buffer_load_dwordx4 v[40:43],v18,s[88:91],0 offen offset:2048
v_mfma_f32_16x16x32_bf16 a[192:195],a[136:139],a[0:3],0
buffer_load_dwordx4 v[44:47],v18,s[88:91],0 offen offset:3072
v_mfma_f32_16x16x32_bf16 a[192:195],a[152:155],a[32:35],a[192:195]
s_mov_b32 s61,0
s_add_u32 s92,s98,s61
v_mfma_f32_16x16x32_bf16 a[200:203],a[136:139],a[4:7],0
s_addc_u32 s93,s99,0
v_mfma_f32_16x16x32_bf16 a[200:203],a[152:155],a[36:39],a[200:203]
s_and_b32 s93,s93,0xffff
s_sub_u32 s94,2048,s61
v_mfma_f32_16x16x32_bf16 a[208:211],a[136:139],a[8:11],0
s_mov_b32 s95,0x20000
v_mfma_f32_16x16x32_bf16 a[208:211],a[152:155],a[40:43],a[208:211]
buffer_load_dwordx4 v[48:51],v18,s[92:95],0 offen offset:0
buffer_load_dwordx4 v[52:55],v18,s[92:95],0 offen offset:1024
v_mfma_f32_16x16x32_bf16 a[216:219],a[136:139],a[12:15],0
s_mov_b32 s61,100663296
v_mfma_f32_16x16x32_bf16 a[216:219],a[152:155],a[44:47],a[216:219]
s_add_u32 s4,s30,s61
s_addc_u32 s5,s31,0
)"
R"(v_mfma_f32_16x16x32_bf16 a[224:227],a[136:139],a[16:19],0
s_and_b32 s5,s5,0xffff
v_mfma_f32_16x16x32_bf16 a[224:227],a[152:155],a[48:51],a[224:227]
s_mov_b32 s6,65536
s_mov_b32 s7,0x20000
v_mfma_f32_16x16x32_bf16 a[232:235],a[136:139],a[20:23],0
v_mov_b32 v28,0
v_mfma_f32_16x16x32_bf16 a[232:235],a[152:155],a[52:55],a[232:235]
s_mov_b32 s61,16384
buffer_load_dword v28,v28,s[4:7],s61 offen sc1
v_mfma_f32_16x16x32_bf16 a[240:243],a[136:139],a[24:27],0
s_and_b32 s67,s70,1
v_xor_b32 v0,0x8000,v0
v_mfma_f32_16x16x32_bf16 a[240:243],a[152:155],a[56:59],a[240:243]
s_lshl_b32 s68,s74,1
s_add_u32 s67,s67,s68
v_xor_b32 v1,0x8000,v1
v_mfma_f32_16x16x32_bf16 a[248:251],a[136:139],a[28:31],0
s_lshl_b32 s67,s67,4
v_add_u32 v2,s58,v2
v_mfma_f32_16x16x32_bf16 a[248:251],a[152:155],a[60:63],a[248:251]
s_lshr_b32 s68,s70,1
s_add_u32 s67,s67,s68
v_add_u32 v3,s58,v3
)"
R"(v_mfma_f32_16x16x32_bf16 a[252:255],a[140:143],a[28:31],0
s_lshl_b32 s68,s71,1
v_add_u32 v4,s58,v4
v_mfma_f32_16x16x32_bf16 a[252:255],a[156:159],a[60:63],a[252:255]
s_add_u32 s67,s67,s68
s_lshl_b32 s67,s67,15
v_add_u32 v5,s58,v5
v_mfma_f32_16x16x32_bf16 a[244:247],a[140:143],a[24:27],0
s_add_u32 s67,s67,16777216
v_add_u32 v6,s58,v6
v_mfma_f32_16x16x32_bf16 a[244:247],a[156:159],a[56:59],a[244:247]
s_add_u32 s48,s30,s67
s_addc_u32 s49,s31,0
v_add_u32 v7,s58,v7
v_mfma_f32_16x16x32_bf16 a[236:239],a[140:143],a[20:23],0
s_and_b32 s49,s49,0xffff
v_add_u32 v8,s58,v8
v_mfma_f32_16x16x32_bf16 a[236:239],a[156:159],a[52:55],a[236:239]
s_sub_u32 s50,33554432,s67
s_mov_b32 s51,0x20000
v_add_u32 v9,s58,v9
v_mfma_f32_16x16x32_bf16 a[228:231],a[140:143],a[16:19],0
s_cmp_lt_u32 s69,16
v_mfma_f32_16x16x32_bf16 a[228:231],a[156:159],a[48:51],a[228:231]
)"
R"(s_cselect_b32 s61,0,16777216
s_add_u32 s48,s48,s61
v_mfma_f32_16x16x32_bf16 a[220:223],a[140:143],a[12:15],0
s_addc_u32 s49,s49,0
v_mfma_f32_16x16x32_bf16 a[220:223],a[156:159],a[44:47],a[220:223]
s_mov_b32 s50,32768
v_mfma_f32_16x16x32_bf16 a[212:215],a[140:143],a[8:11],0
v_mfma_f32_16x16x32_bf16 a[212:215],a[156:159],a[40:43],a[212:215]
v_mfma_f32_16x16x32_bf16 a[204:207],a[140:143],a[4:7],0
v_mfma_f32_16x16x32_bf16 a[204:207],a[156:159],a[36:39],a[204:207]
v_mfma_f32_16x16x32_bf16 a[196:199],a[140:143],a[0:3],0
s_waitcnt vmcnt(15) lgkmcnt(0)
v_mfma_f32_16x16x32_bf16 a[196:199],a[156:159],a[32:35],a[196:199]
s_barrier
v_mfma_f32_16x16x32_bf16 v[64:67],a[160:163],a[0:3],0
s_add_u32 m0,s56,0
s_add_u32 s61,s53,0
ds_read_b64_tr_b16 a[64:65],v2 offset:0
v_mfma_f32_16x16x32_bf16 v[64:67],a[176:179],a[32:35],v[64:67]
buffer_load_dwordx4 v12,s[36:39],s61 offen lds
ds_read_b64_tr_b16 a[66:67],v2 offset:1024
v_mfma_f32_16x16x32_bf16 v[72:75],a[160:163],a[4:7],0
s_add_u32 m0,s56,1024
ds_read_b64_tr_b16 a[96:97],v2 offset:8192
)"
R"(v_mfma_f32_16x16x32_bf16 v[72:75],a[176:179],a[36:39],v[72:75]
s_add_u32 s62,s53,8192
buffer_load_dwordx4 v13,s[36:39],s62 offen lds
ds_read_b64_tr_b16 a[98:99],v2 offset:9216
v_mfma_f32_16x16x32_bf16 v[80:83],a[160:163],a[8:11],0
s_add_u32 m0,s56,2048
ds_read_b128 a[128:131],v0 offset:8192
v_mfma_f32_16x16x32_bf16 v[80:83],a[176:179],a[40:43],v[80:83]
s_add_u32 s61,s53,16384
ds_read_b64_tr_b16 a[68:69],v3 offset:0
v_mfma_f32_16x16x32_bf16 v[88:91],a[160:163],a[12:15],0
buffer_load_dwordx4 v12,s[36:39],s61 offen lds
ds_read_b64_tr_b16 a[70:71],v3 offset:1024
v_mfma_f32_16x16x32_bf16 v[88:91],a[176:179],a[44:47],v[88:91]
s_add_u32 m0,s56,3072
s_add_u32 s62,s53,24576
ds_read_b64_tr_b16 a[100:101],v3 offset:8192
v_mfma_f32_16x16x32_bf16 v[96:99],a[160:163],a[16:19],0
buffer_load_dwordx4 v13,s[36:39],s62 offen lds
ds_read_b64_tr_b16 a[102:103],v3 offset:9216
v_mfma_f32_16x16x32_bf16 v[96:99],a[176:179],a[48:51],v[96:99]
s_add_u32 m0,s56,4096
s_add_u32 s61,s53,32768
ds_read_b128 a[144:147],v1 offset:8192
)"
R"(v_mfma_f32_16x16x32_bf16 v[104:107],a[160:163],a[20:23],0
buffer_load_dwordx4 v12,s[36:39],s61 offen lds
ds_read_b64_tr_b16 a[72:73],v4 offset:0
v_mfma_f32_16x16x32_bf16 v[104:107],a[176:179],a[52:55],v[104:107]
s_add_u32 m0,s56,5120
ds_read_b64_tr_b16 a[74:75],v4 offset:1024
v_mfma_f32_16x16x32_bf16 v[112:115],a[160:163],a[24:27],0
s_add_u32 s62,s53,40960
buffer_load_dwordx4 v13,s[36:39],s62 offen lds
ds_read_b64_tr_b16 a[104:105],v4 offset:8192
v_mfma_f32_16x16x32_bf16 v[112:115],a[176:179],a[56:59],v[112:115]
s_add_u32 m0,s56,6144
ds_read_b64_tr_b16 a[106:107],v4 offset:9216
v_mfma_f32_16x16x32_bf16 v[120:123],a[160:163],a[28:31],0
s_add_u32 s61,s53,49152
ds_read_b128 a[132:135],v0 offset:10240
v_mfma_f32_16x16x32_bf16 v[120:123],a[176:179],a[60:63],v[120:123]
buffer_load_dwordx4 v12,s[36:39],s61 offen lds
ds_read_b64_tr_b16 a[76:77],v5 offset:0
v_mfma_f32_16x16x32_bf16 v[124:127],a[164:167],a[28:31],0
s_add_u32 m0,s56,7168
s_add_u32 s62,s53,57344
ds_read_b64_tr_b16 a[78:79],v5 offset:1024
v_mfma_f32_16x16x32_bf16 v[124:127],a[180:183],a[60:63],v[124:127]
)"
R"(buffer_load_dwordx4 v13,s[36:39],s62 offen lds
ds_read_b64_tr_b16 a[108:109],v5 offset:8192
v_mfma_f32_16x16x32_bf16 v[116:119],a[164:167],a[24:27],0
buffer_load_dword v61,v60,s[12:15],s87 offen
ds_read_b64_tr_b16 a[110:111],v5 offset:9216
v_mfma_f32_16x16x32_bf16 v[116:119],a[180:183],a[56:59],v[116:119]
s_add_u32 s87,s87,67108864
v_lshrrev_b32 v20,4,v19
ds_read_b128 a[148:151],v1 offset:10240
v_mfma_f32_16x16x32_bf16 v[108:111],a[164:167],a[20:23],0
v_add_u32 v20,0,v20
ds_read_b64_tr_b16 a[80:81],v6 offset:0
v_mfma_f32_16x16x32_bf16 v[108:111],a[180:183],a[52:55],v[108:111]
v_and_b32 v21,7,v19
ds_read_b64_tr_b16 a[82:83],v6 offset:1024
v_mfma_f32_16x16x32_bf16 v[100:103],a[164:167],a[16:19],0
v_xor_b32 v20,v20,v21
ds_read_b64_tr_b16 a[112:113],v6 offset:8192
v_mfma_f32_16x16x32_bf16 v[100:103],a[180:183],a[48:51],v[100:103]
v_lshlrev_b32 v20,4,v20
ds_read_b64_tr_b16 a[114:115],v6 offset:9216
v_mfma_f32_16x16x32_bf16 v[92:95],a[164:167],a[12:15],0
v_lshrrev_b32 v21,3,v19
ds_read_b128 a[136:139],v0 offset:12288
)"
R"(v_mfma_f32_16x16x32_bf16 v[92:95],a[180:183],a[44:47],v[92:95]
v_mul_u32_u24 v21,2048,v21
ds_read_b64_tr_b16 a[84:85],v7 offset:0
v_mfma_f32_16x16x32_bf16 v[84:87],a[164:167],a[8:11],0
v_add_u32 v10,v20,v21
ds_read_b64_tr_b16 a[86:87],v7 offset:1024
v_mfma_f32_16x16x32_bf16 v[84:87],a[180:183],a[40:43],v[84:87]
v_lshrrev_b32 v20,4,v19
ds_read_b64_tr_b16 a[116:117],v7 offset:8192
v_mfma_f32_16x16x32_bf16 v[76:79],a[164:167],a[4:7],0
v_or_b32 v20,0,v20
ds_read_b64_tr_b16 a[118:119],v7 offset:9216
v_mfma_f32_16x16x32_bf16 v[76:79],a[180:183],a[36:39],v[76:79]
v_bfe_u32 v21,v19,1,3
ds_read_b128 a[152:155],v1 offset:12288
v_mfma_f32_16x16x32_bf16 v[68:71],a[164:167],a[0:3],0
v_xor_b32 v20,v20,v21
ds_read_b64_tr_b16 a[88:89],v8 offset:0
v_mfma_f32_16x16x32_bf16 v[68:71],a[180:183],a[32:35],v[68:71]
v_add_u32 v22,1,v20
ds_read_b64_tr_b16 a[90:91],v8 offset:1024
v_mfma_f32_16x16x32_bf16 v[128:131],a[168:171],a[0:3],0
v_lshrrev_b32 v22,3,v22
ds_read_b64_tr_b16 a[120:121],v8 offset:8192
)"
R"(v_mfma_f32_16x16x32_bf16 v[128:131],a[184:187],a[32:35],v[128:131]
s_and_b32 s63,s86,1
v_add_u32 v20,s63,v20
ds_read_b64_tr_b16 a[122:123],v8 offset:9216
v_mfma_f32_16x16x32_bf16 v[136:139],a[168:171],a[4:7],0
v_cmp_ne_u32 vcc,7,v20
ds_read_b128 a[140:143],v0 offset:14336
v_mfma_f32_16x16x32_bf16 v[136:139],a[184:187],a[36:39],v[136:139]
v_cndmask_b32 v20,0,v20,vcc
ds_read_b64_tr_b16 a[92:93],v9 offset:0
v_mfma_f32_16x16x32_bf16 v[144:147],a[168:171],a[8:11],0
v_lshl_add_u32 v20,v22,25,v20
ds_read_b64_tr_b16 a[94:95],v9 offset:1024
v_mfma_f32_16x16x32_bf16 v[144:147],a[184:187],a[40:43],v[144:147]
v_lshlrev_b32 v20,5,v20
ds_read_b64_tr_b16 a[124:125],v9 offset:8192
v_mfma_f32_16x16x32_bf16 v[152:155],a[168:171],a[12:15],0
v_and_b32 v21,1,v19
ds_read_b64_tr_b16 a[126:127],v9 offset:9216
v_mfma_f32_16x16x32_bf16 v[152:155],a[184:187],a[44:47],v[152:155]
v_lshl_add_u32 v20,v21,4,v20
ds_read_b128 a[156:159],v1 offset:14336
v_mfma_f32_16x16x32_bf16 v[160:163],a[168:171],a[16:19],0
v_lshrrev_b32 v21,4,v19
)"
R"(v_mul_u32_u24 v21,7168,v21
v_mfma_f32_16x16x32_bf16 v[160:163],a[184:187],a[48:51],v[160:163]
v_add_u32 v14,v20,v21
v_lshrrev_b32 v20,4,v19
v_mfma_f32_16x16x32_bf16 v[168:171],a[168:171],a[20:23],0
v_add_u32 v20,4,v20
v_and_b32 v21,7,v19
v_mfma_f32_16x16x32_bf16 v[168:171],a[184:187],a[52:55],v[168:171]
v_xor_b32 v20,v20,v21
v_lshlrev_b32 v20,4,v20
v_mfma_f32_16x16x32_bf16 v[176:179],a[168:171],a[24:27],0
v_lshrrev_b32 v21,3,v19
v_mul_u32_u24 v21,2048,v21
v_mfma_f32_16x16x32_bf16 v[176:179],a[184:187],a[56:59],v[176:179]
v_add_u32 v11,v20,v21
v_lshrrev_b32 v20,4,v19
v_mfma_f32_16x16x32_bf16 v[184:187],a[168:171],a[28:31],0
v_or_b32 v20,4,v20
v_bfe_u32 v21,v19,1,3
v_mfma_f32_16x16x32_bf16 v[184:187],a[184:187],a[60:63],v[184:187]
v_xor_b32 v20,v20,v21
v_add_u32 v22,1,v20
v_mfma_f32_16x16x32_bf16 v[188:191],a[172:175],a[28:31],0
v_lshrrev_b32 v22,3,v22
)"
R"(s_and_b32 s63,s86,1
v_mfma_f32_16x16x32_bf16 v[188:191],a[188:191],a[60:63],v[188:191]
v_add_u32 v20,s63,v20
v_mfma_f32_16x16x32_bf16 v[180:183],a[172:175],a[24:27],0
v_cmp_ne_u32 vcc,7,v20
v_mfma_f32_16x16x32_bf16 v[180:183],a[188:191],a[56:59],v[180:183]
s_nop 0
v_cndmask_b32 v20,0,v20,vcc
v_lshl_add_u32 v20,v22,25,v20
v_mfma_f32_16x16x32_bf16 v[172:175],a[172:175],a[20:23],0
v_lshlrev_b32 v20,5,v20
v_mfma_f32_16x16x32_bf16 v[172:175],a[188:191],a[52:55],v[172:175]
v_and_b32 v21,1,v19
v_mfma_f32_16x16x32_bf16 v[164:167],a[172:175],a[16:19],0
v_lshl_add_u32 v20,v21,4,v20
v_lshrrev_b32 v21,4,v19
v_mfma_f32_16x16x32_bf16 v[164:167],a[188:191],a[48:51],v[164:167]
v_mul_u32_u24 v21,7168,v21
v_mfma_f32_16x16x32_bf16 v[156:159],a[172:175],a[12:15],0
v_add_u32 v15,v20,v21
v_mfma_f32_16x16x32_bf16 v[156:159],a[188:191],a[44:47],v[156:159]
s_xor_b32 s56,s56,0x8000
s_add_u32 s53,s53,128
v_mfma_f32_16x16x32_bf16 v[148:151],a[172:175],a[8:11],0
)"
R"(s_cmp_eq_u32 s53,1024
v_mfma_f32_16x16x32_bf16 v[148:151],a[188:191],a[40:43],v[148:151]
s_cselect_b32 s53,0,s53
v_mfma_f32_16x16x32_bf16 v[140:143],a[172:175],a[4:7],0
v_mfma_f32_16x16x32_bf16 v[140:143],a[188:191],a[36:39],v[140:143]
v_mfma_f32_16x16x32_bf16 v[132:135],a[172:175],a[0:3],0
s_waitcnt lgkmcnt(0)
v_mfma_f32_16x16x32_bf16 v[132:135],a[188:191],a[32:35],v[132:135]
v_mfma_f32_16x16x32_bf16 v[192:195],a[128:131],a[64:67],v[192:195]
s_add_u32 m0,s57,0
s_add_u32 s63,s55,0
ds_read_b128 a[160:163],v0 offset:0
v_mfma_f32_16x16x32_bf16 v[192:195],a[144:147],a[96:99],v[192:195]
buffer_load_dwordx4 v16,s[44:47],s63 offen lds
ds_read_b128 a[176:179],v1 offset:0
v_mfma_f32_16x16x32_bf16 v[200:203],a[128:131],a[68:71],v[200:203]
s_add_u32 s66,s59,0x8000
ds_read_b128 a[164:167],v0 offset:2048
v_mfma_f32_16x16x32_bf16 v[200:203],a[144:147],a[100:103],v[200:203]
s_cmp_eq_u32 s66,0x18000
s_cselect_b32 s66,0,s66
ds_read_b128 a[180:183],v1 offset:2048
v_mfma_f32_16x16x32_bf16 v[208:211],a[128:131],a[72:75],v[208:211]
s_sub_u32 s58,s66,s59
)"
R"(ds_read_b128 a[168:171],v0 offset:4096
v_mfma_f32_16x16x32_bf16 v[208:211],a[144:147],a[104:107],v[208:211]
s_mov_b32 s59,s66
v_mfma_f32_16x16x32_bf16 v[216:219],a[128:131],a[76:79],v[216:219]
s_add_u32 m0,s57,1024
s_add_u32 s64,s55,32768
ds_read_b128 a[184:187],v1 offset:4096
v_mfma_f32_16x16x32_bf16 v[216:219],a[144:147],a[108:111],v[216:219]
buffer_load_dwordx4 v16,s[44:47],s64 offen lds
ds_read_b128 a[172:175],v0 offset:6144
v_mfma_f32_16x16x32_bf16 v[224:227],a[128:131],a[80:83],v[224:227]
s_add_u32 m0,s57,2048
ds_read_b128 a[188:191],v1 offset:6144
v_mfma_f32_16x16x32_bf16 v[224:227],a[144:147],a[112:115],v[224:227]
s_add_u32 s63,s55,65536
buffer_load_dwordx4 v17,s[44:47],s63 offen lds
v_mfma_f32_16x16x32_bf16 v[232:235],a[128:131],a[84:87],v[232:235]
s_add_u32 m0,s57,3072
v_mfma_f32_16x16x32_bf16 v[232:235],a[144:147],a[116:119],v[232:235]
s_add_u32 s64,s55,98304
v_mfma_f32_16x16x32_bf16 v[240:243],a[128:131],a[88:91],v[240:243]
buffer_load_dwordx4 v17,s[44:47],s64 offen lds
s_add_u32 m0,s57,4096
v_mfma_f32_16x16x32_bf16 v[240:243],a[144:147],a[120:123],v[240:243]
)"
R"(s_add_u32 s63,s55,131072
v_mfma_f32_16x16x32_bf16 v[248:251],a[128:131],a[92:95],v[248:251]
buffer_load_dwordx4 v16,s[44:47],s63 offen lds
v_mfma_f32_16x16x32_bf16 v[248:251],a[144:147],a[124:127],v[248:251]
s_add_u32 m0,s57,5120
s_add_u32 s64,s55,163840
v_mfma_f32_16x16x32_bf16 v[252:255],a[132:135],a[92:95],v[252:255]
buffer_load_dwordx4 v16,s[44:47],s64 offen lds
v_mfma_f32_16x16x32_bf16 v[252:255],a[148:151],a[124:127],v[252:255]
s_add_u32 m0,s57,6144
v_mfma_f32_16x16x32_bf16 v[244:247],a[132:135],a[88:91],v[244:247]
s_add_u32 s63,s55,196608
buffer_load_dwordx4 v17,s[44:47],s63 offen lds
v_mfma_f32_16x16x32_bf16 v[244:247],a[148:151],a[120:123],v[244:247]
s_add_u32 m0,s57,7168
v_mfma_f32_16x16x32_bf16 v[236:239],a[132:135],a[84:87],v[236:239]
s_add_u32 s64,s55,229376
v_mfma_f32_16x16x32_bf16 v[236:239],a[148:151],a[116:119],v[236:239]
buffer_load_dwordx4 v17,s[44:47],s64 offen lds
s_add_u32 s57,s57,0x8000
v_mfma_f32_16x16x32_bf16 v[228:231],a[132:135],a[80:83],v[228:231]
s_cmp_ge_u32 s57,0x28000
v_mfma_f32_16x16x32_bf16 v[228:231],a[148:151],a[112:115],v[228:231]
s_cselect_b32 s65,0x18000,0
)"
R"(v_mfma_f32_16x16x32_bf16 v[220:223],a[132:135],a[76:79],v[220:223]
s_sub_u32 s57,s57,s65
s_add_u32 s55,s55,524288
v_mfma_f32_16x16x32_bf16 v[220:223],a[148:151],a[108:111],v[220:223]
s_cmp_eq_u32 s55,4194304
v_mfma_f32_16x16x32_bf16 v[212:215],a[132:135],a[72:75],v[212:215]
s_cselect_b32 s55,0,s55
v_mfma_f32_16x16x32_bf16 v[212:215],a[148:151],a[104:107],v[212:215]
s_lshl_b32 s67,s71,2
s_add_u32 s67,s67,s70
v_mfma_f32_16x16x32_bf16 v[204:207],a[132:135],a[68:71],v[204:207]
s_mul_i32 s67,s67,131072
v_mfma_f32_16x16x32_bf16 v[204:207],a[148:151],a[100:103],v[204:207]
s_and_b32 s68,s70,1
v_mfma_f32_16x16x32_bf16 v[196:199],a[132:135],a[64:67],v[196:199]
s_mul_i32 s68,s68,229376
s_mul_i32 s62,s86,224
v_mfma_f32_16x16x32_bf16 v[196:199],a[148:151],a[96:99],v[196:199]
s_add_u32 s68,s68,s62
v_mfma_f32_16x16x32_bf16 a[192:195],a[136:139],a[64:67],a[192:195]
s_mul_i32 s52,s73,256
v_mfma_f32_16x16x32_bf16 a[192:195],a[152:155],a[96:99],a[192:195]
s_mul_i32 s54,s73,917504
s_cmp_lt_u32 s70,2
)"
R"(v_mfma_f32_16x16x32_bf16 a[200:203],a[136:139],a[68:71],a[200:203]
s_cselect_b64 s[64:65],s[26:27],s[28:29]
v_mfma_f32_16x16x32_bf16 a[200:203],a[152:155],a[100:103],a[200:203]
s_add_u32 s40,s64,s68
v_mfma_f32_16x16x32_bf16 a[208:211],a[136:139],a[72:75],a[208:211]
s_addc_u32 s41,s65,0
s_and_b32 s41,s41,0xffff
v_mfma_f32_16x16x32_bf16 a[208:211],a[152:155],a[104:107],a[208:211]
s_sub_u32 s42,7340032,s68
v_mfma_f32_16x16x32_bf16 a[216:219],a[136:139],a[76:79],a[216:219]
s_mov_b32 s43,0x20000
v_mfma_f32_16x16x32_bf16 a[216:219],a[152:155],a[108:111],a[216:219]
s_add_u32 s32,s16,s67
s_addc_u32 s33,s17,0
v_mfma_f32_16x16x32_bf16 a[224:227],a[136:139],a[80:83],a[224:227]
s_and_b32 s33,s33,0xffff
v_mfma_f32_16x16x32_bf16 a[224:227],a[152:155],a[112:115],a[224:227]
s_sub_u32 s34,4194304,s67
v_mfma_f32_16x16x32_bf16 a[232:235],a[136:139],a[84:87],a[232:235]
s_mov_b32 s35,0x20000
s_lshl_b32 s67,s71,3
v_mfma_f32_16x16x32_bf16 a[232:235],a[152:155],a[116:119],a[232:235]
s_add_u32 s67,s67,s70
v_mfma_f32_16x16x32_bf16 a[240:243],a[136:139],a[88:91],a[240:243]
)"
R"(s_lshl_b32 s67,s67,15
v_xor_b32 v0,0x8000,v0
v_mfma_f32_16x16x32_bf16 a[240:243],a[152:155],a[120:123],a[240:243]
s_add_u32 s88,s16,s67
s_addc_u32 s89,s17,0
v_xor_b32 v1,0x8000,v1
v_mfma_f32_16x16x32_bf16 a[248:251],a[136:139],a[92:95],a[248:251]
s_and_b32 s89,s89,0xffff
v_add_u32 v2,s58,v2
v_mfma_f32_16x16x32_bf16 a[248:251],a[152:155],a[124:127],a[248:251]
s_sub_u32 s90,4194304,s67
v_add_u32 v3,s58,v3
v_mfma_f32_16x16x32_bf16 a[252:255],a[140:143],a[92:95],a[252:255]
s_mov_b32 s91,0x20000
s_lshl_b32 s67,s70,17
v_add_u32 v4,s58,v4
v_mfma_f32_16x16x32_bf16 a[252:255],a[156:159],a[124:127],a[252:255]
s_lshl_b32 s68,s86,8
v_add_u32 v5,s58,v5
v_mfma_f32_16x16x32_bf16 a[244:247],a[140:143],a[88:91],a[244:247]
s_add_u32 s67,s67,s68
v_add_u32 v6,s58,v6
v_mfma_f32_16x16x32_bf16 a[244:247],a[156:159],a[120:123],a[244:247]
s_add_u32 s92,s20,s67
)"
R"(s_addc_u32 s93,s21,0
v_add_u32 v7,s58,v7
v_mfma_f32_16x16x32_bf16 a[236:239],a[140:143],a[84:87],a[236:239]
s_and_b32 s93,s93,0xffff
v_add_u32 v8,s58,v8
v_mfma_f32_16x16x32_bf16 a[236:239],a[156:159],a[116:119],a[236:239]
s_sub_u32 s94,8388608,s67
v_add_u32 v9,s58,v9
v_mfma_f32_16x16x32_bf16 a[228:231],a[140:143],a[80:83],a[228:231]
s_mov_b32 s95,0x20000
s_and_b32 s61,s70,1
v_mfma_f32_16x16x32_bf16 a[228:231],a[156:159],a[112:115],a[228:231]
s_lshl_b32 s98,s61,13
v_mfma_f32_16x16x32_bf16 a[220:223],a[140:143],a[76:79],a[220:223]
s_lshl_b32 s61,s61,14
v_mfma_f32_16x16x32_bf16 a[220:223],a[156:159],a[108:111],a[220:223]
s_sub_u32 s75,0,s61
v_mfma_f32_16x16x32_bf16 a[212:215],a[140:143],a[72:75],a[212:215]
v_mfma_f32_16x16x32_bf16 a[212:215],a[156:159],a[104:107],a[212:215]
v_mfma_f32_16x16x32_bf16 a[204:207],a[140:143],a[68:71],a[204:207]
v_mfma_f32_16x16x32_bf16 a[204:207],a[156:159],a[100:103],a[204:207]
v_mfma_f32_16x16x32_bf16 a[196:199],a[140:143],a[64:67],a[196:199]
s_waitcnt vmcnt(9) lgkmcnt(0)
v_mfma_f32_16x16x32_bf16 a[196:199],a[156:159],a[96:99],a[196:199]
)"
R"(s_barrier
v_mfma_f32_16x16x32_bf16 v[64:67],a[160:163],a[64:67],v[64:67]
s_add_u32 m0,s56,0
ds_read_b64_tr_b16 a[0:1],v2 offset:0
v_mov_b32 v56,0
v_mfma_f32_16x16x32_bf16 v[64:67],a[176:179],a[96:99],v[64:67]
s_add_u32 s61,s53,0
ds_read_b64_tr_b16 a[2:3],v2 offset:1024
v_mov_b32 v57,0
v_mfma_f32_16x16x32_bf16 v[72:75],a[160:163],a[68:71],v[72:75]
buffer_load_dwordx4 v12,s[36:39],s61 offen lds
ds_read_b64_tr_b16 a[32:33],v2 offset:8192
v_mfma_f32_16x16x32_bf16 v[72:75],a[176:179],a[100:103],v[72:75]
s_add_u32 m0,s56,1024
ds_read_b64_tr_b16 a[34:35],v2 offset:9216
v_dot2c_f32_bf16 v56,v32,v48
v_mfma_f32_16x16x32_bf16 v[80:83],a[160:163],a[72:75],v[80:83]
ds_read_b128 a[128:131],v0 offset:8192
v_dot2c_f32_bf16 v57,v40,v48
v_mfma_f32_16x16x32_bf16 v[80:83],a[176:179],a[104:107],v[80:83]
s_add_u32 s62,s53,8192
ds_read_b64_tr_b16 a[4:5],v3 offset:0
v_dot2c_f32_bf16 v56,v33,v49
v_mfma_f32_16x16x32_bf16 v[88:91],a[160:163],a[76:79],v[88:91]
)"
R"(buffer_load_dwordx4 v13,s[36:39],s62 offen lds
ds_read_b64_tr_b16 a[6:7],v3 offset:1024
v_mfma_f32_16x16x32_bf16 v[88:91],a[176:179],a[108:111],v[88:91]
s_add_u32 m0,s56,2048
ds_read_b64_tr_b16 a[36:37],v3 offset:8192
v_dot2c_f32_bf16 v57,v41,v49
v_mfma_f32_16x16x32_bf16 v[96:99],a[160:163],a[80:83],v[96:99]
ds_read_b64_tr_b16 a[38:39],v3 offset:9216
v_dot2c_f32_bf16 v56,v34,v50
v_mfma_f32_16x16x32_bf16 v[96:99],a[176:179],a[112:115],v[96:99]
s_branch gm_KB_sk5
.p2align 12
gm_KB_pg5:
s_setpc_b64 s[100:101]
gm_KB_sk5:
s_add_u32 s61,s53,16384
ds_read_b128 a[144:147],v1 offset:8192
v_dot2c_f32_bf16 v57,v42,v50
v_mfma_f32_16x16x32_bf16 v[104:107],a[160:163],a[84:87],v[104:107]
buffer_load_dwordx4 v12,s[36:39],s61 offen lds
ds_read_b64_tr_b16 a[8:9],v4 offset:0
v_mfma_f32_16x16x32_bf16 v[104:107],a[176:179],a[116:119],v[104:107]
s_add_u32 m0,s56,3072
ds_read_b64_tr_b16 a[10:11],v4 offset:1024
)"
R"(v_dot2c_f32_bf16 v56,v35,v51
v_mfma_f32_16x16x32_bf16 v[112:115],a[160:163],a[88:91],v[112:115]
ds_read_b64_tr_b16 a[40:41],v4 offset:8192
v_dot2c_f32_bf16 v57,v43,v51
v_mfma_f32_16x16x32_bf16 v[112:115],a[176:179],a[120:123],v[112:115]
s_add_u32 s62,s53,24576
ds_read_b64_tr_b16 a[42:43],v4 offset:9216
v_dot2c_f32_bf16 v56,v36,v52
v_mfma_f32_16x16x32_bf16 v[120:123],a[160:163],a[92:95],v[120:123]
buffer_load_dwordx4 v13,s[36:39],s62 offen lds
ds_read_b128 a[132:135],v0 offset:10240
v_mfma_f32_16x16x32_bf16 v[120:123],a[176:179],a[124:127],v[120:123]
s_add_u32 m0,s56,4096
ds_read_b64_tr_b16 a[12:13],v5 offset:0
v_dot2c_f32_bf16 v57,v44,v52
v_mfma_f32_16x16x32_bf16 v[124:127],a[164:167],a[92:95],v[124:127]
s_add_u32 s61,s53,32768
ds_read_b64_tr_b16 a[14:15],v5 offset:1024
v_dot2c_f32_bf16 v56,v37,v53
v_mfma_f32_16x16x32_bf16 v[124:127],a[180:183],a[124:127],v[124:127]
ds_read_b64_tr_b16 a[44:45],v5 offset:8192
v_dot2c_f32_bf16 v57,v45,v53
v_mfma_f32_16x16x32_bf16 v[116:119],a[164:167],a[88:91],v[116:119]
buffer_load_dwordx4 v12,s[36:39],s61 offen lds
)"
R"(ds_read_b64_tr_b16 a[46:47],v5 offset:9216
v_mfma_f32_16x16x32_bf16 v[116:119],a[180:183],a[120:123],v[116:119]
s_add_u32 m0,s56,5120
ds_read_b128 a[148:151],v1 offset:10240
v_dot2c_f32_bf16 v56,v38,v54
v_mfma_f32_16x16x32_bf16 v[108:111],a[164:167],a[84:87],v[108:111]
s_add_u32 s62,s53,40960
ds_read_b64_tr_b16 a[16:17],v6 offset:0
v_dot2c_f32_bf16 v57,v46,v54
v_mfma_f32_16x16x32_bf16 v[108:111],a[180:183],a[116:119],v[108:111]
ds_read_b64_tr_b16 a[18:19],v6 offset:1024
v_dot2c_f32_bf16 v56,v39,v55
v_mfma_f32_16x16x32_bf16 v[100:103],a[164:167],a[80:83],v[100:103]
buffer_load_dwordx4 v13,s[36:39],s62 offen lds
ds_read_b64_tr_b16 a[48:49],v6 offset:8192
v_mfma_f32_16x16x32_bf16 v[100:103],a[180:183],a[112:115],v[100:103]
s_add_u32 m0,s56,6144
ds_read_b64_tr_b16 a[50:51],v6 offset:9216
v_dot2c_f32_bf16 v57,v47,v55
v_mfma_f32_16x16x32_bf16 v[92:95],a[164:167],a[76:79],v[92:95]
s_add_u32 s61,s53,49152
ds_read_b128 a[136:139],v0 offset:12288
v_lshrrev_b32 v58,5,v19
v_mfma_f32_16x16x32_bf16 v[92:95],a[180:183],a[108:111],v[92:95]
)"
R"(ds_read_b64_tr_b16 a[20:21],v7 offset:0
v_lshlrev_b32 v58,2,v58
v_mfma_f32_16x16x32_bf16 v[84:87],a[164:167],a[72:75],v[84:87]
buffer_load_dwordx4 v12,s[36:39],s61 offen lds
ds_read_b64_tr_b16 a[22:23],v7 offset:1024
v_mfma_f32_16x16x32_bf16 v[84:87],a[180:183],a[104:107],v[84:87]
s_add_u32 m0,s56,7168
ds_read_b64_tr_b16 a[52:53],v7 offset:8192
v_mov_b32 v59,0
v_mfma_f32_16x16x32_bf16 v[76:79],a[164:167],a[68:71],v[76:79]
s_add_u32 s62,s53,57344
ds_read_b64_tr_b16 a[54:55],v7 offset:9216
v_nop
v_mfma_f32_16x16x32_bf16 v[76:79],a[180:183],a[100:103],v[76:79]
buffer_load_dwordx4 v13,s[36:39],s62 offen lds
ds_read_b128 a[152:155],v1 offset:12288
v_mfma_f32_16x16x32_bf16 v[68:71],a[164:167],a[64:67],v[68:71]
ds_read_b64_tr_b16 a[24:25],v8 offset:0
v_permlane32_swap_b32 v56,v57
v_mfma_f32_16x16x32_bf16 v[68:71],a[180:183],a[96:99],v[68:71]
buffer_load_dword v61,v60,s[12:15],s87 offen
ds_read_b64_tr_b16 a[26:27],v8 offset:1024
v_mfma_f32_16x16x32_bf16 v[128:131],a[168:171],a[64:67],v[128:131]
s_add_u32 s87,s87,67108864
)"
R"(ds_read_b64_tr_b16 a[56:57],v8 offset:8192
v_add_f32 v56,v56,v57
v_mfma_f32_16x16x32_bf16 v[128:131],a[184:187],a[96:99],v[128:131]
s_xor_b32 s56,s56,0x8000
ds_read_b64_tr_b16 a[58:59],v8 offset:9216
v_mov_b32 v57,v56
v_mfma_f32_16x16x32_bf16 v[136:139],a[168:171],a[68:71],v[136:139]
ds_read_b128 a[140:143],v0 offset:14336
v_permlane16_swap_b32 v56,v57
v_mfma_f32_16x16x32_bf16 v[136:139],a[184:187],a[100:103],v[136:139]
s_add_u32 s53,s53,128
ds_read_b64_tr_b16 a[28:29],v9 offset:0
v_add_f32 v56,v56,v57
v_mfma_f32_16x16x32_bf16 v[144:147],a[168:171],a[72:75],v[144:147]
s_cmp_eq_u32 s53,1024
ds_read_b64_tr_b16 a[30:31],v9 offset:1024
v_add_f32_dpp v56,v56,v56 row_ror:8 row_mask:0xf bank_mask:0xf
v_mfma_f32_16x16x32_bf16 v[144:147],a[184:187],a[104:107],v[144:147]
s_cselect_b32 s53,0,s53
ds_read_b64_tr_b16 a[60:61],v9 offset:8192
v_add_f32_dpp v56,v56,v56 row_ror:4 row_mask:0xf bank_mask:0xf
v_mfma_f32_16x16x32_bf16 v[152:155],a[168:171],a[76:79],v[152:155]
ds_read_b64_tr_b16 a[62:63],v9 offset:9216
v_add_f32_dpp v56,v56,v56 row_ror:2 row_mask:0xf bank_mask:0xf
)"
R"(v_mfma_f32_16x16x32_bf16 v[152:155],a[184:187],a[108:111],v[152:155]
ds_read_b128 a[156:159],v1 offset:14336
v_add_f32_dpp v56,v56,v56 row_ror:1 row_mask:0xf bank_mask:0xf
v_mfma_f32_16x16x32_bf16 v[160:163],a[168:171],a[80:83],v[160:163]
v_mul_f32 v56,0xbfb8aa3b,v56
v_mfma_f32_16x16x32_bf16 v[160:163],a[184:187],a[112:115],v[160:163]
v_exp_f32 v57,-v56
v_mfma_f32_16x16x32_bf16 v[168:171],a[168:171],a[84:87],v[168:171]
v_exp_f32 v56,v56
v_mfma_f32_16x16x32_bf16 v[168:171],a[184:187],a[116:119],v[168:171]
v_add_f32 v57,1.0,v57
v_add_f32 v56,1.0,v56
v_mfma_f32_16x16x32_bf16 v[176:179],a[168:171],a[88:91],v[176:179]
v_min_f32 v57,0x7149f2ca,v57
v_mfma_f32_16x16x32_bf16 v[176:179],a[184:187],a[120:123],v[176:179]
v_rcp_f32 v56,v56
v_mfma_f32_16x16x32_bf16 v[184:187],a[168:171],a[92:95],v[184:187]
v_add_u32 v32,0x2000,v58
buffer_store_dword v56,v58,s[4:7],s84 offen sc1
v_mfma_f32_16x16x32_bf16 v[184:187],a[184:187],a[124:127],v[184:187]
buffer_store_dword v57,v32,s[4:7],s84 offen sc1
v_xor_b32 v28,0x9e3779b9,v28
v_mfma_f32_16x16x32_bf16 v[188:191],a[172:175],a[92:95],v[188:191]
v_nop
)"
R"(v_readfirstlane_b32 s72,v28
v_mfma_f32_16x16x32_bf16 v[188:191],a[188:191],a[124:127],v[188:191]
v_mfma_f32_16x16x32_bf16 v[180:183],a[172:175],a[88:91],v[180:183]
v_mfma_f32_16x16x32_bf16 v[180:183],a[188:191],a[120:123],v[180:183]
v_mfma_f32_16x16x32_bf16 v[172:175],a[172:175],a[84:87],v[172:175]
v_mfma_f32_16x16x32_bf16 v[172:175],a[188:191],a[116:119],v[172:175]
v_mfma_f32_16x16x32_bf16 v[164:167],a[172:175],a[80:83],v[164:167]
v_mfma_f32_16x16x32_bf16 v[164:167],a[188:191],a[112:115],v[164:167]
v_mfma_f32_16x16x32_bf16 v[156:159],a[172:175],a[76:79],v[156:159]
v_mfma_f32_16x16x32_bf16 v[156:159],a[188:191],a[108:111],v[156:159]
v_mfma_f32_16x16x32_bf16 v[148:151],a[172:175],a[72:75],v[148:151]
v_mfma_f32_16x16x32_bf16 v[148:151],a[188:191],a[104:107],v[148:151]
v_mfma_f32_16x16x32_bf16 v[140:143],a[172:175],a[68:71],v[140:143]
v_mfma_f32_16x16x32_bf16 v[140:143],a[188:191],a[100:103],v[140:143]
v_mfma_f32_16x16x32_bf16 v[132:135],a[172:175],a[64:67],v[132:135]
s_waitcnt lgkmcnt(0)
v_mfma_f32_16x16x32_bf16 v[132:135],a[188:191],a[96:99],v[132:135]
s_mov_b32 s60,2
s_branch gm_KB_sk6
.p2align 12
gm_KB_pg6:
s_setpc_b64 s[100:101]
s_nop 0
gm_KB_sk6:
)"
R"(gm_K1_V_loop:
v_mfma_f32_16x16x32_bf16 v[192:195],a[128:131],a[0:3],v[192:195]
s_add_u32 m0,s57,0
ds_read_b128 a[160:163],v0 offset:0
v_mfma_f32_16x16x32_bf16 v[192:195],a[144:147],a[32:35],v[192:195]
s_add_u32 s63,s55,0
ds_read_b128 a[176:179],v1 offset:0
v_mfma_f32_16x16x32_bf16 v[200:203],a[128:131],a[4:7],v[200:203]
ds_read_b128 a[164:167],v0 offset:2048
v_mfma_f32_16x16x32_bf16 v[200:203],a[144:147],a[36:39],v[200:203]
buffer_load_dwordx4 v16,s[44:47],s63 offen lds
v_mfma_f32_16x16x32_bf16 v[208:211],a[128:131],a[8:11],v[208:211]
s_add_u32 s66,s59,0x8000
ds_read_b128 a[180:183],v1 offset:2048
v_mfma_f32_16x16x32_bf16 v[208:211],a[144:147],a[40:43],v[208:211]
ds_read_b128 a[168:171],v0 offset:4096
v_mfma_f32_16x16x32_bf16 v[216:219],a[128:131],a[12:15],v[216:219]
s_cmp_eq_u32 s66,0x18000
ds_read_b128 a[184:187],v1 offset:4096
v_mfma_f32_16x16x32_bf16 v[216:219],a[144:147],a[44:47],v[216:219]
ds_read_b128 a[172:175],v0 offset:6144
v_mfma_f32_16x16x32_bf16 v[224:227],a[128:131],a[16:19],v[224:227]
s_cselect_b32 s66,0,s66
ds_read_b128 a[188:191],v1 offset:6144
)"
R"(v_mfma_f32_16x16x32_bf16 v[224:227],a[144:147],a[48:51],v[224:227]
s_sub_u32 s58,s66,s59
v_mfma_f32_16x16x32_bf16 v[232:235],a[128:131],a[20:23],v[232:235]
v_mfma_f32_16x16x32_bf16 v[232:235],a[144:147],a[52:55],v[232:235]
s_mov_b32 s59,s66
v_mfma_f32_16x16x32_bf16 v[240:243],a[128:131],a[24:27],v[240:243]
s_add_u32 m0,s57,1024
v_mfma_f32_16x16x32_bf16 v[240:243],a[144:147],a[56:59],v[240:243]
v_mfma_f32_16x16x32_bf16 v[248:251],a[128:131],a[28:31],v[248:251]
s_add_u32 s64,s55,32768
v_mfma_f32_16x16x32_bf16 v[248:251],a[144:147],a[60:63],v[248:251]
v_mfma_f32_16x16x32_bf16 v[252:255],a[132:135],a[28:31],v[252:255]
buffer_load_dwordx4 v16,s[44:47],s64 offen lds
v_mfma_f32_16x16x32_bf16 v[252:255],a[148:151],a[60:63],v[252:255]
s_add_u32 m0,s57,2048
v_mfma_f32_16x16x32_bf16 v[244:247],a[132:135],a[24:27],v[244:247]
v_mfma_f32_16x16x32_bf16 v[244:247],a[148:151],a[56:59],v[244:247]
s_add_u32 s63,s55,65536
v_mfma_f32_16x16x32_bf16 v[236:239],a[132:135],a[20:23],v[236:239]
buffer_load_dwordx4 v17,s[44:47],s63 offen lds
v_mfma_f32_16x16x32_bf16 v[236:239],a[148:151],a[52:55],v[236:239]
v_mfma_f32_16x16x32_bf16 v[228:231],a[132:135],a[16:19],v[228:231]
s_add_u32 m0,s57,3072
v_mfma_f32_16x16x32_bf16 v[228:231],a[148:151],a[48:51],v[228:231]
)"
R"(v_mfma_f32_16x16x32_bf16 v[220:223],a[132:135],a[12:15],v[220:223]
s_add_u32 s64,s55,98304
v_mfma_f32_16x16x32_bf16 v[220:223],a[148:151],a[44:47],v[220:223]
buffer_load_dwordx4 v17,s[44:47],s64 offen lds
v_mfma_f32_16x16x32_bf16 v[212:215],a[132:135],a[8:11],v[212:215]
v_mfma_f32_16x16x32_bf16 v[212:215],a[148:151],a[40:43],v[212:215]
s_add_u32 m0,s57,4096
v_mfma_f32_16x16x32_bf16 v[204:207],a[132:135],a[4:7],v[204:207]
s_add_u32 s63,s55,131072
v_mfma_f32_16x16x32_bf16 v[204:207],a[148:151],a[36:39],v[204:207]
v_mfma_f32_16x16x32_bf16 v[196:199],a[132:135],a[0:3],v[196:199]
buffer_load_dwordx4 v16,s[44:47],s63 offen lds
v_mfma_f32_16x16x32_bf16 v[196:199],a[148:151],a[32:35],v[196:199]
v_mfma_f32_16x16x32_bf16 a[192:195],a[136:139],a[0:3],a[192:195]
s_add_u32 m0,s57,5120
v_mfma_f32_16x16x32_bf16 a[192:195],a[152:155],a[32:35],a[192:195]
s_add_u32 s64,s55,163840
v_mfma_f32_16x16x32_bf16 a[200:203],a[136:139],a[4:7],a[200:203]
v_mfma_f32_16x16x32_bf16 a[200:203],a[152:155],a[36:39],a[200:203]
buffer_load_dwordx4 v16,s[44:47],s64 offen lds
v_mfma_f32_16x16x32_bf16 a[208:211],a[136:139],a[8:11],a[208:211]
s_add_u32 m0,s57,6144
v_mfma_f32_16x16x32_bf16 a[208:211],a[152:155],a[40:43],a[208:211]
v_mfma_f32_16x16x32_bf16 a[216:219],a[136:139],a[12:15],a[216:219]
)"
R"(s_add_u32 s63,s55,196608
v_mfma_f32_16x16x32_bf16 a[216:219],a[152:155],a[44:47],a[216:219]
v_mfma_f32_16x16x32_bf16 a[224:227],a[136:139],a[16:19],a[224:227]
buffer_load_dwordx4 v17,s[44:47],s63 offen lds
v_mfma_f32_16x16x32_bf16 a[224:227],a[152:155],a[48:51],a[224:227]
s_add_u32 m0,s57,7168
v_mfma_f32_16x16x32_bf16 a[232:235],a[136:139],a[20:23],a[232:235]
v_mfma_f32_16x16x32_bf16 a[232:235],a[152:155],a[52:55],a[232:235]
s_add_u32 s64,s55,229376
v_mfma_f32_16x16x32_bf16 a[240:243],a[136:139],a[24:27],a[240:243]
buffer_load_dwordx4 v17,s[44:47],s64 offen lds
v_mfma_f32_16x16x32_bf16 a[240:243],a[152:155],a[56:59],a[240:243]
v_xor_b32 v0,0x8000,v0
v_mfma_f32_16x16x32_bf16 a[248:251],a[136:139],a[28:31],a[248:251]
s_add_u32 s57,s57,0x8000
v_xor_b32 v1,0x8000,v1
v_mfma_f32_16x16x32_bf16 a[248:251],a[152:155],a[60:63],a[248:251]
v_add_u32 v2,s58,v2
v_mfma_f32_16x16x32_bf16 a[252:255],a[140:143],a[28:31],a[252:255]
s_cmp_ge_u32 s57,0x28000
v_add_u32 v3,s58,v3
v_mfma_f32_16x16x32_bf16 a[252:255],a[156:159],a[60:63],a[252:255]
s_cselect_b32 s65,0x18000,0
v_add_u32 v4,s58,v4
)"
R"(v_mfma_f32_16x16x32_bf16 a[244:247],a[140:143],a[24:27],a[244:247]
v_add_u32 v5,s58,v5
v_mfma_f32_16x16x32_bf16 a[244:247],a[156:159],a[56:59],a[244:247]
s_sub_u32 s57,s57,s65
v_add_u32 v6,s58,v6
v_mfma_f32_16x16x32_bf16 a[236:239],a[140:143],a[20:23],a[236:239]
s_add_u32 s55,s55,524288
v_add_u32 v7,s58,v7
v_mfma_f32_16x16x32_bf16 a[236:239],a[156:159],a[52:55],a[236:239]
v_add_u32 v8,s58,v8
v_mfma_f32_16x16x32_bf16 a[228:231],a[140:143],a[16:19],a[228:231]
s_cmp_eq_u32 s55,4194304
v_add_u32 v9,s58,v9
v_mfma_f32_16x16x32_bf16 a[228:231],a[156:159],a[48:51],a[228:231]
v_mfma_f32_16x16x32_bf16 a[220:223],a[140:143],a[12:15],a[220:223]
s_cselect_b32 s55,0,s55
v_mfma_f32_16x16x32_bf16 a[220:223],a[156:159],a[44:47],a[220:223]
v_mfma_f32_16x16x32_bf16 a[212:215],a[140:143],a[8:11],a[212:215]
v_mfma_f32_16x16x32_bf16 a[212:215],a[156:159],a[40:43],a[212:215]
v_mfma_f32_16x16x32_bf16 a[204:207],a[140:143],a[4:7],a[204:207]
v_mfma_f32_16x16x32_bf16 a[204:207],a[156:159],a[36:39],a[204:207]
v_mfma_f32_16x16x32_bf16 a[196:199],a[140:143],a[0:3],a[196:199]
s_waitcnt vmcnt(9) lgkmcnt(0)
v_mfma_f32_16x16x32_bf16 a[196:199],a[156:159],a[32:35],a[196:199]
)"
R"(s_barrier
v_mfma_f32_16x16x32_bf16 v[64:67],a[160:163],a[0:3],v[64:67]
s_add_u32 m0,s56,0
ds_read_b64_tr_b16 a[64:65],v2 offset:0
v_mfma_f32_16x16x32_bf16 v[64:67],a[176:179],a[32:35],v[64:67]
s_add_u32 s61,s53,0
ds_read_b64_tr_b16 a[66:67],v2 offset:1024
v_mfma_f32_16x16x32_bf16 v[72:75],a[160:163],a[4:7],v[72:75]
buffer_load_dwordx4 v12,s[36:39],s61 offen lds
v_mfma_f32_16x16x32_bf16 v[72:75],a[176:179],a[36:39],v[72:75]
s_add_u32 m0,s56,1024
ds_read_b64_tr_b16 a[96:97],v2 offset:8192
v_mfma_f32_16x16x32_bf16 v[80:83],a[160:163],a[8:11],v[80:83]
ds_read_b64_tr_b16 a[98:99],v2 offset:9216
v_mfma_f32_16x16x32_bf16 v[80:83],a[176:179],a[40:43],v[80:83]
s_add_u32 s62,s53,8192
ds_read_b128 a[128:131],v0 offset:8192
v_mfma_f32_16x16x32_bf16 v[88:91],a[160:163],a[12:15],v[88:91]
buffer_load_dwordx4 v13,s[36:39],s62 offen lds
v_mfma_f32_16x16x32_bf16 v[88:91],a[176:179],a[44:47],v[88:91]
s_add_u32 m0,s56,2048
ds_read_b64_tr_b16 a[68:69],v3 offset:0
v_mfma_f32_16x16x32_bf16 v[96:99],a[160:163],a[16:19],v[96:99]
ds_read_b64_tr_b16 a[70:71],v3 offset:1024
)"
R"(v_mfma_f32_16x16x32_bf16 v[96:99],a[176:179],a[48:51],v[96:99]
s_add_u32 s61,s53,16384
ds_read_b64_tr_b16 a[100:101],v3 offset:8192
v_mfma_f32_16x16x32_bf16 v[104:107],a[160:163],a[20:23],v[104:107]
buffer_load_dwordx4 v12,s[36:39],s61 offen lds
v_mfma_f32_16x16x32_bf16 v[104:107],a[176:179],a[52:55],v[104:107]
s_add_u32 m0,s56,3072
ds_read_b64_tr_b16 a[102:103],v3 offset:9216
v_mfma_f32_16x16x32_bf16 v[112:115],a[160:163],a[24:27],v[112:115]
ds_read_b128 a[144:147],v1 offset:8192
v_mfma_f32_16x16x32_bf16 v[112:115],a[176:179],a[56:59],v[112:115]
s_add_u32 s62,s53,24576
ds_read_b64_tr_b16 a[72:73],v4 offset:0
v_mfma_f32_16x16x32_bf16 v[120:123],a[160:163],a[28:31],v[120:123]
buffer_load_dwordx4 v13,s[36:39],s62 offen lds
v_mfma_f32_16x16x32_bf16 v[120:123],a[176:179],a[60:63],v[120:123]
s_add_u32 m0,s56,4096
ds_read_b64_tr_b16 a[74:75],v4 offset:1024
v_mfma_f32_16x16x32_bf16 v[124:127],a[164:167],a[28:31],v[124:127]
s_add_u32 s61,s53,32768
ds_read_b64_tr_b16 a[104:105],v4 offset:8192
v_mfma_f32_16x16x32_bf16 v[124:127],a[180:183],a[60:63],v[124:127]
ds_read_b64_tr_b16 a[106:107],v4 offset:9216
v_mfma_f32_16x16x32_bf16 v[116:119],a[164:167],a[24:27],v[116:119]
)"
R"(buffer_load_dwordx4 v12,s[36:39],s61 offen lds
v_mfma_f32_16x16x32_bf16 v[116:119],a[180:183],a[56:59],v[116:119]
s_add_u32 m0,s56,5120
ds_read_b128 a[132:135],v0 offset:10240
v_mfma_f32_16x16x32_bf16 v[108:111],a[164:167],a[20:23],v[108:111]
s_add_u32 s62,s53,40960
ds_read_b64_tr_b16 a[76:77],v5 offset:0
v_mfma_f32_16x16x32_bf16 v[108:111],a[180:183],a[52:55],v[108:111]
ds_read_b64_tr_b16 a[78:79],v5 offset:1024
v_mfma_f32_16x16x32_bf16 v[100:103],a[164:167],a[16:19],v[100:103]
buffer_load_dwordx4 v13,s[36:39],s62 offen lds
v_mfma_f32_16x16x32_bf16 v[100:103],a[180:183],a[48:51],v[100:103]
s_add_u32 m0,s56,6144
ds_read_b64_tr_b16 a[108:109],v5 offset:8192
v_mfma_f32_16x16x32_bf16 v[92:95],a[164:167],a[12:15],v[92:95]
s_add_u32 s61,s53,49152
ds_read_b64_tr_b16 a[110:111],v5 offset:9216
v_mfma_f32_16x16x32_bf16 v[92:95],a[180:183],a[44:47],v[92:95]
ds_read_b128 a[148:151],v1 offset:10240
v_mfma_f32_16x16x32_bf16 v[84:87],a[164:167],a[8:11],v[84:87]
buffer_load_dwordx4 v12,s[36:39],s61 offen lds
v_mfma_f32_16x16x32_bf16 v[84:87],a[180:183],a[40:43],v[84:87]
s_add_u32 m0,s56,7168
ds_read_b64_tr_b16 a[80:81],v6 offset:0
)"
R"(v_mfma_f32_16x16x32_bf16 v[76:79],a[164:167],a[4:7],v[76:79]
s_add_u32 s62,s53,57344
ds_read_b64_tr_b16 a[82:83],v6 offset:1024
v_mfma_f32_16x16x32_bf16 v[76:79],a[180:183],a[36:39],v[76:79]
buffer_load_dwordx4 v13,s[36:39],s62 offen lds
v_mfma_f32_16x16x32_bf16 v[68:71],a[164:167],a[0:3],v[68:71]
ds_read_b64_tr_b16 a[112:113],v6 offset:8192
v_mfma_f32_16x16x32_bf16 v[68:71],a[180:183],a[32:35],v[68:71]
buffer_load_dword v61,v60,s[12:15],s87 offen
v_mfma_f32_16x16x32_bf16 v[128:131],a[168:171],a[0:3],v[128:131]
s_add_u32 s87,s87,67108864
ds_read_b64_tr_b16 a[114:115],v6 offset:9216
v_mfma_f32_16x16x32_bf16 v[128:131],a[184:187],a[32:35],v[128:131]
s_xor_b32 s56,s56,0x8000
ds_read_b128 a[136:139],v0 offset:12288
v_mfma_f32_16x16x32_bf16 v[136:139],a[168:171],a[4:7],v[136:139]
ds_read_b64_tr_b16 a[84:85],v7 offset:0
v_mfma_f32_16x16x32_bf16 v[136:139],a[184:187],a[36:39],v[136:139]
s_add_u32 s53,s53,128
ds_read_b64_tr_b16 a[86:87],v7 offset:1024
v_mfma_f32_16x16x32_bf16 v[144:147],a[168:171],a[8:11],v[144:147]
s_cmp_eq_u32 s53,1024
ds_read_b64_tr_b16 a[116:117],v7 offset:8192
v_mfma_f32_16x16x32_bf16 v[144:147],a[184:187],a[40:43],v[144:147]
)"
R"(s_cselect_b32 s53,0,s53
ds_read_b64_tr_b16 a[118:119],v7 offset:9216
v_mfma_f32_16x16x32_bf16 v[152:155],a[168:171],a[12:15],v[152:155]
ds_read_b128 a[152:155],v1 offset:12288
v_mfma_f32_16x16x32_bf16 v[152:155],a[184:187],a[44:47],v[152:155]
ds_read_b64_tr_b16 a[88:89],v8 offset:0
v_mfma_f32_16x16x32_bf16 v[160:163],a[168:171],a[16:19],v[160:163]
ds_read_b64_tr_b16 a[90:91],v8 offset:1024
v_mfma_f32_16x16x32_bf16 v[160:163],a[184:187],a[48:51],v[160:163]
ds_read_b64_tr_b16 a[120:121],v8 offset:8192
v_mfma_f32_16x16x32_bf16 v[168:171],a[168:171],a[20:23],v[168:171]
ds_read_b64_tr_b16 a[122:123],v8 offset:9216
v_mfma_f32_16x16x32_bf16 v[168:171],a[184:187],a[52:55],v[168:171]
ds_read_b128 a[140:143],v0 offset:14336
v_mfma_f32_16x16x32_bf16 v[176:179],a[168:171],a[24:27],v[176:179]
ds_read_b64_tr_b16 a[92:93],v9 offset:0
v_mfma_f32_16x16x32_bf16 v[176:179],a[184:187],a[56:59],v[176:179]
ds_read_b64_tr_b16 a[94:95],v9 offset:1024
v_mfma_f32_16x16x32_bf16 v[184:187],a[168:171],a[28:31],v[184:187]
ds_read_b64_tr_b16 a[124:125],v9 offset:8192
v_mfma_f32_16x16x32_bf16 v[184:187],a[184:187],a[60:63],v[184:187]
ds_read_b64_tr_b16 a[126:127],v9 offset:9216
v_mfma_f32_16x16x32_bf16 v[188:191],a[172:175],a[28:31],v[188:191]
ds_read_b128 a[156:159],v1 offset:14336
)"
R"(v_mfma_f32_16x16x32_bf16 v[188:191],a[188:191],a[60:63],v[188:191]
v_mfma_f32_16x16x32_bf16 v[180:183],a[172:175],a[24:27],v[180:183]
v_mfma_f32_16x16x32_bf16 v[180:183],a[188:191],a[56:59],v[180:183]
v_mfma_f32_16x16x32_bf16 v[172:175],a[172:175],a[20:23],v[172:175]
v_mfma_f32_16x16x32_bf16 v[172:175],a[188:191],a[52:55],v[172:175]
v_mfma_f32_16x16x32_bf16 v[164:167],a[172:175],a[16:19],v[164:167]
v_mfma_f32_16x16x32_bf16 v[164:167],a[188:191],a[48:51],v[164:167]
v_mfma_f32_16x16x32_bf16 v[156:159],a[172:175],a[12:15],v[156:159]
v_mfma_f32_16x16x32_bf16 v[156:159],a[188:191],a[44:47],v[156:159]
v_mfma_f32_16x16x32_bf16 v[148:151],a[172:175],a[8:11],v[148:151]
v_mfma_f32_16x16x32_bf16 v[148:151],a[188:191],a[40:43],v[148:151]
v_mfma_f32_16x16x32_bf16 v[140:143],a[172:175],a[4:7],v[140:143]
v_mfma_f32_16x16x32_bf16 v[140:143],a[188:191],a[36:39],v[140:143]
v_mfma_f32_16x16x32_bf16 v[132:135],a[172:175],a[0:3],v[132:135]
s_waitcnt lgkmcnt(0)
v_mfma_f32_16x16x32_bf16 v[132:135],a[188:191],a[32:35],v[132:135]
v_mfma_f32_16x16x32_bf16 v[192:195],a[128:131],a[64:67],v[192:195]
s_add_u32 m0,s57,0
ds_read_b128 a[160:163],v0 offset:0
v_mfma_f32_16x16x32_bf16 v[192:195],a[144:147],a[96:99],v[192:195]
s_add_u32 s63,s55,0
ds_read_b128 a[176:179],v1 offset:0
v_mfma_f32_16x16x32_bf16 v[200:203],a[128:131],a[68:71],v[200:203]
ds_read_b128 a[164:167],v0 offset:2048
)"
R"(v_mfma_f32_16x16x32_bf16 v[200:203],a[144:147],a[100:103],v[200:203]
buffer_load_dwordx4 v16,s[44:47],s63 offen lds
v_mfma_f32_16x16x32_bf16 v[208:211],a[128:131],a[72:75],v[208:211]
s_add_u32 s66,s59,0x8000
ds_read_b128 a[180:183],v1 offset:2048
v_mfma_f32_16x16x32_bf16 v[208:211],a[144:147],a[104:107],v[208:211]
ds_read_b128 a[168:171],v0 offset:4096
v_mfma_f32_16x16x32_bf16 v[216:219],a[128:131],a[76:79],v[216:219]
s_cmp_eq_u32 s66,0x18000
ds_read_b128 a[184:187],v1 offset:4096
v_mfma_f32_16x16x32_bf16 v[216:219],a[144:147],a[108:111],v[216:219]
ds_read_b128 a[172:175],v0 offset:6144
v_mfma_f32_16x16x32_bf16 v[224:227],a[128:131],a[80:83],v[224:227]
s_cselect_b32 s66,0,s66
ds_read_b128 a[188:191],v1 offset:6144
v_mfma_f32_16x16x32_bf16 v[224:227],a[144:147],a[112:115],v[224:227]
s_sub_u32 s58,s66,s59
v_mfma_f32_16x16x32_bf16 v[232:235],a[128:131],a[84:87],v[232:235]
v_mfma_f32_16x16x32_bf16 v[232:235],a[144:147],a[116:119],v[232:235]
s_mov_b32 s59,s66
v_mfma_f32_16x16x32_bf16 v[240:243],a[128:131],a[88:91],v[240:243]
s_add_u32 m0,s57,1024
v_mfma_f32_16x16x32_bf16 v[240:243],a[144:147],a[120:123],v[240:243]
v_mfma_f32_16x16x32_bf16 v[248:251],a[128:131],a[92:95],v[248:251]
)"
R"(s_add_u32 s64,s55,32768
v_mfma_f32_16x16x32_bf16 v[248:251],a[144:147],a[124:127],v[248:251]
v_mfma_f32_16x16x32_bf16 v[252:255],a[132:135],a[92:95],v[252:255]
buffer_load_dwordx4 v16,s[44:47],s64 offen lds
v_mfma_f32_16x16x32_bf16 v[252:255],a[148:151],a[124:127],v[252:255]
s_add_u32 m0,s57,2048
v_mfma_f32_16x16x32_bf16 v[244:247],a[132:135],a[88:91],v[244:247]
v_mfma_f32_16x16x32_bf16 v[244:247],a[148:151],a[120:123],v[244:247]
s_add_u32 s63,s55,65536
v_mfma_f32_16x16x32_bf16 v[236:239],a[132:135],a[84:87],v[236:239]
buffer_load_dwordx4 v17,s[44:47],s63 offen lds
v_mfma_f32_16x16x32_bf16 v[236:239],a[148:151],a[116:119],v[236:239]
v_mfma_f32_16x16x32_bf16 v[228:231],a[132:135],a[80:83],v[228:231]
s_add_u32 m0,s57,3072
v_mfma_f32_16x16x32_bf16 v[228:231],a[148:151],a[112:115],v[228:231]
v_mfma_f32_16x16x32_bf16 v[220:223],a[132:135],a[76:79],v[220:223]
s_add_u32 s64,s55,98304
v_mfma_f32_16x16x32_bf16 v[220:223],a[148:151],a[108:111],v[220:223]
buffer_load_dwordx4 v17,s[44:47],s64 offen lds
v_mfma_f32_16x16x32_bf16 v[212:215],a[132:135],a[72:75],v[212:215]
v_mfma_f32_16x16x32_bf16 v[212:215],a[148:151],a[104:107],v[212:215]
s_add_u32 m0,s57,4096
v_mfma_f32_16x16x32_bf16 v[204:207],a[132:135],a[68:71],v[204:207]
s_add_u32 s63,s55,131072
)"
R"(v_mfma_f32_16x16x32_bf16 v[204:207],a[148:151],a[100:103],v[204:207]
v_mfma_f32_16x16x32_bf16 v[196:199],a[132:135],a[64:67],v[196:199]
buffer_load_dwordx4 v16,s[44:47],s63 offen lds
v_mfma_f32_16x16x32_bf16 v[196:199],a[148:151],a[96:99],v[196:199]
v_mfma_f32_16x16x32_bf16 a[192:195],a[136:139],a[64:67],a[192:195]
s_add_u32 m0,s57,5120
v_mfma_f32_16x16x32_bf16 a[192:195],a[152:155],a[96:99],a[192:195]
s_add_u32 s64,s55,163840
v_mfma_f32_16x16x32_bf16 a[200:203],a[136:139],a[68:71],a[200:203]
v_mfma_f32_16x16x32_bf16 a[200:203],a[152:155],a[100:103],a[200:203]
buffer_load_dwordx4 v16,s[44:47],s64 offen lds
v_mfma_f32_16x16x32_bf16 a[208:211],a[136:139],a[72:75],a[208:211]
s_add_u32 m0,s57,6144
v_mfma_f32_16x16x32_bf16 a[208:211],a[152:155],a[104:107],a[208:211]
v_mfma_f32_16x16x32_bf16 a[216:219],a[136:139],a[76:79],a[216:219]
s_add_u32 s63,s55,196608
v_mfma_f32_16x16x32_bf16 a[216:219],a[152:155],a[108:111],a[216:219]
v_mfma_f32_16x16x32_bf16 a[224:227],a[136:139],a[80:83],a[224:227]
buffer_load_dwordx4 v17,s[44:47],s63 offen lds
v_mfma_f32_16x16x32_bf16 a[224:227],a[152:155],a[112:115],a[224:227]
s_add_u32 m0,s57,7168
v_mfma_f32_16x16x32_bf16 a[232:235],a[136:139],a[84:87],a[232:235]
v_mfma_f32_16x16x32_bf16 a[232:235],a[152:155],a[116:119],a[232:235]
s_add_u32 s64,s55,229376
)"
R"(v_mfma_f32_16x16x32_bf16 a[240:243],a[136:139],a[88:91],a[240:243]
buffer_load_dwordx4 v17,s[44:47],s64 offen lds
v_mfma_f32_16x16x32_bf16 a[240:243],a[152:155],a[120:123],a[240:243]
v_xor_b32 v0,0x8000,v0
v_mfma_f32_16x16x32_bf16 a[248:251],a[136:139],a[92:95],a[248:251]
s_add_u32 s57,s57,0x8000
v_xor_b32 v1,0x8000,v1
v_mfma_f32_16x16x32_bf16 a[248:251],a[152:155],a[124:127],a[248:251]
v_add_u32 v2,s58,v2
v_mfma_f32_16x16x32_bf16 a[252:255],a[140:143],a[92:95],a[252:255]
s_cmp_ge_u32 s57,0x28000
v_add_u32 v3,s58,v3
v_mfma_f32_16x16x32_bf16 a[252:255],a[156:159],a[124:127],a[252:255]
s_cselect_b32 s65,0x18000,0
v_add_u32 v4,s58,v4
v_mfma_f32_16x16x32_bf16 a[244:247],a[140:143],a[88:91],a[244:247]
v_add_u32 v5,s58,v5
v_mfma_f32_16x16x32_bf16 a[244:247],a[156:159],a[120:123],a[244:247]
s_sub_u32 s57,s57,s65
v_add_u32 v6,s58,v6
v_mfma_f32_16x16x32_bf16 a[236:239],a[140:143],a[84:87],a[236:239]
s_add_u32 s55,s55,524288
v_add_u32 v7,s58,v7
v_mfma_f32_16x16x32_bf16 a[236:239],a[156:159],a[116:119],a[236:239]
)"
R"(v_add_u32 v8,s58,v8
v_mfma_f32_16x16x32_bf16 a[228:231],a[140:143],a[80:83],a[228:231]
s_cmp_eq_u32 s55,4194304
v_add_u32 v9,s58,v9
v_mfma_f32_16x16x32_bf16 a[228:231],a[156:159],a[112:115],a[228:231]
v_mfma_f32_16x16x32_bf16 a[220:223],a[140:143],a[76:79],a[220:223]
s_cselect_b32 s55,0,s55
v_mfma_f32_16x16x32_bf16 a[220:223],a[156:159],a[108:111],a[220:223]
v_mfma_f32_16x16x32_bf16 a[212:215],a[140:143],a[72:75],a[212:215]
v_mfma_f32_16x16x32_bf16 a[212:215],a[156:159],a[104:107],a[212:215]
v_mfma_f32_16x16x32_bf16 a[204:207],a[140:143],a[68:71],a[204:207]
v_mfma_f32_16x16x32_bf16 a[204:207],a[156:159],a[100:103],a[204:207]
v_mfma_f32_16x16x32_bf16 a[196:199],a[140:143],a[64:67],a[196:199]
s_waitcnt vmcnt(9) lgkmcnt(0)
v_mfma_f32_16x16x32_bf16 a[196:199],a[156:159],a[96:99],a[196:199]
s_barrier
v_mfma_f32_16x16x32_bf16 v[64:67],a[160:163],a[64:67],v[64:67]
s_add_u32 m0,s56,0
ds_read_b64_tr_b16 a[0:1],v2 offset:0
v_mfma_f32_16x16x32_bf16 v[64:67],a[176:179],a[96:99],v[64:67]
s_add_u32 s61,s53,0
ds_read_b64_tr_b16 a[2:3],v2 offset:1024
v_mfma_f32_16x16x32_bf16 v[72:75],a[160:163],a[68:71],v[72:75]
buffer_load_dwordx4 v12,s[36:39],s61 offen lds
)"
R"(v_mfma_f32_16x16x32_bf16 v[72:75],a[176:179],a[100:103],v[72:75]
s_add_u32 m0,s56,1024
ds_read_b64_tr_b16 a[32:33],v2 offset:8192
v_mfma_f32_16x16x32_bf16 v[80:83],a[160:163],a[72:75],v[80:83]
ds_read_b64_tr_b16 a[34:35],v2 offset:9216
v_mfma_f32_16x16x32_bf16 v[80:83],a[176:179],a[104:107],v[80:83]
s_add_u32 s62,s53,8192
ds_read_b128 a[128:131],v0 offset:8192
v_mfma_f32_16x16x32_bf16 v[88:91],a[160:163],a[76:79],v[88:91]
buffer_load_dwordx4 v13,s[36:39],s62 offen lds
v_mfma_f32_16x16x32_bf16 v[88:91],a[176:179],a[108:111],v[88:91]
s_add_u32 m0,s56,2048
ds_read_b64_tr_b16 a[4:5],v3 offset:0
v_mfma_f32_16x16x32_bf16 v[96:99],a[160:163],a[80:83],v[96:99]
ds_read_b64_tr_b16 a[6:7],v3 offset:1024
v_mfma_f32_16x16x32_bf16 v[96:99],a[176:179],a[112:115],v[96:99]
s_add_u32 s61,s53,16384
ds_read_b64_tr_b16 a[36:37],v3 offset:8192
v_mfma_f32_16x16x32_bf16 v[104:107],a[160:163],a[84:87],v[104:107]
buffer_load_dwordx4 v12,s[36:39],s61 offen lds
v_mfma_f32_16x16x32_bf16 v[104:107],a[176:179],a[116:119],v[104:107]
s_add_u32 m0,s56,3072
ds_read_b64_tr_b16 a[38:39],v3 offset:9216
v_mfma_f32_16x16x32_bf16 v[112:115],a[160:163],a[88:91],v[112:115]
)"
R"(ds_read_b128 a[144:147],v1 offset:8192
v_mfma_f32_16x16x32_bf16 v[112:115],a[176:179],a[120:123],v[112:115]
s_add_u32 s62,s53,24576
ds_read_b64_tr_b16 a[8:9],v4 offset:0
v_mfma_f32_16x16x32_bf16 v[120:123],a[160:163],a[92:95],v[120:123]
buffer_load_dwordx4 v13,s[36:39],s62 offen lds
v_mfma_f32_16x16x32_bf16 v[120:123],a[176:179],a[124:127],v[120:123]
s_add_u32 m0,s56,4096
ds_read_b64_tr_b16 a[10:11],v4 offset:1024
v_mfma_f32_16x16x32_bf16 v[124:127],a[164:167],a[92:95],v[124:127]
s_add_u32 s61,s53,32768
ds_read_b64_tr_b16 a[40:41],v4 offset:8192
v_mfma_f32_16x16x32_bf16 v[124:127],a[180:183],a[124:127],v[124:127]
ds_read_b64_tr_b16 a[42:43],v4 offset:9216
v_mfma_f32_16x16x32_bf16 v[116:119],a[164:167],a[88:91],v[116:119]
buffer_load_dwordx4 v12,s[36:39],s61 offen lds
v_mfma_f32_16x16x32_bf16 v[116:119],a[180:183],a[120:123],v[116:119]
s_add_u32 m0,s56,5120
ds_read_b128 a[132:135],v0 offset:10240
v_mfma_f32_16x16x32_bf16 v[108:111],a[164:167],a[84:87],v[108:111]
s_add_u32 s62,s53,40960
ds_read_b64_tr_b16 a[12:13],v5 offset:0
v_mfma_f32_16x16x32_bf16 v[108:111],a[180:183],a[116:119],v[108:111]
ds_read_b64_tr_b16 a[14:15],v5 offset:1024
)"
R"(v_mfma_f32_16x16x32_bf16 v[100:103],a[164:167],a[80:83],v[100:103]
buffer_load_dwordx4 v13,s[36:39],s62 offen lds
v_mfma_f32_16x16x32_bf16 v[100:103],a[180:183],a[112:115],v[100:103]
s_add_u32 m0,s56,6144
ds_read_b64_tr_b16 a[44:45],v5 offset:8192
v_mfma_f32_16x16x32_bf16 v[92:95],a[164:167],a[76:79],v[92:95]
s_add_u32 s61,s53,49152
ds_read_b64_tr_b16 a[46:47],v5 offset:9216
v_mfma_f32_16x16x32_bf16 v[92:95],a[180:183],a[108:111],v[92:95]
ds_read_b128 a[148:151],v1 offset:10240
v_mfma_f32_16x16x32_bf16 v[84:87],a[164:167],a[72:75],v[84:87]
buffer_load_dwordx4 v12,s[36:39],s61 offen lds
v_mfma_f32_16x16x32_bf16 v[84:87],a[180:183],a[104:107],v[84:87]
s_add_u32 m0,s56,7168
ds_read_b64_tr_b16 a[16:17],v6 offset:0
v_mfma_f32_16x16x32_bf16 v[76:79],a[164:167],a[68:71],v[76:79]
s_add_u32 s62,s53,57344
ds_read_b64_tr_b16 a[18:19],v6 offset:1024
v_mfma_f32_16x16x32_bf16 v[76:79],a[180:183],a[100:103],v[76:79]
buffer_load_dwordx4 v13,s[36:39],s62 offen lds
v_mfma_f32_16x16x32_bf16 v[68:71],a[164:167],a[64:67],v[68:71]
ds_read_b64_tr_b16 a[48:49],v6 offset:8192
v_mfma_f32_16x16x32_bf16 v[68:71],a[180:183],a[96:99],v[68:71]
buffer_load_dword v61,v60,s[12:15],s87 offen
)"
R"(v_mfma_f32_16x16x32_bf16 v[128:131],a[168:171],a[64:67],v[128:131]
s_add_u32 s87,s87,67108864
ds_read_b64_tr_b16 a[50:51],v6 offset:9216
v_mfma_f32_16x16x32_bf16 v[128:131],a[184:187],a[96:99],v[128:131]
s_xor_b32 s56,s56,0x8000
ds_read_b128 a[136:139],v0 offset:12288
v_mfma_f32_16x16x32_bf16 v[136:139],a[168:171],a[68:71],v[136:139]
ds_read_b64_tr_b16 a[20:21],v7 offset:0
v_mfma_f32_16x16x32_bf16 v[136:139],a[184:187],a[100:103],v[136:139]
s_add_u32 s53,s53,128
ds_read_b64_tr_b16 a[22:23],v7 offset:1024
v_mfma_f32_16x16x32_bf16 v[144:147],a[168:171],a[72:75],v[144:147]
s_cmp_eq_u32 s53,1024
ds_read_b64_tr_b16 a[52:53],v7 offset:8192
v_mfma_f32_16x16x32_bf16 v[144:147],a[184:187],a[104:107],v[144:147]
s_cselect_b32 s53,0,s53
ds_read_b64_tr_b16 a[54:55],v7 offset:9216
v_mfma_f32_16x16x32_bf16 v[152:155],a[168:171],a[76:79],v[152:155]
ds_read_b128 a[152:155],v1 offset:12288
v_mfma_f32_16x16x32_bf16 v[152:155],a[184:187],a[108:111],v[152:155]
s_sub_u32 s60,s60,1
ds_read_b64_tr_b16 a[24:25],v8 offset:0
v_mfma_f32_16x16x32_bf16 v[160:163],a[168:171],a[80:83],v[160:163]
s_cmp_lg_u32 s60,0
)"
R"(ds_read_b64_tr_b16 a[26:27],v8 offset:1024
v_mfma_f32_16x16x32_bf16 v[160:163],a[184:187],a[112:115],v[160:163]
ds_read_b64_tr_b16 a[56:57],v8 offset:8192
v_mfma_f32_16x16x32_bf16 v[168:171],a[168:171],a[84:87],v[168:171]
ds_read_b64_tr_b16 a[58:59],v8 offset:9216
v_mfma_f32_16x16x32_bf16 v[168:171],a[184:187],a[116:119],v[168:171]
ds_read_b128 a[140:143],v0 offset:14336
v_mfma_f32_16x16x32_bf16 v[176:179],a[168:171],a[88:91],v[176:179]
ds_read_b64_tr_b16 a[28:29],v9 offset:0
v_mfma_f32_16x16x32_bf16 v[176:179],a[184:187],a[120:123],v[176:179]
ds_read_b64_tr_b16 a[30:31],v9 offset:1024
v_mfma_f32_16x16x32_bf16 v[184:187],a[168:171],a[92:95],v[184:187]
ds_read_b64_tr_b16 a[60:61],v9 offset:8192
v_mfma_f32_16x16x32_bf16 v[184:187],a[184:187],a[124:127],v[184:187]
ds_read_b64_tr_b16 a[62:63],v9 offset:9216
v_mfma_f32_16x16x32_bf16 v[188:191],a[172:175],a[92:95],v[188:191]
ds_read_b128 a[156:159],v1 offset:14336
v_mfma_f32_16x16x32_bf16 v[188:191],a[188:191],a[124:127],v[188:191]
v_mfma_f32_16x16x32_bf16 v[180:183],a[172:175],a[88:91],v[180:183]
v_mfma_f32_16x16x32_bf16 v[180:183],a[188:191],a[120:123],v[180:183]
v_mfma_f32_16x16x32_bf16 v[172:175],a[172:175],a[84:87],v[172:175]
v_mfma_f32_16x16x32_bf16 v[172:175],a[188:191],a[116:119],v[172:175]
v_mfma_f32_16x16x32_bf16 v[164:167],a[172:175],a[80:83],v[164:167]
v_mfma_f32_16x16x32_bf16 v[164:167],a[188:191],a[112:115],v[164:167]
)"
R"(v_mfma_f32_16x16x32_bf16 v[156:159],a[172:175],a[76:79],v[156:159]
v_mfma_f32_16x16x32_bf16 v[156:159],a[188:191],a[108:111],v[156:159]
v_mfma_f32_16x16x32_bf16 v[148:151],a[172:175],a[72:75],v[148:151]
v_mfma_f32_16x16x32_bf16 v[148:151],a[188:191],a[104:107],v[148:151]
v_mfma_f32_16x16x32_bf16 v[140:143],a[172:175],a[68:71],v[140:143]
v_mfma_f32_16x16x32_bf16 v[140:143],a[188:191],a[100:103],v[140:143]
v_mfma_f32_16x16x32_bf16 v[132:135],a[172:175],a[64:67],v[132:135]
s_waitcnt lgkmcnt(0)
v_mfma_f32_16x16x32_bf16 v[132:135],a[188:191],a[96:99],v[132:135]
s_cbranch_scc1 gm_K1_V_loop
v_mfma_f32_16x16x32_bf16 v[192:195],a[128:131],a[0:3],v[192:195]
s_add_u32 s66,s59,0x8000
s_cmp_eq_u32 s66,0x18000
ds_read_b128 a[160:163],v0 offset:0
v_mfma_f32_16x16x32_bf16 v[192:195],a[144:147],a[32:35],v[192:195]
s_cselect_b32 s66,0,s66
s_sub_u32 s58,s66,s59
ds_read_b128 a[176:179],v1 offset:0
v_mfma_f32_16x16x32_bf16 v[200:203],a[128:131],a[4:7],v[200:203]
s_mov_b32 s59,s66
s_mul_i32 s67,s73,256
ds_read_b128 a[164:167],v0 offset:2048
v_mfma_f32_16x16x32_bf16 v[200:203],a[144:147],a[36:39],v[200:203]
s_mul_i32 s74,s73,1048576
)"
R"(s_lshl_b32 s68,s71,1
ds_read_b128 a[180:183],v1 offset:2048
v_mfma_f32_16x16x32_bf16 v[208:211],a[128:131],a[8:11],v[208:211]
s_sub_u32 s68,15,s68
s_lshl_b32 s68,s68,18
ds_read_b128 a[168:171],v0 offset:4096
v_mfma_f32_16x16x32_bf16 v[208:211],a[144:147],a[40:43],v[208:211]
s_lshl_b32 s56,s70,16
s_branch gm_KB_sk7
.p2align 12
gm_KB_pg7:
s_setpc_b64 s[100:101]
gm_KB_sk7:
s_sub_u32 s56,229376,s56
v_add_u32 v12,s56,v10
v_mfma_f32_16x16x32_bf16 v[216:219],a[128:131],a[12:15],v[216:219]
v_add_u32 v13,s56,v11
ds_read_b128 a[184:187],v1 offset:4096
v_mfma_f32_16x16x32_bf16 v[216:219],a[144:147],a[44:47],v[216:219]
s_lshl_b32 s56,s70,13
s_lshl_b32 s57,s70,12
ds_read_b128 a[172:175],v0 offset:6144
v_mfma_f32_16x16x32_bf16 v[224:227],a[128:131],a[16:19],v[224:227]
s_add_u32 m0,s57,131072
)"
R"(s_add_u32 s82,s74,0
ds_read_b128 a[188:191],v1 offset:6144
v_mfma_f32_16x16x32_bf16 v[224:227],a[144:147],a[48:51],v[224:227]
buffer_load_dwordx4 v16,s[92:95],s82 offen lds
v_mfma_f32_16x16x32_bf16 v[232:235],a[128:131],a[20:23],v[232:235]
v_mfma_f32_16x16x32_bf16 v[232:235],a[144:147],a[52:55],v[232:235]
v_mfma_f32_16x16x32_bf16 v[240:243],a[128:131],a[24:27],v[240:243]
v_mfma_f32_16x16x32_bf16 v[240:243],a[144:147],a[56:59],v[240:243]
v_mfma_f32_16x16x32_bf16 v[248:251],a[128:131],a[28:31],v[248:251]
v_mfma_f32_16x16x32_bf16 v[248:251],a[144:147],a[60:63],v[248:251]
v_mfma_f32_16x16x32_bf16 v[252:255],a[132:135],a[28:31],v[252:255]
v_mfma_f32_16x16x32_bf16 v[252:255],a[148:151],a[60:63],v[252:255]
s_add_u32 m0,s57,132096
s_add_u32 s83,s74,32768
v_mfma_f32_16x16x32_bf16 v[244:247],a[132:135],a[24:27],v[244:247]
v_mfma_f32_16x16x32_bf16 v[244:247],a[148:151],a[56:59],v[244:247]
v_mfma_f32_16x16x32_bf16 v[236:239],a[132:135],a[20:23],v[236:239]
v_mfma_f32_16x16x32_bf16 v[236:239],a[148:151],a[52:55],v[236:239]
v_mfma_f32_16x16x32_bf16 v[228:231],a[132:135],a[16:19],v[228:231]
v_mfma_f32_16x16x32_bf16 v[228:231],a[148:151],a[48:51],v[228:231]
buffer_load_dwordx4 v16,s[92:95],s83 offen lds
v_mfma_f32_16x16x32_bf16 v[220:223],a[132:135],a[12:15],v[220:223]
v_mfma_f32_16x16x32_bf16 v[220:223],a[148:151],a[44:47],v[220:223]
v_mfma_f32_16x16x32_bf16 v[212:215],a[132:135],a[8:11],v[212:215]
)"
R"(v_mfma_f32_16x16x32_bf16 v[212:215],a[148:151],a[40:43],v[212:215]
v_mfma_f32_16x16x32_bf16 v[204:207],a[132:135],a[4:7],v[204:207]
v_mfma_f32_16x16x32_bf16 v[204:207],a[148:151],a[36:39],v[204:207]
v_mfma_f32_16x16x32_bf16 v[196:199],a[132:135],a[0:3],v[196:199]
v_mfma_f32_16x16x32_bf16 v[196:199],a[148:151],a[32:35],v[196:199]
v_mfma_f32_16x16x32_bf16 a[192:195],a[136:139],a[0:3],a[192:195]
v_mfma_f32_16x16x32_bf16 a[192:195],a[152:155],a[32:35],a[192:195]
v_mfma_f32_16x16x32_bf16 a[200:203],a[136:139],a[4:7],a[200:203]
s_add_u32 m0,s57,133120
s_add_u32 s82,s74,65536
v_mfma_f32_16x16x32_bf16 a[200:203],a[152:155],a[36:39],a[200:203]
v_mfma_f32_16x16x32_bf16 a[208:211],a[136:139],a[8:11],a[208:211]
v_mfma_f32_16x16x32_bf16 a[208:211],a[152:155],a[40:43],a[208:211]
v_mfma_f32_16x16x32_bf16 a[216:219],a[136:139],a[12:15],a[216:219]
v_mfma_f32_16x16x32_bf16 a[216:219],a[152:155],a[44:47],a[216:219]
v_mfma_f32_16x16x32_bf16 a[224:227],a[136:139],a[16:19],a[224:227]
buffer_load_dwordx4 v17,s[92:95],s82 offen lds
v_mfma_f32_16x16x32_bf16 a[224:227],a[152:155],a[48:51],a[224:227]
v_mfma_f32_16x16x32_bf16 a[232:235],a[136:139],a[20:23],a[232:235]
v_mfma_f32_16x16x32_bf16 a[232:235],a[152:155],a[52:55],a[232:235]
v_mfma_f32_16x16x32_bf16 a[240:243],a[136:139],a[24:27],a[240:243]
v_xor_b32 v0,0x8000,v0
v_mfma_f32_16x16x32_bf16 a[240:243],a[152:155],a[56:59],a[240:243]
v_xor_b32 v1,0x8000,v1
)"
R"(v_mfma_f32_16x16x32_bf16 a[248:251],a[136:139],a[28:31],a[248:251]
v_add_u32 v2,s58,v2
v_mfma_f32_16x16x32_bf16 a[248:251],a[152:155],a[60:63],a[248:251]
v_add_u32 v3,s58,v3
v_mfma_f32_16x16x32_bf16 a[252:255],a[140:143],a[28:31],a[252:255]
v_add_u32 v4,s58,v4
v_mfma_f32_16x16x32_bf16 a[252:255],a[156:159],a[60:63],a[252:255]
v_add_u32 v5,s58,v5
v_mfma_f32_16x16x32_bf16 a[244:247],a[140:143],a[24:27],a[244:247]
v_add_u32 v6,s58,v6
v_mfma_f32_16x16x32_bf16 a[244:247],a[156:159],a[56:59],a[244:247]
v_add_u32 v7,s58,v7
v_mfma_f32_16x16x32_bf16 a[236:239],a[140:143],a[20:23],a[236:239]
s_add_u32 m0,s57,134144
s_add_u32 s83,s74,98304
v_add_u32 v8,s58,v8
v_mfma_f32_16x16x32_bf16 a[236:239],a[156:159],a[52:55],a[236:239]
v_add_u32 v9,s58,v9
v_mfma_f32_16x16x32_bf16 a[228:231],a[140:143],a[16:19],a[228:231]
v_mfma_f32_16x16x32_bf16 a[228:231],a[156:159],a[48:51],a[228:231]
v_mfma_f32_16x16x32_bf16 a[220:223],a[140:143],a[12:15],a[220:223]
v_mfma_f32_16x16x32_bf16 a[220:223],a[156:159],a[44:47],a[220:223]
v_mfma_f32_16x16x32_bf16 a[212:215],a[140:143],a[8:11],a[212:215]
buffer_load_dwordx4 v17,s[92:95],s83 offen lds
)"
R"(s_add_u32 s74,s74,524288
v_mfma_f32_16x16x32_bf16 a[212:215],a[156:159],a[40:43],a[212:215]
s_and_b32 s74,s74,0x7fffff
v_mfma_f32_16x16x32_bf16 a[204:207],a[140:143],a[4:7],a[204:207]
v_mfma_f32_16x16x32_bf16 a[204:207],a[156:159],a[36:39],a[204:207]
v_mfma_f32_16x16x32_bf16 a[196:199],a[140:143],a[0:3],a[196:199]
s_waitcnt vmcnt(5) lgkmcnt(0)
v_mfma_f32_16x16x32_bf16 a[196:199],a[156:159],a[32:35],a[196:199]
s_barrier
v_mfma_f32_16x16x32_bf16 v[64:67],a[160:163],a[0:3],v[64:67]
ds_read_b64_tr_b16 a[64:65],v2 offset:0
v_mfma_f32_16x16x32_bf16 v[64:67],a[176:179],a[32:35],v[64:67]
buffer_load_dword v61,v60,s[12:15],s87 offen
v_mfma_f32_16x16x32_bf16 v[72:75],a[160:163],a[4:7],v[72:75]
ds_read_b64_tr_b16 a[66:67],v2 offset:1024
v_mfma_f32_16x16x32_bf16 v[72:75],a[176:179],a[36:39],v[72:75]
ds_read_b64_tr_b16 a[96:97],v2 offset:8192
v_mfma_f32_16x16x32_bf16 v[80:83],a[160:163],a[8:11],v[80:83]
ds_read_b64_tr_b16 a[98:99],v2 offset:9216
v_mfma_f32_16x16x32_bf16 v[80:83],a[176:179],a[40:43],v[80:83]
s_add_u32 s87,s87,67108864
s_lshl_b32 s99,%1,2
v_mfma_f32_16x16x32_bf16 v[88:91],a[160:163],a[12:15],v[88:91]
s_add_u32 s99,s99,32768
)"
R"(s_cmp_lg_u32 %2,0
s_cbranch_scc1 gm_KB_w29
v_mov_b32 v22,s72
v_mov_b32 v23,0
s_mov_b64 exec,1
buffer_store_dword v22,v23,s[4:7],s99 offen sc1
s_mov_b64 exec,-1
gm_KB_w29:
v_mfma_f32_16x16x32_bf16 v[88:91],a[176:179],a[44:47],v[88:91]
s_add_u32 s84,s67,s68
s_add_u32 m0,s56,0
v_mfma_f32_16x16x32_bf16 v[96:99],a[160:163],a[16:19],v[96:99]
s_add_u32 s82,s67,0
buffer_load_dwordx4 v10,s[88:91],s82 offen lds
v_mfma_f32_16x16x32_bf16 v[96:99],a[176:179],a[48:51],v[96:99]
ds_read_b128 a[128:131],v0 offset:8192
v_mfma_f32_16x16x32_bf16 v[104:107],a[160:163],a[20:23],v[104:107]
ds_read_b64_tr_b16 a[68:69],v3 offset:0
v_mfma_f32_16x16x32_bf16 v[104:107],a[176:179],a[52:55],v[104:107]
ds_read_b64_tr_b16 a[70:71],v3 offset:1024
v_mfma_f32_16x16x32_bf16 v[112:115],a[160:163],a[24:27],v[112:115]
s_add_u32 m0,s56,1024
s_add_u32 s83,s67,16384
v_mfma_f32_16x16x32_bf16 v[112:115],a[176:179],a[56:59],v[112:115]
)"
R"(ds_read_b64_tr_b16 a[100:101],v3 offset:8192
v_mfma_f32_16x16x32_bf16 v[120:123],a[160:163],a[28:31],v[120:123]
ds_read_b64_tr_b16 a[102:103],v3 offset:9216
v_mfma_f32_16x16x32_bf16 v[120:123],a[176:179],a[60:63],v[120:123]
buffer_load_dwordx4 v11,s[88:91],s83 offen lds
v_mfma_f32_16x16x32_bf16 v[124:127],a[164:167],a[28:31],v[124:127]
ds_read_b128 a[144:147],v1 offset:8192
v_mfma_f32_16x16x32_bf16 v[124:127],a[180:183],a[60:63],v[124:127]
ds_read_b64_tr_b16 a[72:73],v4 offset:0
v_mfma_f32_16x16x32_bf16 v[116:119],a[164:167],a[24:27],v[116:119]
ds_read_b64_tr_b16 a[74:75],v4 offset:1024
v_mfma_f32_16x16x32_bf16 v[116:119],a[180:183],a[56:59],v[116:119]
s_add_u32 m0,s56,2048
s_add_u32 s82,s67,0
v_mfma_f32_16x16x32_bf16 v[108:111],a[164:167],a[20:23],v[108:111]
ds_read_b64_tr_b16 a[104:105],v4 offset:8192
v_mfma_f32_16x16x32_bf16 v[108:111],a[180:183],a[52:55],v[108:111]
ds_read_b64_tr_b16 a[106:107],v4 offset:9216
v_mfma_f32_16x16x32_bf16 v[100:103],a[164:167],a[16:19],v[100:103]
buffer_load_dwordx4 v12,s[88:91],s82 offen lds
v_mfma_f32_16x16x32_bf16 v[100:103],a[180:183],a[48:51],v[100:103]
ds_read_b128 a[132:135],v0 offset:10240
v_mfma_f32_16x16x32_bf16 v[92:95],a[164:167],a[12:15],v[92:95]
ds_read_b64_tr_b16 a[76:77],v5 offset:0
)"
R"(v_mfma_f32_16x16x32_bf16 v[92:95],a[180:183],a[44:47],v[92:95]
ds_read_b64_tr_b16 a[78:79],v5 offset:1024
v_mfma_f32_16x16x32_bf16 v[84:87],a[164:167],a[8:11],v[84:87]
s_add_u32 m0,s56,3072
s_add_u32 s83,s67,16384
v_mfma_f32_16x16x32_bf16 v[84:87],a[180:183],a[40:43],v[84:87]
ds_read_b64_tr_b16 a[108:109],v5 offset:8192
v_mfma_f32_16x16x32_bf16 v[76:79],a[164:167],a[4:7],v[76:79]
ds_read_b64_tr_b16 a[110:111],v5 offset:9216
v_mfma_f32_16x16x32_bf16 v[76:79],a[180:183],a[36:39],v[76:79]
buffer_load_dwordx4 v13,s[88:91],s83 offen lds
v_mfma_f32_16x16x32_bf16 v[68:71],a[164:167],a[0:3],v[68:71]
ds_read_b128 a[148:151],v1 offset:10240
v_mfma_f32_16x16x32_bf16 v[68:71],a[180:183],a[32:35],v[68:71]
ds_read_b64_tr_b16 a[80:81],v6 offset:0
v_mfma_f32_16x16x32_bf16 v[128:131],a[168:171],a[0:3],v[128:131]
ds_read_b64_tr_b16 a[82:83],v6 offset:1024
v_mfma_f32_16x16x32_bf16 v[128:131],a[184:187],a[32:35],v[128:131]
s_add_u32 m0,s56,4096
s_add_u32 s82,s84,0
v_mfma_f32_16x16x32_bf16 v[136:139],a[168:171],a[4:7],v[136:139]
ds_read_b64_tr_b16 a[112:113],v6 offset:8192
v_mfma_f32_16x16x32_bf16 v[136:139],a[184:187],a[36:39],v[136:139]
ds_read_b64_tr_b16 a[114:115],v6 offset:9216
)"
R"(v_mfma_f32_16x16x32_bf16 v[144:147],a[168:171],a[8:11],v[144:147]
buffer_load_dwordx4 v10,s[88:91],s82 offen lds
v_mfma_f32_16x16x32_bf16 v[144:147],a[184:187],a[40:43],v[144:147]
ds_read_b128 a[136:139],v0 offset:12288
v_mfma_f32_16x16x32_bf16 v[152:155],a[168:171],a[12:15],v[152:155]
ds_read_b64_tr_b16 a[84:85],v7 offset:0
v_mfma_f32_16x16x32_bf16 v[152:155],a[184:187],a[44:47],v[152:155]
ds_read_b64_tr_b16 a[86:87],v7 offset:1024
v_mfma_f32_16x16x32_bf16 v[160:163],a[168:171],a[16:19],v[160:163]
s_add_u32 m0,s56,5120
s_add_u32 s83,s84,16384
v_mfma_f32_16x16x32_bf16 v[160:163],a[184:187],a[48:51],v[160:163]
ds_read_b64_tr_b16 a[116:117],v7 offset:8192
v_mfma_f32_16x16x32_bf16 v[168:171],a[168:171],a[20:23],v[168:171]
ds_read_b64_tr_b16 a[118:119],v7 offset:9216
v_mfma_f32_16x16x32_bf16 v[168:171],a[184:187],a[52:55],v[168:171]
buffer_load_dwordx4 v11,s[88:91],s83 offen lds
v_mfma_f32_16x16x32_bf16 v[176:179],a[168:171],a[24:27],v[176:179]
ds_read_b128 a[152:155],v1 offset:12288
v_mfma_f32_16x16x32_bf16 v[176:179],a[184:187],a[56:59],v[176:179]
ds_read_b64_tr_b16 a[88:89],v8 offset:0
v_mfma_f32_16x16x32_bf16 v[184:187],a[168:171],a[28:31],v[184:187]
ds_read_b64_tr_b16 a[90:91],v8 offset:1024
v_mfma_f32_16x16x32_bf16 v[184:187],a[184:187],a[60:63],v[184:187]
)"
R"(s_add_u32 m0,s56,6144
s_add_u32 s82,s84,0
v_mfma_f32_16x16x32_bf16 v[188:191],a[172:175],a[28:31],v[188:191]
ds_read_b64_tr_b16 a[120:121],v8 offset:8192
v_mfma_f32_16x16x32_bf16 v[188:191],a[188:191],a[60:63],v[188:191]
ds_read_b64_tr_b16 a[122:123],v8 offset:9216
v_mfma_f32_16x16x32_bf16 v[180:183],a[172:175],a[24:27],v[180:183]
buffer_load_dwordx4 v12,s[88:91],s82 offen lds
v_mfma_f32_16x16x32_bf16 v[180:183],a[188:191],a[56:59],v[180:183]
ds_read_b128 a[140:143],v0 offset:14336
v_mfma_f32_16x16x32_bf16 v[172:175],a[172:175],a[20:23],v[172:175]
ds_read_b64_tr_b16 a[92:93],v9 offset:0
v_mfma_f32_16x16x32_bf16 v[172:175],a[188:191],a[52:55],v[172:175]
ds_read_b64_tr_b16 a[94:95],v9 offset:1024
v_mfma_f32_16x16x32_bf16 v[164:167],a[172:175],a[16:19],v[164:167]
ds_read_b64_tr_b16 a[124:125],v9 offset:8192
v_mfma_f32_16x16x32_bf16 v[164:167],a[188:191],a[48:51],v[164:167]
s_add_u32 m0,s56,7168
s_add_u32 s83,s84,16384
v_mfma_f32_16x16x32_bf16 v[156:159],a[172:175],a[12:15],v[156:159]
ds_read_b64_tr_b16 a[126:127],v9 offset:9216
v_mfma_f32_16x16x32_bf16 v[156:159],a[188:191],a[44:47],v[156:159]
ds_read_b128 a[156:159],v1 offset:14336
v_mfma_f32_16x16x32_bf16 v[148:151],a[172:175],a[8:11],v[148:151]
)"
R"(buffer_load_dwordx4 v13,s[88:91],s83 offen lds
v_mfma_f32_16x16x32_bf16 v[148:151],a[188:191],a[40:43],v[148:151]
s_add_u32 s67,s67,128
s_and_b32 s67,s67,2047
v_mfma_f32_16x16x32_bf16 v[140:143],a[172:175],a[4:7],v[140:143]
v_mfma_f32_16x16x32_bf16 v[140:143],a[188:191],a[36:39],v[140:143]
v_mfma_f32_16x16x32_bf16 v[132:135],a[172:175],a[0:3],v[132:135]
s_waitcnt lgkmcnt(0)
v_mfma_f32_16x16x32_bf16 v[132:135],a[188:191],a[32:35],v[132:135]
v_mfma_f32_16x16x32_bf16 v[192:195],a[128:131],a[64:67],v[192:195]
s_add_u32 s66,s59,0x8000
s_cmp_eq_u32 s66,0x18000
ds_read_b128 a[160:163],v0 offset:0
v_mfma_f32_16x16x32_bf16 v[192:195],a[144:147],a[96:99],v[192:195]
s_cselect_b32 s66,0,s66
s_sub_u32 s58,s66,s59
ds_read_b128 a[176:179],v1 offset:0
v_mfma_f32_16x16x32_bf16 v[200:203],a[128:131],a[68:71],v[200:203]
s_mov_b32 s59,s66
s_add_u32 m0,s57,147456
ds_read_b128 a[164:167],v0 offset:2048
v_mfma_f32_16x16x32_bf16 v[200:203],a[144:147],a[100:103],v[200:203]
s_add_u32 s82,s74,0
ds_read_b128 a[180:183],v1 offset:2048
)"
R"(v_mfma_f32_16x16x32_bf16 v[208:211],a[128:131],a[72:75],v[208:211]
ds_read_b128 a[168:171],v0 offset:4096
v_mfma_f32_16x16x32_bf16 v[208:211],a[144:147],a[104:107],v[208:211]
v_mfma_f32_16x16x32_bf16 v[216:219],a[128:131],a[76:79],v[216:219]
buffer_load_dwordx4 v16,s[92:95],s82 offen lds
ds_read_b128 a[184:187],v1 offset:4096
v_mfma_f32_16x16x32_bf16 v[216:219],a[144:147],a[108:111],v[216:219]
s_add_u32 m0,s57,148480
s_add_u32 s83,s74,32768
ds_read_b128 a[172:175],v0 offset:6144
v_mfma_f32_16x16x32_bf16 v[224:227],a[128:131],a[80:83],v[224:227]
ds_read_b128 a[188:191],v1 offset:6144
v_mfma_f32_16x16x32_bf16 v[224:227],a[144:147],a[112:115],v[224:227]
v_mfma_f32_16x16x32_bf16 v[232:235],a[128:131],a[84:87],v[232:235]
buffer_load_dwordx4 v16,s[92:95],s83 offen lds
s_add_u32 m0,s57,149504
v_mfma_f32_16x16x32_bf16 v[232:235],a[144:147],a[116:119],v[232:235]
s_add_u32 s82,s74,65536
v_mfma_f32_16x16x32_bf16 v[240:243],a[128:131],a[88:91],v[240:243]
v_mfma_f32_16x16x32_bf16 v[240:243],a[144:147],a[120:123],v[240:243]
v_mfma_f32_16x16x32_bf16 v[248:251],a[128:131],a[92:95],v[248:251]
v_mfma_f32_16x16x32_bf16 v[248:251],a[144:147],a[124:127],v[248:251]
buffer_load_dwordx4 v17,s[92:95],s82 offen lds
s_add_u32 m0,s57,150528
)"
R"(v_mfma_f32_16x16x32_bf16 v[252:255],a[132:135],a[92:95],v[252:255]
s_add_u32 s83,s74,98304
v_mfma_f32_16x16x32_bf16 v[252:255],a[148:151],a[124:127],v[252:255]
v_mfma_f32_16x16x32_bf16 v[244:247],a[132:135],a[88:91],v[244:247]
v_mfma_f32_16x16x32_bf16 v[244:247],a[148:151],a[120:123],v[244:247]
v_mfma_f32_16x16x32_bf16 v[236:239],a[132:135],a[84:87],v[236:239]
buffer_load_dwordx4 v17,s[92:95],s83 offen lds
s_add_u32 s74,s74,524288
v_mfma_f32_16x16x32_bf16 v[236:239],a[148:151],a[116:119],v[236:239]
s_and_b32 s74,s74,0x7fffff
s_add_u32 s84,s67,s68
s_add_u32 m0,s56,65536
v_mfma_f32_16x16x32_bf16 v[228:231],a[132:135],a[80:83],v[228:231]
s_add_u32 s82,s67,0
v_mfma_f32_16x16x32_bf16 v[228:231],a[148:151],a[112:115],v[228:231]
v_mfma_f32_16x16x32_bf16 v[220:223],a[132:135],a[76:79],v[220:223]
buffer_load_dwordx4 v10,s[88:91],s82 offen lds
s_add_u32 m0,s56,66560
v_mfma_f32_16x16x32_bf16 v[220:223],a[148:151],a[108:111],v[220:223]
s_add_u32 s83,s67,16384
v_mfma_f32_16x16x32_bf16 v[212:215],a[132:135],a[72:75],v[212:215]
v_mfma_f32_16x16x32_bf16 v[212:215],a[148:151],a[104:107],v[212:215]
v_mfma_f32_16x16x32_bf16 v[204:207],a[132:135],a[68:71],v[204:207]
v_mfma_f32_16x16x32_bf16 v[204:207],a[148:151],a[100:103],v[204:207]
)"
R"(buffer_load_dwordx4 v11,s[88:91],s83 offen lds
s_add_u32 m0,s56,67584
v_mfma_f32_16x16x32_bf16 v[196:199],a[132:135],a[64:67],v[196:199]
s_add_u32 s82,s67,0
v_mfma_f32_16x16x32_bf16 v[196:199],a[148:151],a[96:99],v[196:199]
v_mfma_f32_16x16x32_bf16 a[192:195],a[136:139],a[64:67],a[192:195]
v_mfma_f32_16x16x32_bf16 a[192:195],a[152:155],a[96:99],a[192:195]
v_mfma_f32_16x16x32_bf16 a[200:203],a[136:139],a[68:71],a[200:203]
buffer_load_dwordx4 v12,s[88:91],s82 offen lds
s_add_u32 m0,s56,68608
v_mfma_f32_16x16x32_bf16 a[200:203],a[152:155],a[100:103],a[200:203]
s_add_u32 s83,s67,16384
v_mfma_f32_16x16x32_bf16 a[208:211],a[136:139],a[72:75],a[208:211]
v_mfma_f32_16x16x32_bf16 a[208:211],a[152:155],a[104:107],a[208:211]
v_mfma_f32_16x16x32_bf16 a[216:219],a[136:139],a[76:79],a[216:219]
v_mfma_f32_16x16x32_bf16 a[216:219],a[152:155],a[108:111],a[216:219]
buffer_load_dwordx4 v13,s[88:91],s83 offen lds
s_add_u32 m0,s56,69632
v_mfma_f32_16x16x32_bf16 a[224:227],a[136:139],a[80:83],a[224:227]
s_add_u32 s82,s84,0
v_mfma_f32_16x16x32_bf16 a[224:227],a[152:155],a[112:115],a[224:227]
v_mfma_f32_16x16x32_bf16 a[232:235],a[136:139],a[84:87],a[232:235]
v_mfma_f32_16x16x32_bf16 a[232:235],a[152:155],a[116:119],a[232:235]
buffer_load_dwordx4 v10,s[88:91],s82 offen lds
)"
R"(s_add_u32 m0,s56,70656
v_mfma_f32_16x16x32_bf16 a[240:243],a[136:139],a[88:91],a[240:243]
s_add_u32 s83,s84,16384
v_xor_b32 v0,0x8000,v0
v_mfma_f32_16x16x32_bf16 a[240:243],a[152:155],a[120:123],a[240:243]
v_xor_b32 v1,0x8000,v1
v_mfma_f32_16x16x32_bf16 a[248:251],a[136:139],a[92:95],a[248:251]
v_add_u32 v2,s58,v2
v_mfma_f32_16x16x32_bf16 a[248:251],a[152:155],a[124:127],a[248:251]
v_add_u32 v3,s58,v3
v_mfma_f32_16x16x32_bf16 a[252:255],a[140:143],a[92:95],a[252:255]
buffer_load_dwordx4 v11,s[88:91],s83 offen lds
v_add_u32 v4,s58,v4
v_mfma_f32_16x16x32_bf16 a[252:255],a[156:159],a[124:127],a[252:255]
s_add_u32 m0,s56,71680
s_add_u32 s82,s84,0
v_add_u32 v5,s58,v5
v_mfma_f32_16x16x32_bf16 a[244:247],a[140:143],a[88:91],a[244:247]
v_add_u32 v6,s58,v6
v_mfma_f32_16x16x32_bf16 a[244:247],a[156:159],a[120:123],a[244:247]
v_add_u32 v7,s58,v7
v_mfma_f32_16x16x32_bf16 a[236:239],a[140:143],a[84:87],a[236:239]
v_add_u32 v8,s58,v8
v_mfma_f32_16x16x32_bf16 a[236:239],a[156:159],a[116:119],a[236:239]
)"
R"(buffer_load_dwordx4 v12,s[88:91],s82 offen lds
v_add_u32 v9,s58,v9
v_mfma_f32_16x16x32_bf16 a[228:231],a[140:143],a[80:83],a[228:231]
s_add_u32 m0,s56,72704
s_add_u32 s83,s84,16384
v_mfma_f32_16x16x32_bf16 a[228:231],a[156:159],a[112:115],a[228:231]
v_mfma_f32_16x16x32_bf16 a[220:223],a[140:143],a[76:79],a[220:223]
v_mfma_f32_16x16x32_bf16 a[220:223],a[156:159],a[108:111],a[220:223]
v_mfma_f32_16x16x32_bf16 a[212:215],a[140:143],a[72:75],a[212:215]
buffer_load_dwordx4 v13,s[88:91],s83 offen lds
s_add_u32 s67,s67,128
v_mfma_f32_16x16x32_bf16 a[212:215],a[156:159],a[104:107],a[212:215]
s_and_b32 s67,s67,2047
v_mfma_f32_16x16x32_bf16 a[204:207],a[140:143],a[68:71],a[204:207]
v_mfma_f32_16x16x32_bf16 a[204:207],a[156:159],a[100:103],a[204:207]
v_mfma_f32_16x16x32_bf16 a[196:199],a[140:143],a[64:67],a[196:199]
s_waitcnt lgkmcnt(0)
v_mfma_f32_16x16x32_bf16 a[196:199],a[156:159],a[96:99],a[196:199]
s_barrier
v_mfma_f32_16x16x32_bf16 v[64:67],a[160:163],a[64:67],v[64:67]
v_mfma_f32_16x16x32_bf16 v[64:67],a[176:179],a[96:99],v[64:67]
buffer_load_dword v61,v60,s[12:15],s87 offen
v_mfma_f32_16x16x32_bf16 v[72:75],a[160:163],a[68:71],v[72:75]
s_add_u32 s87,s87,67108864
)"
R"(s_add_u32 m0,s57,98304
s_add_u32 s82,s74,0
v_mfma_f32_16x16x32_bf16 v[72:75],a[176:179],a[100:103],v[72:75]
v_mfma_f32_16x16x32_bf16 v[80:83],a[160:163],a[72:75],v[80:83]
v_cvt_pk_bf16_f32 v255,v254,v255
v_cvt_pk_bf16_f32 v254,v252,v253
v_mfma_f32_16x16x32_bf16 v[80:83],a[176:179],a[104:107],v[80:83]
buffer_load_dwordx4 v16,s[92:95],s82 offen lds
v_cvt_pk_bf16_f32 v253,v250,v251
v_mfma_f32_16x16x32_bf16 v[88:91],a[160:163],a[76:79],v[88:91]
v_cvt_pk_bf16_f32 v252,v248,v249
v_cvt_pk_bf16_f32 v251,v246,v247
v_mfma_f32_16x16x32_bf16 v[88:91],a[176:179],a[108:111],v[88:91]
s_add_u32 m0,s57,99328
s_add_u32 s83,s74,32768
v_cvt_pk_bf16_f32 v250,v244,v245
v_mfma_f32_16x16x32_bf16 v[96:99],a[160:163],a[80:83],v[96:99]
v_cvt_pk_bf16_f32 v249,v242,v243
v_cvt_pk_bf16_f32 v248,v240,v241
v_mfma_f32_16x16x32_bf16 v[96:99],a[176:179],a[112:115],v[96:99]
v_cvt_pk_bf16_f32 v247,v238,v239
v_cvt_pk_bf16_f32 v246,v236,v237
v_mfma_f32_16x16x32_bf16 v[104:107],a[160:163],a[84:87],v[104:107]
buffer_load_dwordx4 v16,s[92:95],s83 offen lds
)"
R"(v_cvt_pk_bf16_f32 v245,v234,v235
v_mfma_f32_16x16x32_bf16 v[104:107],a[176:179],a[116:119],v[104:107]
v_cvt_pk_bf16_f32 v244,v232,v233
v_cvt_pk_bf16_f32 v243,v230,v231
v_mfma_f32_16x16x32_bf16 v[112:115],a[160:163],a[88:91],v[112:115]
s_add_u32 m0,s57,100352
s_add_u32 s82,s74,65536
v_cvt_pk_bf16_f32 v242,v228,v229
v_mfma_f32_16x16x32_bf16 v[112:115],a[176:179],a[120:123],v[112:115]
v_cvt_pk_bf16_f32 v241,v226,v227
v_cvt_pk_bf16_f32 v240,v224,v225
v_mfma_f32_16x16x32_bf16 v[120:123],a[160:163],a[92:95],v[120:123]
v_cvt_pk_bf16_f32 v239,v222,v223
v_cvt_pk_bf16_f32 v238,v220,v221
v_mfma_f32_16x16x32_bf16 v[120:123],a[176:179],a[124:127],v[120:123]
buffer_load_dwordx4 v17,s[92:95],s82 offen lds
v_cvt_pk_bf16_f32 v237,v218,v219
v_mfma_f32_16x16x32_bf16 v[124:127],a[164:167],a[92:95],v[124:127]
v_cvt_pk_bf16_f32 v236,v216,v217
v_cvt_pk_bf16_f32 v235,v214,v215
v_mfma_f32_16x16x32_bf16 v[124:127],a[180:183],a[124:127],v[124:127]
s_add_u32 m0,s57,101376
s_add_u32 s83,s74,98304
v_cvt_pk_bf16_f32 v234,v212,v213
)"
R"(v_mfma_f32_16x16x32_bf16 v[116:119],a[164:167],a[88:91],v[116:119]
v_cvt_pk_bf16_f32 v233,v210,v211
v_cvt_pk_bf16_f32 v232,v208,v209
v_mfma_f32_16x16x32_bf16 v[116:119],a[180:183],a[120:123],v[116:119]
v_cvt_pk_bf16_f32 v231,v206,v207
v_cvt_pk_bf16_f32 v230,v204,v205
v_mfma_f32_16x16x32_bf16 v[108:111],a[164:167],a[84:87],v[108:111]
buffer_load_dwordx4 v17,s[92:95],s83 offen lds
v_cvt_pk_bf16_f32 v229,v202,v203
v_mfma_f32_16x16x32_bf16 v[108:111],a[180:183],a[116:119],v[108:111]
s_add_u32 s74,s74,524288
s_and_b32 s74,s74,0x7fffff
v_cvt_pk_bf16_f32 v228,v200,v201
v_mfma_f32_16x16x32_bf16 v[100:103],a[164:167],a[80:83],v[100:103]
s_add_u32 s84,s67,s68
s_add_u32 m0,s56,32768
v_cvt_pk_bf16_f32 v227,v198,v199
v_mfma_f32_16x16x32_bf16 v[100:103],a[180:183],a[112:115],v[100:103]
s_add_u32 s82,s67,0
v_cvt_pk_bf16_f32 v226,v196,v197
v_cvt_pk_bf16_f32 v225,v194,v195
v_mfma_f32_16x16x32_bf16 v[92:95],a[164:167],a[76:79],v[92:95]
buffer_load_dwordx4 v10,s[88:91],s82 offen lds
v_cvt_pk_bf16_f32 v224,v192,v193
)"
R"(v_mfma_f32_16x16x32_bf16 v[92:95],a[180:183],a[108:111],v[92:95]
v_accvgpr_read_b32 v24,a192
v_accvgpr_read_b32 v25,a193
v_mfma_f32_16x16x32_bf16 v[84:87],a[164:167],a[72:75],v[84:87]
s_add_u32 m0,s56,33792
s_add_u32 s83,s67,16384
v_accvgpr_read_b32 v26,a194
v_mfma_f32_16x16x32_bf16 v[84:87],a[180:183],a[104:107],v[84:87]
v_accvgpr_read_b32 v27,a195
v_accvgpr_read_b32 v28,a196
v_mfma_f32_16x16x32_bf16 v[76:79],a[164:167],a[68:71],v[76:79]
v_accvgpr_read_b32 v29,a197
v_accvgpr_read_b32 v30,a198
v_mfma_f32_16x16x32_bf16 v[76:79],a[180:183],a[100:103],v[76:79]
buffer_load_dwordx4 v11,s[88:91],s83 offen lds
v_accvgpr_read_b32 v31,a199
v_mfma_f32_16x16x32_bf16 v[68:71],a[164:167],a[64:67],v[68:71]
v_cvt_pk_bf16_f32 v192,v24,v25
v_cvt_pk_bf16_f32 v193,v26,v27
v_mfma_f32_16x16x32_bf16 v[68:71],a[180:183],a[96:99],v[68:71]
s_add_u32 m0,s56,34816
s_add_u32 s82,s67,0
v_cvt_pk_bf16_f32 v194,v28,v29
v_mfma_f32_16x16x32_bf16 v[128:131],a[168:171],a[64:67],v[128:131]
)"
R"(v_cvt_pk_bf16_f32 v195,v30,v31
v_accvgpr_read_b32 v32,a200
v_mfma_f32_16x16x32_bf16 v[128:131],a[184:187],a[96:99],v[128:131]
v_accvgpr_read_b32 v33,a201
v_accvgpr_read_b32 v34,a202
v_mfma_f32_16x16x32_bf16 v[136:139],a[168:171],a[68:71],v[136:139]
buffer_load_dwordx4 v12,s[88:91],s82 offen lds
v_accvgpr_read_b32 v35,a203
v_mfma_f32_16x16x32_bf16 v[136:139],a[184:187],a[100:103],v[136:139]
v_accvgpr_read_b32 v36,a204
v_accvgpr_read_b32 v37,a205
v_mfma_f32_16x16x32_bf16 v[144:147],a[168:171],a[72:75],v[144:147]
s_add_u32 m0,s56,35840
s_add_u32 s83,s67,16384
v_accvgpr_read_b32 v38,a206
v_mfma_f32_16x16x32_bf16 v[144:147],a[184:187],a[104:107],v[144:147]
v_accvgpr_read_b32 v39,a207
v_cvt_pk_bf16_f32 v196,v32,v33
v_mfma_f32_16x16x32_bf16 v[152:155],a[168:171],a[76:79],v[152:155]
v_cvt_pk_bf16_f32 v197,v34,v35
v_cvt_pk_bf16_f32 v198,v36,v37
v_mfma_f32_16x16x32_bf16 v[152:155],a[184:187],a[108:111],v[152:155]
buffer_load_dwordx4 v13,s[88:91],s83 offen lds
v_cvt_pk_bf16_f32 v199,v38,v39
)"
R"(v_mfma_f32_16x16x32_bf16 v[160:163],a[168:171],a[80:83],v[160:163]
s_add_u32 m0,s56,36864
s_add_u32 s82,s84,0
v_accvgpr_read_b32 v24,a208
v_mfma_f32_16x16x32_bf16 v[160:163],a[184:187],a[112:115],v[160:163]
v_accvgpr_read_b32 v25,a209
v_accvgpr_read_b32 v26,a210
v_mfma_f32_16x16x32_bf16 v[168:171],a[168:171],a[84:87],v[168:171]
v_accvgpr_read_b32 v27,a211
v_accvgpr_read_b32 v28,a212
v_mfma_f32_16x16x32_bf16 v[168:171],a[184:187],a[116:119],v[168:171]
buffer_load_dwordx4 v10,s[88:91],s82 offen lds
v_accvgpr_read_b32 v29,a213
v_mfma_f32_16x16x32_bf16 v[176:179],a[168:171],a[88:91],v[176:179]
v_accvgpr_read_b32 v30,a214
v_accvgpr_read_b32 v31,a215
v_mfma_f32_16x16x32_bf16 v[176:179],a[184:187],a[120:123],v[176:179]
s_add_u32 m0,s56,37888
s_add_u32 s83,s84,16384
v_cvt_pk_bf16_f32 v200,v24,v25
v_mfma_f32_16x16x32_bf16 v[184:187],a[168:171],a[92:95],v[184:187]
v_cvt_pk_bf16_f32 v201,v26,v27
v_cvt_pk_bf16_f32 v202,v28,v29
v_mfma_f32_16x16x32_bf16 v[184:187],a[184:187],a[124:127],v[184:187]
)"
R"(v_cvt_pk_bf16_f32 v203,v30,v31
v_accvgpr_read_b32 v32,a216
v_mfma_f32_16x16x32_bf16 v[188:191],a[172:175],a[92:95],v[188:191]
buffer_load_dwordx4 v11,s[88:91],s83 offen lds
v_accvgpr_read_b32 v33,a217
v_mfma_f32_16x16x32_bf16 v[188:191],a[188:191],a[124:127],v[188:191]
v_accvgpr_read_b32 v34,a218
v_accvgpr_read_b32 v35,a219
v_mfma_f32_16x16x32_bf16 v[180:183],a[172:175],a[88:91],v[180:183]
s_add_u32 m0,s56,38912
s_add_u32 s82,s84,0
v_accvgpr_read_b32 v36,a220
v_mfma_f32_16x16x32_bf16 v[180:183],a[188:191],a[120:123],v[180:183]
v_accvgpr_read_b32 v37,a221
v_accvgpr_read_b32 v38,a222
v_mfma_f32_16x16x32_bf16 v[172:175],a[172:175],a[84:87],v[172:175]
v_accvgpr_read_b32 v39,a223
v_cvt_pk_bf16_f32 v204,v32,v33
v_mfma_f32_16x16x32_bf16 v[172:175],a[188:191],a[116:119],v[172:175]
buffer_load_dwordx4 v12,s[88:91],s82 offen lds
v_cvt_pk_bf16_f32 v205,v34,v35
v_mfma_f32_16x16x32_bf16 v[164:167],a[172:175],a[80:83],v[164:167]
v_cvt_pk_bf16_f32 v206,v36,v37
s_branch gm_KB_sk8
)"
R"(.p2align 12
gm_KB_pg8:
s_setpc_b64 s[100:101]
gm_KB_sk8:
v_cvt_pk_bf16_f32 v207,v38,v39
v_mfma_f32_16x16x32_bf16 v[164:167],a[188:191],a[112:115],v[164:167]
s_add_u32 m0,s56,39936
s_add_u32 s83,s84,16384
v_accvgpr_read_b32 v24,a224
v_mfma_f32_16x16x32_bf16 v[156:159],a[172:175],a[76:79],v[156:159]
v_accvgpr_read_b32 v25,a225
v_accvgpr_read_b32 v26,a226
v_mfma_f32_16x16x32_bf16 v[156:159],a[188:191],a[108:111],v[156:159]
v_accvgpr_read_b32 v27,a227
v_accvgpr_read_b32 v28,a228
v_mfma_f32_16x16x32_bf16 v[148:151],a[172:175],a[72:75],v[148:151]
buffer_load_dwordx4 v13,s[88:91],s83 offen lds
v_accvgpr_read_b32 v29,a229
v_mfma_f32_16x16x32_bf16 v[148:151],a[188:191],a[104:107],v[148:151]
s_add_u32 s67,s67,128
s_and_b32 s67,s67,2047
v_accvgpr_read_b32 v30,a230
v_mfma_f32_16x16x32_bf16 v[140:143],a[172:175],a[68:71],v[140:143]
v_accvgpr_read_b32 v31,a231
)"
R"(v_cvt_pk_bf16_f32 v208,v24,v25
v_mfma_f32_16x16x32_bf16 v[140:143],a[188:191],a[100:103],v[140:143]
v_cvt_pk_bf16_f32 v209,v26,v27
v_cvt_pk_bf16_f32 v210,v28,v29
v_mfma_f32_16x16x32_bf16 v[132:135],a[172:175],a[64:67],v[132:135]
v_cvt_pk_bf16_f32 v211,v30,v31
s_waitcnt lgkmcnt(0)
v_mfma_f32_16x16x32_bf16 v[132:135],a[188:191],a[96:99],v[132:135]
s_branch gm_KB_tp_done_x
gm_KB_tp_done:
s_setpc_b64 s[100:101]
gm_KB_tp_done_x:
gm_K1_kvdone:
v_add_u32 v0,s98,v0
v_add_u32 v1,s98,v1
v_add_u32 v2,s75,v2
v_add_u32 v3,s75,v3
v_add_u32 v4,s75,v4
v_add_u32 v5,s75,v5
v_add_u32 v6,s75,v6
v_add_u32 v7,s75,v7
v_add_u32 v8,s75,v8
v_add_u32 v9,s75,v9
s_waitcnt vmcnt(25)
)"
R"(s_barrier
ds_read_b64_tr_b16 a[16:17],v2 offset:0
ds_read_b64_tr_b16 a[18:19],v2 offset:1024
ds_read_b64_tr_b16 a[20:21],v3 offset:0
ds_read_b64_tr_b16 a[22:23],v3 offset:1024
ds_read_b128 a[0:3],v0 offset:0
ds_read_b64_tr_b16 a[24:25],v4 offset:0
ds_read_b64_tr_b16 a[26:27],v4 offset:1024
ds_read_b64_tr_b16 a[28:29],v5 offset:0
ds_read_b64_tr_b16 a[30:31],v5 offset:1024
ds_read_b128 a[4:7],v0 offset:2048
ds_read_b64_tr_b16 a[32:33],v6 offset:0
ds_read_b64_tr_b16 a[34:35],v6 offset:1024
ds_read_b64_tr_b16 a[36:37],v7 offset:0
ds_read_b64_tr_b16 a[38:39],v7 offset:1024
ds_read_b128 a[8:11],v0 offset:4096
ds_read_b64_tr_b16 a[40:41],v8 offset:0
ds_read_b64_tr_b16 a[42:43],v8 offset:1024
ds_read_b64_tr_b16 a[44:45],v9 offset:0
ds_read_b64_tr_b16 a[46:47],v9 offset:1024
ds_read_b128 a[12:15],v0 offset:6144
ds_read_b64_tr_b16 a[64:65],v2 offset:8192
ds_read_b64_tr_b16 a[66:67],v2 offset:9216
ds_read_b64_tr_b16 a[68:69],v3 offset:8192
)"
R"(ds_read_b64_tr_b16 a[70:71],v3 offset:9216
ds_read_b128 a[48:51],v1 offset:0
ds_read_b64_tr_b16 a[72:73],v4 offset:8192
ds_read_b64_tr_b16 a[74:75],v4 offset:9216
ds_read_b64_tr_b16 a[76:77],v5 offset:8192
ds_read_b64_tr_b16 a[78:79],v5 offset:9216
ds_read_b128 a[52:55],v1 offset:2048
ds_read_b64_tr_b16 a[80:81],v6 offset:8192
ds_read_b64_tr_b16 a[82:83],v6 offset:9216
ds_read_b64_tr_b16 a[84:85],v7 offset:8192
ds_read_b64_tr_b16 a[86:87],v7 offset:9216
ds_read_b128 a[56:59],v1 offset:4096
ds_read_b64_tr_b16 a[88:89],v8 offset:8192
ds_read_b64_tr_b16 a[90:91],v8 offset:9216
ds_read_b64_tr_b16 a[92:93],v9 offset:8192
ds_read_b64_tr_b16 a[94:95],v9 offset:9216
ds_read_b128 a[60:63],v1 offset:6144
v_add_u32 v0,65536,v0
v_add_u32 v1,65536,v1
v_add_u32 v2,16384,v2
v_add_u32 v3,16384,v3
v_add_u32 v4,16384,v4
v_add_u32 v5,16384,v5
v_add_u32 v6,16384,v6
)"
R"(v_add_u32 v7,16384,v7
v_add_u32 v8,16384,v8
v_add_u32 v9,16384,v9
s_mov_b32 s99,20480
buffer_store_dwordx4 v[252:255],v18,s[48:51],s99 offen offset:3072 sc1
v_accvgpr_read_b32 v32,a232
v_accvgpr_read_b32 v33,a233
v_accvgpr_read_b32 v34,a234
v_accvgpr_read_b32 v35,a235
v_accvgpr_read_b32 v36,a236
v_accvgpr_read_b32 v37,a237
v_accvgpr_read_b32 v38,a238
v_accvgpr_read_b32 v39,a239
v_cvt_pk_bf16_f32 v212,v32,v33
v_cvt_pk_bf16_f32 v213,v34,v35
v_cvt_pk_bf16_f32 v214,v36,v37
v_cvt_pk_bf16_f32 v215,v38,v39
v_accvgpr_read_b32 v24,a240
v_accvgpr_read_b32 v25,a241
v_accvgpr_read_b32 v26,a242
s_mov_b32 s99,20480
buffer_store_dwordx4 v[248:251],v18,s[48:51],s99 offen offset:2048 sc1
v_accvgpr_read_b32 v27,a243
v_accvgpr_read_b32 v28,a244
)"
R"(v_accvgpr_read_b32 v29,a245
v_accvgpr_read_b32 v30,a246
v_accvgpr_read_b32 v31,a247
v_cvt_pk_bf16_f32 v216,v24,v25
v_cvt_pk_bf16_f32 v217,v26,v27
v_cvt_pk_bf16_f32 v218,v28,v29
v_cvt_pk_bf16_f32 v219,v30,v31
v_accvgpr_read_b32 v32,a248
v_accvgpr_read_b32 v33,a249
v_accvgpr_read_b32 v34,a250
v_accvgpr_read_b32 v35,a251
v_accvgpr_read_b32 v36,a252
v_accvgpr_read_b32 v37,a253
s_mov_b32 s99,20480
buffer_store_dwordx4 v[244:247],v18,s[48:51],s99 offen offset:1024 sc1
v_accvgpr_read_b32 v38,a254
v_accvgpr_read_b32 v39,a255
v_cvt_pk_bf16_f32 v220,v32,v33
v_cvt_pk_bf16_f32 v221,v34,v35
v_cvt_pk_bf16_f32 v222,v36,v37
v_cvt_pk_bf16_f32 v223,v38,v39
v_cvt_pk_bf16_f32 v191,v190,v191
v_cvt_pk_bf16_f32 v190,v188,v189
v_cvt_pk_bf16_f32 v189,v186,v187
)"
R"(v_cvt_pk_bf16_f32 v188,v184,v185
v_cvt_pk_bf16_f32 v187,v182,v183
v_cvt_pk_bf16_f32 v186,v180,v181
v_cvt_pk_bf16_f32 v185,v178,v179
v_cvt_pk_bf16_f32 v184,v176,v177
v_cvt_pk_bf16_f32 v183,v174,v175
s_mov_b32 s99,20480
buffer_store_dwordx4 v[240:243],v18,s[48:51],s99 offen offset:0 sc1
v_cvt_pk_bf16_f32 v182,v172,v173
v_cvt_pk_bf16_f32 v181,v170,v171
v_cvt_pk_bf16_f32 v180,v168,v169
v_cvt_pk_bf16_f32 v179,v166,v167
v_cvt_pk_bf16_f32 v178,v164,v165
v_cvt_pk_bf16_f32 v177,v162,v163
v_cvt_pk_bf16_f32 v176,v160,v161
v_cvt_pk_bf16_f32 v175,v158,v159
v_cvt_pk_bf16_f32 v174,v156,v157
v_cvt_pk_bf16_f32 v173,v154,v155
v_cvt_pk_bf16_f32 v172,v152,v153
v_cvt_pk_bf16_f32 v171,v150,v151
v_cvt_pk_bf16_f32 v170,v148,v149
v_cvt_pk_bf16_f32 v169,v146,v147
v_cvt_pk_bf16_f32 v168,v144,v145
s_mov_b32 s99,16384
)"
R"(buffer_store_dwordx4 v[236:239],v18,s[48:51],s99 offen offset:3072 sc1
v_cvt_pk_bf16_f32 v167,v142,v143
v_cvt_pk_bf16_f32 v166,v140,v141
v_cvt_pk_bf16_f32 v165,v138,v139
v_cvt_pk_bf16_f32 v164,v136,v137
v_cvt_pk_bf16_f32 v163,v134,v135
v_cvt_pk_bf16_f32 v162,v132,v133
v_cvt_pk_bf16_f32 v161,v130,v131
v_cvt_pk_bf16_f32 v160,v128,v129
v_cvt_pk_bf16_f32 v159,v126,v127
v_cvt_pk_bf16_f32 v158,v124,v125
v_cvt_pk_bf16_f32 v157,v122,v123
v_cvt_pk_bf16_f32 v156,v120,v121
v_cvt_pk_bf16_f32 v155,v118,v119
v_cvt_pk_bf16_f32 v154,v116,v117
v_cvt_pk_bf16_f32 v153,v114,v115
s_mov_b32 s99,16384
buffer_store_dwordx4 v[232:235],v18,s[48:51],s99 offen offset:2048 sc1
v_cvt_pk_bf16_f32 v152,v112,v113
v_cvt_pk_bf16_f32 v151,v110,v111
v_cvt_pk_bf16_f32 v150,v108,v109
v_cvt_pk_bf16_f32 v149,v106,v107
v_cvt_pk_bf16_f32 v148,v104,v105
v_cvt_pk_bf16_f32 v147,v102,v103
)"
R"(v_cvt_pk_bf16_f32 v146,v100,v101
v_cvt_pk_bf16_f32 v145,v98,v99
v_cvt_pk_bf16_f32 v144,v96,v97
v_cvt_pk_bf16_f32 v143,v94,v95
v_cvt_pk_bf16_f32 v142,v92,v93
v_cvt_pk_bf16_f32 v141,v90,v91
v_cvt_pk_bf16_f32 v140,v88,v89
v_cvt_pk_bf16_f32 v139,v86,v87
v_cvt_pk_bf16_f32 v138,v84,v85
v_cvt_pk_bf16_f32 v137,v82,v83
v_cvt_pk_bf16_f32 v136,v80,v81
v_cvt_pk_bf16_f32 v135,v78,v79
v_cvt_pk_bf16_f32 v134,v76,v77
v_cvt_pk_bf16_f32 v133,v74,v75
v_cvt_pk_bf16_f32 v132,v72,v73
v_cvt_pk_bf16_f32 v131,v70,v71
v_cvt_pk_bf16_f32 v130,v68,v69
v_cvt_pk_bf16_f32 v129,v66,v67
v_cvt_pk_bf16_f32 v128,v64,v65
s_waitcnt lgkmcnt(0)
v_mfma_f32_16x16x32_bf16 v[64:67],a[16:19],a[0:3],0
s_waitcnt vmcnt(19)
s_barrier
v_mfma_f32_16x16x32_bf16 v[64:67],a[64:67],a[48:51],v[64:67]
)"
R"(s_add_u32 m0,s57,131072
s_add_u32 s82,s74,0
ds_read_b64_tr_b16 a[112:113],v2 offset:0
v_mfma_f32_16x16x32_bf16 v[96:99],a[16:19],a[4:7],0
ds_read_b64_tr_b16 a[114:115],v2 offset:1024
v_mfma_f32_16x16x32_bf16 v[96:99],a[64:67],a[52:55],v[96:99]
buffer_load_dwordx4 v16,s[92:95],s82 offen lds
ds_read_b64_tr_b16 a[116:117],v3 offset:0
v_mfma_f32_16x16x32_bf16 a[192:195],a[16:19],a[8:11],0
s_add_u32 m0,s57,132096
s_add_u32 s83,s74,32768
ds_read_b64_tr_b16 a[118:119],v3 offset:1024
v_mfma_f32_16x16x32_bf16 a[192:195],a[64:67],a[56:59],a[192:195]
ds_read_b128 a[96:99],v0 offset:0
v_mfma_f32_16x16x32_bf16 a[224:227],a[16:19],a[12:15],0
buffer_load_dwordx4 v16,s[92:95],s83 offen lds
ds_read_b64_tr_b16 a[120:121],v4 offset:0
v_mfma_f32_16x16x32_bf16 a[224:227],a[64:67],a[60:63],a[224:227]
s_add_u32 m0,s57,133120
s_add_u32 s82,s74,65536
ds_read_b64_tr_b16 a[122:123],v4 offset:1024
v_mfma_f32_16x16x32_bf16 a[228:231],a[20:23],a[12:15],0
ds_read_b64_tr_b16 a[124:125],v5 offset:0
v_mfma_f32_16x16x32_bf16 a[228:231],a[68:71],a[60:63],a[228:231]
)"
R"(ds_read_b64_tr_b16 a[126:127],v5 offset:1024
v_mfma_f32_16x16x32_bf16 a[196:199],a[20:23],a[8:11],0
buffer_load_dwordx4 v17,s[92:95],s82 offen lds
ds_read_b128 a[100:103],v0 offset:2048
v_mfma_f32_16x16x32_bf16 a[196:199],a[68:71],a[56:59],a[196:199]
s_add_u32 m0,s57,134144
s_add_u32 s83,s74,98304
v_mfma_f32_16x16x32_bf16 v[100:103],a[20:23],a[4:7],0
ds_read_b64_tr_b16 a[128:129],v6 offset:0
v_mfma_f32_16x16x32_bf16 v[100:103],a[68:71],a[52:55],v[100:103]
ds_read_b64_tr_b16 a[130:131],v6 offset:1024
v_mfma_f32_16x16x32_bf16 v[68:71],a[20:23],a[0:3],0
buffer_load_dwordx4 v17,s[92:95],s83 offen lds
ds_read_b64_tr_b16 a[132:133],v7 offset:0
v_mfma_f32_16x16x32_bf16 v[68:71],a[68:71],a[48:51],v[68:71]
s_add_u32 s74,s74,524288
s_and_b32 s74,s74,0x7fffff
ds_read_b64_tr_b16 a[134:135],v7 offset:1024
v_mfma_f32_16x16x32_bf16 v[72:75],a[24:27],a[0:3],0
s_add_u32 s84,s67,s68
s_add_u32 m0,s56,0
ds_read_b128 a[104:107],v0 offset:4096
v_mfma_f32_16x16x32_bf16 v[72:75],a[72:75],a[48:51],v[72:75]
s_add_u32 s82,s67,0
)"
R"(ds_read_b64_tr_b16 a[136:137],v8 offset:0
v_mfma_f32_16x16x32_bf16 v[104:107],a[24:27],a[4:7],0
buffer_load_dwordx4 v10,s[88:91],s82 offen lds
ds_read_b64_tr_b16 a[138:139],v8 offset:1024
v_mfma_f32_16x16x32_bf16 v[104:107],a[72:75],a[52:55],v[104:107]
s_add_u32 m0,s56,1024
s_add_u32 s83,s67,16384
ds_read_b64_tr_b16 a[140:141],v9 offset:0
v_mfma_f32_16x16x32_bf16 a[200:203],a[24:27],a[8:11],0
ds_read_b64_tr_b16 a[142:143],v9 offset:1024
v_mfma_f32_16x16x32_bf16 a[200:203],a[72:75],a[56:59],a[200:203]
ds_read_b128 a[108:111],v0 offset:6144
v_mfma_f32_16x16x32_bf16 a[232:235],a[24:27],a[12:15],0
buffer_load_dwordx4 v11,s[88:91],s83 offen lds
v_mfma_f32_16x16x32_bf16 a[232:235],a[72:75],a[60:63],a[232:235]
s_add_u32 m0,s56,2048
s_add_u32 s82,s67,0
ds_read_b64_tr_b16 a[160:161],v2 offset:8192
v_mfma_f32_16x16x32_bf16 a[236:239],a[28:31],a[12:15],0
ds_read_b64_tr_b16 a[162:163],v2 offset:9216
v_mfma_f32_16x16x32_bf16 a[236:239],a[76:79],a[60:63],a[236:239]
ds_read_b64_tr_b16 a[164:165],v3 offset:8192
v_mfma_f32_16x16x32_bf16 a[204:207],a[28:31],a[8:11],0
buffer_load_dwordx4 v12,s[88:91],s82 offen lds
)"
R"(ds_read_b64_tr_b16 a[166:167],v3 offset:9216
v_mfma_f32_16x16x32_bf16 a[204:207],a[76:79],a[56:59],a[204:207]
s_add_u32 m0,s56,3072
s_add_u32 s83,s67,16384
ds_read_b128 a[144:147],v1 offset:0
v_mfma_f32_16x16x32_bf16 v[108:111],a[28:31],a[4:7],0
ds_read_b64_tr_b16 a[168:169],v4 offset:8192
v_mfma_f32_16x16x32_bf16 v[108:111],a[76:79],a[52:55],v[108:111]
ds_read_b64_tr_b16 a[170:171],v4 offset:9216
v_mfma_f32_16x16x32_bf16 v[76:79],a[28:31],a[0:3],0
buffer_load_dwordx4 v13,s[88:91],s83 offen lds
ds_read_b64_tr_b16 a[172:173],v5 offset:8192
v_mfma_f32_16x16x32_bf16 v[76:79],a[76:79],a[48:51],v[76:79]
s_add_u32 m0,s56,4096
s_add_u32 s82,s84,0
ds_read_b64_tr_b16 a[174:175],v5 offset:9216
v_mfma_f32_16x16x32_bf16 v[80:83],a[32:35],a[0:3],0
ds_read_b128 a[148:151],v1 offset:2048
v_mfma_f32_16x16x32_bf16 v[80:83],a[80:83],a[48:51],v[80:83]
buffer_load_dwordx4 v10,s[88:91],s82 offen lds
v_mfma_f32_16x16x32_bf16 v[112:115],a[32:35],a[4:7],0
s_add_u32 m0,s56,5120
s_add_u32 s83,s84,16384
ds_read_b64_tr_b16 a[176:177],v6 offset:8192
)"
R"(v_mfma_f32_16x16x32_bf16 v[112:115],a[80:83],a[52:55],v[112:115]
ds_read_b64_tr_b16 a[178:179],v6 offset:9216
v_mfma_f32_16x16x32_bf16 a[208:211],a[32:35],a[8:11],0
ds_read_b64_tr_b16 a[180:181],v7 offset:8192
v_mfma_f32_16x16x32_bf16 a[208:211],a[80:83],a[56:59],a[208:211]
buffer_load_dwordx4 v11,s[88:91],s83 offen lds
ds_read_b64_tr_b16 a[182:183],v7 offset:9216
v_mfma_f32_16x16x32_bf16 a[240:243],a[32:35],a[12:15],0
s_add_u32 m0,s56,6144
s_add_u32 s82,s84,0
ds_read_b128 a[152:155],v1 offset:4096
v_mfma_f32_16x16x32_bf16 a[240:243],a[80:83],a[60:63],a[240:243]
ds_read_b64_tr_b16 a[184:185],v8 offset:8192
v_mfma_f32_16x16x32_bf16 a[244:247],a[36:39],a[12:15],0
ds_read_b64_tr_b16 a[186:187],v8 offset:9216
v_mfma_f32_16x16x32_bf16 a[244:247],a[84:87],a[60:63],a[244:247]
buffer_load_dwordx4 v12,s[88:91],s82 offen lds
ds_read_b64_tr_b16 a[188:189],v9 offset:8192
v_mfma_f32_16x16x32_bf16 a[212:215],a[36:39],a[8:11],0
s_add_u32 m0,s56,7168
s_add_u32 s83,s84,16384
ds_read_b64_tr_b16 a[190:191],v9 offset:9216
v_mfma_f32_16x16x32_bf16 a[212:215],a[84:87],a[56:59],a[212:215]
ds_read_b128 a[156:159],v1 offset:6144
)"
R"(v_mfma_f32_16x16x32_bf16 v[116:119],a[36:39],a[4:7],0
v_mfma_f32_16x16x32_bf16 v[116:119],a[84:87],a[52:55],v[116:119]
buffer_load_dwordx4 v13,s[88:91],s83 offen lds
v_mfma_f32_16x16x32_bf16 v[84:87],a[36:39],a[0:3],0
s_add_u32 s67,s67,128
s_and_b32 s67,s67,2047
s_mov_b32 s99,16384
v_mfma_f32_16x16x32_bf16 v[84:87],a[84:87],a[48:51],v[84:87]
v_mfma_f32_16x16x32_bf16 v[88:91],a[40:43],a[0:3],0
v_mfma_f32_16x16x32_bf16 v[88:91],a[88:91],a[48:51],v[88:91]
buffer_store_dwordx4 v[228:231],v18,s[48:51],s99 offen offset:1024 sc1
v_add_u32 v0,-32768,v0
v_mfma_f32_16x16x32_bf16 v[120:123],a[40:43],a[4:7],0
s_mov_b32 s99,16384
v_add_u32 v1,-32768,v1
v_add_u32 v2,-49152,v2
v_mfma_f32_16x16x32_bf16 v[120:123],a[88:91],a[52:55],v[120:123]
v_add_u32 v3,-49152,v3
v_add_u32 v4,-49152,v4
v_mfma_f32_16x16x32_bf16 a[216:219],a[40:43],a[8:11],0
v_add_u32 v5,-49152,v5
v_mfma_f32_16x16x32_bf16 a[216:219],a[88:91],a[56:59],a[216:219]
buffer_store_dwordx4 v[224:227],v18,s[48:51],s99 offen offset:0 sc1
v_add_u32 v6,-49152,v6
)"
R"(v_mfma_f32_16x16x32_bf16 a[248:251],a[40:43],a[12:15],0
s_mov_b32 s99,24576
v_add_u32 v7,-49152,v7
v_add_u32 v8,-49152,v8
v_mfma_f32_16x16x32_bf16 a[248:251],a[88:91],a[60:63],a[248:251]
v_add_u32 v9,-49152,v9
v_mfma_f32_16x16x32_bf16 a[252:255],a[44:47],a[12:15],0
v_mfma_f32_16x16x32_bf16 a[252:255],a[92:95],a[60:63],a[252:255]
buffer_store_dwordx4 v[192:195],v18,s[48:51],s99 offen offset:0 sc1
v_mfma_f32_16x16x32_bf16 a[220:223],a[44:47],a[8:11],0
v_mfma_f32_16x16x32_bf16 a[220:223],a[92:95],a[56:59],a[220:223]
v_mfma_f32_16x16x32_bf16 v[124:127],a[44:47],a[4:7],0
v_mfma_f32_16x16x32_bf16 v[124:127],a[92:95],a[52:55],v[124:127]
v_mfma_f32_16x16x32_bf16 v[92:95],a[44:47],a[0:3],0
s_waitcnt lgkmcnt(0)
v_mfma_f32_16x16x32_bf16 v[92:95],a[92:95],a[48:51],v[92:95]
v_mfma_f32_16x16x32_bf16 v[64:67],a[112:115],a[96:99],v[64:67]
s_waitcnt vmcnt(21)
s_barrier
v_mfma_f32_16x16x32_bf16 v[64:67],a[160:163],a[144:147],v[64:67]
s_add_u32 m0,s57,147456
s_add_u32 s82,s74,0
ds_read_b64_tr_b16 a[16:17],v2 offset:0
v_mfma_f32_16x16x32_bf16 v[96:99],a[112:115],a[100:103],v[96:99]
)"
R"(ds_read_b64_tr_b16 a[18:19],v2 offset:1024
v_mfma_f32_16x16x32_bf16 v[96:99],a[160:163],a[148:151],v[96:99]
buffer_load_dwordx4 v16,s[92:95],s82 offen lds
ds_read_b64_tr_b16 a[20:21],v3 offset:0
v_mfma_f32_16x16x32_bf16 a[192:195],a[112:115],a[104:107],a[192:195]
s_add_u32 m0,s57,148480
s_add_u32 s83,s74,32768
ds_read_b64_tr_b16 a[22:23],v3 offset:1024
v_mfma_f32_16x16x32_bf16 a[192:195],a[160:163],a[152:155],a[192:195]
ds_read_b128 a[0:3],v0 offset:0
v_mfma_f32_16x16x32_bf16 a[224:227],a[112:115],a[108:111],a[224:227]
buffer_load_dwordx4 v16,s[92:95],s83 offen lds
ds_read_b64_tr_b16 a[24:25],v4 offset:0
v_mfma_f32_16x16x32_bf16 a[224:227],a[160:163],a[156:159],a[224:227]
s_add_u32 m0,s57,149504
s_add_u32 s82,s74,65536
ds_read_b64_tr_b16 a[26:27],v4 offset:1024
v_mfma_f32_16x16x32_bf16 a[228:231],a[116:119],a[108:111],a[228:231]
ds_read_b64_tr_b16 a[28:29],v5 offset:0
v_mfma_f32_16x16x32_bf16 a[228:231],a[164:167],a[156:159],a[228:231]
ds_read_b64_tr_b16 a[30:31],v5 offset:1024
v_mfma_f32_16x16x32_bf16 a[196:199],a[116:119],a[104:107],a[196:199]
buffer_load_dwordx4 v17,s[92:95],s82 offen lds
ds_read_b128 a[4:7],v0 offset:2048
)"
R"(v_mfma_f32_16x16x32_bf16 a[196:199],a[164:167],a[152:155],a[196:199]
s_add_u32 m0,s57,150528
s_add_u32 s83,s74,98304
v_mfma_f32_16x16x32_bf16 v[100:103],a[116:119],a[100:103],v[100:103]
ds_read_b64_tr_b16 a[32:33],v6 offset:0
v_mfma_f32_16x16x32_bf16 v[100:103],a[164:167],a[148:151],v[100:103]
ds_read_b64_tr_b16 a[34:35],v6 offset:1024
v_mfma_f32_16x16x32_bf16 v[68:71],a[116:119],a[96:99],v[68:71]
buffer_load_dwordx4 v17,s[92:95],s83 offen lds
ds_read_b64_tr_b16 a[36:37],v7 offset:0
v_mfma_f32_16x16x32_bf16 v[68:71],a[164:167],a[144:147],v[68:71]
s_add_u32 s74,s74,524288
s_and_b32 s74,s74,0x7fffff
ds_read_b64_tr_b16 a[38:39],v7 offset:1024
v_mfma_f32_16x16x32_bf16 v[72:75],a[120:123],a[96:99],v[72:75]
s_add_u32 s84,s67,s68
s_add_u32 m0,s56,65536
ds_read_b128 a[8:11],v0 offset:4096
v_mfma_f32_16x16x32_bf16 v[72:75],a[168:171],a[144:147],v[72:75]
s_add_u32 s82,s67,0
ds_read_b64_tr_b16 a[40:41],v8 offset:0
v_mfma_f32_16x16x32_bf16 v[104:107],a[120:123],a[100:103],v[104:107]
buffer_load_dwordx4 v10,s[88:91],s82 offen lds
ds_read_b64_tr_b16 a[42:43],v8 offset:1024
)"
R"(v_mfma_f32_16x16x32_bf16 v[104:107],a[168:171],a[148:151],v[104:107]
s_add_u32 m0,s56,66560
s_add_u32 s83,s67,16384
ds_read_b64_tr_b16 a[44:45],v9 offset:0
v_mfma_f32_16x16x32_bf16 a[200:203],a[120:123],a[104:107],a[200:203]
ds_read_b64_tr_b16 a[46:47],v9 offset:1024
v_mfma_f32_16x16x32_bf16 a[200:203],a[168:171],a[152:155],a[200:203]
ds_read_b128 a[12:15],v0 offset:6144
v_mfma_f32_16x16x32_bf16 a[232:235],a[120:123],a[108:111],a[232:235]
buffer_load_dwordx4 v11,s[88:91],s83 offen lds
v_mfma_f32_16x16x32_bf16 a[232:235],a[168:171],a[156:159],a[232:235]
s_add_u32 m0,s56,67584
s_add_u32 s82,s67,0
ds_read_b64_tr_b16 a[64:65],v2 offset:8192
v_mfma_f32_16x16x32_bf16 a[236:239],a[124:127],a[108:111],a[236:239]
ds_read_b64_tr_b16 a[66:67],v2 offset:9216
v_mfma_f32_16x16x32_bf16 a[236:239],a[172:175],a[156:159],a[236:239]
ds_read_b64_tr_b16 a[68:69],v3 offset:8192
v_mfma_f32_16x16x32_bf16 a[204:207],a[124:127],a[104:107],a[204:207]
buffer_load_dwordx4 v12,s[88:91],s82 offen lds
ds_read_b64_tr_b16 a[70:71],v3 offset:9216
v_mfma_f32_16x16x32_bf16 a[204:207],a[172:175],a[152:155],a[204:207]
s_add_u32 m0,s56,68608
s_add_u32 s83,s67,16384
)"
R"(ds_read_b128 a[48:51],v1 offset:0
v_mfma_f32_16x16x32_bf16 v[108:111],a[124:127],a[100:103],v[108:111]
ds_read_b64_tr_b16 a[72:73],v4 offset:8192
v_mfma_f32_16x16x32_bf16 v[108:111],a[172:175],a[148:151],v[108:111]
ds_read_b64_tr_b16 a[74:75],v4 offset:9216
v_mfma_f32_16x16x32_bf16 v[76:79],a[124:127],a[96:99],v[76:79]
buffer_load_dwordx4 v13,s[88:91],s83 offen lds
ds_read_b64_tr_b16 a[76:77],v5 offset:8192
v_mfma_f32_16x16x32_bf16 v[76:79],a[172:175],a[144:147],v[76:79]
s_add_u32 m0,s56,69632
s_add_u32 s82,s84,0
ds_read_b64_tr_b16 a[78:79],v5 offset:9216
v_mfma_f32_16x16x32_bf16 v[80:83],a[128:131],a[96:99],v[80:83]
ds_read_b128 a[52:55],v1 offset:2048
v_mfma_f32_16x16x32_bf16 v[80:83],a[176:179],a[144:147],v[80:83]
buffer_load_dwordx4 v10,s[88:91],s82 offen lds
v_mfma_f32_16x16x32_bf16 v[112:115],a[128:131],a[100:103],v[112:115]
s_add_u32 m0,s56,70656
s_add_u32 s83,s84,16384
ds_read_b64_tr_b16 a[80:81],v6 offset:8192
v_mfma_f32_16x16x32_bf16 v[112:115],a[176:179],a[148:151],v[112:115]
ds_read_b64_tr_b16 a[82:83],v6 offset:9216
v_mfma_f32_16x16x32_bf16 a[208:211],a[128:131],a[104:107],a[208:211]
ds_read_b64_tr_b16 a[84:85],v7 offset:8192
)"
R"(v_mfma_f32_16x16x32_bf16 a[208:211],a[176:179],a[152:155],a[208:211]
buffer_load_dwordx4 v11,s[88:91],s83 offen lds
ds_read_b64_tr_b16 a[86:87],v7 offset:9216
v_mfma_f32_16x16x32_bf16 a[240:243],a[128:131],a[108:111],a[240:243]
s_add_u32 m0,s56,71680
s_add_u32 s82,s84,0
ds_read_b128 a[56:59],v1 offset:4096
v_mfma_f32_16x16x32_bf16 a[240:243],a[176:179],a[156:159],a[240:243]
ds_read_b64_tr_b16 a[88:89],v8 offset:8192
v_mfma_f32_16x16x32_bf16 a[244:247],a[132:135],a[108:111],a[244:247]
ds_read_b64_tr_b16 a[90:91],v8 offset:9216
v_mfma_f32_16x16x32_bf16 a[244:247],a[180:183],a[156:159],a[244:247]
buffer_load_dwordx4 v12,s[88:91],s82 offen lds
ds_read_b64_tr_b16 a[92:93],v9 offset:8192
v_mfma_f32_16x16x32_bf16 a[212:215],a[132:135],a[104:107],a[212:215]
s_add_u32 m0,s56,72704
s_add_u32 s83,s84,16384
ds_read_b64_tr_b16 a[94:95],v9 offset:9216
v_mfma_f32_16x16x32_bf16 a[212:215],a[180:183],a[152:155],a[212:215]
ds_read_b128 a[60:63],v1 offset:6144
v_mfma_f32_16x16x32_bf16 v[116:119],a[132:135],a[100:103],v[116:119]
v_mfma_f32_16x16x32_bf16 v[116:119],a[180:183],a[148:151],v[116:119]
buffer_load_dwordx4 v13,s[88:91],s83 offen lds
v_mfma_f32_16x16x32_bf16 v[84:87],a[132:135],a[96:99],v[84:87]
)"
R"(s_add_u32 s67,s67,128
s_and_b32 s67,s67,2047
s_mov_b32 s99,24576
v_mfma_f32_16x16x32_bf16 v[84:87],a[180:183],a[144:147],v[84:87]
v_mfma_f32_16x16x32_bf16 v[88:91],a[136:139],a[96:99],v[88:91]
v_mfma_f32_16x16x32_bf16 v[88:91],a[184:187],a[144:147],v[88:91]
buffer_store_dwordx4 v[196:199],v18,s[48:51],s99 offen offset:1024 sc1
v_add_u32 v0,-32768,v0
v_mfma_f32_16x16x32_bf16 v[120:123],a[136:139],a[100:103],v[120:123]
s_mov_b32 s99,24576
v_add_u32 v1,-32768,v1
v_add_u32 v2,32768,v2
v_mfma_f32_16x16x32_bf16 v[120:123],a[184:187],a[148:151],v[120:123]
v_add_u32 v3,32768,v3
v_add_u32 v4,32768,v4
v_mfma_f32_16x16x32_bf16 a[216:219],a[136:139],a[104:107],a[216:219]
v_add_u32 v5,32768,v5
v_mfma_f32_16x16x32_bf16 a[216:219],a[184:187],a[152:155],a[216:219]
buffer_store_dwordx4 v[200:203],v18,s[48:51],s99 offen offset:2048 sc1
v_add_u32 v6,32768,v6
v_mfma_f32_16x16x32_bf16 a[248:251],a[136:139],a[108:111],a[248:251]
s_mov_b32 s99,24576
v_add_u32 v7,32768,v7
v_add_u32 v8,32768,v8
)"
R"(v_mfma_f32_16x16x32_bf16 a[248:251],a[184:187],a[156:159],a[248:251]
v_add_u32 v9,32768,v9
v_mfma_f32_16x16x32_bf16 a[252:255],a[140:143],a[108:111],a[252:255]
v_mfma_f32_16x16x32_bf16 a[252:255],a[188:191],a[156:159],a[252:255]
buffer_store_dwordx4 v[204:207],v18,s[48:51],s99 offen offset:3072 sc1
v_mfma_f32_16x16x32_bf16 a[220:223],a[140:143],a[104:107],a[220:223]
s_branch gm_KB_sk9
.p2align 12
gm_KB_pg9:
s_setpc_b64 s[100:101]
gm_KB_sk9:
v_mfma_f32_16x16x32_bf16 a[220:223],a[188:191],a[152:155],a[220:223]
v_mfma_f32_16x16x32_bf16 v[124:127],a[140:143],a[100:103],v[124:127]
v_mfma_f32_16x16x32_bf16 v[124:127],a[188:191],a[148:151],v[124:127]
v_mfma_f32_16x16x32_bf16 v[92:95],a[140:143],a[96:99],v[92:95]
s_waitcnt lgkmcnt(0)
v_mfma_f32_16x16x32_bf16 v[92:95],a[188:191],a[144:147],v[92:95]
v_mfma_f32_16x16x32_bf16 v[64:67],a[16:19],a[0:3],v[64:67]
s_waitcnt vmcnt(18)
s_barrier
v_mfma_f32_16x16x32_bf16 v[64:67],a[64:67],a[48:51],v[64:67]
s_add_u32 m0,s57,98304
s_add_u32 s82,s74,0
ds_read_b64_tr_b16 a[112:113],v2 offset:0
)"
R"(v_mfma_f32_16x16x32_bf16 v[96:99],a[16:19],a[4:7],v[96:99]
ds_read_b64_tr_b16 a[114:115],v2 offset:1024
v_mfma_f32_16x16x32_bf16 v[96:99],a[64:67],a[52:55],v[96:99]
buffer_load_dwordx4 v16,s[92:95],s82 offen lds
ds_read_b64_tr_b16 a[116:117],v3 offset:0
v_mfma_f32_16x16x32_bf16 a[192:195],a[16:19],a[8:11],a[192:195]
s_add_u32 m0,s57,99328
s_add_u32 s83,s74,32768
ds_read_b64_tr_b16 a[118:119],v3 offset:1024
v_mfma_f32_16x16x32_bf16 a[192:195],a[64:67],a[56:59],a[192:195]
ds_read_b128 a[96:99],v0 offset:0
v_mfma_f32_16x16x32_bf16 a[224:227],a[16:19],a[12:15],a[224:227]
buffer_load_dwordx4 v16,s[92:95],s83 offen lds
ds_read_b64_tr_b16 a[120:121],v4 offset:0
v_mfma_f32_16x16x32_bf16 a[224:227],a[64:67],a[60:63],a[224:227]
s_add_u32 m0,s57,100352
s_add_u32 s82,s74,65536
ds_read_b64_tr_b16 a[122:123],v4 offset:1024
v_mfma_f32_16x16x32_bf16 a[228:231],a[20:23],a[12:15],a[228:231]
ds_read_b64_tr_b16 a[124:125],v5 offset:0
v_mfma_f32_16x16x32_bf16 a[228:231],a[68:71],a[60:63],a[228:231]
ds_read_b64_tr_b16 a[126:127],v5 offset:1024
v_mfma_f32_16x16x32_bf16 a[196:199],a[20:23],a[8:11],a[196:199]
buffer_load_dwordx4 v17,s[92:95],s82 offen lds
)"
R"(ds_read_b128 a[100:103],v0 offset:2048
v_mfma_f32_16x16x32_bf16 a[196:199],a[68:71],a[56:59],a[196:199]
s_add_u32 m0,s57,101376
s_add_u32 s83,s74,98304
v_mfma_f32_16x16x32_bf16 v[100:103],a[20:23],a[4:7],v[100:103]
ds_read_b64_tr_b16 a[128:129],v6 offset:0
v_mfma_f32_16x16x32_bf16 v[100:103],a[68:71],a[52:55],v[100:103]
ds_read_b64_tr_b16 a[130:131],v6 offset:1024
v_mfma_f32_16x16x32_bf16 v[68:71],a[20:23],a[0:3],v[68:71]
buffer_load_dwordx4 v17,s[92:95],s83 offen lds
ds_read_b64_tr_b16 a[132:133],v7 offset:0
v_mfma_f32_16x16x32_bf16 v[68:71],a[68:71],a[48:51],v[68:71]
s_add_u32 s74,s74,524288
s_and_b32 s74,s74,0x7fffff
ds_read_b64_tr_b16 a[134:135],v7 offset:1024
v_mfma_f32_16x16x32_bf16 v[72:75],a[24:27],a[0:3],v[72:75]
s_add_u32 s84,s67,s68
s_add_u32 m0,s56,32768
ds_read_b128 a[104:107],v0 offset:4096
v_mfma_f32_16x16x32_bf16 v[72:75],a[72:75],a[48:51],v[72:75]
s_add_u32 s82,s67,0
ds_read_b64_tr_b16 a[136:137],v8 offset:0
v_mfma_f32_16x16x32_bf16 v[104:107],a[24:27],a[4:7],v[104:107]
buffer_load_dwordx4 v10,s[88:91],s82 offen lds
)"
R"(ds_read_b64_tr_b16 a[138:139],v8 offset:1024
v_mfma_f32_16x16x32_bf16 v[104:107],a[72:75],a[52:55],v[104:107]
s_add_u32 m0,s56,33792
s_add_u32 s83,s67,16384
ds_read_b64_tr_b16 a[140:141],v9 offset:0
v_mfma_f32_16x16x32_bf16 a[200:203],a[24:27],a[8:11],a[200:203]
ds_read_b64_tr_b16 a[142:143],v9 offset:1024
v_mfma_f32_16x16x32_bf16 a[200:203],a[72:75],a[56:59],a[200:203]
ds_read_b128 a[108:111],v0 offset:6144
v_mfma_f32_16x16x32_bf16 a[232:235],a[24:27],a[12:15],a[232:235]
buffer_load_dwordx4 v11,s[88:91],s83 offen lds
v_mfma_f32_16x16x32_bf16 a[232:235],a[72:75],a[60:63],a[232:235]
s_add_u32 m0,s56,34816
s_add_u32 s82,s67,0
ds_read_b64_tr_b16 a[160:161],v2 offset:8192
v_mfma_f32_16x16x32_bf16 a[236:239],a[28:31],a[12:15],a[236:239]
ds_read_b64_tr_b16 a[162:163],v2 offset:9216
v_mfma_f32_16x16x32_bf16 a[236:239],a[76:79],a[60:63],a[236:239]
ds_read_b64_tr_b16 a[164:165],v3 offset:8192
v_mfma_f32_16x16x32_bf16 a[204:207],a[28:31],a[8:11],a[204:207]
buffer_load_dwordx4 v12,s[88:91],s82 offen lds
ds_read_b64_tr_b16 a[166:167],v3 offset:9216
v_mfma_f32_16x16x32_bf16 a[204:207],a[76:79],a[56:59],a[204:207]
s_add_u32 m0,s56,35840
)"
R"(s_add_u32 s83,s67,16384
ds_read_b128 a[144:147],v1 offset:0
v_mfma_f32_16x16x32_bf16 v[108:111],a[28:31],a[4:7],v[108:111]
ds_read_b64_tr_b16 a[168:169],v4 offset:8192
v_mfma_f32_16x16x32_bf16 v[108:111],a[76:79],a[52:55],v[108:111]
ds_read_b64_tr_b16 a[170:171],v4 offset:9216
v_mfma_f32_16x16x32_bf16 v[76:79],a[28:31],a[0:3],v[76:79]
buffer_load_dwordx4 v13,s[88:91],s83 offen lds
ds_read_b64_tr_b16 a[172:173],v5 offset:8192
v_mfma_f32_16x16x32_bf16 v[76:79],a[76:79],a[48:51],v[76:79]
s_add_u32 m0,s56,36864
s_add_u32 s82,s84,0
ds_read_b64_tr_b16 a[174:175],v5 offset:9216
v_mfma_f32_16x16x32_bf16 v[80:83],a[32:35],a[0:3],v[80:83]
ds_read_b128 a[148:151],v1 offset:2048
v_mfma_f32_16x16x32_bf16 v[80:83],a[80:83],a[48:51],v[80:83]
buffer_load_dwordx4 v10,s[88:91],s82 offen lds
v_mfma_f32_16x16x32_bf16 v[112:115],a[32:35],a[4:7],v[112:115]
s_add_u32 m0,s56,37888
s_add_u32 s83,s84,16384
ds_read_b64_tr_b16 a[176:177],v6 offset:8192
v_mfma_f32_16x16x32_bf16 v[112:115],a[80:83],a[52:55],v[112:115]
ds_read_b64_tr_b16 a[178:179],v6 offset:9216
v_mfma_f32_16x16x32_bf16 a[208:211],a[32:35],a[8:11],a[208:211]
)"
R"(ds_read_b64_tr_b16 a[180:181],v7 offset:8192
v_mfma_f32_16x16x32_bf16 a[208:211],a[80:83],a[56:59],a[208:211]
buffer_load_dwordx4 v11,s[88:91],s83 offen lds
ds_read_b64_tr_b16 a[182:183],v7 offset:9216
v_mfma_f32_16x16x32_bf16 a[240:243],a[32:35],a[12:15],a[240:243]
s_add_u32 m0,s56,38912
s_add_u32 s82,s84,0
ds_read_b128 a[152:155],v1 offset:4096
v_mfma_f32_16x16x32_bf16 a[240:243],a[80:83],a[60:63],a[240:243]
ds_read_b64_tr_b16 a[184:185],v8 offset:8192
v_mfma_f32_16x16x32_bf16 a[244:247],a[36:39],a[12:15],a[244:247]
ds_read_b64_tr_b16 a[186:187],v8 offset:9216
v_mfma_f32_16x16x32_bf16 a[244:247],a[84:87],a[60:63],a[244:247]
buffer_load_dwordx4 v12,s[88:91],s82 offen lds
ds_read_b64_tr_b16 a[188:189],v9 offset:8192
v_mfma_f32_16x16x32_bf16 a[212:215],a[36:39],a[8:11],a[212:215]
s_add_u32 m0,s56,39936
s_add_u32 s83,s84,16384
ds_read_b64_tr_b16 a[190:191],v9 offset:9216
v_mfma_f32_16x16x32_bf16 a[212:215],a[84:87],a[56:59],a[212:215]
ds_read_b128 a[156:159],v1 offset:6144
v_mfma_f32_16x16x32_bf16 v[116:119],a[36:39],a[4:7],v[116:119]
v_mfma_f32_16x16x32_bf16 v[116:119],a[84:87],a[52:55],v[116:119]
buffer_load_dwordx4 v13,s[88:91],s83 offen lds
)"
R"(v_mfma_f32_16x16x32_bf16 v[84:87],a[36:39],a[0:3],v[84:87]
s_add_u32 s67,s67,128
s_and_b32 s67,s67,2047
s_mov_b32 s99,28672
v_mfma_f32_16x16x32_bf16 v[84:87],a[84:87],a[48:51],v[84:87]
v_mfma_f32_16x16x32_bf16 v[88:91],a[40:43],a[0:3],v[88:91]
v_mfma_f32_16x16x32_bf16 v[88:91],a[88:91],a[48:51],v[88:91]
buffer_store_dwordx4 v[208:211],v18,s[48:51],s99 offen offset:0 sc1
v_add_u32 v0,65536,v0
v_mfma_f32_16x16x32_bf16 v[120:123],a[40:43],a[4:7],v[120:123]
s_mov_b32 s99,28672
v_add_u32 v1,65536,v1
v_add_u32 v2,16384,v2
v_mfma_f32_16x16x32_bf16 v[120:123],a[88:91],a[52:55],v[120:123]
v_add_u32 v3,16384,v3
v_add_u32 v4,16384,v4
v_mfma_f32_16x16x32_bf16 a[216:219],a[40:43],a[8:11],a[216:219]
v_add_u32 v5,16384,v5
v_mfma_f32_16x16x32_bf16 a[216:219],a[88:91],a[56:59],a[216:219]
buffer_store_dwordx4 v[212:215],v18,s[48:51],s99 offen offset:1024 sc1
v_add_u32 v6,16384,v6
v_mfma_f32_16x16x32_bf16 a[248:251],a[40:43],a[12:15],a[248:251]
s_mov_b32 s99,28672
v_add_u32 v7,16384,v7
)"
R"(v_add_u32 v8,16384,v8
v_mfma_f32_16x16x32_bf16 a[248:251],a[88:91],a[60:63],a[248:251]
v_add_u32 v9,16384,v9
v_mfma_f32_16x16x32_bf16 a[252:255],a[44:47],a[12:15],a[252:255]
v_mfma_f32_16x16x32_bf16 a[252:255],a[92:95],a[60:63],a[252:255]
buffer_store_dwordx4 v[216:219],v18,s[48:51],s99 offen offset:2048 sc1
v_mfma_f32_16x16x32_bf16 a[220:223],a[44:47],a[8:11],a[220:223]
v_mfma_f32_16x16x32_bf16 a[220:223],a[92:95],a[56:59],a[220:223]
v_mfma_f32_16x16x32_bf16 v[124:127],a[44:47],a[4:7],v[124:127]
v_mfma_f32_16x16x32_bf16 v[124:127],a[92:95],a[52:55],v[124:127]
v_mfma_f32_16x16x32_bf16 v[92:95],a[44:47],a[0:3],v[92:95]
s_waitcnt lgkmcnt(0)
v_mfma_f32_16x16x32_bf16 v[92:95],a[92:95],a[48:51],v[92:95]
v_mfma_f32_16x16x32_bf16 v[64:67],a[112:115],a[96:99],v[64:67]
s_waitcnt vmcnt(18)
s_barrier
v_mfma_f32_16x16x32_bf16 v[64:67],a[160:163],a[144:147],v[64:67]
s_add_u32 m0,s57,131072
s_add_u32 s82,s74,0
ds_read_b64_tr_b16 a[16:17],v2 offset:0
v_mfma_f32_16x16x32_bf16 v[96:99],a[112:115],a[100:103],v[96:99]
ds_read_b64_tr_b16 a[18:19],v2 offset:1024
v_mfma_f32_16x16x32_bf16 v[96:99],a[160:163],a[148:151],v[96:99]
buffer_load_dwordx4 v16,s[92:95],s82 offen lds
)"
R"(ds_read_b64_tr_b16 a[20:21],v3 offset:0
v_mfma_f32_16x16x32_bf16 a[192:195],a[112:115],a[104:107],a[192:195]
s_add_u32 m0,s57,132096
s_add_u32 s83,s74,32768
ds_read_b64_tr_b16 a[22:23],v3 offset:1024
v_mfma_f32_16x16x32_bf16 a[192:195],a[160:163],a[152:155],a[192:195]
ds_read_b128 a[0:3],v0 offset:0
v_mfma_f32_16x16x32_bf16 a[224:227],a[112:115],a[108:111],a[224:227]
buffer_load_dwordx4 v16,s[92:95],s83 offen lds
ds_read_b64_tr_b16 a[24:25],v4 offset:0
v_mfma_f32_16x16x32_bf16 a[224:227],a[160:163],a[156:159],a[224:227]
s_add_u32 m0,s57,133120
s_add_u32 s82,s74,65536
ds_read_b64_tr_b16 a[26:27],v4 offset:1024
v_mfma_f32_16x16x32_bf16 a[228:231],a[116:119],a[108:111],a[228:231]
ds_read_b64_tr_b16 a[28:29],v5 offset:0
v_mfma_f32_16x16x32_bf16 a[228:231],a[164:167],a[156:159],a[228:231]
ds_read_b64_tr_b16 a[30:31],v5 offset:1024
v_mfma_f32_16x16x32_bf16 a[196:199],a[116:119],a[104:107],a[196:199]
buffer_load_dwordx4 v17,s[92:95],s82 offen lds
ds_read_b128 a[4:7],v0 offset:2048
v_mfma_f32_16x16x32_bf16 a[196:199],a[164:167],a[152:155],a[196:199]
s_add_u32 m0,s57,134144
s_add_u32 s83,s74,98304
)"
R"(v_mfma_f32_16x16x32_bf16 v[100:103],a[116:119],a[100:103],v[100:103]
ds_read_b64_tr_b16 a[32:33],v6 offset:0
v_mfma_f32_16x16x32_bf16 v[100:103],a[164:167],a[148:151],v[100:103]
ds_read_b64_tr_b16 a[34:35],v6 offset:1024
v_mfma_f32_16x16x32_bf16 v[68:71],a[116:119],a[96:99],v[68:71]
buffer_load_dwordx4 v17,s[92:95],s83 offen lds
ds_read_b64_tr_b16 a[36:37],v7 offset:0
v_mfma_f32_16x16x32_bf16 v[68:71],a[164:167],a[144:147],v[68:71]
s_add_u32 s74,s74,524288
s_and_b32 s74,s74,0x7fffff
ds_read_b64_tr_b16 a[38:39],v7 offset:1024
v_mfma_f32_16x16x32_bf16 v[72:75],a[120:123],a[96:99],v[72:75]
s_add_u32 s84,s67,s68
s_add_u32 m0,s56,0
ds_read_b128 a[8:11],v0 offset:4096
v_mfma_f32_16x16x32_bf16 v[72:75],a[168:171],a[144:147],v[72:75]
s_add_u32 s82,s67,0
ds_read_b64_tr_b16 a[40:41],v8 offset:0
v_mfma_f32_16x16x32_bf16 v[104:107],a[120:123],a[100:103],v[104:107]
buffer_load_dwordx4 v10,s[88:91],s82 offen lds
ds_read_b64_tr_b16 a[42:43],v8 offset:1024
v_mfma_f32_16x16x32_bf16 v[104:107],a[168:171],a[148:151],v[104:107]
s_add_u32 m0,s56,1024
s_add_u32 s83,s67,16384
)"
R"(ds_read_b64_tr_b16 a[44:45],v9 offset:0
v_mfma_f32_16x16x32_bf16 a[200:203],a[120:123],a[104:107],a[200:203]
ds_read_b64_tr_b16 a[46:47],v9 offset:1024
v_mfma_f32_16x16x32_bf16 a[200:203],a[168:171],a[152:155],a[200:203]
ds_read_b128 a[12:15],v0 offset:6144
v_mfma_f32_16x16x32_bf16 a[232:235],a[120:123],a[108:111],a[232:235]
buffer_load_dwordx4 v11,s[88:91],s83 offen lds
v_mfma_f32_16x16x32_bf16 a[232:235],a[168:171],a[156:159],a[232:235]
s_add_u32 m0,s56,2048
s_add_u32 s82,s67,0
ds_read_b64_tr_b16 a[64:65],v2 offset:8192
v_mfma_f32_16x16x32_bf16 a[236:239],a[124:127],a[108:111],a[236:239]
ds_read_b64_tr_b16 a[66:67],v2 offset:9216
v_mfma_f32_16x16x32_bf16 a[236:239],a[172:175],a[156:159],a[236:239]
ds_read_b64_tr_b16 a[68:69],v3 offset:8192
v_mfma_f32_16x16x32_bf16 a[204:207],a[124:127],a[104:107],a[204:207]
buffer_load_dwordx4 v12,s[88:91],s82 offen lds
ds_read_b64_tr_b16 a[70:71],v3 offset:9216
v_mfma_f32_16x16x32_bf16 a[204:207],a[172:175],a[152:155],a[204:207]
s_add_u32 m0,s56,3072
s_add_u32 s83,s67,16384
ds_read_b128 a[48:51],v1 offset:0
v_mfma_f32_16x16x32_bf16 v[108:111],a[124:127],a[100:103],v[108:111]
ds_read_b64_tr_b16 a[72:73],v4 offset:8192
)"
R"(v_mfma_f32_16x16x32_bf16 v[108:111],a[172:175],a[148:151],v[108:111]
ds_read_b64_tr_b16 a[74:75],v4 offset:9216
v_mfma_f32_16x16x32_bf16 v[76:79],a[124:127],a[96:99],v[76:79]
buffer_load_dwordx4 v13,s[88:91],s83 offen lds
ds_read_b64_tr_b16 a[76:77],v5 offset:8192
v_mfma_f32_16x16x32_bf16 v[76:79],a[172:175],a[144:147],v[76:79]
s_add_u32 m0,s56,4096
s_add_u32 s82,s84,0
ds_read_b64_tr_b16 a[78:79],v5 offset:9216
v_mfma_f32_16x16x32_bf16 v[80:83],a[128:131],a[96:99],v[80:83]
ds_read_b128 a[52:55],v1 offset:2048
v_mfma_f32_16x16x32_bf16 v[80:83],a[176:179],a[144:147],v[80:83]
buffer_load_dwordx4 v10,s[88:91],s82 offen lds
v_mfma_f32_16x16x32_bf16 v[112:115],a[128:131],a[100:103],v[112:115]
s_add_u32 m0,s56,5120
s_add_u32 s83,s84,16384
ds_read_b64_tr_b16 a[80:81],v6 offset:8192
v_mfma_f32_16x16x32_bf16 v[112:115],a[176:179],a[148:151],v[112:115]
ds_read_b64_tr_b16 a[82:83],v6 offset:9216
v_mfma_f32_16x16x32_bf16 a[208:211],a[128:131],a[104:107],a[208:211]
ds_read_b64_tr_b16 a[84:85],v7 offset:8192
v_mfma_f32_16x16x32_bf16 a[208:211],a[176:179],a[152:155],a[208:211]
buffer_load_dwordx4 v11,s[88:91],s83 offen lds
ds_read_b64_tr_b16 a[86:87],v7 offset:9216
)"
R"(v_mfma_f32_16x16x32_bf16 a[240:243],a[128:131],a[108:111],a[240:243]
s_add_u32 m0,s56,6144
s_add_u32 s82,s84,0
ds_read_b128 a[56:59],v1 offset:4096
v_mfma_f32_16x16x32_bf16 a[240:243],a[176:179],a[156:159],a[240:243]
ds_read_b64_tr_b16 a[88:89],v8 offset:8192
v_mfma_f32_16x16x32_bf16 a[244:247],a[132:135],a[108:111],a[244:247]
ds_read_b64_tr_b16 a[90:91],v8 offset:9216
v_mfma_f32_16x16x32_bf16 a[244:247],a[180:183],a[156:159],a[244:247]
buffer_load_dwordx4 v12,s[88:91],s82 offen lds
ds_read_b64_tr_b16 a[92:93],v9 offset:8192
v_mfma_f32_16x16x32_bf16 a[212:215],a[132:135],a[104:107],a[212:215]
s_add_u32 m0,s56,7168
s_add_u32 s83,s84,16384
ds_read_b64_tr_b16 a[94:95],v9 offset:9216
v_mfma_f32_16x16x32_bf16 a[212:215],a[180:183],a[152:155],a[212:215]
ds_read_b128 a[60:63],v1 offset:6144
v_mfma_f32_16x16x32_bf16 v[116:119],a[132:135],a[100:103],v[116:119]
v_mfma_f32_16x16x32_bf16 v[116:119],a[180:183],a[148:151],v[116:119]
buffer_load_dwordx4 v13,s[88:91],s83 offen lds
v_mfma_f32_16x16x32_bf16 v[84:87],a[132:135],a[96:99],v[84:87]
s_add_u32 s67,s67,128
s_and_b32 s67,s67,2047
s_mov_b32 s99,28672
)"
R"(v_mfma_f32_16x16x32_bf16 v[84:87],a[180:183],a[144:147],v[84:87]
v_mfma_f32_16x16x32_bf16 v[88:91],a[136:139],a[96:99],v[88:91]
v_mfma_f32_16x16x32_bf16 v[88:91],a[184:187],a[144:147],v[88:91]
buffer_store_dwordx4 v[220:223],v18,s[48:51],s99 offen offset:3072 sc1
v_add_u32 v0,-32768,v0
v_mfma_f32_16x16x32_bf16 v[120:123],a[136:139],a[100:103],v[120:123]
s_mov_b32 s99,12288
v_add_u32 v1,-32768,v1
v_add_u32 v2,-49152,v2
v_mfma_f32_16x16x32_bf16 v[120:123],a[184:187],a[148:151],v[120:123]
v_add_u32 v3,-49152,v3
v_add_u32 v4,-49152,v4
v_mfma_f32_16x16x32_bf16 a[216:219],a[136:139],a[104:107],a[216:219]
v_add_u32 v5,-49152,v5
v_mfma_f32_16x16x32_bf16 a[216:219],a[184:187],a[152:155],a[216:219]
buffer_store_dwordx4 v[188:191],v18,s[48:51],s99 offen offset:3072 sc1
v_add_u32 v6,-49152,v6
v_mfma_f32_16x16x32_bf16 a[248:251],a[136:139],a[108:111],a[248:251]
s_mov_b32 s99,12288
v_add_u32 v7,-49152,v7
v_add_u32 v8,-49152,v8
v_mfma_f32_16x16x32_bf16 a[248:251],a[184:187],a[156:159],a[248:251]
v_add_u32 v9,-49152,v9
v_mfma_f32_16x16x32_bf16 a[252:255],a[140:143],a[108:111],a[252:255]
)"
R"(v_mfma_f32_16x16x32_bf16 a[252:255],a[188:191],a[156:159],a[252:255]
buffer_store_dwordx4 v[184:187],v18,s[48:51],s99 offen offset:2048 sc1
v_mfma_f32_16x16x32_bf16 a[220:223],a[140:143],a[104:107],a[220:223]
v_mfma_f32_16x16x32_bf16 a[220:223],a[188:191],a[152:155],a[220:223]
v_mfma_f32_16x16x32_bf16 v[124:127],a[140:143],a[100:103],v[124:127]
v_mfma_f32_16x16x32_bf16 v[124:127],a[188:191],a[148:151],v[124:127]
v_mfma_f32_16x16x32_bf16 v[92:95],a[140:143],a[96:99],v[92:95]
s_waitcnt lgkmcnt(0)
v_mfma_f32_16x16x32_bf16 v[92:95],a[188:191],a[144:147],v[92:95]
v_mfma_f32_16x16x32_bf16 v[64:67],a[16:19],a[0:3],v[64:67]
s_waitcnt vmcnt(18)
s_barrier
v_mfma_f32_16x16x32_bf16 v[64:67],a[64:67],a[48:51],v[64:67]
s_add_u32 m0,s57,147456
s_add_u32 s82,s74,0
ds_read_b64_tr_b16 a[112:113],v2 offset:0
v_mfma_f32_16x16x32_bf16 v[96:99],a[16:19],a[4:7],v[96:99]
ds_read_b64_tr_b16 a[114:115],v2 offset:1024
v_mfma_f32_16x16x32_bf16 v[96:99],a[64:67],a[52:55],v[96:99]
buffer_load_dwordx4 v16,s[92:95],s82 offen lds
ds_read_b64_tr_b16 a[116:117],v3 offset:0
v_mfma_f32_16x16x32_bf16 a[192:195],a[16:19],a[8:11],a[192:195]
s_add_u32 m0,s57,148480
s_add_u32 s83,s74,32768
)"
R"(ds_read_b64_tr_b16 a[118:119],v3 offset:1024
v_mfma_f32_16x16x32_bf16 a[192:195],a[64:67],a[56:59],a[192:195]
ds_read_b128 a[96:99],v0 offset:0
v_mfma_f32_16x16x32_bf16 a[224:227],a[16:19],a[12:15],a[224:227]
buffer_load_dwordx4 v16,s[92:95],s83 offen lds
ds_read_b64_tr_b16 a[120:121],v4 offset:0
v_mfma_f32_16x16x32_bf16 a[224:227],a[64:67],a[60:63],a[224:227]
s_add_u32 m0,s57,149504
s_add_u32 s82,s74,65536
ds_read_b64_tr_b16 a[122:123],v4 offset:1024
v_mfma_f32_16x16x32_bf16 a[228:231],a[20:23],a[12:15],a[228:231]
ds_read_b64_tr_b16 a[124:125],v5 offset:0
v_mfma_f32_16x16x32_bf16 a[228:231],a[68:71],a[60:63],a[228:231]
ds_read_b64_tr_b16 a[126:127],v5 offset:1024
v_mfma_f32_16x16x32_bf16 a[196:199],a[20:23],a[8:11],a[196:199]
buffer_load_dwordx4 v17,s[92:95],s82 offen lds
ds_read_b128 a[100:103],v0 offset:2048
v_mfma_f32_16x16x32_bf16 a[196:199],a[68:71],a[56:59],a[196:199]
s_add_u32 m0,s57,150528
s_add_u32 s83,s74,98304
v_mfma_f32_16x16x32_bf16 v[100:103],a[20:23],a[4:7],v[100:103]
ds_read_b64_tr_b16 a[128:129],v6 offset:0
v_mfma_f32_16x16x32_bf16 v[100:103],a[68:71],a[52:55],v[100:103]
ds_read_b64_tr_b16 a[130:131],v6 offset:1024
)"
R"(v_mfma_f32_16x16x32_bf16 v[68:71],a[20:23],a[0:3],v[68:71]
buffer_load_dwordx4 v17,s[92:95],s83 offen lds
ds_read_b64_tr_b16 a[132:133],v7 offset:0
v_mfma_f32_16x16x32_bf16 v[68:71],a[68:71],a[48:51],v[68:71]
s_add_u32 s74,s74,524288
s_and_b32 s74,s74,0x7fffff
ds_read_b64_tr_b16 a[134:135],v7 offset:1024
v_mfma_f32_16x16x32_bf16 v[72:75],a[24:27],a[0:3],v[72:75]
s_add_u32 s84,s67,s68
s_add_u32 m0,s56,65536
ds_read_b128 a[104:107],v0 offset:4096
v_mfma_f32_16x16x32_bf16 v[72:75],a[72:75],a[48:51],v[72:75]
s_add_u32 s82,s67,0
ds_read_b64_tr_b16 a[136:137],v8 offset:0
v_mfma_f32_16x16x32_bf16 v[104:107],a[24:27],a[4:7],v[104:107]
buffer_load_dwordx4 v10,s[88:91],s82 offen lds
ds_read_b64_tr_b16 a[138:139],v8 offset:1024
v_mfma_f32_16x16x32_bf16 v[104:107],a[72:75],a[52:55],v[104:107]
s_add_u32 m0,s56,66560
s_add_u32 s83,s67,16384
ds_read_b64_tr_b16 a[140:141],v9 offset:0
v_mfma_f32_16x16x32_bf16 a[200:203],a[24:27],a[8:11],a[200:203]
ds_read_b64_tr_b16 a[142:143],v9 offset:1024
v_mfma_f32_16x16x32_bf16 a[200:203],a[72:75],a[56:59],a[200:203]
)"
R"(ds_read_b128 a[108:111],v0 offset:6144
v_mfma_f32_16x16x32_bf16 a[232:235],a[24:27],a[12:15],a[232:235]
buffer_load_dwordx4 v11,s[88:91],s83 offen lds
v_mfma_f32_16x16x32_bf16 a[232:235],a[72:75],a[60:63],a[232:235]
s_add_u32 m0,s56,67584
s_add_u32 s82,s67,0
ds_read_b64_tr_b16 a[160:161],v2 offset:8192
v_mfma_f32_16x16x32_bf16 a[236:239],a[28:31],a[12:15],a[236:239]
ds_read_b64_tr_b16 a[162:163],v2 offset:9216
v_mfma_f32_16x16x32_bf16 a[236:239],a[76:79],a[60:63],a[236:239]
ds_read_b64_tr_b16 a[164:165],v3 offset:8192
v_mfma_f32_16x16x32_bf16 a[204:207],a[28:31],a[8:11],a[204:207]
buffer_load_dwordx4 v12,s[88:91],s82 offen lds
ds_read_b64_tr_b16 a[166:167],v3 offset:9216
v_mfma_f32_16x16x32_bf16 a[204:207],a[76:79],a[56:59],a[204:207]
s_add_u32 m0,s56,68608
s_add_u32 s83,s67,16384
ds_read_b128 a[144:147],v1 offset:0
v_mfma_f32_16x16x32_bf16 v[108:111],a[28:31],a[4:7],v[108:111]
ds_read_b64_tr_b16 a[168:169],v4 offset:8192
v_mfma_f32_16x16x32_bf16 v[108:111],a[76:79],a[52:55],v[108:111]
ds_read_b64_tr_b16 a[170:171],v4 offset:9216
v_mfma_f32_16x16x32_bf16 v[76:79],a[28:31],a[0:3],v[76:79]
buffer_load_dwordx4 v13,s[88:91],s83 offen lds
)"
R"(ds_read_b64_tr_b16 a[172:173],v5 offset:8192
v_mfma_f32_16x16x32_bf16 v[76:79],a[76:79],a[48:51],v[76:79]
s_add_u32 m0,s56,69632
s_add_u32 s82,s84,0
ds_read_b64_tr_b16 a[174:175],v5 offset:9216
v_mfma_f32_16x16x32_bf16 v[80:83],a[32:35],a[0:3],v[80:83]
ds_read_b128 a[148:151],v1 offset:2048
v_mfma_f32_16x16x32_bf16 v[80:83],a[80:83],a[48:51],v[80:83]
buffer_load_dwordx4 v10,s[88:91],s82 offen lds
v_mfma_f32_16x16x32_bf16 v[112:115],a[32:35],a[4:7],v[112:115]
s_add_u32 m0,s56,70656
s_add_u32 s83,s84,16384
ds_read_b64_tr_b16 a[176:177],v6 offset:8192
v_mfma_f32_16x16x32_bf16 v[112:115],a[80:83],a[52:55],v[112:115]
ds_read_b64_tr_b16 a[178:179],v6 offset:9216
v_mfma_f32_16x16x32_bf16 a[208:211],a[32:35],a[8:11],a[208:211]
ds_read_b64_tr_b16 a[180:181],v7 offset:8192
v_mfma_f32_16x16x32_bf16 a[208:211],a[80:83],a[56:59],a[208:211]
buffer_load_dwordx4 v11,s[88:91],s83 offen lds
ds_read_b64_tr_b16 a[182:183],v7 offset:9216
v_mfma_f32_16x16x32_bf16 a[240:243],a[32:35],a[12:15],a[240:243]
s_add_u32 m0,s56,71680
s_add_u32 s82,s84,0
ds_read_b128 a[152:155],v1 offset:4096
)"
R"(v_mfma_f32_16x16x32_bf16 a[240:243],a[80:83],a[60:63],a[240:243]
ds_read_b64_tr_b16 a[184:185],v8 offset:8192
v_mfma_f32_16x16x32_bf16 a[244:247],a[36:39],a[12:15],a[244:247]
ds_read_b64_tr_b16 a[186:187],v8 offset:9216
v_mfma_f32_16x16x32_bf16 a[244:247],a[84:87],a[60:63],a[244:247]
buffer_load_dwordx4 v12,s[88:91],s82 offen lds
ds_read_b64_tr_b16 a[188:189],v9 offset:8192
v_mfma_f32_16x16x32_bf16 a[212:215],a[36:39],a[8:11],a[212:215]
s_add_u32 m0,s56,72704
s_add_u32 s83,s84,16384
ds_read_b64_tr_b16 a[190:191],v9 offset:9216
v_mfma_f32_16x16x32_bf16 a[212:215],a[84:87],a[56:59],a[212:215]
ds_read_b128 a[156:159],v1 offset:6144
v_mfma_f32_16x16x32_bf16 v[116:119],a[36:39],a[4:7],v[116:119]
v_mfma_f32_16x16x32_bf16 v[116:119],a[84:87],a[52:55],v[116:119]
buffer_load_dwordx4 v13,s[88:91],s83 offen lds
v_mfma_f32_16x16x32_bf16 v[84:87],a[36:39],a[0:3],v[84:87]
s_add_u32 s67,s67,128
s_and_b32 s67,s67,2047
s_mov_b32 s99,12288
v_mfma_f32_16x16x32_bf16 v[84:87],a[84:87],a[48:51],v[84:87]
v_mfma_f32_16x16x32_bf16 v[88:91],a[40:43],a[0:3],v[88:91]
v_mfma_f32_16x16x32_bf16 v[88:91],a[88:91],a[48:51],v[88:91]
buffer_store_dwordx4 v[180:183],v18,s[48:51],s99 offen offset:1024 sc1
)"
R"(v_add_u32 v0,-32768,v0
v_mfma_f32_16x16x32_bf16 v[120:123],a[40:43],a[4:7],v[120:123]
s_mov_b32 s99,12288
v_add_u32 v1,-32768,v1
v_add_u32 v2,32768,v2
v_mfma_f32_16x16x32_bf16 v[120:123],a[88:91],a[52:55],v[120:123]
v_add_u32 v3,32768,v3
v_add_u32 v4,32768,v4
v_mfma_f32_16x16x32_bf16 a[216:219],a[40:43],a[8:11],a[216:219]
v_add_u32 v5,32768,v5
v_mfma_f32_16x16x32_bf16 a[216:219],a[88:91],a[56:59],a[216:219]
buffer_store_dwordx4 v[176:179],v18,s[48:51],s99 offen offset:0 sc1
v_add_u32 v6,32768,v6
v_mfma_f32_16x16x32_bf16 a[248:251],a[40:43],a[12:15],a[248:251]
s_mov_b32 s99,8192
v_add_u32 v7,32768,v7
v_add_u32 v8,32768,v8
v_mfma_f32_16x16x32_bf16 a[248:251],a[88:91],a[60:63],a[248:251]
v_add_u32 v9,32768,v9
v_mfma_f32_16x16x32_bf16 a[252:255],a[44:47],a[12:15],a[252:255]
v_mfma_f32_16x16x32_bf16 a[252:255],a[92:95],a[60:63],a[252:255]
buffer_store_dwordx4 v[172:175],v18,s[48:51],s99 offen offset:3072 sc1
v_mfma_f32_16x16x32_bf16 a[220:223],a[44:47],a[8:11],a[220:223]
v_mfma_f32_16x16x32_bf16 a[220:223],a[92:95],a[56:59],a[220:223]
)"
R"(v_mfma_f32_16x16x32_bf16 v[124:127],a[44:47],a[4:7],v[124:127]
v_mfma_f32_16x16x32_bf16 v[124:127],a[92:95],a[52:55],v[124:127]
v_mfma_f32_16x16x32_bf16 v[92:95],a[44:47],a[0:3],v[92:95]
s_waitcnt lgkmcnt(0)
v_mfma_f32_16x16x32_bf16 v[92:95],a[92:95],a[48:51],v[92:95]
v_mfma_f32_16x16x32_bf16 v[64:67],a[112:115],a[96:99],v[64:67]
s_waitcnt vmcnt(18)
s_barrier
v_mfma_f32_16x16x32_bf16 v[64:67],a[160:163],a[144:147],v[64:67]
s_add_u32 m0,s57,98304
s_add_u32 s82,s74,0
ds_read_b64_tr_b16 a[16:17],v2 offset:0
v_mfma_f32_16x16x32_bf16 v[96:99],a[112:115],a[100:103],v[96:99]
ds_read_b64_tr_b16 a[18:19],v2 offset:1024
v_mfma_f32_16x16x32_bf16 v[96:99],a[160:163],a[148:151],v[96:99]
buffer_load_dwordx4 v16,s[92:95],s82 offen lds
ds_read_b64_tr_b16 a[20:21],v3 offset:0
v_mfma_f32_16x16x32_bf16 a[192:195],a[112:115],a[104:107],a[192:195]
s_add_u32 m0,s57,99328
s_add_u32 s83,s74,32768
ds_read_b64_tr_b16 a[22:23],v3 offset:1024
v_mfma_f32_16x16x32_bf16 a[192:195],a[160:163],a[152:155],a[192:195]
ds_read_b128 a[0:3],v0 offset:0
v_mfma_f32_16x16x32_bf16 a[224:227],a[112:115],a[108:111],a[224:227]
)"
R"(buffer_load_dwordx4 v16,s[92:95],s83 offen lds
ds_read_b64_tr_b16 a[24:25],v4 offset:0
v_mfma_f32_16x16x32_bf16 a[224:227],a[160:163],a[156:159],a[224:227]
s_add_u32 m0,s57,100352
s_add_u32 s82,s74,65536
ds_read_b64_tr_b16 a[26:27],v4 offset:1024
v_mfma_f32_16x16x32_bf16 a[228:231],a[116:119],a[108:111],a[228:231]
ds_read_b64_tr_b16 a[28:29],v5 offset:0
v_mfma_f32_16x16x32_bf16 a[228:231],a[164:167],a[156:159],a[228:231]
s_branch gm_KB_sk10
.p2align 12
gm_KB_pg10:
s_setpc_b64 s[100:101]
gm_KB_sk10:
ds_read_b64_tr_b16 a[30:31],v5 offset:1024
v_mfma_f32_16x16x32_bf16 a[196:199],a[116:119],a[104:107],a[196:199]
buffer_load_dwordx4 v17,s[92:95],s82 offen lds
ds_read_b128 a[4:7],v0 offset:2048
v_mfma_f32_16x16x32_bf16 a[196:199],a[164:167],a[152:155],a[196:199]
s_add_u32 m0,s57,101376
s_add_u32 s83,s74,98304
v_mfma_f32_16x16x32_bf16 v[100:103],a[116:119],a[100:103],v[100:103]
ds_read_b64_tr_b16 a[32:33],v6 offset:0
v_mfma_f32_16x16x32_bf16 v[100:103],a[164:167],a[148:151],v[100:103]
)"
R"(ds_read_b64_tr_b16 a[34:35],v6 offset:1024
v_mfma_f32_16x16x32_bf16 v[68:71],a[116:119],a[96:99],v[68:71]
buffer_load_dwordx4 v17,s[92:95],s83 offen lds
ds_read_b64_tr_b16 a[36:37],v7 offset:0
v_mfma_f32_16x16x32_bf16 v[68:71],a[164:167],a[144:147],v[68:71]
s_add_u32 s74,s74,524288
s_and_b32 s74,s74,0x7fffff
ds_read_b64_tr_b16 a[38:39],v7 offset:1024
v_mfma_f32_16x16x32_bf16 v[72:75],a[120:123],a[96:99],v[72:75]
s_add_u32 s84,s67,s68
s_add_u32 m0,s56,32768
ds_read_b128 a[8:11],v0 offset:4096
v_mfma_f32_16x16x32_bf16 v[72:75],a[168:171],a[144:147],v[72:75]
s_add_u32 s82,s67,0
ds_read_b64_tr_b16 a[40:41],v8 offset:0
v_mfma_f32_16x16x32_bf16 v[104:107],a[120:123],a[100:103],v[104:107]
buffer_load_dwordx4 v10,s[88:91],s82 offen lds
ds_read_b64_tr_b16 a[42:43],v8 offset:1024
v_mfma_f32_16x16x32_bf16 v[104:107],a[168:171],a[148:151],v[104:107]
s_add_u32 m0,s56,33792
s_add_u32 s83,s67,16384
ds_read_b64_tr_b16 a[44:45],v9 offset:0
v_mfma_f32_16x16x32_bf16 a[200:203],a[120:123],a[104:107],a[200:203]
ds_read_b64_tr_b16 a[46:47],v9 offset:1024
)"
R"(v_mfma_f32_16x16x32_bf16 a[200:203],a[168:171],a[152:155],a[200:203]
ds_read_b128 a[12:15],v0 offset:6144
v_mfma_f32_16x16x32_bf16 a[232:235],a[120:123],a[108:111],a[232:235]
buffer_load_dwordx4 v11,s[88:91],s83 offen lds
v_mfma_f32_16x16x32_bf16 a[232:235],a[168:171],a[156:159],a[232:235]
s_add_u32 m0,s56,34816
s_add_u32 s82,s67,0
ds_read_b64_tr_b16 a[64:65],v2 offset:8192
v_mfma_f32_16x16x32_bf16 a[236:239],a[124:127],a[108:111],a[236:239]
ds_read_b64_tr_b16 a[66:67],v2 offset:9216
v_mfma_f32_16x16x32_bf16 a[236:239],a[172:175],a[156:159],a[236:239]
ds_read_b64_tr_b16 a[68:69],v3 offset:8192
v_mfma_f32_16x16x32_bf16 a[204:207],a[124:127],a[104:107],a[204:207]
buffer_load_dwordx4 v12,s[88:91],s82 offen lds
ds_read_b64_tr_b16 a[70:71],v3 offset:9216
v_mfma_f32_16x16x32_bf16 a[204:207],a[172:175],a[152:155],a[204:207]
s_add_u32 m0,s56,35840
s_add_u32 s83,s67,16384
ds_read_b128 a[48:51],v1 offset:0
v_mfma_f32_16x16x32_bf16 v[108:111],a[124:127],a[100:103],v[108:111]
ds_read_b64_tr_b16 a[72:73],v4 offset:8192
v_mfma_f32_16x16x32_bf16 v[108:111],a[172:175],a[148:151],v[108:111]
ds_read_b64_tr_b16 a[74:75],v4 offset:9216
v_mfma_f32_16x16x32_bf16 v[76:79],a[124:127],a[96:99],v[76:79]
)"
R"(buffer_load_dwordx4 v13,s[88:91],s83 offen lds
ds_read_b64_tr_b16 a[76:77],v5 offset:8192
v_mfma_f32_16x16x32_bf16 v[76:79],a[172:175],a[144:147],v[76:79]
s_add_u32 m0,s56,36864
s_add_u32 s82,s84,0
ds_read_b64_tr_b16 a[78:79],v5 offset:9216
v_mfma_f32_16x16x32_bf16 v[80:83],a[128:131],a[96:99],v[80:83]
ds_read_b128 a[52:55],v1 offset:2048
v_mfma_f32_16x16x32_bf16 v[80:83],a[176:179],a[144:147],v[80:83]
buffer_load_dwordx4 v10,s[88:91],s82 offen lds
v_mfma_f32_16x16x32_bf16 v[112:115],a[128:131],a[100:103],v[112:115]
s_add_u32 m0,s56,37888
s_add_u32 s83,s84,16384
ds_read_b64_tr_b16 a[80:81],v6 offset:8192
v_mfma_f32_16x16x32_bf16 v[112:115],a[176:179],a[148:151],v[112:115]
ds_read_b64_tr_b16 a[82:83],v6 offset:9216
v_mfma_f32_16x16x32_bf16 a[208:211],a[128:131],a[104:107],a[208:211]
ds_read_b64_tr_b16 a[84:85],v7 offset:8192
v_mfma_f32_16x16x32_bf16 a[208:211],a[176:179],a[152:155],a[208:211]
buffer_load_dwordx4 v11,s[88:91],s83 offen lds
ds_read_b64_tr_b16 a[86:87],v7 offset:9216
v_mfma_f32_16x16x32_bf16 a[240:243],a[128:131],a[108:111],a[240:243]
s_add_u32 m0,s56,38912
s_add_u32 s82,s84,0
)"
R"(ds_read_b128 a[56:59],v1 offset:4096
v_mfma_f32_16x16x32_bf16 a[240:243],a[176:179],a[156:159],a[240:243]
ds_read_b64_tr_b16 a[88:89],v8 offset:8192
v_mfma_f32_16x16x32_bf16 a[244:247],a[132:135],a[108:111],a[244:247]
ds_read_b64_tr_b16 a[90:91],v8 offset:9216
v_mfma_f32_16x16x32_bf16 a[244:247],a[180:183],a[156:159],a[244:247]
buffer_load_dwordx4 v12,s[88:91],s82 offen lds
ds_read_b64_tr_b16 a[92:93],v9 offset:8192
v_mfma_f32_16x16x32_bf16 a[212:215],a[132:135],a[104:107],a[212:215]
s_add_u32 m0,s56,39936
s_add_u32 s83,s84,16384
ds_read_b64_tr_b16 a[94:95],v9 offset:9216
v_mfma_f32_16x16x32_bf16 a[212:215],a[180:183],a[152:155],a[212:215]
ds_read_b128 a[60:63],v1 offset:6144
v_mfma_f32_16x16x32_bf16 v[116:119],a[132:135],a[100:103],v[116:119]
v_mfma_f32_16x16x32_bf16 v[116:119],a[180:183],a[148:151],v[116:119]
buffer_load_dwordx4 v13,s[88:91],s83 offen lds
v_mfma_f32_16x16x32_bf16 v[84:87],a[132:135],a[96:99],v[84:87]
s_add_u32 s67,s67,128
s_and_b32 s67,s67,2047
s_mov_b32 s99,8192
v_mfma_f32_16x16x32_bf16 v[84:87],a[180:183],a[144:147],v[84:87]
v_mfma_f32_16x16x32_bf16 v[88:91],a[136:139],a[96:99],v[88:91]
v_mfma_f32_16x16x32_bf16 v[88:91],a[184:187],a[144:147],v[88:91]
)"
R"(buffer_store_dwordx4 v[168:171],v18,s[48:51],s99 offen offset:2048 sc1
v_add_u32 v0,65536,v0
v_mfma_f32_16x16x32_bf16 v[120:123],a[136:139],a[100:103],v[120:123]
s_mov_b32 s99,8192
v_add_u32 v1,65536,v1
v_add_u32 v2,16384,v2
v_mfma_f32_16x16x32_bf16 v[120:123],a[184:187],a[148:151],v[120:123]
v_add_u32 v3,16384,v3
v_add_u32 v4,16384,v4
v_mfma_f32_16x16x32_bf16 a[216:219],a[136:139],a[104:107],a[216:219]
v_add_u32 v5,16384,v5
v_mfma_f32_16x16x32_bf16 a[216:219],a[184:187],a[152:155],a[216:219]
buffer_store_dwordx4 v[164:167],v18,s[48:51],s99 offen offset:1024 sc1
v_add_u32 v6,16384,v6
v_mfma_f32_16x16x32_bf16 a[248:251],a[136:139],a[108:111],a[248:251]
s_mov_b32 s99,8192
v_add_u32 v7,16384,v7
v_add_u32 v8,16384,v8
v_mfma_f32_16x16x32_bf16 a[248:251],a[184:187],a[156:159],a[248:251]
v_add_u32 v9,16384,v9
v_mfma_f32_16x16x32_bf16 a[252:255],a[140:143],a[108:111],a[252:255]
v_mfma_f32_16x16x32_bf16 a[252:255],a[188:191],a[156:159],a[252:255]
buffer_store_dwordx4 v[160:163],v18,s[48:51],s99 offen offset:0 sc1
v_mfma_f32_16x16x32_bf16 a[220:223],a[140:143],a[104:107],a[220:223]
)"
R"(v_mfma_f32_16x16x32_bf16 a[220:223],a[188:191],a[152:155],a[220:223]
v_mfma_f32_16x16x32_bf16 v[124:127],a[140:143],a[100:103],v[124:127]
v_mfma_f32_16x16x32_bf16 v[124:127],a[188:191],a[148:151],v[124:127]
v_mfma_f32_16x16x32_bf16 v[92:95],a[140:143],a[96:99],v[92:95]
s_waitcnt lgkmcnt(0)
v_mfma_f32_16x16x32_bf16 v[92:95],a[188:191],a[144:147],v[92:95]
v_mfma_f32_16x16x32_bf16 v[64:67],a[16:19],a[0:3],v[64:67]
s_waitcnt vmcnt(18)
s_barrier
v_mfma_f32_16x16x32_bf16 v[64:67],a[64:67],a[48:51],v[64:67]
s_add_u32 m0,s57,131072
s_add_u32 s82,s74,0
ds_read_b64_tr_b16 a[112:113],v2 offset:0
v_mfma_f32_16x16x32_bf16 v[96:99],a[16:19],a[4:7],v[96:99]
ds_read_b64_tr_b16 a[114:115],v2 offset:1024
v_mfma_f32_16x16x32_bf16 v[96:99],a[64:67],a[52:55],v[96:99]
buffer_load_dwordx4 v16,s[92:95],s82 offen lds
ds_read_b64_tr_b16 a[116:117],v3 offset:0
v_mfma_f32_16x16x32_bf16 a[192:195],a[16:19],a[8:11],a[192:195]
s_add_u32 m0,s57,132096
s_add_u32 s83,s74,32768
ds_read_b64_tr_b16 a[118:119],v3 offset:1024
v_mfma_f32_16x16x32_bf16 a[192:195],a[64:67],a[56:59],a[192:195]
ds_read_b128 a[96:99],v0 offset:0
)"
R"(v_mfma_f32_16x16x32_bf16 a[224:227],a[16:19],a[12:15],a[224:227]
buffer_load_dwordx4 v16,s[92:95],s83 offen lds
ds_read_b64_tr_b16 a[120:121],v4 offset:0
v_mfma_f32_16x16x32_bf16 a[224:227],a[64:67],a[60:63],a[224:227]
s_add_u32 m0,s57,133120
s_add_u32 s82,s74,65536
ds_read_b64_tr_b16 a[122:123],v4 offset:1024
v_mfma_f32_16x16x32_bf16 a[228:231],a[20:23],a[12:15],a[228:231]
ds_read_b64_tr_b16 a[124:125],v5 offset:0
v_mfma_f32_16x16x32_bf16 a[228:231],a[68:71],a[60:63],a[228:231]
ds_read_b64_tr_b16 a[126:127],v5 offset:1024
v_mfma_f32_16x16x32_bf16 a[196:199],a[20:23],a[8:11],a[196:199]
buffer_load_dwordx4 v17,s[92:95],s82 offen lds
ds_read_b128 a[100:103],v0 offset:2048
v_mfma_f32_16x16x32_bf16 a[196:199],a[68:71],a[56:59],a[196:199]
s_add_u32 m0,s57,134144
s_add_u32 s83,s74,98304
v_mfma_f32_16x16x32_bf16 v[100:103],a[20:23],a[4:7],v[100:103]
ds_read_b64_tr_b16 a[128:129],v6 offset:0
v_mfma_f32_16x16x32_bf16 v[100:103],a[68:71],a[52:55],v[100:103]
ds_read_b64_tr_b16 a[130:131],v6 offset:1024
v_mfma_f32_16x16x32_bf16 v[68:71],a[20:23],a[0:3],v[68:71]
buffer_load_dwordx4 v17,s[92:95],s83 offen lds
ds_read_b64_tr_b16 a[132:133],v7 offset:0
)"
R"(v_mfma_f32_16x16x32_bf16 v[68:71],a[68:71],a[48:51],v[68:71]
s_add_u32 s74,s74,524288
s_and_b32 s74,s74,0x7fffff
ds_read_b64_tr_b16 a[134:135],v7 offset:1024
v_mfma_f32_16x16x32_bf16 v[72:75],a[24:27],a[0:3],v[72:75]
s_add_u32 s84,s67,s68
s_add_u32 m0,s56,0
ds_read_b128 a[104:107],v0 offset:4096
v_mfma_f32_16x16x32_bf16 v[72:75],a[72:75],a[48:51],v[72:75]
s_add_u32 s82,s67,0
ds_read_b64_tr_b16 a[136:137],v8 offset:0
v_mfma_f32_16x16x32_bf16 v[104:107],a[24:27],a[4:7],v[104:107]
buffer_load_dwordx4 v10,s[88:91],s82 offen lds
ds_read_b64_tr_b16 a[138:139],v8 offset:1024
v_mfma_f32_16x16x32_bf16 v[104:107],a[72:75],a[52:55],v[104:107]
s_add_u32 m0,s56,1024
s_add_u32 s83,s67,16384
ds_read_b64_tr_b16 a[140:141],v9 offset:0
v_mfma_f32_16x16x32_bf16 a[200:203],a[24:27],a[8:11],a[200:203]
ds_read_b64_tr_b16 a[142:143],v9 offset:1024
v_mfma_f32_16x16x32_bf16 a[200:203],a[72:75],a[56:59],a[200:203]
ds_read_b128 a[108:111],v0 offset:6144
v_mfma_f32_16x16x32_bf16 a[232:235],a[24:27],a[12:15],a[232:235]
buffer_load_dwordx4 v11,s[88:91],s83 offen lds
)"
R"(v_mfma_f32_16x16x32_bf16 a[232:235],a[72:75],a[60:63],a[232:235]
s_add_u32 m0,s56,2048
s_add_u32 s82,s67,0
ds_read_b64_tr_b16 a[160:161],v2 offset:8192
v_mfma_f32_16x16x32_bf16 a[236:239],a[28:31],a[12:15],a[236:239]
ds_read_b64_tr_b16 a[162:163],v2 offset:9216
v_mfma_f32_16x16x32_bf16 a[236:239],a[76:79],a[60:63],a[236:239]
ds_read_b64_tr_b16 a[164:165],v3 offset:8192
v_mfma_f32_16x16x32_bf16 a[204:207],a[28:31],a[8:11],a[204:207]
buffer_load_dwordx4 v12,s[88:91],s82 offen lds
ds_read_b64_tr_b16 a[166:167],v3 offset:9216
v_mfma_f32_16x16x32_bf16 a[204:207],a[76:79],a[56:59],a[204:207]
s_add_u32 m0,s56,3072
s_add_u32 s83,s67,16384
ds_read_b128 a[144:147],v1 offset:0
v_mfma_f32_16x16x32_bf16 v[108:111],a[28:31],a[4:7],v[108:111]
ds_read_b64_tr_b16 a[168:169],v4 offset:8192
v_mfma_f32_16x16x32_bf16 v[108:111],a[76:79],a[52:55],v[108:111]
ds_read_b64_tr_b16 a[170:171],v4 offset:9216
v_mfma_f32_16x16x32_bf16 v[76:79],a[28:31],a[0:3],v[76:79]
buffer_load_dwordx4 v13,s[88:91],s83 offen lds
ds_read_b64_tr_b16 a[172:173],v5 offset:8192
v_mfma_f32_16x16x32_bf16 v[76:79],a[76:79],a[48:51],v[76:79]
s_add_u32 m0,s56,4096
)"
R"(s_add_u32 s82,s84,0
ds_read_b64_tr_b16 a[174:175],v5 offset:9216
v_mfma_f32_16x16x32_bf16 v[80:83],a[32:35],a[0:3],v[80:83]
ds_read_b128 a[148:151],v1 offset:2048
v_mfma_f32_16x16x32_bf16 v[80:83],a[80:83],a[48:51],v[80:83]
buffer_load_dwordx4 v10,s[88:91],s82 offen lds
v_mfma_f32_16x16x32_bf16 v[112:115],a[32:35],a[4:7],v[112:115]
s_add_u32 m0,s56,5120
s_add_u32 s83,s84,16384
ds_read_b64_tr_b16 a[176:177],v6 offset:8192
v_mfma_f32_16x16x32_bf16 v[112:115],a[80:83],a[52:55],v[112:115]
ds_read_b64_tr_b16 a[178:179],v6 offset:9216
v_mfma_f32_16x16x32_bf16 a[208:211],a[32:35],a[8:11],a[208:211]
ds_read_b64_tr_b16 a[180:181],v7 offset:8192
v_mfma_f32_16x16x32_bf16 a[208:211],a[80:83],a[56:59],a[208:211]
buffer_load_dwordx4 v11,s[88:91],s83 offen lds
ds_read_b64_tr_b16 a[182:183],v7 offset:9216
v_mfma_f32_16x16x32_bf16 a[240:243],a[32:35],a[12:15],a[240:243]
s_add_u32 m0,s56,6144
s_add_u32 s82,s84,0
ds_read_b128 a[152:155],v1 offset:4096
v_mfma_f32_16x16x32_bf16 a[240:243],a[80:83],a[60:63],a[240:243]
ds_read_b64_tr_b16 a[184:185],v8 offset:8192
v_mfma_f32_16x16x32_bf16 a[244:247],a[36:39],a[12:15],a[244:247]
)"
R"(ds_read_b64_tr_b16 a[186:187],v8 offset:9216
v_mfma_f32_16x16x32_bf16 a[244:247],a[84:87],a[60:63],a[244:247]
buffer_load_dwordx4 v12,s[88:91],s82 offen lds
ds_read_b64_tr_b16 a[188:189],v9 offset:8192
v_mfma_f32_16x16x32_bf16 a[212:215],a[36:39],a[8:11],a[212:215]
s_add_u32 m0,s56,7168
s_add_u32 s83,s84,16384
ds_read_b64_tr_b16 a[190:191],v9 offset:9216
v_mfma_f32_16x16x32_bf16 a[212:215],a[84:87],a[56:59],a[212:215]
ds_read_b128 a[156:159],v1 offset:6144
v_mfma_f32_16x16x32_bf16 v[116:119],a[36:39],a[4:7],v[116:119]
v_mfma_f32_16x16x32_bf16 v[116:119],a[84:87],a[52:55],v[116:119]
buffer_load_dwordx4 v13,s[88:91],s83 offen lds
v_mfma_f32_16x16x32_bf16 v[84:87],a[36:39],a[0:3],v[84:87]
s_add_u32 s67,s67,128
s_and_b32 s67,s67,2047
s_mov_b32 s99,4096
v_mfma_f32_16x16x32_bf16 v[84:87],a[84:87],a[48:51],v[84:87]
v_mfma_f32_16x16x32_bf16 v[88:91],a[40:43],a[0:3],v[88:91]
v_mfma_f32_16x16x32_bf16 v[88:91],a[88:91],a[48:51],v[88:91]
buffer_store_dwordx4 v[156:159],v18,s[48:51],s99 offen offset:3072 sc1
v_add_u32 v0,-32768,v0
v_mfma_f32_16x16x32_bf16 v[120:123],a[40:43],a[4:7],v[120:123]
s_mov_b32 s99,4096
)"
R"(v_add_u32 v1,-32768,v1
v_add_u32 v2,-49152,v2
v_mfma_f32_16x16x32_bf16 v[120:123],a[88:91],a[52:55],v[120:123]
v_add_u32 v3,-49152,v3
v_add_u32 v4,-49152,v4
v_mfma_f32_16x16x32_bf16 a[216:219],a[40:43],a[8:11],a[216:219]
v_add_u32 v5,-49152,v5
v_mfma_f32_16x16x32_bf16 a[216:219],a[88:91],a[56:59],a[216:219]
buffer_store_dwordx4 v[152:155],v18,s[48:51],s99 offen offset:2048 sc1
v_add_u32 v6,-49152,v6
v_mfma_f32_16x16x32_bf16 a[248:251],a[40:43],a[12:15],a[248:251]
s_mov_b32 s99,4096
v_add_u32 v7,-49152,v7
v_add_u32 v8,-49152,v8
v_mfma_f32_16x16x32_bf16 a[248:251],a[88:91],a[60:63],a[248:251]
v_add_u32 v9,-49152,v9
v_mfma_f32_16x16x32_bf16 a[252:255],a[44:47],a[12:15],a[252:255]
v_mfma_f32_16x16x32_bf16 a[252:255],a[92:95],a[60:63],a[252:255]
buffer_store_dwordx4 v[148:151],v18,s[48:51],s99 offen offset:1024 sc1
v_mfma_f32_16x16x32_bf16 a[220:223],a[44:47],a[8:11],a[220:223]
v_mfma_f32_16x16x32_bf16 a[220:223],a[92:95],a[56:59],a[220:223]
v_mfma_f32_16x16x32_bf16 v[124:127],a[44:47],a[4:7],v[124:127]
v_mfma_f32_16x16x32_bf16 v[124:127],a[92:95],a[52:55],v[124:127]
v_mfma_f32_16x16x32_bf16 v[92:95],a[44:47],a[0:3],v[92:95]
)"
R"(s_waitcnt lgkmcnt(0)
v_mfma_f32_16x16x32_bf16 v[92:95],a[92:95],a[48:51],v[92:95]
v_mfma_f32_16x16x32_bf16 v[64:67],a[112:115],a[96:99],v[64:67]
s_waitcnt vmcnt(18)
s_barrier
v_mfma_f32_16x16x32_bf16 v[64:67],a[160:163],a[144:147],v[64:67]
s_add_u32 m0,s57,147456
s_add_u32 s82,s74,0
ds_read_b64_tr_b16 a[16:17],v2 offset:0
v_mfma_f32_16x16x32_bf16 v[96:99],a[112:115],a[100:103],v[96:99]
ds_read_b64_tr_b16 a[18:19],v2 offset:1024
v_mfma_f32_16x16x32_bf16 v[96:99],a[160:163],a[148:151],v[96:99]
buffer_load_dwordx4 v16,s[92:95],s82 offen lds
ds_read_b64_tr_b16 a[20:21],v3 offset:0
v_mfma_f32_16x16x32_bf16 a[192:195],a[112:115],a[104:107],a[192:195]
s_add_u32 m0,s57,148480
s_add_u32 s83,s74,32768
ds_read_b64_tr_b16 a[22:23],v3 offset:1024
v_mfma_f32_16x16x32_bf16 a[192:195],a[160:163],a[152:155],a[192:195]
ds_read_b128 a[0:3],v0 offset:0
v_mfma_f32_16x16x32_bf16 a[224:227],a[112:115],a[108:111],a[224:227]
buffer_load_dwordx4 v16,s[92:95],s83 offen lds
ds_read_b64_tr_b16 a[24:25],v4 offset:0
v_mfma_f32_16x16x32_bf16 a[224:227],a[160:163],a[156:159],a[224:227]
)"
R"(s_add_u32 m0,s57,149504
s_add_u32 s82,s74,65536
ds_read_b64_tr_b16 a[26:27],v4 offset:1024
v_mfma_f32_16x16x32_bf16 a[228:231],a[116:119],a[108:111],a[228:231]
ds_read_b64_tr_b16 a[28:29],v5 offset:0
v_mfma_f32_16x16x32_bf16 a[228:231],a[164:167],a[156:159],a[228:231]
ds_read_b64_tr_b16 a[30:31],v5 offset:1024
v_mfma_f32_16x16x32_bf16 a[196:199],a[116:119],a[104:107],a[196:199]
buffer_load_dwordx4 v17,s[92:95],s82 offen lds
ds_read_b128 a[4:7],v0 offset:2048
v_mfma_f32_16x16x32_bf16 a[196:199],a[164:167],a[152:155],a[196:199]
s_add_u32 m0,s57,150528
s_add_u32 s83,s74,98304
v_mfma_f32_16x16x32_bf16 v[100:103],a[116:119],a[100:103],v[100:103]
ds_read_b64_tr_b16 a[32:33],v6 offset:0
v_mfma_f32_16x16x32_bf16 v[100:103],a[164:167],a[148:151],v[100:103]
ds_read_b64_tr_b16 a[34:35],v6 offset:1024
v_mfma_f32_16x16x32_bf16 v[68:71],a[116:119],a[96:99],v[68:71]
buffer_load_dwordx4 v17,s[92:95],s83 offen lds
ds_read_b64_tr_b16 a[36:37],v7 offset:0
v_mfma_f32_16x16x32_bf16 v[68:71],a[164:167],a[144:147],v[68:71]
s_add_u32 s74,s74,524288
s_and_b32 s74,s74,0x7fffff
ds_read_b64_tr_b16 a[38:39],v7 offset:1024
)"
R"(v_mfma_f32_16x16x32_bf16 v[72:75],a[120:123],a[96:99],v[72:75]
s_add_u32 s84,s67,s68
s_add_u32 m0,s56,65536
ds_read_b128 a[8:11],v0 offset:4096
v_mfma_f32_16x16x32_bf16 v[72:75],a[168:171],a[144:147],v[72:75]
s_add_u32 s82,s67,0
ds_read_b64_tr_b16 a[40:41],v8 offset:0
v_mfma_f32_16x16x32_bf16 v[104:107],a[120:123],a[100:103],v[104:107]
buffer_load_dwordx4 v10,s[88:91],s82 offen lds
ds_read_b64_tr_b16 a[42:43],v8 offset:1024
v_mfma_f32_16x16x32_bf16 v[104:107],a[168:171],a[148:151],v[104:107]
s_add_u32 m0,s56,66560
s_add_u32 s83,s67,16384
ds_read_b64_tr_b16 a[44:45],v9 offset:0
v_mfma_f32_16x16x32_bf16 a[200:203],a[120:123],a[104:107],a[200:203]
ds_read_b64_tr_b16 a[46:47],v9 offset:1024
v_mfma_f32_16x16x32_bf16 a[200:203],a[168:171],a[152:155],a[200:203]
ds_read_b128 a[12:15],v0 offset:6144
v_mfma_f32_16x16x32_bf16 a[232:235],a[120:123],a[108:111],a[232:235]
buffer_load_dwordx4 v11,s[88:91],s83 offen lds
v_mfma_f32_16x16x32_bf16 a[232:235],a[168:171],a[156:159],a[232:235]
s_add_u32 m0,s56,67584
s_add_u32 s82,s67,0
ds_read_b64_tr_b16 a[64:65],v2 offset:8192
)"
R"(v_mfma_f32_16x16x32_bf16 a[236:239],a[124:127],a[108:111],a[236:239]
ds_read_b64_tr_b16 a[66:67],v2 offset:9216
v_mfma_f32_16x16x32_bf16 a[236:239],a[172:175],a[156:159],a[236:239]
ds_read_b64_tr_b16 a[68:69],v3 offset:8192
v_mfma_f32_16x16x32_bf16 a[204:207],a[124:127],a[104:107],a[204:207]
buffer_load_dwordx4 v12,s[88:91],s82 offen lds
ds_read_b64_tr_b16 a[70:71],v3 offset:9216
v_mfma_f32_16x16x32_bf16 a[204:207],a[172:175],a[152:155],a[204:207]
s_add_u32 m0,s56,68608
s_add_u32 s83,s67,16384
ds_read_b128 a[48:51],v1 offset:0
v_mfma_f32_16x16x32_bf16 v[108:111],a[124:127],a[100:103],v[108:111]
ds_read_b64_tr_b16 a[72:73],v4 offset:8192
v_mfma_f32_16x16x32_bf16 v[108:111],a[172:175],a[148:151],v[108:111]
ds_read_b64_tr_b16 a[74:75],v4 offset:9216
v_mfma_f32_16x16x32_bf16 v[76:79],a[124:127],a[96:99],v[76:79]
buffer_load_dwordx4 v13,s[88:91],s83 offen lds
ds_read_b64_tr_b16 a[76:77],v5 offset:8192
v_mfma_f32_16x16x32_bf16 v[76:79],a[172:175],a[144:147],v[76:79]
s_add_u32 m0,s56,69632
s_add_u32 s82,s84,0
ds_read_b64_tr_b16 a[78:79],v5 offset:9216
v_mfma_f32_16x16x32_bf16 v[80:83],a[128:131],a[96:99],v[80:83]
ds_read_b128 a[52:55],v1 offset:2048
)"
R"(v_mfma_f32_16x16x32_bf16 v[80:83],a[176:179],a[144:147],v[80:83]
buffer_load_dwordx4 v10,s[88:91],s82 offen lds
v_mfma_f32_16x16x32_bf16 v[112:115],a[128:131],a[100:103],v[112:115]
s_add_u32 m0,s56,70656
s_add_u32 s83,s84,16384
ds_read_b64_tr_b16 a[80:81],v6 offset:8192
v_mfma_f32_16x16x32_bf16 v[112:115],a[176:179],a[148:151],v[112:115]
ds_read_b64_tr_b16 a[82:83],v6 offset:9216
v_mfma_f32_16x16x32_bf16 a[208:211],a[128:131],a[104:107],a[208:211]
ds_read_b64_tr_b16 a[84:85],v7 offset:8192
v_mfma_f32_16x16x32_bf16 a[208:211],a[176:179],a[152:155],a[208:211]
buffer_load_dwordx4 v11,s[88:91],s83 offen lds
ds_read_b64_tr_b16 a[86:87],v7 offset:9216
v_mfma_f32_16x16x32_bf16 a[240:243],a[128:131],a[108:111],a[240:243]
s_add_u32 m0,s56,71680
s_add_u32 s82,s84,0
ds_read_b128 a[56:59],v1 offset:4096
v_mfma_f32_16x16x32_bf16 a[240:243],a[176:179],a[156:159],a[240:243]
ds_read_b64_tr_b16 a[88:89],v8 offset:8192
v_mfma_f32_16x16x32_bf16 a[244:247],a[132:135],a[108:111],a[244:247]
ds_read_b64_tr_b16 a[90:91],v8 offset:9216
v_mfma_f32_16x16x32_bf16 a[244:247],a[180:183],a[156:159],a[244:247]
buffer_load_dwordx4 v12,s[88:91],s82 offen lds
ds_read_b64_tr_b16 a[92:93],v9 offset:8192
)"
R"(v_mfma_f32_16x16x32_bf16 a[212:215],a[132:135],a[104:107],a[212:215]
s_add_u32 m0,s56,72704
s_add_u32 s83,s84,16384
ds_read_b64_tr_b16 a[94:95],v9 offset:9216
v_mfma_f32_16x16x32_bf16 a[212:215],a[180:183],a[152:155],a[212:215]
ds_read_b128 a[60:63],v1 offset:6144
v_mfma_f32_16x16x32_bf16 v[116:119],a[132:135],a[100:103],v[116:119]
v_mfma_f32_16x16x32_bf16 v[116:119],a[180:183],a[148:151],v[116:119]
buffer_load_dwordx4 v13,s[88:91],s83 offen lds
v_mfma_f32_16x16x32_bf16 v[84:87],a[132:135],a[96:99],v[84:87]
s_add_u32 s67,s67,128
s_and_b32 s67,s67,2047
s_mov_b32 s99,4096
v_mfma_f32_16x16x32_bf16 v[84:87],a[180:183],a[144:147],v[84:87]
v_mfma_f32_16x16x32_bf16 v[88:91],a[136:139],a[96:99],v[88:91]
v_mfma_f32_16x16x32_bf16 v[88:91],a[184:187],a[144:147],v[88:91]
buffer_store_dwordx4 v[144:147],v18,s[48:51],s99 offen offset:0 sc1
v_add_u32 v0,-32768,v0
v_mfma_f32_16x16x32_bf16 v[120:123],a[136:139],a[100:103],v[120:123]
s_mov_b32 s99,0
v_add_u32 v1,-32768,v1
v_add_u32 v2,32768,v2
v_mfma_f32_16x16x32_bf16 v[120:123],a[184:187],a[148:151],v[120:123]
v_add_u32 v3,32768,v3
)"
R"(v_add_u32 v4,32768,v4
v_mfma_f32_16x16x32_bf16 a[216:219],a[136:139],a[104:107],a[216:219]
v_add_u32 v5,32768,v5
v_mfma_f32_16x16x32_bf16 a[216:219],a[184:187],a[152:155],a[216:219]
buffer_store_dwordx4 v[140:143],v18,s[48:51],s99 offen offset:3072 sc1
v_add_u32 v6,32768,v6
v_mfma_f32_16x16x32_bf16 a[248:251],a[136:139],a[108:111],a[248:251]
s_mov_b32 s99,0
v_add_u32 v7,32768,v7
v_add_u32 v8,32768,v8
v_mfma_f32_16x16x32_bf16 a[248:251],a[184:187],a[156:159],a[248:251]
v_add_u32 v9,32768,v9
v_mfma_f32_16x16x32_bf16 a[252:255],a[140:143],a[108:111],a[252:255]
v_mfma_f32_16x16x32_bf16 a[252:255],a[188:191],a[156:159],a[252:255]
buffer_store_dwordx4 v[136:139],v18,s[48:51],s99 offen offset:2048 sc1
v_mfma_f32_16x16x32_bf16 a[220:223],a[140:143],a[104:107],a[220:223]
v_mfma_f32_16x16x32_bf16 a[220:223],a[188:191],a[152:155],a[220:223]
v_mfma_f32_16x16x32_bf16 v[124:127],a[140:143],a[100:103],v[124:127]
v_mfma_f32_16x16x32_bf16 v[124:127],a[188:191],a[148:151],v[124:127]
v_mfma_f32_16x16x32_bf16 v[92:95],a[140:143],a[96:99],v[92:95]
s_waitcnt lgkmcnt(0)
v_mfma_f32_16x16x32_bf16 v[92:95],a[188:191],a[144:147],v[92:95]
v_mfma_f32_16x16x32_bf16 v[64:67],a[16:19],a[0:3],v[64:67]
s_waitcnt vmcnt(18)
)"
R"(s_barrier
v_mfma_f32_16x16x32_bf16 v[64:67],a[64:67],a[48:51],v[64:67]
s_add_u32 m0,s57,98304
s_add_u32 s82,s74,0
ds_read_b64_tr_b16 a[112:113],v2 offset:0
v_mfma_f32_16x16x32_bf16 v[96:99],a[16:19],a[4:7],v[96:99]
ds_read_b64_tr_b16 a[114:115],v2 offset:1024
v_mfma_f32_16x16x32_bf16 v[96:99],a[64:67],a[52:55],v[96:99]
buffer_load_dwordx4 v16,s[92:95],s82 offen lds
ds_read_b64_tr_b16 a[116:117],v3 offset:0
v_mfma_f32_16x16x32_bf16 a[192:195],a[16:19],a[8:11],a[192:195]
s_add_u32 m0,s57,99328
s_add_u32 s83,s74,32768
ds_read_b64_tr_b16 a[118:119],v3 offset:1024
v_mfma_f32_16x16x32_bf16 a[192:195],a[64:67],a[56:59],a[192:195]
ds_read_b128 a[96:99],v0 offset:0
v_mfma_f32_16x16x32_bf16 a[224:227],a[16:19],a[12:15],a[224:227]
ds_read_b64_tr_b16 a[120:121],v4 offset:0
v_mfma_f32_16x16x32_bf16 a[224:227],a[64:67],a[60:63],a[224:227]
buffer_load_dwordx4 v16,s[92:95],s83 offen lds
ds_read_b64_tr_b16 a[122:123],v4 offset:1024
v_mfma_f32_16x16x32_bf16 a[228:231],a[20:23],a[12:15],a[228:231]
s_add_u32 m0,s57,100352
s_add_u32 s82,s74,65536
)"
R"(ds_read_b64_tr_b16 a[124:125],v5 offset:0
v_mfma_f32_16x16x32_bf16 a[228:231],a[68:71],a[60:63],a[228:231]
ds_read_b64_tr_b16 a[126:127],v5 offset:1024
v_mfma_f32_16x16x32_bf16 a[196:199],a[20:23],a[8:11],a[196:199]
ds_read_b128 a[100:103],v0 offset:2048
v_mfma_f32_16x16x32_bf16 a[196:199],a[68:71],a[56:59],a[196:199]
buffer_load_dwordx4 v17,s[92:95],s82 offen lds
v_mfma_f32_16x16x32_bf16 v[100:103],a[20:23],a[4:7],v[100:103]
s_add_u32 m0,s57,101376
s_add_u32 s83,s74,98304
ds_read_b64_tr_b16 a[128:129],v6 offset:0
v_mfma_f32_16x16x32_bf16 v[100:103],a[68:71],a[52:55],v[100:103]
ds_read_b64_tr_b16 a[130:131],v6 offset:1024
v_mfma_f32_16x16x32_bf16 v[68:71],a[20:23],a[0:3],v[68:71]
ds_read_b64_tr_b16 a[132:133],v7 offset:0
v_mfma_f32_16x16x32_bf16 v[68:71],a[68:71],a[48:51],v[68:71]
buffer_load_dwordx4 v17,s[92:95],s83 offen lds
ds_read_b64_tr_b16 a[134:135],v7 offset:1024
v_mfma_f32_16x16x32_bf16 v[72:75],a[24:27],a[0:3],v[72:75]
s_add_u32 s74,s74,524288
s_and_b32 s74,s74,0x7fffff
ds_read_b128 a[104:107],v0 offset:4096
v_mfma_f32_16x16x32_bf16 v[72:75],a[72:75],a[48:51],v[72:75]
s_add_u32 s84,s67,s68
)"
R"(s_add_u32 m0,s56,32768
ds_read_b64_tr_b16 a[136:137],v8 offset:0
v_mfma_f32_16x16x32_bf16 v[104:107],a[24:27],a[4:7],v[104:107]
s_add_u32 s82,s67,0
ds_read_b64_tr_b16 a[138:139],v8 offset:1024
v_mfma_f32_16x16x32_bf16 v[104:107],a[72:75],a[52:55],v[104:107]
buffer_load_dwordx4 v10,s[88:91],s82 offen lds
ds_read_b64_tr_b16 a[140:141],v9 offset:0
v_mfma_f32_16x16x32_bf16 a[200:203],a[24:27],a[8:11],a[200:203]
s_add_u32 m0,s56,33792
s_add_u32 s83,s67,16384
ds_read_b64_tr_b16 a[142:143],v9 offset:1024
s_branch gm_KB_sk11
.p2align 12
gm_KB_pg11:
s_setpc_b64 s[100:101]
gm_KB_sk11:
v_mfma_f32_16x16x32_bf16 a[200:203],a[72:75],a[56:59],a[200:203]
ds_read_b128 a[108:111],v0 offset:6144
v_mfma_f32_16x16x32_bf16 a[232:235],a[24:27],a[12:15],a[232:235]
v_mfma_f32_16x16x32_bf16 a[232:235],a[72:75],a[60:63],a[232:235]
buffer_load_dwordx4 v11,s[88:91],s83 offen lds
ds_read_b64_tr_b16 a[160:161],v2 offset:8192
v_mfma_f32_16x16x32_bf16 a[236:239],a[28:31],a[12:15],a[236:239]
)"
R"(s_add_u32 m0,s56,34816
s_add_u32 s82,s67,0
ds_read_b64_tr_b16 a[162:163],v2 offset:9216
v_mfma_f32_16x16x32_bf16 a[236:239],a[76:79],a[60:63],a[236:239]
ds_read_b64_tr_b16 a[164:165],v3 offset:8192
v_mfma_f32_16x16x32_bf16 a[204:207],a[28:31],a[8:11],a[204:207]
ds_read_b64_tr_b16 a[166:167],v3 offset:9216
v_mfma_f32_16x16x32_bf16 a[204:207],a[76:79],a[56:59],a[204:207]
buffer_load_dwordx4 v12,s[88:91],s82 offen lds
ds_read_b128 a[144:147],v1 offset:0
v_mfma_f32_16x16x32_bf16 v[108:111],a[28:31],a[4:7],v[108:111]
ds_read_b64_tr_b16 a[168:169],v4 offset:8192
v_mfma_f32_16x16x32_bf16 v[108:111],a[76:79],a[52:55],v[108:111]
s_add_u32 m0,s56,35840
s_add_u32 s83,s67,16384
ds_read_b64_tr_b16 a[170:171],v4 offset:9216
v_mfma_f32_16x16x32_bf16 v[76:79],a[28:31],a[0:3],v[76:79]
ds_read_b64_tr_b16 a[172:173],v5 offset:8192
v_mfma_f32_16x16x32_bf16 v[76:79],a[76:79],a[48:51],v[76:79]
ds_read_b64_tr_b16 a[174:175],v5 offset:9216
v_mfma_f32_16x16x32_bf16 v[80:83],a[32:35],a[0:3],v[80:83]
buffer_load_dwordx4 v13,s[88:91],s83 offen lds
ds_read_b128 a[148:151],v1 offset:2048
v_mfma_f32_16x16x32_bf16 v[80:83],a[80:83],a[48:51],v[80:83]
)"
R"(s_add_u32 m0,s56,36864
s_add_u32 s82,s84,0
v_mfma_f32_16x16x32_bf16 v[112:115],a[32:35],a[4:7],v[112:115]
ds_read_b64_tr_b16 a[176:177],v6 offset:8192
v_mfma_f32_16x16x32_bf16 v[112:115],a[80:83],a[52:55],v[112:115]
ds_read_b64_tr_b16 a[178:179],v6 offset:9216
v_mfma_f32_16x16x32_bf16 a[208:211],a[32:35],a[8:11],a[208:211]
buffer_load_dwordx4 v10,s[88:91],s82 offen lds
ds_read_b64_tr_b16 a[180:181],v7 offset:8192
v_mfma_f32_16x16x32_bf16 a[208:211],a[80:83],a[56:59],a[208:211]
s_add_u32 m0,s56,37888
s_add_u32 s83,s84,16384
ds_read_b64_tr_b16 a[182:183],v7 offset:9216
v_mfma_f32_16x16x32_bf16 a[240:243],a[32:35],a[12:15],a[240:243]
ds_read_b128 a[152:155],v1 offset:4096
v_mfma_f32_16x16x32_bf16 a[240:243],a[80:83],a[60:63],a[240:243]
ds_read_b64_tr_b16 a[184:185],v8 offset:8192
v_mfma_f32_16x16x32_bf16 a[244:247],a[36:39],a[12:15],a[244:247]
buffer_load_dwordx4 v11,s[88:91],s83 offen lds
ds_read_b64_tr_b16 a[186:187],v8 offset:9216
v_mfma_f32_16x16x32_bf16 a[244:247],a[84:87],a[60:63],a[244:247]
s_add_u32 m0,s56,38912
s_add_u32 s82,s84,0
ds_read_b64_tr_b16 a[188:189],v9 offset:8192
)"
R"(v_mfma_f32_16x16x32_bf16 a[212:215],a[36:39],a[8:11],a[212:215]
ds_read_b64_tr_b16 a[190:191],v9 offset:9216
v_mfma_f32_16x16x32_bf16 a[212:215],a[84:87],a[56:59],a[212:215]
ds_read_b128 a[156:159],v1 offset:6144
v_mfma_f32_16x16x32_bf16 v[116:119],a[36:39],a[4:7],v[116:119]
buffer_load_dwordx4 v12,s[88:91],s82 offen lds
v_mfma_f32_16x16x32_bf16 v[116:119],a[84:87],a[52:55],v[116:119]
s_add_u32 m0,s56,39936
s_add_u32 s83,s84,16384
v_mfma_f32_16x16x32_bf16 v[84:87],a[36:39],a[0:3],v[84:87]
v_mfma_f32_16x16x32_bf16 v[84:87],a[84:87],a[48:51],v[84:87]
v_mfma_f32_16x16x32_bf16 v[88:91],a[40:43],a[0:3],v[88:91]
buffer_load_dwordx4 v13,s[88:91],s83 offen lds
v_mfma_f32_16x16x32_bf16 v[88:91],a[88:91],a[48:51],v[88:91]
s_add_u32 s67,s67,128
s_and_b32 s67,s67,2047
v_add_u32 v0,65536,v0
v_mfma_f32_16x16x32_bf16 v[120:123],a[40:43],a[4:7],v[120:123]
s_mov_b32 s99,0
v_add_u32 v1,65536,v1
v_add_u32 v2,16384,v2
v_mfma_f32_16x16x32_bf16 v[120:123],a[88:91],a[52:55],v[120:123]
v_add_u32 v3,16384,v3
v_add_u32 v4,16384,v4
)"
R"(v_mfma_f32_16x16x32_bf16 a[216:219],a[40:43],a[8:11],a[216:219]
buffer_store_dwordx4 v[132:135],v18,s[48:51],s99 offen offset:1024 sc1
v_add_u32 v5,16384,v5
v_mfma_f32_16x16x32_bf16 a[216:219],a[88:91],a[56:59],a[216:219]
v_add_u32 v6,16384,v6
v_add_u32 v7,16384,v7
v_mfma_f32_16x16x32_bf16 a[248:251],a[40:43],a[12:15],a[248:251]
s_mov_b32 s99,0
v_add_u32 v8,16384,v8
v_mfma_f32_16x16x32_bf16 a[248:251],a[88:91],a[60:63],a[248:251]
v_add_u32 v9,16384,v9
v_mfma_f32_16x16x32_bf16 a[252:255],a[44:47],a[12:15],a[252:255]
v_mfma_f32_16x16x32_bf16 a[252:255],a[92:95],a[60:63],a[252:255]
buffer_store_dwordx4 v[128:131],v18,s[48:51],s99 offen offset:0 sc1
v_mfma_f32_16x16x32_bf16 a[220:223],a[44:47],a[8:11],a[220:223]
v_mfma_f32_16x16x32_bf16 a[220:223],a[92:95],a[56:59],a[220:223]
v_mfma_f32_16x16x32_bf16 v[124:127],a[44:47],a[4:7],v[124:127]
v_mfma_f32_16x16x32_bf16 v[124:127],a[92:95],a[52:55],v[124:127]
v_mfma_f32_16x16x32_bf16 v[92:95],a[44:47],a[0:3],v[92:95]
s_waitcnt lgkmcnt(0)
v_mfma_f32_16x16x32_bf16 v[92:95],a[92:95],a[48:51],v[92:95]
v_mfma_f32_16x16x32_bf16 v[64:67],a[112:115],a[96:99],v[64:67]
s_waitcnt vmcnt(17)
s_barrier
)"
R"(v_mfma_f32_16x16x32_bf16 v[64:67],a[160:163],a[144:147],v[64:67]
s_add_u32 m0,s57,131072
s_add_u32 s82,s74,0
ds_read_b64_tr_b16 a[16:17],v2 offset:0
v_mfma_f32_16x16x32_bf16 v[96:99],a[112:115],a[100:103],v[96:99]
ds_read_b64_tr_b16 a[18:19],v2 offset:1024
v_mfma_f32_16x16x32_bf16 v[96:99],a[160:163],a[148:151],v[96:99]
buffer_load_dwordx4 v16,s[92:95],s82 offen lds
ds_read_b64_tr_b16 a[20:21],v3 offset:0
v_mfma_f32_16x16x32_bf16 a[192:195],a[112:115],a[104:107],a[192:195]
s_lshr_b32 s12,%1,3
s_and_b32 s55,s12,7
ds_read_b64_tr_b16 a[22:23],v3 offset:1024
v_mfma_f32_16x16x32_bf16 a[192:195],a[160:163],a[152:155],a[192:195]
s_add_u32 m0,s57,132096
s_add_u32 s83,s74,32768
ds_read_b128 a[0:3],v0 offset:0
v_mfma_f32_16x16x32_bf16 a[224:227],a[112:115],a[108:111],a[224:227]
ds_read_b64_tr_b16 a[24:25],v4 offset:0
v_mfma_f32_16x16x32_bf16 a[224:227],a[160:163],a[156:159],a[224:227]
buffer_load_dwordx4 v16,s[92:95],s83 offen lds
ds_read_b64_tr_b16 a[26:27],v4 offset:1024
v_mfma_f32_16x16x32_bf16 a[228:231],a[116:119],a[108:111],a[228:231]
ds_read_b64_tr_b16 a[28:29],v5 offset:0
)"
R"(v_mfma_f32_16x16x32_bf16 a[228:231],a[164:167],a[156:159],a[228:231]
s_lshr_b32 s12,s12,3
s_lshl_b32 s53,s73,2
ds_read_b64_tr_b16 a[30:31],v5 offset:1024
v_mfma_f32_16x16x32_bf16 a[196:199],a[116:119],a[104:107],a[196:199]
s_add_u32 m0,s57,133120
s_add_u32 s82,s74,65536
ds_read_b128 a[4:7],v0 offset:2048
v_mfma_f32_16x16x32_bf16 a[196:199],a[164:167],a[152:155],a[196:199]
v_mfma_f32_16x16x32_bf16 v[100:103],a[116:119],a[100:103],v[100:103]
buffer_load_dwordx4 v17,s[92:95],s82 offen lds
ds_read_b64_tr_b16 a[32:33],v6 offset:0
v_mfma_f32_16x16x32_bf16 v[100:103],a[164:167],a[148:151],v[100:103]
ds_read_b64_tr_b16 a[34:35],v6 offset:1024
v_mfma_f32_16x16x32_bf16 v[68:71],a[116:119],a[96:99],v[68:71]
s_add_u32 s53,s53,s12
s_lshl_b32 s65,s70,13
ds_read_b64_tr_b16 a[36:37],v7 offset:0
v_mfma_f32_16x16x32_bf16 v[68:71],a[164:167],a[144:147],v[68:71]
s_add_u32 m0,s57,134144
s_add_u32 s83,s74,98304
ds_read_b64_tr_b16 a[38:39],v7 offset:1024
v_mfma_f32_16x16x32_bf16 v[72:75],a[120:123],a[96:99],v[72:75]
ds_read_b128 a[8:11],v0 offset:4096
)"
R"(v_mfma_f32_16x16x32_bf16 v[72:75],a[168:171],a[144:147],v[72:75]
buffer_load_dwordx4 v17,s[92:95],s83 offen lds
ds_read_b64_tr_b16 a[40:41],v8 offset:0
v_mfma_f32_16x16x32_bf16 v[104:107],a[120:123],a[100:103],v[104:107]
ds_read_b64_tr_b16 a[42:43],v8 offset:1024
v_mfma_f32_16x16x32_bf16 v[104:107],a[168:171],a[148:151],v[104:107]
s_add_u32 s63,s65,0x8000
s_add_u32 s64,s65,0x10000
ds_read_b64_tr_b16 a[44:45],v9 offset:0
v_mfma_f32_16x16x32_bf16 a[200:203],a[120:123],a[104:107],a[200:203]
s_add_u32 s74,s74,524288
s_and_b32 s74,s74,0x7fffff
ds_read_b64_tr_b16 a[46:47],v9 offset:1024
v_mfma_f32_16x16x32_bf16 a[200:203],a[168:171],a[152:155],a[200:203]
s_add_u32 s84,s67,s68
s_add_u32 m0,s56,0
ds_read_b128 a[12:15],v0 offset:6144
v_mfma_f32_16x16x32_bf16 a[232:235],a[120:123],a[108:111],a[232:235]
s_add_u32 s82,s67,0
buffer_load_dwordx4 v10,s[88:91],s82 offen lds
v_mfma_f32_16x16x32_bf16 a[232:235],a[168:171],a[156:159],a[232:235]
ds_read_b64_tr_b16 a[64:65],v2 offset:8192
v_mfma_f32_16x16x32_bf16 a[236:239],a[124:127],a[108:111],a[236:239]
s_add_u32 s66,s65,98304
)"
R"(s_mov_b32 s58,0
ds_read_b64_tr_b16 a[66:67],v2 offset:9216
v_mfma_f32_16x16x32_bf16 a[236:239],a[172:175],a[156:159],a[236:239]
s_add_u32 m0,s56,1024
s_add_u32 s83,s67,16384
ds_read_b64_tr_b16 a[68:69],v3 offset:8192
v_mfma_f32_16x16x32_bf16 a[204:207],a[124:127],a[104:107],a[204:207]
ds_read_b64_tr_b16 a[70:71],v3 offset:9216
v_mfma_f32_16x16x32_bf16 a[204:207],a[172:175],a[152:155],a[204:207]
buffer_load_dwordx4 v11,s[88:91],s83 offen lds
ds_read_b128 a[48:51],v1 offset:0
v_mfma_f32_16x16x32_bf16 v[108:111],a[124:127],a[100:103],v[108:111]
ds_read_b64_tr_b16 a[72:73],v4 offset:8192
v_mfma_f32_16x16x32_bf16 v[108:111],a[172:175],a[148:151],v[108:111]
s_mov_b32 s60,0
s_add_u32 s61,s55,1
ds_read_b64_tr_b16 a[74:75],v4 offset:9216
v_mfma_f32_16x16x32_bf16 v[76:79],a[124:127],a[96:99],v[76:79]
s_add_u32 m0,s56,2048
s_add_u32 s82,s67,0
ds_read_b64_tr_b16 a[76:77],v5 offset:8192
v_mfma_f32_16x16x32_bf16 v[76:79],a[172:175],a[144:147],v[76:79]
ds_read_b64_tr_b16 a[78:79],v5 offset:9216
v_mfma_f32_16x16x32_bf16 v[80:83],a[128:131],a[96:99],v[80:83]
)"
R"(buffer_load_dwordx4 v12,s[88:91],s82 offen lds
ds_read_b128 a[52:55],v1 offset:2048
v_mfma_f32_16x16x32_bf16 v[80:83],a[176:179],a[144:147],v[80:83]
v_mfma_f32_16x16x32_bf16 v[112:115],a[128:131],a[100:103],v[112:115]
s_lshl_b32 s61,s61,15
s_lshl_b32 s12,s53,19
ds_read_b64_tr_b16 a[80:81],v6 offset:8192
v_mfma_f32_16x16x32_bf16 v[112:115],a[176:179],a[148:151],v[112:115]
s_add_u32 m0,s56,3072
s_add_u32 s83,s67,16384
ds_read_b64_tr_b16 a[82:83],v6 offset:9216
v_mfma_f32_16x16x32_bf16 a[208:211],a[128:131],a[104:107],a[208:211]
ds_read_b64_tr_b16 a[84:85],v7 offset:8192
v_mfma_f32_16x16x32_bf16 a[208:211],a[176:179],a[152:155],a[208:211]
buffer_load_dwordx4 v13,s[88:91],s83 offen lds
ds_read_b64_tr_b16 a[86:87],v7 offset:9216
v_mfma_f32_16x16x32_bf16 a[240:243],a[128:131],a[108:111],a[240:243]
ds_read_b128 a[56:59],v1 offset:4096
v_mfma_f32_16x16x32_bf16 a[240:243],a[176:179],a[156:159],a[240:243]
s_add_u32 s12,s12,s65
s_add_u32 s13,s12,16777216
ds_read_b64_tr_b16 a[88:89],v8 offset:8192
v_mfma_f32_16x16x32_bf16 a[244:247],a[132:135],a[108:111],a[244:247]
s_add_u32 m0,s56,4096
)"
R"(s_add_u32 s82,s84,0
ds_read_b64_tr_b16 a[90:91],v8 offset:9216
v_mfma_f32_16x16x32_bf16 a[244:247],a[180:183],a[156:159],a[244:247]
ds_read_b64_tr_b16 a[92:93],v9 offset:8192
v_mfma_f32_16x16x32_bf16 a[212:215],a[132:135],a[104:107],a[212:215]
buffer_load_dwordx4 v10,s[88:91],s82 offen lds
ds_read_b64_tr_b16 a[94:95],v9 offset:9216
v_mfma_f32_16x16x32_bf16 a[212:215],a[180:183],a[152:155],a[212:215]
ds_read_b128 a[60:63],v1 offset:6144
v_mfma_f32_16x16x32_bf16 v[116:119],a[132:135],a[100:103],v[116:119]
s_add_u32 m0,s56,5120
s_add_u32 s83,s84,16384
v_mfma_f32_16x16x32_bf16 v[116:119],a[180:183],a[148:151],v[116:119]
v_mfma_f32_16x16x32_bf16 v[84:87],a[132:135],a[96:99],v[84:87]
v_mfma_f32_16x16x32_bf16 v[84:87],a[180:183],a[144:147],v[84:87]
buffer_load_dwordx4 v11,s[88:91],s83 offen lds
v_mfma_f32_16x16x32_bf16 v[88:91],a[136:139],a[96:99],v[88:91]
v_mfma_f32_16x16x32_bf16 v[88:91],a[184:187],a[144:147],v[88:91]
s_add_u32 m0,s56,6144
s_add_u32 s82,s84,0
v_add_u32 v0,-32768,v0
v_mfma_f32_16x16x32_bf16 v[120:123],a[136:139],a[100:103],v[120:123]
v_add_u32 v1,-32768,v1
v_add_u32 v2,-49152,v2
)"
R"(v_mfma_f32_16x16x32_bf16 v[120:123],a[184:187],a[148:151],v[120:123]
v_add_u32 v3,-49152,v3
v_add_u32 v4,-49152,v4
v_mfma_f32_16x16x32_bf16 a[216:219],a[136:139],a[104:107],a[216:219]
buffer_load_dwordx4 v12,s[88:91],s82 offen lds
v_add_u32 v5,-49152,v5
v_mfma_f32_16x16x32_bf16 a[216:219],a[184:187],a[152:155],a[216:219]
v_add_u32 v6,-49152,v6
v_add_u32 v7,-49152,v7
v_mfma_f32_16x16x32_bf16 a[248:251],a[136:139],a[108:111],a[248:251]
s_add_u32 m0,s56,7168
s_add_u32 s83,s84,16384
v_add_u32 v8,-49152,v8
v_mfma_f32_16x16x32_bf16 a[248:251],a[184:187],a[156:159],a[248:251]
v_add_u32 v9,-49152,v9
v_mfma_f32_16x16x32_bf16 a[252:255],a[140:143],a[108:111],a[252:255]
v_mfma_f32_16x16x32_bf16 a[252:255],a[188:191],a[156:159],a[252:255]
buffer_load_dwordx4 v13,s[88:91],s83 offen lds
s_add_u32 s67,s67,128
v_mfma_f32_16x16x32_bf16 a[220:223],a[140:143],a[104:107],a[220:223]
s_and_b32 s67,s67,2047
v_mfma_f32_16x16x32_bf16 a[220:223],a[188:191],a[152:155],a[220:223]
v_mfma_f32_16x16x32_bf16 v[124:127],a[140:143],a[100:103],v[124:127]
v_mfma_f32_16x16x32_bf16 v[124:127],a[188:191],a[148:151],v[124:127]
)"
R"(v_mfma_f32_16x16x32_bf16 v[92:95],a[140:143],a[96:99],v[92:95]
s_waitcnt lgkmcnt(0)
v_mfma_f32_16x16x32_bf16 v[92:95],a[188:191],a[144:147],v[92:95]
v_mfma_f32_16x16x32_bf16 v[64:67],a[16:19],a[0:3],v[64:67]
s_waitcnt vmcnt(14)
s_barrier
v_mfma_f32_16x16x32_bf16 v[64:67],a[64:67],a[48:51],v[64:67]
s_add_u32 m0,s57,147456
s_add_u32 s82,s74,0
ds_read_b64_tr_b16 a[112:113],v2 offset:0
v_mfma_f32_16x16x32_bf16 v[96:99],a[16:19],a[4:7],v[96:99]
ds_read_b64_tr_b16 a[114:115],v2 offset:1024
v_mfma_f32_16x16x32_bf16 v[96:99],a[64:67],a[52:55],v[96:99]
buffer_load_dwordx4 v16,s[92:95],s82 offen lds
ds_read_b64_tr_b16 a[116:117],v3 offset:0
v_mfma_f32_16x16x32_bf16 a[192:195],a[16:19],a[8:11],a[192:195]
s_add_u32 s36,s30,s13
s_addc_u32 s37,s31,0
v_mfma_f32_16x16x32_bf16 a[192:195],a[64:67],a[56:59],a[192:195]
s_and_b32 s37,s37,0xffff
s_add_u32 m0,s57,148480
ds_read_b64_tr_b16 a[118:119],v3 offset:1024
v_mfma_f32_16x16x32_bf16 a[224:227],a[16:19],a[12:15],a[224:227]
s_add_u32 s83,s74,32768
)"
R"(ds_read_b128 a[96:99],v0 offset:0
ds_read_b64_tr_b16 a[120:121],v4 offset:0
v_mfma_f32_16x16x32_bf16 a[224:227],a[64:67],a[60:63],a[224:227]
buffer_load_dwordx4 v16,s[92:95],s83 offen lds
ds_read_b64_tr_b16 a[122:123],v4 offset:1024
v_mfma_f32_16x16x32_bf16 a[228:231],a[20:23],a[12:15],a[228:231]
ds_read_b64_tr_b16 a[124:125],v5 offset:0
v_mfma_f32_16x16x32_bf16 a[228:231],a[68:71],a[60:63],a[228:231]
s_mov_b32 s38,499712
s_mov_b32 s39,0x20000
ds_read_b64_tr_b16 a[126:127],v5 offset:1024
v_mfma_f32_16x16x32_bf16 a[196:199],a[20:23],a[8:11],a[196:199]
s_add_u32 m0,s57,149504
s_add_u32 s82,s74,65536
ds_read_b128 a[100:103],v0 offset:2048
v_mfma_f32_16x16x32_bf16 a[196:199],a[68:71],a[56:59],a[196:199]
v_mfma_f32_16x16x32_bf16 v[100:103],a[20:23],a[4:7],v[100:103]
buffer_load_dwordx4 v17,s[92:95],s82 offen lds
ds_read_b64_tr_b16 a[128:129],v6 offset:0
v_mfma_f32_16x16x32_bf16 v[100:103],a[68:71],a[52:55],v[100:103]
ds_read_b64_tr_b16 a[130:131],v6 offset:1024
v_mfma_f32_16x16x32_bf16 v[68:71],a[20:23],a[0:3],v[68:71]
s_add_u32 s13,s12,33554432
s_add_u32 s48,s30,s13
)"
R"(s_addc_u32 s49,s31,0
v_mfma_f32_16x16x32_bf16 v[68:71],a[68:71],a[48:51],v[68:71]
s_add_u32 m0,s57,150528
s_add_u32 s83,s74,98304
ds_read_b64_tr_b16 a[132:133],v7 offset:0
v_mfma_f32_16x16x32_bf16 v[72:75],a[24:27],a[0:3],v[72:75]
ds_read_b64_tr_b16 a[134:135],v7 offset:1024
ds_read_b128 a[104:107],v0 offset:4096
v_mfma_f32_16x16x32_bf16 v[72:75],a[72:75],a[48:51],v[72:75]
buffer_load_dwordx4 v17,s[92:95],s83 offen lds
ds_read_b64_tr_b16 a[136:137],v8 offset:0
v_mfma_f32_16x16x32_bf16 v[104:107],a[24:27],a[4:7],v[104:107]
ds_read_b64_tr_b16 a[138:139],v8 offset:1024
v_mfma_f32_16x16x32_bf16 v[104:107],a[72:75],a[52:55],v[104:107]
s_and_b32 s49,s49,0xffff
s_mov_b32 s50,499712
ds_read_b64_tr_b16 a[140:141],v9 offset:0
v_mfma_f32_16x16x32_bf16 a[200:203],a[24:27],a[8:11],a[200:203]
s_add_u32 s74,s74,524288
s_and_b32 s74,s74,0x7fffff
ds_read_b64_tr_b16 a[142:143],v9 offset:1024
v_mfma_f32_16x16x32_bf16 a[200:203],a[72:75],a[56:59],a[200:203]
s_add_u32 s84,s67,s68
s_add_u32 m0,s56,65536
)"
R"(ds_read_b128 a[108:111],v0 offset:6144
v_mfma_f32_16x16x32_bf16 a[232:235],a[24:27],a[12:15],a[232:235]
s_add_u32 s82,s67,0
buffer_load_dwordx4 v10,s[88:91],s82 offen lds
v_mfma_f32_16x16x32_bf16 a[232:235],a[72:75],a[60:63],a[232:235]
ds_read_b64_tr_b16 a[160:161],v2 offset:8192
v_mfma_f32_16x16x32_bf16 a[236:239],a[28:31],a[12:15],a[236:239]
s_mov_b32 s51,0x20000
s_lshl_b32 s13,s55,9
ds_read_b64_tr_b16 a[162:163],v2 offset:9216
v_mfma_f32_16x16x32_bf16 a[236:239],a[76:79],a[60:63],a[236:239]
s_add_u32 m0,s56,66560
s_add_u32 s83,s67,16384
ds_read_b64_tr_b16 a[164:165],v3 offset:8192
v_mfma_f32_16x16x32_bf16 a[204:207],a[28:31],a[8:11],a[204:207]
ds_read_b64_tr_b16 a[166:167],v3 offset:9216
v_mfma_f32_16x16x32_bf16 a[204:207],a[76:79],a[56:59],a[204:207]
buffer_load_dwordx4 v11,s[88:91],s83 offen lds
ds_read_b128 a[144:147],v1 offset:0
v_mfma_f32_16x16x32_bf16 v[108:111],a[28:31],a[4:7],v[108:111]
ds_read_b64_tr_b16 a[168:169],v4 offset:8192
v_mfma_f32_16x16x32_bf16 v[108:111],a[76:79],a[52:55],v[108:111]
s_lshl_b32 s12,s70,6
s_sub_u32 s14,7680,s13
)"
R"(ds_read_b64_tr_b16 a[170:171],v4 offset:9216
v_mfma_f32_16x16x32_bf16 v[76:79],a[28:31],a[0:3],v[76:79]
s_add_u32 m0,s56,67584
s_add_u32 s82,s67,0
ds_read_b64_tr_b16 a[172:173],v5 offset:8192
v_mfma_f32_16x16x32_bf16 v[76:79],a[76:79],a[48:51],v[76:79]
ds_read_b64_tr_b16 a[174:175],v5 offset:9216
v_mfma_f32_16x16x32_bf16 v[80:83],a[32:35],a[0:3],v[80:83]
buffer_load_dwordx4 v12,s[88:91],s82 offen lds
ds_read_b128 a[148:151],v1 offset:2048
v_mfma_f32_16x16x32_bf16 v[80:83],a[80:83],a[48:51],v[80:83]
v_mfma_f32_16x16x32_bf16 v[112:115],a[32:35],a[4:7],v[112:115]
s_add_u32 s13,s13,s12
s_add_u32 s14,s14,s12
ds_read_b64_tr_b16 a[176:177],v6 offset:8192
v_mfma_f32_16x16x32_bf16 v[112:115],a[80:83],a[52:55],v[112:115]
s_add_u32 m0,s56,68608
s_add_u32 s83,s67,16384
ds_read_b64_tr_b16 a[178:179],v6 offset:9216
v_mfma_f32_16x16x32_bf16 a[208:211],a[32:35],a[8:11],a[208:211]
ds_read_b64_tr_b16 a[180:181],v7 offset:8192
v_mfma_f32_16x16x32_bf16 a[208:211],a[80:83],a[56:59],a[208:211]
buffer_load_dwordx4 v13,s[88:91],s83 offen lds
ds_read_b64_tr_b16 a[182:183],v7 offset:9216
)"
R"(v_mfma_f32_16x16x32_bf16 a[240:243],a[32:35],a[12:15],a[240:243]
ds_read_b128 a[152:155],v1 offset:4096
v_mfma_f32_16x16x32_bf16 a[240:243],a[80:83],a[60:63],a[240:243]
s_lshl_b32 s15,s70,1
s_sub_u32 s15,7,s15
ds_read_b64_tr_b16 a[184:185],v8 offset:8192
v_mfma_f32_16x16x32_bf16 a[244:247],a[36:39],a[12:15],a[244:247]
s_add_u32 m0,s56,69632
s_add_u32 s82,s84,0
ds_read_b64_tr_b16 a[186:187],v8 offset:9216
v_mfma_f32_16x16x32_bf16 a[244:247],a[84:87],a[60:63],a[244:247]
ds_read_b64_tr_b16 a[188:189],v9 offset:8192
v_mfma_f32_16x16x32_bf16 a[212:215],a[36:39],a[8:11],a[212:215]
buffer_load_dwordx4 v10,s[88:91],s82 offen lds
ds_read_b64_tr_b16 a[190:191],v9 offset:9216
v_mfma_f32_16x16x32_bf16 a[212:215],a[84:87],a[56:59],a[212:215]
ds_read_b128 a[156:159],v1 offset:6144
v_mfma_f32_16x16x32_bf16 v[116:119],a[36:39],a[4:7],v[116:119]
s_lshl_b32 s99,s73,5
s_add_u32 s99,s99,s69
s_add_u32 m0,s56,70656
v_mfma_f32_16x16x32_bf16 v[116:119],a[84:87],a[52:55],v[116:119]
s_add_u32 s83,s84,16384
v_mfma_f32_16x16x32_bf16 v[84:87],a[36:39],a[0:3],v[84:87]
)"
R"(v_mfma_f32_16x16x32_bf16 v[84:87],a[84:87],a[48:51],v[84:87]
buffer_load_dwordx4 v11,s[88:91],s83 offen lds
v_mfma_f32_16x16x32_bf16 v[88:91],a[40:43],a[0:3],v[88:91]
v_mfma_f32_16x16x32_bf16 v[88:91],a[88:91],a[48:51],v[88:91]
s_lshl_b32 s99,s99,2
s_add_u32 s99,s99,20480
v_add_u32 v0,-32768,v0
v_mfma_f32_16x16x32_bf16 v[120:123],a[40:43],a[4:7],v[120:123]
s_add_u32 m0,s56,71680
s_add_u32 s82,s84,0
v_add_u32 v1,-32768,v1
v_mfma_f32_16x16x32_bf16 v[120:123],a[88:91],a[52:55],v[120:123]
v_add_u32 v2,32768,v2
v_add_u32 v3,32768,v3
v_mfma_f32_16x16x32_bf16 a[216:219],a[40:43],a[8:11],a[216:219]
buffer_load_dwordx4 v12,s[88:91],s82 offen lds
v_add_u32 v4,32768,v4
v_mfma_f32_16x16x32_bf16 a[216:219],a[88:91],a[56:59],a[216:219]
v_add_u32 v5,32768,v5
v_add_u32 v6,32768,v6
v_mfma_f32_16x16x32_bf16 a[248:251],a[40:43],a[12:15],a[248:251]
s_add_u32 m0,s56,72704
s_add_u32 s83,s84,16384
v_add_u32 v7,32768,v7
)"
R"(v_mfma_f32_16x16x32_bf16 a[248:251],a[88:91],a[60:63],a[248:251]
v_add_u32 v8,32768,v8
v_add_u32 v9,32768,v9
v_mfma_f32_16x16x32_bf16 a[252:255],a[44:47],a[12:15],a[252:255]
v_mfma_f32_16x16x32_bf16 a[252:255],a[92:95],a[60:63],a[252:255]
buffer_load_dwordx4 v13,s[88:91],s83 offen lds
s_add_u32 s67,s67,128
v_mfma_f32_16x16x32_bf16 a[220:223],a[44:47],a[8:11],a[220:223]
s_and_b32 s67,s67,2047
v_mfma_f32_16x16x32_bf16 a[220:223],a[92:95],a[56:59],a[220:223]
v_mfma_f32_16x16x32_bf16 v[124:127],a[44:47],a[4:7],v[124:127]
v_mfma_f32_16x16x32_bf16 v[124:127],a[92:95],a[52:55],v[124:127]
v_mfma_f32_16x16x32_bf16 v[92:95],a[44:47],a[0:3],v[92:95]
s_waitcnt lgkmcnt(0)
v_mfma_f32_16x16x32_bf16 v[92:95],a[92:95],a[48:51],v[92:95]
v_mfma_f32_16x16x32_bf16 v[64:67],a[112:115],a[96:99],v[64:67]
s_waitcnt vmcnt(12)
s_barrier
v_mfma_f32_16x16x32_bf16 v[64:67],a[160:163],a[144:147],v[64:67]
s_add_u32 m0,s57,98304
s_add_u32 s82,s74,0
ds_read_b64_tr_b16 a[16:17],v2 offset:0
v_mfma_f32_16x16x32_bf16 v[96:99],a[112:115],a[100:103],v[96:99]
ds_read_b64_tr_b16 a[18:19],v2 offset:1024
)"
R"(v_add_u32 v241,131072,v18
v_mfma_f32_16x16x32_bf16 v[96:99],a[160:163],a[148:151],v[96:99]
buffer_load_dwordx4 v16,s[92:95],s82 offen lds
ds_read_b64_tr_b16 a[20:21],v3 offset:0
v_mfma_f32_16x16x32_bf16 a[192:195],a[112:115],a[104:107],a[192:195]
s_cmp_lg_u32 %2,0
s_cbranch_scc1 gm_KB_w30
v_mov_b32 v22,s72
v_mov_b32 v23,0
s_mov_b64 exec,1
buffer_store_dword v22,v23,s[4:7],s99 offen sc1
s_mov_b64 exec,-1
gm_KB_w30:
v_mfma_f32_16x16x32_bf16 a[192:195],a[160:163],a[152:155],a[192:195]
s_add_u32 m0,s57,99328
s_add_u32 s83,s74,32768
ds_read_b64_tr_b16 a[22:23],v3 offset:1024
v_mfma_f32_16x16x32_bf16 a[224:227],a[112:115],a[108:111],a[224:227]
ds_read_b128 a[0:3],v0 offset:0
v_add_u32 v240,0x10000,v18
v_mfma_f32_16x16x32_bf16 a[224:227],a[160:163],a[156:159],a[224:227]
buffer_load_dwordx4 v16,s[92:95],s83 offen lds
ds_read_b64_tr_b16 a[24:25],v4 offset:0
v_mfma_f32_16x16x32_bf16 a[228:231],a[116:119],a[108:111],a[228:231]
)"
R"(ds_read_b64_tr_b16 a[26:27],v4 offset:1024
v_and_b32 v24,15,v19
v_mfma_f32_16x16x32_bf16 a[228:231],a[164:167],a[156:159],a[228:231]
s_add_u32 m0,s57,100352
s_add_u32 s82,s74,65536
ds_read_b64_tr_b16 a[28:29],v5 offset:0
v_mfma_f32_16x16x32_bf16 a[196:199],a[116:119],a[104:107],a[196:199]
ds_read_b64_tr_b16 a[30:31],v5 offset:1024
v_lshlrev_b32 v253,2,v24
v_mfma_f32_16x16x32_bf16 a[196:199],a[164:167],a[152:155],a[196:199]
ds_read_b128 a[4:7],v0 offset:2048
v_lshrrev_b32 v25,4,v19
v_mfma_f32_16x16x32_bf16 v[100:103],a[116:119],a[100:103],v[100:103]
buffer_load_dwordx4 v17,s[92:95],s82 offen lds
ds_read_b64_tr_b16 a[32:33],v6 offset:0
v_mfma_f32_16x16x32_bf16 v[100:103],a[164:167],a[148:151],v[100:103]
ds_read_b64_tr_b16 a[34:35],v6 offset:1024
v_lshlrev_b32 v25,2,v25
v_mfma_f32_16x16x32_bf16 v[68:71],a[116:119],a[96:99],v[68:71]
s_add_u32 m0,s57,101376
s_add_u32 s83,s74,98304
ds_read_b64_tr_b16 a[36:37],v7 offset:0
v_mfma_f32_16x16x32_bf16 v[68:71],a[164:167],a[144:147],v[68:71]
ds_read_b64_tr_b16 a[38:39],v7 offset:1024
)"
R"(v_sub_u32 v24,v24,v25
v_mfma_f32_16x16x32_bf16 v[72:75],a[120:123],a[96:99],v[72:75]
ds_read_b128 a[8:11],v0 offset:4096
v_mov_b32 v252,0xf149f2ca
v_mfma_f32_16x16x32_bf16 v[72:75],a[168:171],a[144:147],v[72:75]
buffer_load_dwordx4 v17,s[92:95],s83 offen lds
ds_read_b64_tr_b16 a[40:41],v8 offset:0
v_mfma_f32_16x16x32_bf16 v[104:107],a[120:123],a[100:103],v[104:107]
ds_read_b64_tr_b16 a[42:43],v8 offset:1024
v_mov_b32 v25,0x7f61b1e6
v_mfma_f32_16x16x32_bf16 v[104:107],a[168:171],a[148:151],v[104:107]
s_add_u32 s74,s74,524288
s_and_b32 s74,s74,0x7fffff
ds_read_b64_tr_b16 a[44:45],v9 offset:0
v_mfma_f32_16x16x32_bf16 a[200:203],a[120:123],a[104:107],a[200:203]
s_add_u32 s84,s67,s68
s_add_u32 m0,s56,32768
ds_read_b64_tr_b16 a[46:47],v9 offset:1024
v_mfma_f32_16x16x32_bf16 a[200:203],a[168:171],a[152:155],a[200:203]
s_add_u32 s82,s67,0
ds_read_b128 a[12:15],v0 offset:6144
v_lshl_add_u32 v26,s70,4,v24
v_mfma_f32_16x16x32_bf16 a[232:235],a[120:123],a[108:111],a[232:235]
buffer_load_dwordx4 v10,s[88:91],s82 offen lds
)"
R"(v_lshl_add_u32 v24,s15,4,v26
v_mfma_f32_16x16x32_bf16 a[232:235],a[168:171],a[156:159],a[232:235]
ds_read_b64_tr_b16 a[64:65],v2 offset:8192
v_add_u32 v24,-64,v24
v_mfma_f32_16x16x32_bf16 a[236:239],a[124:127],a[108:111],a[236:239]
s_add_u32 m0,s56,33792
s_add_u32 s83,s67,16384
ds_read_b64_tr_b16 a[66:67],v2 offset:9216
v_mfma_f32_16x16x32_bf16 a[236:239],a[172:175],a[156:159],a[236:239]
s_branch gm_KB_sk12
.p2align 12
gm_KB_pg12:
s_setpc_b64 s[100:101]
gm_KB_sk12:
ds_read_b64_tr_b16 a[68:69],v3 offset:8192
v_mfma_f32_16x16x32_bf16 a[204:207],a[124:127],a[104:107],a[204:207]
ds_read_b64_tr_b16 a[70:71],v3 offset:9216
v_mfma_f32_16x16x32_bf16 a[204:207],a[172:175],a[152:155],a[204:207]
buffer_load_dwordx4 v11,s[88:91],s83 offen lds
ds_read_b128 a[48:51],v1 offset:0
v_mfma_f32_16x16x32_bf16 v[108:111],a[124:127],a[100:103],v[108:111]
ds_read_b64_tr_b16 a[72:73],v4 offset:8192
v_mfma_f32_16x16x32_bf16 v[108:111],a[172:175],a[148:151],v[108:111]
s_add_u32 m0,s56,34816
)"
R"(s_add_u32 s82,s67,0
ds_read_b64_tr_b16 a[74:75],v4 offset:9216
v_mfma_f32_16x16x32_bf16 v[76:79],a[124:127],a[96:99],v[76:79]
ds_read_b64_tr_b16 a[76:77],v5 offset:8192
v_mfma_f32_16x16x32_bf16 v[76:79],a[172:175],a[144:147],v[76:79]
ds_read_b64_tr_b16 a[78:79],v5 offset:9216
v_mfma_f32_16x16x32_bf16 v[80:83],a[128:131],a[96:99],v[80:83]
buffer_load_dwordx4 v12,s[88:91],s82 offen lds
ds_read_b128 a[52:55],v1 offset:2048
v_mfma_f32_16x16x32_bf16 v[80:83],a[176:179],a[144:147],v[80:83]
v_mfma_f32_16x16x32_bf16 v[112:115],a[128:131],a[100:103],v[112:115]
s_add_u32 m0,s56,35840
s_add_u32 s83,s67,16384
ds_read_b64_tr_b16 a[80:81],v6 offset:8192
v_mfma_f32_16x16x32_bf16 v[112:115],a[176:179],a[148:151],v[112:115]
ds_read_b64_tr_b16 a[82:83],v6 offset:9216
v_mfma_f32_16x16x32_bf16 a[208:211],a[128:131],a[104:107],a[208:211]
ds_read_b64_tr_b16 a[84:85],v7 offset:8192
v_mfma_f32_16x16x32_bf16 a[208:211],a[176:179],a[152:155],a[208:211]
buffer_load_dwordx4 v13,s[88:91],s83 offen lds
ds_read_b64_tr_b16 a[86:87],v7 offset:9216
v_mfma_f32_16x16x32_bf16 a[240:243],a[128:131],a[108:111],a[240:243]
ds_read_b128 a[56:59],v1 offset:4096
v_mfma_f32_16x16x32_bf16 a[240:243],a[176:179],a[156:159],a[240:243]
)"
R"(s_add_u32 m0,s56,36864
s_add_u32 s82,s84,0
ds_read_b64_tr_b16 a[88:89],v8 offset:8192
v_mfma_f32_16x16x32_bf16 a[244:247],a[132:135],a[108:111],a[244:247]
ds_read_b64_tr_b16 a[90:91],v8 offset:9216
v_mfma_f32_16x16x32_bf16 a[244:247],a[180:183],a[156:159],a[244:247]
ds_read_b64_tr_b16 a[92:93],v9 offset:8192
v_mfma_f32_16x16x32_bf16 a[212:215],a[132:135],a[104:107],a[212:215]
buffer_load_dwordx4 v10,s[88:91],s82 offen lds
ds_read_b64_tr_b16 a[94:95],v9 offset:9216
v_mfma_f32_16x16x32_bf16 a[212:215],a[180:183],a[152:155],a[212:215]
ds_read_b128 a[60:63],v1 offset:6144
v_mfma_f32_16x16x32_bf16 v[116:119],a[132:135],a[100:103],v[116:119]
s_add_u32 m0,s56,37888
s_add_u32 s83,s84,16384
v_mfma_f32_16x16x32_bf16 v[116:119],a[180:183],a[148:151],v[116:119]
v_mfma_f32_16x16x32_bf16 v[84:87],a[132:135],a[96:99],v[84:87]
v_mfma_f32_16x16x32_bf16 v[84:87],a[180:183],a[144:147],v[84:87]
buffer_load_dwordx4 v11,s[88:91],s83 offen lds
v_mfma_f32_16x16x32_bf16 v[88:91],a[136:139],a[96:99],v[88:91]
v_mfma_f32_16x16x32_bf16 v[88:91],a[184:187],a[144:147],v[88:91]
s_add_u32 m0,s56,38912
s_add_u32 s82,s84,0
v_add_u32 v0,65536,v0
)"
R"(v_mfma_f32_16x16x32_bf16 v[120:123],a[136:139],a[100:103],v[120:123]
v_add_u32 v1,65536,v1
v_add_u32 v2,16384,v2
v_mfma_f32_16x16x32_bf16 v[120:123],a[184:187],a[148:151],v[120:123]
v_add_u32 v3,16384,v3
v_add_u32 v4,16384,v4
v_mfma_f32_16x16x32_bf16 a[216:219],a[136:139],a[104:107],a[216:219]
buffer_load_dwordx4 v12,s[88:91],s82 offen lds
v_add_u32 v5,16384,v5
v_mfma_f32_16x16x32_bf16 a[216:219],a[184:187],a[152:155],a[216:219]
v_add_u32 v6,16384,v6
v_add_u32 v7,16384,v7
v_mfma_f32_16x16x32_bf16 a[248:251],a[136:139],a[108:111],a[248:251]
s_add_u32 m0,s56,39936
s_add_u32 s83,s84,16384
v_add_u32 v8,16384,v8
v_mfma_f32_16x16x32_bf16 a[248:251],a[184:187],a[156:159],a[248:251]
v_add_u32 v9,16384,v9
v_mfma_f32_16x16x32_bf16 a[252:255],a[140:143],a[108:111],a[252:255]
v_mfma_f32_16x16x32_bf16 a[252:255],a[188:191],a[156:159],a[252:255]
buffer_load_dwordx4 v13,s[88:91],s83 offen lds
s_add_u32 s67,s67,128
v_mfma_f32_16x16x32_bf16 a[220:223],a[140:143],a[104:107],a[220:223]
s_and_b32 s67,s67,2047
)"
R"(v_mfma_f32_16x16x32_bf16 a[220:223],a[188:191],a[152:155],a[220:223]
v_mfma_f32_16x16x32_bf16 v[124:127],a[140:143],a[100:103],v[124:127]
v_mfma_f32_16x16x32_bf16 v[124:127],a[188:191],a[148:151],v[124:127]
v_mfma_f32_16x16x32_bf16 v[92:95],a[140:143],a[96:99],v[92:95]
s_waitcnt lgkmcnt(0)
v_mfma_f32_16x16x32_bf16 v[92:95],a[188:191],a[144:147],v[92:95]
v_mfma_f32_16x16x32_bf16 v[64:67],a[16:19],a[0:3],v[64:67]
s_waitcnt vmcnt(12)
s_barrier
v_mfma_f32_16x16x32_bf16 v[64:67],a[64:67],a[48:51],v[64:67]
s_add_u32 m0,s57,131072
s_add_u32 s82,s74,0
ds_read_b64_tr_b16 a[112:113],v2 offset:0
v_mfma_f32_16x16x32_bf16 v[96:99],a[16:19],a[4:7],v[96:99]
ds_read_b64_tr_b16 a[114:115],v2 offset:1024
v_mfma_f32_16x16x32_bf16 v[96:99],a[64:67],a[52:55],v[96:99]
buffer_load_dwordx4 v16,s[92:95],s82 offen lds
ds_read_b64_tr_b16 a[116:117],v3 offset:0
v_mfma_f32_16x16x32_bf16 a[192:195],a[16:19],a[8:11],a[192:195]
s_add_u32 m0,s57,132096
s_add_u32 s83,s74,32768
ds_read_b64_tr_b16 a[118:119],v3 offset:1024
v_mfma_f32_16x16x32_bf16 a[192:195],a[64:67],a[56:59],a[192:195]
ds_read_b128 a[96:99],v0 offset:0
)"
R"(v_mfma_f32_16x16x32_bf16 a[224:227],a[16:19],a[12:15],a[224:227]
ds_read_b64_tr_b16 a[120:121],v4 offset:0
v_mfma_f32_16x16x32_bf16 a[224:227],a[64:67],a[60:63],a[224:227]
buffer_load_dwordx4 v16,s[92:95],s83 offen lds
ds_read_b64_tr_b16 a[122:123],v4 offset:1024
v_mfma_f32_16x16x32_bf16 a[228:231],a[20:23],a[12:15],a[228:231]
ds_read_b64_tr_b16 a[124:125],v5 offset:0
v_mfma_f32_16x16x32_bf16 a[228:231],a[68:71],a[60:63],a[228:231]
s_add_u32 m0,s57,133120
s_add_u32 s82,s74,65536
ds_read_b64_tr_b16 a[126:127],v5 offset:1024
v_mfma_f32_16x16x32_bf16 a[196:199],a[20:23],a[8:11],a[196:199]
ds_read_b128 a[100:103],v0 offset:2048
v_mfma_f32_16x16x32_bf16 a[196:199],a[68:71],a[56:59],a[196:199]
v_mfma_f32_16x16x32_bf16 v[100:103],a[20:23],a[4:7],v[100:103]
buffer_load_dwordx4 v17,s[92:95],s82 offen lds
ds_read_b64_tr_b16 a[128:129],v6 offset:0
v_mfma_f32_16x16x32_bf16 v[100:103],a[68:71],a[52:55],v[100:103]
ds_read_b64_tr_b16 a[130:131],v6 offset:1024
v_mfma_f32_16x16x32_bf16 v[68:71],a[20:23],a[0:3],v[68:71]
s_add_u32 m0,s57,134144
s_add_u32 s83,s74,98304
ds_read_b64_tr_b16 a[132:133],v7 offset:0
v_mfma_f32_16x16x32_bf16 v[68:71],a[68:71],a[48:51],v[68:71]
)"
R"(ds_read_b64_tr_b16 a[134:135],v7 offset:1024
v_mfma_f32_16x16x32_bf16 v[72:75],a[24:27],a[0:3],v[72:75]
ds_read_b128 a[104:107],v0 offset:4096
v_mfma_f32_16x16x32_bf16 v[72:75],a[72:75],a[48:51],v[72:75]
buffer_load_dwordx4 v17,s[92:95],s83 offen lds
ds_read_b64_tr_b16 a[136:137],v8 offset:0
v_mfma_f32_16x16x32_bf16 v[104:107],a[24:27],a[4:7],v[104:107]
ds_read_b64_tr_b16 a[138:139],v8 offset:1024
v_mfma_f32_16x16x32_bf16 v[104:107],a[72:75],a[52:55],v[104:107]
s_add_u32 s74,s74,524288
s_and_b32 s74,s74,0x7fffff
ds_read_b64_tr_b16 a[140:141],v9 offset:0
v_mfma_f32_16x16x32_bf16 a[200:203],a[24:27],a[8:11],a[200:203]
s_add_u32 s84,s67,s68
s_add_u32 m0,s56,0
ds_read_b64_tr_b16 a[142:143],v9 offset:1024
v_mfma_f32_16x16x32_bf16 a[200:203],a[72:75],a[56:59],a[200:203]
s_add_u32 s82,s67,0
ds_read_b128 a[108:111],v0 offset:6144
v_mfma_f32_16x16x32_bf16 a[232:235],a[24:27],a[12:15],a[232:235]
buffer_load_dwordx4 v10,s[88:91],s82 offen lds
v_mfma_f32_16x16x32_bf16 a[232:235],a[72:75],a[60:63],a[232:235]
ds_read_b64_tr_b16 a[160:161],v2 offset:8192
v_mfma_f32_16x16x32_bf16 a[236:239],a[28:31],a[12:15],a[236:239]
)"
R"(s_add_u32 m0,s56,1024
s_add_u32 s83,s67,16384
ds_read_b64_tr_b16 a[162:163],v2 offset:9216
v_mfma_f32_16x16x32_bf16 a[236:239],a[76:79],a[60:63],a[236:239]
ds_read_b64_tr_b16 a[164:165],v3 offset:8192
v_mfma_f32_16x16x32_bf16 a[204:207],a[28:31],a[8:11],a[204:207]
ds_read_b64_tr_b16 a[166:167],v3 offset:9216
v_mfma_f32_16x16x32_bf16 a[204:207],a[76:79],a[56:59],a[204:207]
buffer_load_dwordx4 v11,s[88:91],s83 offen lds
ds_read_b128 a[144:147],v1 offset:0
v_mfma_f32_16x16x32_bf16 v[108:111],a[28:31],a[4:7],v[108:111]
ds_read_b64_tr_b16 a[168:169],v4 offset:8192
v_mfma_f32_16x16x32_bf16 v[108:111],a[76:79],a[52:55],v[108:111]
s_add_u32 m0,s56,2048
s_add_u32 s82,s67,0
ds_read_b64_tr_b16 a[170:171],v4 offset:9216
v_mfma_f32_16x16x32_bf16 v[76:79],a[28:31],a[0:3],v[76:79]
ds_read_b64_tr_b16 a[172:173],v5 offset:8192
v_mfma_f32_16x16x32_bf16 v[76:79],a[76:79],a[48:51],v[76:79]
ds_read_b64_tr_b16 a[174:175],v5 offset:9216
v_mfma_f32_16x16x32_bf16 v[80:83],a[32:35],a[0:3],v[80:83]
buffer_load_dwordx4 v12,s[88:91],s82 offen lds
ds_read_b128 a[148:151],v1 offset:2048
v_mfma_f32_16x16x32_bf16 v[80:83],a[80:83],a[48:51],v[80:83]
)"
R"(v_mfma_f32_16x16x32_bf16 v[112:115],a[32:35],a[4:7],v[112:115]
s_add_u32 m0,s56,3072
s_add_u32 s83,s67,16384
ds_read_b64_tr_b16 a[176:177],v6 offset:8192
v_mfma_f32_16x16x32_bf16 v[112:115],a[80:83],a[52:55],v[112:115]
ds_read_b64_tr_b16 a[178:179],v6 offset:9216
v_mfma_f32_16x16x32_bf16 a[208:211],a[32:35],a[8:11],a[208:211]
ds_read_b64_tr_b16 a[180:181],v7 offset:8192
v_mfma_f32_16x16x32_bf16 a[208:211],a[80:83],a[56:59],a[208:211]
buffer_load_dwordx4 v13,s[88:91],s83 offen lds
ds_read_b64_tr_b16 a[182:183],v7 offset:9216
v_mfma_f32_16x16x32_bf16 a[240:243],a[32:35],a[12:15],a[240:243]
ds_read_b128 a[152:155],v1 offset:4096
v_mfma_f32_16x16x32_bf16 a[240:243],a[80:83],a[60:63],a[240:243]
s_add_u32 m0,s56,4096
s_add_u32 s82,s84,0
ds_read_b64_tr_b16 a[184:185],v8 offset:8192
v_mfma_f32_16x16x32_bf16 a[244:247],a[36:39],a[12:15],a[244:247]
ds_read_b64_tr_b16 a[186:187],v8 offset:9216
v_mfma_f32_16x16x32_bf16 a[244:247],a[84:87],a[60:63],a[244:247]
ds_read_b64_tr_b16 a[188:189],v9 offset:8192
v_mfma_f32_16x16x32_bf16 a[212:215],a[36:39],a[8:11],a[212:215]
buffer_load_dwordx4 v10,s[88:91],s82 offen lds
ds_read_b64_tr_b16 a[190:191],v9 offset:9216
)"
R"(v_mfma_f32_16x16x32_bf16 a[212:215],a[84:87],a[56:59],a[212:215]
ds_read_b128 a[156:159],v1 offset:6144
v_mfma_f32_16x16x32_bf16 v[116:119],a[36:39],a[4:7],v[116:119]
s_add_u32 m0,s56,5120
s_add_u32 s83,s84,16384
v_mfma_f32_16x16x32_bf16 v[116:119],a[84:87],a[52:55],v[116:119]
v_mfma_f32_16x16x32_bf16 v[84:87],a[36:39],a[0:3],v[84:87]
v_mfma_f32_16x16x32_bf16 v[84:87],a[84:87],a[48:51],v[84:87]
buffer_load_dwordx4 v11,s[88:91],s83 offen lds
v_mfma_f32_16x16x32_bf16 v[88:91],a[40:43],a[0:3],v[88:91]
v_mfma_f32_16x16x32_bf16 v[88:91],a[88:91],a[48:51],v[88:91]
s_add_u32 m0,s56,6144
s_add_u32 s82,s84,0
v_add_u32 v0,-32768,v0
v_mfma_f32_16x16x32_bf16 v[120:123],a[40:43],a[4:7],v[120:123]
v_add_u32 v1,-32768,v1
v_add_u32 v2,-49152,v2
v_mfma_f32_16x16x32_bf16 v[120:123],a[88:91],a[52:55],v[120:123]
v_add_u32 v3,-49152,v3
v_add_u32 v4,-49152,v4
v_mfma_f32_16x16x32_bf16 a[216:219],a[40:43],a[8:11],a[216:219]
buffer_load_dwordx4 v12,s[88:91],s82 offen lds
v_add_u32 v5,-49152,v5
v_mfma_f32_16x16x32_bf16 a[216:219],a[88:91],a[56:59],a[216:219]
)"
R"(v_add_u32 v6,-49152,v6
v_add_u32 v7,-49152,v7
v_mfma_f32_16x16x32_bf16 a[248:251],a[40:43],a[12:15],a[248:251]
s_add_u32 m0,s56,7168
s_add_u32 s83,s84,16384
v_add_u32 v8,-49152,v8
v_mfma_f32_16x16x32_bf16 a[248:251],a[88:91],a[60:63],a[248:251]
v_add_u32 v9,-49152,v9
v_mfma_f32_16x16x32_bf16 a[252:255],a[44:47],a[12:15],a[252:255]
v_mfma_f32_16x16x32_bf16 a[252:255],a[92:95],a[60:63],a[252:255]
buffer_load_dwordx4 v13,s[88:91],s83 offen lds
s_add_u32 s67,s67,128
v_mfma_f32_16x16x32_bf16 a[220:223],a[44:47],a[8:11],a[220:223]
s_and_b32 s67,s67,2047
v_mfma_f32_16x16x32_bf16 a[220:223],a[92:95],a[56:59],a[220:223]
v_mfma_f32_16x16x32_bf16 v[124:127],a[44:47],a[4:7],v[124:127]
v_mfma_f32_16x16x32_bf16 v[124:127],a[92:95],a[52:55],v[124:127]
v_mfma_f32_16x16x32_bf16 v[92:95],a[44:47],a[0:3],v[92:95]
s_waitcnt lgkmcnt(0)
v_mfma_f32_16x16x32_bf16 v[92:95],a[92:95],a[48:51],v[92:95]
v_mfma_f32_16x16x32_bf16 v[64:67],a[112:115],a[96:99],v[64:67]
s_waitcnt vmcnt(12)
s_barrier
v_mfma_f32_16x16x32_bf16 v[64:67],a[160:163],a[144:147],v[64:67]
)"
R"(ds_read_b64_tr_b16 a[16:17],v2 offset:0
v_cmp_le_i32 vcc,0,v26
v_mfma_f32_16x16x32_bf16 v[96:99],a[112:115],a[100:103],v[96:99]
ds_read_b64_tr_b16 a[18:19],v2 offset:1024
v_cndmask_b32 v12,v252,v25,vcc
v_mfma_f32_16x16x32_bf16 v[96:99],a[160:163],a[148:151],v[96:99]
ds_read_b64_tr_b16 a[20:21],v3 offset:0
v_cmp_le_i32 vcc,1,v26
v_mfma_f32_16x16x32_bf16 a[192:195],a[112:115],a[104:107],a[192:195]
ds_read_b64_tr_b16 a[22:23],v3 offset:1024
v_cndmask_b32 v13,v252,v25,vcc
v_mfma_f32_16x16x32_bf16 a[192:195],a[160:163],a[152:155],a[192:195]
ds_read_b128 a[0:3],v0 offset:0
v_cmp_le_i32 vcc,2,v26
v_mfma_f32_16x16x32_bf16 a[224:227],a[112:115],a[108:111],a[224:227]
ds_read_b64_tr_b16 a[24:25],v4 offset:0
v_cndmask_b32 v16,v252,v25,vcc
v_mfma_f32_16x16x32_bf16 a[224:227],a[160:163],a[156:159],a[224:227]
ds_read_b64_tr_b16 a[26:27],v4 offset:1024
v_cmp_le_i32 vcc,3,v26
v_mfma_f32_16x16x32_bf16 a[228:231],a[116:119],a[108:111],a[228:231]
ds_read_b64_tr_b16 a[28:29],v5 offset:0
v_cndmask_b32 v17,v252,v25,vcc
v_mfma_f32_16x16x32_bf16 a[228:231],a[164:167],a[156:159],a[228:231]
)"
R"(ds_read_b64_tr_b16 a[30:31],v5 offset:1024
v_cmp_le_i32 vcc,16,v26
v_mfma_f32_16x16x32_bf16 a[196:199],a[116:119],a[104:107],a[196:199]
ds_read_b128 a[4:7],v0 offset:2048
v_cndmask_b32 v20,v252,v25,vcc
v_mfma_f32_16x16x32_bf16 a[196:199],a[164:167],a[152:155],a[196:199]
v_cmp_le_i32 vcc,17,v26
s_nop 1
v_cndmask_b32 v21,v252,v25,vcc
v_mfma_f32_16x16x32_bf16 v[100:103],a[116:119],a[100:103],v[100:103]
ds_read_b64_tr_b16 a[32:33],v6 offset:0
v_cmp_le_i32 vcc,32,v26
v_mfma_f32_16x16x32_bf16 v[100:103],a[164:167],a[148:151],v[100:103]
ds_read_b64_tr_b16 a[34:35],v6 offset:1024
v_cndmask_b32 v56,v252,v25,vcc
v_mfma_f32_16x16x32_bf16 v[68:71],a[116:119],a[96:99],v[68:71]
ds_read_b64_tr_b16 a[36:37],v7 offset:0
v_cmp_le_i32 vcc,33,v26
v_mfma_f32_16x16x32_bf16 v[68:71],a[164:167],a[144:147],v[68:71]
ds_read_b64_tr_b16 a[38:39],v7 offset:1024
v_cndmask_b32 v57,v252,v25,vcc
v_mfma_f32_16x16x32_bf16 v[72:75],a[120:123],a[96:99],v[72:75]
ds_read_b128 a[8:11],v0 offset:4096
v_cmp_le_i32 vcc,34,v26
)"
R"(v_mfma_f32_16x16x32_bf16 v[72:75],a[168:171],a[144:147],v[72:75]
ds_read_b64_tr_b16 a[40:41],v8 offset:0
v_cndmask_b32 v59,v252,v25,vcc
v_mfma_f32_16x16x32_bf16 v[104:107],a[120:123],a[100:103],v[104:107]
ds_read_b64_tr_b16 a[42:43],v8 offset:1024
v_cmp_le_i32 vcc,35,v26
v_mfma_f32_16x16x32_bf16 v[104:107],a[168:171],a[148:151],v[104:107]
ds_read_b64_tr_b16 a[44:45],v9 offset:0
v_cndmask_b32 v60,v252,v25,vcc
v_mfma_f32_16x16x32_bf16 a[200:203],a[120:123],a[104:107],a[200:203]
ds_read_b64_tr_b16 a[46:47],v9 offset:1024
v_cmp_le_i32 vcc,48,v26
v_mfma_f32_16x16x32_bf16 a[200:203],a[168:171],a[152:155],a[200:203]
ds_read_b128 a[12:15],v0 offset:6144
v_cndmask_b32 v61,v252,v25,vcc
v_mfma_f32_16x16x32_bf16 a[232:235],a[120:123],a[108:111],a[232:235]
v_cmp_le_i32 vcc,49,v26
s_nop 1
v_cndmask_b32 v62,v252,v25,vcc
v_mfma_f32_16x16x32_bf16 a[232:235],a[168:171],a[156:159],a[232:235]
ds_read_b64_tr_b16 a[64:65],v2 offset:8192
v_cmp_le_i32 vcc,50,v26
v_mfma_f32_16x16x32_bf16 a[236:239],a[124:127],a[108:111],a[236:239]
ds_read_b64_tr_b16 a[66:67],v2 offset:9216
)"
R"(v_cndmask_b32 v192,v252,v25,vcc
v_mfma_f32_16x16x32_bf16 a[236:239],a[172:175],a[156:159],a[236:239]
ds_read_b64_tr_b16 a[68:69],v3 offset:8192
v_cmp_le_i32 vcc,51,v26
v_mfma_f32_16x16x32_bf16 a[204:207],a[124:127],a[104:107],a[204:207]
ds_read_b64_tr_b16 a[70:71],v3 offset:9216
v_cndmask_b32 v193,v252,v25,vcc
v_mfma_f32_16x16x32_bf16 a[204:207],a[172:175],a[152:155],a[204:207]
ds_read_b128 a[48:51],v1 offset:0
v_cmp_le_i32 vcc,0,v24
v_mfma_f32_16x16x32_bf16 v[108:111],a[124:127],a[100:103],v[108:111]
ds_read_b64_tr_b16 a[72:73],v4 offset:8192
v_cndmask_b32 v194,v252,v25,vcc
v_mfma_f32_16x16x32_bf16 v[108:111],a[172:175],a[148:151],v[108:111]
ds_read_b64_tr_b16 a[74:75],v4 offset:9216
v_cmp_le_i32 vcc,1,v24
v_mfma_f32_16x16x32_bf16 v[76:79],a[124:127],a[96:99],v[76:79]
ds_read_b64_tr_b16 a[76:77],v5 offset:8192
v_cndmask_b32 v195,v252,v25,vcc
v_mfma_f32_16x16x32_bf16 v[76:79],a[172:175],a[144:147],v[76:79]
ds_read_b64_tr_b16 a[78:79],v5 offset:9216
v_cmp_le_i32 vcc,2,v24
v_mfma_f32_16x16x32_bf16 v[80:83],a[128:131],a[96:99],v[80:83]
ds_read_b128 a[52:55],v1 offset:2048
)"
R"(v_cndmask_b32 v196,v252,v25,vcc
v_mfma_f32_16x16x32_bf16 v[80:83],a[176:179],a[144:147],v[80:83]
v_cmp_le_i32 vcc,3,v24
s_nop 1
v_cndmask_b32 v197,v252,v25,vcc
v_mfma_f32_16x16x32_bf16 v[112:115],a[128:131],a[100:103],v[112:115]
ds_read_b64_tr_b16 a[80:81],v6 offset:8192
v_cmp_le_i32 vcc,16,v24
v_mfma_f32_16x16x32_bf16 v[112:115],a[176:179],a[148:151],v[112:115]
ds_read_b64_tr_b16 a[82:83],v6 offset:9216
v_cndmask_b32 v198,v252,v25,vcc
v_mfma_f32_16x16x32_bf16 a[208:211],a[128:131],a[104:107],a[208:211]
ds_read_b64_tr_b16 a[84:85],v7 offset:8192
v_cmp_le_i32 vcc,17,v24
v_mfma_f32_16x16x32_bf16 a[208:211],a[176:179],a[152:155],a[208:211]
ds_read_b64_tr_b16 a[86:87],v7 offset:9216
v_cndmask_b32 v199,v252,v25,vcc
v_mfma_f32_16x16x32_bf16 a[240:243],a[128:131],a[108:111],a[240:243]
ds_read_b128 a[56:59],v1 offset:4096
v_cmp_le_i32 vcc,18,v24
v_mfma_f32_16x16x32_bf16 a[240:243],a[176:179],a[156:159],a[240:243]
ds_read_b64_tr_b16 a[88:89],v8 offset:8192
v_cndmask_b32 v224,v252,v25,vcc
v_mfma_f32_16x16x32_bf16 a[244:247],a[132:135],a[108:111],a[244:247]
)"
R"(ds_read_b64_tr_b16 a[90:91],v8 offset:9216
v_cmp_le_i32 vcc,19,v24
v_mfma_f32_16x16x32_bf16 a[244:247],a[180:183],a[156:159],a[244:247]
ds_read_b64_tr_b16 a[92:93],v9 offset:8192
v_cndmask_b32 v225,v252,v25,vcc
v_mfma_f32_16x16x32_bf16 a[212:215],a[132:135],a[104:107],a[212:215]
ds_read_b64_tr_b16 a[94:95],v9 offset:9216
v_not_b32 v28,s72
v_mfma_f32_16x16x32_bf16 a[212:215],a[180:183],a[152:155],a[212:215]
ds_read_b128 a[60:63],v1 offset:6144
v_not_b32 v29,s72
v_mfma_f32_16x16x32_bf16 v[116:119],a[132:135],a[100:103],v[116:119]
v_not_b32 v30,s72
v_not_b32 v31,s72
v_mfma_f32_16x16x32_bf16 v[116:119],a[180:183],a[148:151],v[116:119]
v_not_b32 v32,s72
v_not_b32 v33,s72
v_mfma_f32_16x16x32_bf16 v[84:87],a[132:135],a[96:99],v[84:87]
v_not_b32 v34,s72
v_not_b32 v35,s72
v_mfma_f32_16x16x32_bf16 v[84:87],a[180:183],a[144:147],v[84:87]
v_and_b32 v36,0x70,v18
v_mfma_f32_16x16x32_bf16 v[88:91],a[136:139],a[96:99],v[88:91]
v_mfma_f32_16x16x32_bf16 v[88:91],a[184:187],a[144:147],v[88:91]
)"
R"(v_add_u32 v0,-32768,v0
v_add_u32 v1,-32768,v1
v_mfma_f32_16x16x32_bf16 v[120:123],a[136:139],a[100:103],v[120:123]
v_add_u32 v2,32768,v2
v_mfma_f32_16x16x32_bf16 v[120:123],a[184:187],a[148:151],v[120:123]
v_add_u32 v3,32768,v3
v_add_u32 v4,32768,v4
v_mfma_f32_16x16x32_bf16 a[216:219],a[136:139],a[104:107],a[216:219]
v_add_u32 v5,32768,v5
v_mfma_f32_16x16x32_bf16 a[216:219],a[184:187],a[152:155],a[216:219]
v_add_u32 v6,32768,v6
v_add_u32 v7,32768,v7
v_mfma_f32_16x16x32_bf16 a[248:251],a[136:139],a[108:111],a[248:251]
v_add_u32 v8,32768,v8
v_mfma_f32_16x16x32_bf16 a[248:251],a[184:187],a[156:159],a[248:251]
v_add_u32 v9,32768,v9
v_mfma_f32_16x16x32_bf16 a[252:255],a[140:143],a[108:111],a[252:255]
v_mfma_f32_16x16x32_bf16 a[252:255],a[188:191],a[156:159],a[252:255]
v_mfma_f32_16x16x32_bf16 a[220:223],a[140:143],a[104:107],a[220:223]
v_mfma_f32_16x16x32_bf16 a[220:223],a[188:191],a[152:155],a[220:223]
v_mfma_f32_16x16x32_bf16 v[124:127],a[140:143],a[100:103],v[124:127]
v_mfma_f32_16x16x32_bf16 v[124:127],a[188:191],a[148:151],v[124:127]
v_mfma_f32_16x16x32_bf16 v[92:95],a[140:143],a[96:99],v[92:95]
s_waitcnt lgkmcnt(0)
)"
R"(v_mfma_f32_16x16x32_bf16 v[92:95],a[188:191],a[144:147],v[92:95]
v_mfma_f32_16x16x32_bf16 v[64:67],a[16:19],a[0:3],v[64:67]
s_waitcnt vmcnt(0)
s_barrier
v_mfma_f32_16x16x32_bf16 v[64:67],a[64:67],a[48:51],v[64:67]
s_lshl_b32 s99,s73,7
s_add_u32 s99,s99,20480
ds_read_b64_tr_b16 a[112:113],v2 offset:0
v_mfma_f32_16x16x32_bf16 v[96:99],a[16:19],a[4:7],v[96:99]
s_mov_b32 s87,32768
s_cmp_lg_u32 %2,0
s_cbranch_scc1 gm_KB_w31
buffer_load_dwordx4 v[28:31],v36,s[4:7],s99 offen sc1
gm_KB_w31:
v_mfma_f32_16x16x32_bf16 v[96:99],a[64:67],a[52:55],v[96:99]
s_cmp_lg_u32 %2,0
s_cbranch_scc1 gm_KB_w32
buffer_load_dwordx4 v[32:35],v18,s[4:7],s87 offen sc1
gm_KB_w32:
v_mfma_f32_16x16x32_bf16 a[192:195],a[16:19],a[8:11],a[192:195]
ds_read_b64_tr_b16 a[114:115],v2 offset:1024
v_cmp_le_i32 vcc,32,v24
v_mfma_f32_16x16x32_bf16 a[192:195],a[64:67],a[56:59],a[192:195]
ds_read_b64_tr_b16 a[116:117],v3 offset:0
)"
R"(v_cndmask_b32 v226,v252,v25,vcc
v_mfma_f32_16x16x32_bf16 a[224:227],a[16:19],a[12:15],a[224:227]
ds_read_b64_tr_b16 a[118:119],v3 offset:1024
v_cmp_le_i32 vcc,33,v24
v_mfma_f32_16x16x32_bf16 a[224:227],a[64:67],a[60:63],a[224:227]
ds_read_b128 a[96:99],v0 offset:0
v_cndmask_b32 v227,v252,v25,vcc
v_mfma_f32_16x16x32_bf16 a[228:231],a[20:23],a[12:15],a[228:231]
ds_read_b64_tr_b16 a[120:121],v4 offset:0
v_cmp_le_i32 vcc,34,v24
v_mfma_f32_16x16x32_bf16 a[228:231],a[68:71],a[60:63],a[228:231]
ds_read_b64_tr_b16 a[122:123],v4 offset:1024
v_cndmask_b32 v245,v252,v25,vcc
v_mfma_f32_16x16x32_bf16 a[196:199],a[20:23],a[8:11],a[196:199]
ds_read_b64_tr_b16 a[124:125],v5 offset:0
v_cmp_le_i32 vcc,35,v24
v_mfma_f32_16x16x32_bf16 a[196:199],a[68:71],a[56:59],a[196:199]
ds_read_b64_tr_b16 a[126:127],v5 offset:1024
v_cndmask_b32 v246,v252,v25,vcc
v_mfma_f32_16x16x32_bf16 v[100:103],a[20:23],a[4:7],v[100:103]
ds_read_b128 a[100:103],v0 offset:2048
v_cmp_le_i32 vcc,48,v24
v_mfma_f32_16x16x32_bf16 v[100:103],a[68:71],a[52:55],v[100:103]
ds_read_b64_tr_b16 a[128:129],v6 offset:0
)"
R"(v_cndmask_b32 v247,v252,v25,vcc
v_mfma_f32_16x16x32_bf16 v[68:71],a[20:23],a[0:3],v[68:71]
ds_read_b64_tr_b16 a[130:131],v6 offset:1024
v_cmp_le_i32 vcc,49,v24
v_mfma_f32_16x16x32_bf16 v[68:71],a[68:71],a[48:51],v[68:71]
ds_read_b64_tr_b16 a[132:133],v7 offset:0
v_cndmask_b32 v249,v252,v25,vcc
v_mfma_f32_16x16x32_bf16 v[72:75],a[24:27],a[0:3],v[72:75]
ds_read_b64_tr_b16 a[134:135],v7 offset:1024
v_cmp_le_i32 vcc,50,v24
v_mfma_f32_16x16x32_bf16 v[72:75],a[72:75],a[48:51],v[72:75]
ds_read_b128 a[104:107],v0 offset:4096
v_cndmask_b32 v250,v252,v25,vcc
v_mfma_f32_16x16x32_bf16 v[104:107],a[24:27],a[4:7],v[104:107]
ds_read_b64_tr_b16 a[136:137],v8 offset:0
v_cmp_le_i32 vcc,51,v24
v_mfma_f32_16x16x32_bf16 v[104:107],a[72:75],a[52:55],v[104:107]
ds_read_b64_tr_b16 a[138:139],v8 offset:1024
v_cndmask_b32 v251,v252,v25,vcc
v_mfma_f32_16x16x32_bf16 a[200:203],a[24:27],a[8:11],a[200:203]
ds_read_b64_tr_b16 a[140:141],v9 offset:0
v_mov_b32 v208,0
v_mfma_f32_16x16x32_bf16 a[200:203],a[72:75],a[56:59],a[200:203]
ds_read_b64_tr_b16 a[142:143],v9 offset:1024
)"
R"(v_mov_b32 v209,0
v_mfma_f32_16x16x32_bf16 a[232:235],a[24:27],a[12:15],a[232:235]
ds_read_b128 a[108:111],v0 offset:6144
v_mov_b32 v210,0
v_mfma_f32_16x16x32_bf16 a[232:235],a[72:75],a[60:63],a[232:235]
ds_read_b64_tr_b16 a[160:161],v2 offset:8192
v_mov_b32 v211,0
v_mfma_f32_16x16x32_bf16 a[236:239],a[28:31],a[12:15],a[236:239]
ds_read_b64_tr_b16 a[162:163],v2 offset:9216
v_mov_b32 v212,0
v_mfma_f32_16x16x32_bf16 a[236:239],a[76:79],a[60:63],a[236:239]
ds_read_b64_tr_b16 a[164:165],v3 offset:8192
v_mov_b32 v213,0
v_mfma_f32_16x16x32_bf16 a[204:207],a[28:31],a[8:11],a[204:207]
ds_read_b64_tr_b16 a[166:167],v3 offset:9216
v_mov_b32 v214,0
v_mfma_f32_16x16x32_bf16 a[204:207],a[76:79],a[56:59],a[204:207]
ds_read_b128 a[144:147],v1 offset:0
v_mov_b32 v215,0
v_mfma_f32_16x16x32_bf16 v[108:111],a[28:31],a[4:7],v[108:111]
ds_read_b64_tr_b16 a[168:169],v4 offset:8192
v_mov_b32 v216,0
v_mfma_f32_16x16x32_bf16 v[108:111],a[76:79],a[52:55],v[108:111]
ds_read_b64_tr_b16 a[170:171],v4 offset:9216
)"
R"(v_mov_b32 v217,0
v_mfma_f32_16x16x32_bf16 v[76:79],a[28:31],a[0:3],v[76:79]
ds_read_b64_tr_b16 a[172:173],v5 offset:8192
v_mov_b32 v218,0
v_mfma_f32_16x16x32_bf16 v[76:79],a[76:79],a[48:51],v[76:79]
ds_read_b64_tr_b16 a[174:175],v5 offset:9216
v_mov_b32 v219,0
v_mfma_f32_16x16x32_bf16 v[80:83],a[32:35],a[0:3],v[80:83]
ds_read_b128 a[148:151],v1 offset:2048
v_mov_b32 v220,0
v_mfma_f32_16x16x32_bf16 v[80:83],a[80:83],a[48:51],v[80:83]
v_mov_b32 v221,0
v_mov_b32 v222,0
v_mfma_f32_16x16x32_bf16 v[112:115],a[32:35],a[4:7],v[112:115]
ds_read_b64_tr_b16 a[176:177],v6 offset:8192
v_mov_b32 v223,0
v_mfma_f32_16x16x32_bf16 v[112:115],a[80:83],a[52:55],v[112:115]
ds_read_b64_tr_b16 a[178:179],v6 offset:9216
v_mov_b32 v228,0x3f803f80
v_mfma_f32_16x16x32_bf16 a[208:211],a[32:35],a[8:11],a[208:211]
ds_read_b64_tr_b16 a[180:181],v7 offset:8192
v_mov_b32 v229,0x3f803f80
v_mfma_f32_16x16x32_bf16 a[208:211],a[80:83],a[56:59],a[208:211]
ds_read_b64_tr_b16 a[182:183],v7 offset:9216
)"
R"(v_mov_b32 v230,0x3f803f80
v_mfma_f32_16x16x32_bf16 a[240:243],a[32:35],a[12:15],a[240:243]
ds_read_b128 a[152:155],v1 offset:4096
v_mov_b32 v231,0x3f803f80
v_mfma_f32_16x16x32_bf16 a[240:243],a[80:83],a[60:63],a[240:243]
ds_read_b64_tr_b16 a[184:185],v8 offset:8192
v_lshl_add_u32 v58,s15,10,v18
v_mfma_f32_16x16x32_bf16 a[244:247],a[36:39],a[12:15],a[244:247]
ds_read_b64_tr_b16 a[186:187],v8 offset:9216
v_lshl_add_u32 v244,s15,6,v253
v_mfma_f32_16x16x32_bf16 a[244:247],a[84:87],a[60:63],a[244:247]
ds_read_b64_tr_b16 a[188:189],v9 offset:8192
v_mfma_f32_16x16x32_bf16 a[212:215],a[36:39],a[8:11],a[212:215]
ds_read_b64_tr_b16 a[190:191],v9 offset:9216
v_mfma_f32_16x16x32_bf16 a[212:215],a[84:87],a[56:59],a[212:215]
ds_read_b128 a[156:159],v1 offset:6144
v_mfma_f32_16x16x32_bf16 v[116:119],a[36:39],a[4:7],v[116:119]
v_mfma_f32_16x16x32_bf16 v[116:119],a[84:87],a[52:55],v[116:119]
v_mfma_f32_16x16x32_bf16 v[84:87],a[36:39],a[0:3],v[84:87]
v_mfma_f32_16x16x32_bf16 v[84:87],a[84:87],a[48:51],v[84:87]
v_mfma_f32_16x16x32_bf16 v[88:91],a[40:43],a[0:3],v[88:91]
v_mfma_f32_16x16x32_bf16 v[88:91],a[88:91],a[48:51],v[88:91]
v_mfma_f32_16x16x32_bf16 v[120:123],a[40:43],a[4:7],v[120:123]
v_mfma_f32_16x16x32_bf16 v[120:123],a[88:91],a[52:55],v[120:123]
)"
R"(v_mfma_f32_16x16x32_bf16 a[216:219],a[40:43],a[8:11],a[216:219]
v_mfma_f32_16x16x32_bf16 a[216:219],a[88:91],a[56:59],a[216:219]
v_mfma_f32_16x16x32_bf16 a[248:251],a[40:43],a[12:15],a[248:251]
v_mfma_f32_16x16x32_bf16 a[248:251],a[88:91],a[60:63],a[248:251]
v_mfma_f32_16x16x32_bf16 a[252:255],a[44:47],a[12:15],a[252:255]
s_branch gm_KB_sk13
.p2align 12
gm_KB_pg13:
s_setpc_b64 s[100:101]
gm_KB_sk13:
v_mfma_f32_16x16x32_bf16 a[252:255],a[92:95],a[60:63],a[252:255]
v_mfma_f32_16x16x32_bf16 a[220:223],a[44:47],a[8:11],a[220:223]
v_mfma_f32_16x16x32_bf16 a[220:223],a[92:95],a[56:59],a[220:223]
v_mfma_f32_16x16x32_bf16 v[124:127],a[44:47],a[4:7],v[124:127]
v_mfma_f32_16x16x32_bf16 v[124:127],a[92:95],a[52:55],v[124:127]
v_mfma_f32_16x16x32_bf16 v[92:95],a[44:47],a[0:3],v[92:95]
s_waitcnt lgkmcnt(0)
v_mfma_f32_16x16x32_bf16 v[92:95],a[92:95],a[48:51],v[92:95]
s_cmp_lg_u32 %2,0
s_cbranch_scc1 gm_KB_gotg
s_mov_b32 s98,0x10000
gm_KB_pollg:
v_xor_b32 v22,s72,v32
v_xor_b32 v23,s72,v33
)"
R"(v_or_b32 v22,v22,v23
v_xor_b32 v23,s72,v34
v_or_b32 v22,v22,v23
v_xor_b32 v23,s72,v35
v_or_b32 v22,v22,v23
v_cmp_ne_u32 vcc,0,v22
s_nop 1
s_cmp_eq_u64 vcc,0
s_cbranch_scc1 gm_KB_gotg
s_sub_u32 s98,s98,1
s_cmp_eq_u32 s98,0
s_cbranch_scc1 gm_KB_gotg
buffer_load_dwordx4 v[32:35],v18,s[4:7],s87 offen sc1
s_waitcnt vmcnt(0)
s_branch gm_KB_pollg
gm_KB_gotg:
s_cmp_lg_u32 %2,0
s_cbranch_scc1 gm_KB_gotkv
s_mov_b32 s98,0x10000
gm_KB_pollkv:
v_xor_b32 v22,s72,v28
v_xor_b32 v23,s72,v29
v_or_b32 v22,v22,v23
v_xor_b32 v23,s72,v30
)"
R"(v_or_b32 v22,v22,v23
v_xor_b32 v23,s72,v31
v_or_b32 v22,v22,v23
v_cmp_ne_u32 vcc,0,v22
s_nop 1
s_cmp_eq_u64 vcc,0
s_cbranch_scc1 gm_KB_gotkv
s_sub_u32 s98,s98,1
s_cmp_eq_u32 s98,0
s_cbranch_scc1 gm_KB_gotkv
buffer_load_dwordx4 v[28:31],v36,s[4:7],s99 offen sc1
s_waitcnt vmcnt(0)
s_branch gm_KB_pollkv
gm_KB_gotkv:
v_mfma_f32_16x16x32_bf16 v[64:67],a[112:115],a[96:99],v[64:67]
s_barrier
v_mfma_f32_16x16x32_bf16 v[64:67],a[160:163],a[144:147],v[64:67]
s_add_u32 m0,s64,0
v_mfma_f32_16x16x32_bf16 v[96:99],a[112:115],a[100:103],v[96:99]
v_mfma_f32_16x16x32_bf16 v[96:99],a[160:163],a[148:151],v[96:99]
buffer_load_dwordx4 v18,s[36:39],s58 offen offset:0 sc1 lds
v_mfma_f32_16x16x32_bf16 a[192:195],a[112:115],a[104:107],a[192:195]
v_mfma_f32_16x16x32_bf16 a[192:195],a[160:163],a[152:155],a[192:195]
buffer_load_dwordx4 v18,s[36:39],s58 offen offset:1024 sc1 lds
)"
R"(v_mfma_f32_16x16x32_bf16 a[224:227],a[112:115],a[108:111],a[224:227]
v_mfma_f32_16x16x32_bf16 a[224:227],a[160:163],a[156:159],a[224:227]
v_mfma_f32_16x16x32_bf16 a[228:231],a[116:119],a[108:111],a[228:231]
buffer_load_dwordx4 v18,s[36:39],s58 offen offset:2048 sc1 lds
v_mfma_f32_16x16x32_bf16 a[228:231],a[164:167],a[156:159],a[228:231]
v_mfma_f32_16x16x32_bf16 a[196:199],a[116:119],a[104:107],a[196:199]
v_mfma_f32_16x16x32_bf16 a[196:199],a[164:167],a[152:155],a[196:199]
buffer_load_dwordx4 v18,s[36:39],s58 offen offset:3072 sc1 lds
s_add_u32 m0,s64,4096
v_mfma_f32_16x16x32_bf16 v[100:103],a[116:119],a[100:103],v[100:103]
s_add_u32 s75,s58,4096
v_mfma_f32_16x16x32_bf16 v[100:103],a[164:167],a[148:151],v[100:103]
v_mfma_f32_16x16x32_bf16 v[68:71],a[116:119],a[96:99],v[68:71]
buffer_load_dwordx4 v18,s[36:39],s75 offen offset:0 sc1 lds
v_mfma_f32_16x16x32_bf16 v[68:71],a[164:167],a[144:147],v[68:71]
v_mfma_f32_16x16x32_bf16 v[72:75],a[120:123],a[96:99],v[72:75]
v_mfma_f32_16x16x32_bf16 v[72:75],a[168:171],a[144:147],v[72:75]
buffer_load_dwordx4 v18,s[36:39],s75 offen offset:1024 sc1 lds
v_mfma_f32_16x16x32_bf16 v[104:107],a[120:123],a[100:103],v[104:107]
v_mfma_f32_16x16x32_bf16 v[104:107],a[168:171],a[148:151],v[104:107]
v_mfma_f32_16x16x32_bf16 a[200:203],a[120:123],a[104:107],a[200:203]
buffer_load_dwordx4 v18,s[36:39],s75 offen offset:2048 sc1 lds
v_mfma_f32_16x16x32_bf16 a[200:203],a[168:171],a[152:155],a[200:203]
v_mfma_f32_16x16x32_bf16 a[232:235],a[120:123],a[108:111],a[232:235]
)"
R"(buffer_load_dwordx4 v18,s[36:39],s75 offen offset:3072 sc1 lds
v_mfma_f32_16x16x32_bf16 a[232:235],a[168:171],a[156:159],a[232:235]
v_mfma_f32_16x16x32_bf16 a[236:239],a[124:127],a[108:111],a[236:239]
v_mfma_f32_16x16x32_bf16 a[236:239],a[172:175],a[156:159],a[236:239]
buffer_load_dword v232,v253,s[4:7],s13 offen sc1
v_mfma_f32_16x16x32_bf16 a[204:207],a[124:127],a[104:107],a[204:207]
v_mfma_f32_16x16x32_bf16 a[204:207],a[172:175],a[152:155],a[204:207]
v_mfma_f32_16x16x32_bf16 v[108:111],a[124:127],a[100:103],v[108:111]
buffer_load_dword v233,v244,s[4:7],s13 offen sc1
v_mfma_f32_16x16x32_bf16 v[108:111],a[172:175],a[148:151],v[108:111]
v_mfma_f32_16x16x32_bf16 v[76:79],a[124:127],a[96:99],v[76:79]
v_mfma_f32_16x16x32_bf16 v[76:79],a[172:175],a[144:147],v[76:79]
buffer_load_dword v234,v253,s[4:7],s14 offen sc1
v_mfma_f32_16x16x32_bf16 v[80:83],a[128:131],a[96:99],v[80:83]
v_mfma_f32_16x16x32_bf16 v[80:83],a[176:179],a[144:147],v[80:83]
v_mfma_f32_16x16x32_bf16 v[112:115],a[128:131],a[100:103],v[112:115]
buffer_load_dword v235,v244,s[4:7],s14 offen sc1
s_add_u32 m0,s66,0
v_mfma_f32_16x16x32_bf16 v[112:115],a[176:179],a[148:151],v[112:115]
v_mfma_f32_16x16x32_bf16 a[208:211],a[128:131],a[104:107],a[208:211]
v_mfma_f32_16x16x32_bf16 a[208:211],a[176:179],a[152:155],a[208:211]
buffer_load_dwordx4 v18,s[48:51],s60 offen offset:0 sc1 lds
v_mfma_f32_16x16x32_bf16 a[240:243],a[128:131],a[108:111],a[240:243]
v_mfma_f32_16x16x32_bf16 a[240:243],a[176:179],a[156:159],a[240:243]
)"
R"(buffer_load_dwordx4 v18,s[48:51],s60 offen offset:1024 sc1 lds
v_mfma_f32_16x16x32_bf16 a[244:247],a[132:135],a[108:111],a[244:247]
v_mfma_f32_16x16x32_bf16 a[244:247],a[180:183],a[156:159],a[244:247]
v_mfma_f32_16x16x32_bf16 a[212:215],a[132:135],a[104:107],a[212:215]
buffer_load_dwordx4 v18,s[48:51],s60 offen offset:2048 sc1 lds
v_mfma_f32_16x16x32_bf16 a[212:215],a[180:183],a[152:155],a[212:215]
v_mfma_f32_16x16x32_bf16 v[116:119],a[132:135],a[100:103],v[116:119]
v_mfma_f32_16x16x32_bf16 v[116:119],a[180:183],a[148:151],v[116:119]
buffer_load_dwordx4 v18,s[48:51],s60 offen offset:3072 sc1 lds
s_add_u32 m0,s66,4096
v_mfma_f32_16x16x32_bf16 v[84:87],a[132:135],a[96:99],v[84:87]
s_add_u32 s75,s60,4096
v_mfma_f32_16x16x32_bf16 v[84:87],a[180:183],a[144:147],v[84:87]
v_mfma_f32_16x16x32_bf16 v[88:91],a[136:139],a[96:99],v[88:91]
buffer_load_dwordx4 v18,s[48:51],s75 offen offset:0 sc1 lds
v_mfma_f32_16x16x32_bf16 v[88:91],a[184:187],a[144:147],v[88:91]
v_mfma_f32_16x16x32_bf16 v[120:123],a[136:139],a[100:103],v[120:123]
v_mfma_f32_16x16x32_bf16 v[120:123],a[184:187],a[148:151],v[120:123]
buffer_load_dwordx4 v18,s[48:51],s75 offen offset:1024 sc1 lds
v_mfma_f32_16x16x32_bf16 a[216:219],a[136:139],a[104:107],a[216:219]
v_mfma_f32_16x16x32_bf16 a[216:219],a[184:187],a[152:155],a[216:219]
v_mfma_f32_16x16x32_bf16 a[248:251],a[136:139],a[108:111],a[248:251]
buffer_load_dwordx4 v18,s[48:51],s75 offen offset:2048 sc1 lds
v_mfma_f32_16x16x32_bf16 a[248:251],a[184:187],a[156:159],a[248:251]
)"
R"(v_mfma_f32_16x16x32_bf16 a[252:255],a[140:143],a[108:111],a[252:255]
v_mfma_f32_16x16x32_bf16 a[252:255],a[188:191],a[156:159],a[252:255]
buffer_load_dwordx4 v18,s[48:51],s75 offen offset:3072 sc1 lds
v_mfma_f32_16x16x32_bf16 a[220:223],a[140:143],a[104:107],a[220:223]
v_mfma_f32_16x16x32_bf16 a[220:223],a[188:191],a[152:155],a[220:223]
v_mfma_f32_16x16x32_bf16 v[124:127],a[140:143],a[100:103],v[124:127]
v_mfma_f32_16x16x32_bf16 v[124:127],a[188:191],a[148:151],v[124:127]
v_mfma_f32_16x16x32_bf16 v[92:95],a[140:143],a[96:99],v[92:95]
s_waitcnt lgkmcnt(0)
v_mfma_f32_16x16x32_bf16 v[92:95],a[188:191],a[144:147],v[92:95]
s_add_u32 s58,s58,0x8000
s_cmp_eq_u32 s58,s61
s_cselect_b32 s58,0,s58
s_cselect_b32 s61,-1,s61
s_add_u32 m0,s65,0
s_nop 0
buffer_load_dwordx4 v18,s[36:39],s58 offen offset:0 sc1 lds
v_pk_mul_f32 v[64:65],v[64:65],s[80:81]
v_pk_mul_f32 v[66:67],v[66:67],s[80:81]
v_pk_mul_f32 v[68:69],v[68:69],s[80:81]
v_pk_mul_f32 v[70:71],v[70:71],s[80:81]
v_cvt_pk_bf16_f32 v64,v64,v65
v_cvt_pk_bf16_f32 v65,v66,v67
v_cvt_pk_bf16_f32 v66,v68,v69
)"
R"(v_cvt_pk_bf16_f32 v67,v70,v71
v_accvgpr_write_b32 a0,v64
v_accvgpr_write_b32 a1,v65
v_accvgpr_write_b32 a2,v66
v_accvgpr_write_b32 a3,v67
v_pk_mul_f32 v[72:73],v[72:73],s[80:81]
v_pk_mul_f32 v[74:75],v[74:75],s[80:81]
v_pk_mul_f32 v[76:77],v[76:77],s[80:81]
v_pk_mul_f32 v[78:79],v[78:79],s[80:81]
buffer_load_dwordx4 v18,s[36:39],s58 offen offset:1024 sc1 lds
v_cvt_pk_bf16_f32 v72,v72,v73
v_cvt_pk_bf16_f32 v73,v74,v75
v_cvt_pk_bf16_f32 v74,v76,v77
v_cvt_pk_bf16_f32 v75,v78,v79
v_accvgpr_write_b32 a4,v72
v_accvgpr_write_b32 a5,v73
v_accvgpr_write_b32 a6,v74
v_accvgpr_write_b32 a7,v75
v_pk_mul_f32 v[80:81],v[80:81],s[80:81]
v_pk_mul_f32 v[82:83],v[82:83],s[80:81]
v_pk_mul_f32 v[84:85],v[84:85],s[80:81]
v_pk_mul_f32 v[86:87],v[86:87],s[80:81]
v_cvt_pk_bf16_f32 v80,v80,v81
v_cvt_pk_bf16_f32 v81,v82,v83
)"
R"(v_cvt_pk_bf16_f32 v82,v84,v85
v_cvt_pk_bf16_f32 v83,v86,v87
buffer_load_dwordx4 v18,s[36:39],s58 offen offset:2048 sc1 lds
v_accvgpr_write_b32 a8,v80
v_accvgpr_write_b32 a9,v81
v_accvgpr_write_b32 a10,v82
v_accvgpr_write_b32 a11,v83
v_pk_mul_f32 v[88:89],v[88:89],s[80:81]
v_pk_mul_f32 v[90:91],v[90:91],s[80:81]
v_pk_mul_f32 v[92:93],v[92:93],s[80:81]
v_pk_mul_f32 v[94:95],v[94:95],s[80:81]
v_cvt_pk_bf16_f32 v88,v88,v89
v_cvt_pk_bf16_f32 v89,v90,v91
v_cvt_pk_bf16_f32 v90,v92,v93
v_cvt_pk_bf16_f32 v91,v94,v95
v_accvgpr_write_b32 a12,v88
v_accvgpr_write_b32 a13,v89
v_accvgpr_write_b32 a14,v90
v_accvgpr_write_b32 a15,v91
buffer_load_dwordx4 v18,s[36:39],s58 offen offset:3072 sc1 lds
v_pk_mul_f32 v[96:97],v[96:97],s[80:81]
v_pk_mul_f32 v[98:99],v[98:99],s[80:81]
v_pk_mul_f32 v[100:101],v[100:101],s[80:81]
v_pk_mul_f32 v[102:103],v[102:103],s[80:81]
)"
R"(v_cvt_pk_bf16_f32 v96,v96,v97
v_cvt_pk_bf16_f32 v97,v98,v99
v_cvt_pk_bf16_f32 v98,v100,v101
v_cvt_pk_bf16_f32 v99,v102,v103
v_accvgpr_write_b32 a16,v96
v_accvgpr_write_b32 a17,v97
v_accvgpr_write_b32 a18,v98
v_accvgpr_write_b32 a19,v99
v_pk_mul_f32 v[104:105],v[104:105],s[80:81]
v_pk_mul_f32 v[106:107],v[106:107],s[80:81]
v_pk_mul_f32 v[108:109],v[108:109],s[80:81]
v_pk_mul_f32 v[110:111],v[110:111],s[80:81]
s_add_u32 m0,s65,4096
s_add_u32 s75,s58,4096
buffer_load_dwordx4 v18,s[36:39],s75 offen offset:0 sc1 lds
v_cvt_pk_bf16_f32 v104,v104,v105
v_cvt_pk_bf16_f32 v105,v106,v107
v_cvt_pk_bf16_f32 v106,v108,v109
v_cvt_pk_bf16_f32 v107,v110,v111
v_accvgpr_write_b32 a20,v104
v_accvgpr_write_b32 a21,v105
v_accvgpr_write_b32 a22,v106
v_accvgpr_write_b32 a23,v107
v_pk_mul_f32 v[112:113],v[112:113],s[80:81]
)"
R"(v_pk_mul_f32 v[114:115],v[114:115],s[80:81]
v_pk_mul_f32 v[116:117],v[116:117],s[80:81]
v_pk_mul_f32 v[118:119],v[118:119],s[80:81]
v_cvt_pk_bf16_f32 v112,v112,v113
v_cvt_pk_bf16_f32 v113,v114,v115
v_cvt_pk_bf16_f32 v114,v116,v117
v_cvt_pk_bf16_f32 v115,v118,v119
buffer_load_dwordx4 v18,s[36:39],s75 offen offset:1024 sc1 lds
v_accvgpr_write_b32 a24,v112
v_accvgpr_write_b32 a25,v113
v_accvgpr_write_b32 a26,v114
v_accvgpr_write_b32 a27,v115
v_pk_mul_f32 v[120:121],v[120:121],s[80:81]
v_pk_mul_f32 v[122:123],v[122:123],s[80:81]
v_pk_mul_f32 v[124:125],v[124:125],s[80:81]
v_pk_mul_f32 v[126:127],v[126:127],s[80:81]
v_cvt_pk_bf16_f32 v120,v120,v121
v_cvt_pk_bf16_f32 v121,v122,v123
v_cvt_pk_bf16_f32 v122,v124,v125
v_cvt_pk_bf16_f32 v123,v126,v127
v_accvgpr_write_b32 a28,v120
v_accvgpr_write_b32 a29,v121
v_accvgpr_write_b32 a30,v122
v_accvgpr_write_b32 a31,v123
)"
R"(buffer_load_dwordx4 v18,s[36:39],s75 offen offset:2048 sc1 lds
v_accvgpr_read_b32 v64,a192
v_accvgpr_read_b32 v65,a193
v_accvgpr_read_b32 v66,a194
v_accvgpr_read_b32 v67,a195
v_accvgpr_read_b32 v68,a196
v_accvgpr_read_b32 v69,a197
v_accvgpr_read_b32 v70,a198
v_accvgpr_read_b32 v71,a199
v_pk_mul_f32 v[64:65],v[64:65],s[80:81]
v_pk_mul_f32 v[66:67],v[66:67],s[80:81]
v_pk_mul_f32 v[68:69],v[68:69],s[80:81]
v_pk_mul_f32 v[70:71],v[70:71],s[80:81]
v_cvt_pk_bf16_f32 v64,v64,v65
v_cvt_pk_bf16_f32 v65,v66,v67
v_cvt_pk_bf16_f32 v66,v68,v69
v_cvt_pk_bf16_f32 v67,v70,v71
buffer_load_dwordx4 v18,s[36:39],s75 offen offset:3072 sc1 lds
v_accvgpr_write_b32 a32,v64
v_accvgpr_write_b32 a33,v65
v_accvgpr_write_b32 a34,v66
v_accvgpr_write_b32 a35,v67
v_accvgpr_read_b32 v72,a200
v_accvgpr_read_b32 v73,a201
)"
R"(v_accvgpr_read_b32 v74,a202
v_accvgpr_read_b32 v75,a203
v_accvgpr_read_b32 v76,a204
v_accvgpr_read_b32 v77,a205
v_accvgpr_read_b32 v78,a206
v_accvgpr_read_b32 v79,a207
v_pk_mul_f32 v[72:73],v[72:73],s[80:81]
v_pk_mul_f32 v[74:75],v[74:75],s[80:81]
v_pk_mul_f32 v[76:77],v[76:77],s[80:81]
v_pk_mul_f32 v[78:79],v[78:79],s[80:81]
v_cvt_pk_bf16_f32 v72,v72,v73
v_cvt_pk_bf16_f32 v73,v74,v75
v_cvt_pk_bf16_f32 v74,v76,v77
v_cvt_pk_bf16_f32 v75,v78,v79
v_accvgpr_write_b32 a36,v72
v_accvgpr_write_b32 a37,v73
v_accvgpr_write_b32 a38,v74
v_accvgpr_write_b32 a39,v75
v_accvgpr_read_b32 v80,a208
v_accvgpr_read_b32 v81,a209
v_accvgpr_read_b32 v82,a210
v_accvgpr_read_b32 v83,a211
v_accvgpr_read_b32 v84,a212
v_accvgpr_read_b32 v85,a213
)"
R"(v_accvgpr_read_b32 v86,a214
v_accvgpr_read_b32 v87,a215
v_pk_mul_f32 v[80:81],v[80:81],s[80:81]
v_pk_mul_f32 v[82:83],v[82:83],s[80:81]
v_pk_mul_f32 v[84:85],v[84:85],s[80:81]
v_pk_mul_f32 v[86:87],v[86:87],s[80:81]
v_cvt_pk_bf16_f32 v80,v80,v81
v_cvt_pk_bf16_f32 v81,v82,v83
v_cvt_pk_bf16_f32 v82,v84,v85
v_cvt_pk_bf16_f32 v83,v86,v87
v_accvgpr_write_b32 a40,v80
v_accvgpr_write_b32 a41,v81
v_accvgpr_write_b32 a42,v82
v_accvgpr_write_b32 a43,v83
v_accvgpr_read_b32 v88,a216
v_accvgpr_read_b32 v89,a217
v_accvgpr_read_b32 v90,a218
v_accvgpr_read_b32 v91,a219
v_accvgpr_read_b32 v92,a220
v_accvgpr_read_b32 v93,a221
v_accvgpr_read_b32 v94,a222
v_accvgpr_read_b32 v95,a223
v_pk_mul_f32 v[88:89],v[88:89],s[80:81]
v_pk_mul_f32 v[90:91],v[90:91],s[80:81]
)"
R"(v_pk_mul_f32 v[92:93],v[92:93],s[80:81]
v_pk_mul_f32 v[94:95],v[94:95],s[80:81]
v_cvt_pk_bf16_f32 v88,v88,v89
v_cvt_pk_bf16_f32 v89,v90,v91
v_cvt_pk_bf16_f32 v90,v92,v93
v_cvt_pk_bf16_f32 v91,v94,v95
v_accvgpr_write_b32 a44,v88
v_accvgpr_write_b32 a45,v89
v_accvgpr_write_b32 a46,v90
v_accvgpr_write_b32 a47,v91
v_accvgpr_read_b32 v64,a224
v_accvgpr_read_b32 v65,a225
v_accvgpr_read_b32 v66,a226
v_accvgpr_read_b32 v67,a227
v_accvgpr_read_b32 v68,a228
v_accvgpr_read_b32 v69,a229
v_accvgpr_read_b32 v70,a230
v_accvgpr_read_b32 v71,a231
v_pk_mul_f32 v[64:65],v[64:65],s[80:81]
v_pk_mul_f32 v[66:67],v[66:67],s[80:81]
v_pk_mul_f32 v[68:69],v[68:69],s[80:81]
v_pk_mul_f32 v[70:71],v[70:71],s[80:81]
v_cvt_pk_bf16_f32 v64,v64,v65
v_cvt_pk_bf16_f32 v65,v66,v67
)"
R"(v_cvt_pk_bf16_f32 v66,v68,v69
v_cvt_pk_bf16_f32 v67,v70,v71
v_accvgpr_write_b32 a48,v64
v_accvgpr_write_b32 a49,v65
v_accvgpr_write_b32 a50,v66
v_accvgpr_write_b32 a51,v67
v_accvgpr_read_b32 v72,a232
v_accvgpr_read_b32 v73,a233
v_accvgpr_read_b32 v74,a234
v_accvgpr_read_b32 v75,a235
v_accvgpr_read_b32 v76,a236
v_accvgpr_read_b32 v77,a237
v_accvgpr_read_b32 v78,a238
v_accvgpr_read_b32 v79,a239
v_pk_mul_f32 v[72:73],v[72:73],s[80:81]
v_pk_mul_f32 v[74:75],v[74:75],s[80:81]
v_pk_mul_f32 v[76:77],v[76:77],s[80:81]
v_pk_mul_f32 v[78:79],v[78:79],s[80:81]
v_cvt_pk_bf16_f32 v72,v72,v73
v_cvt_pk_bf16_f32 v73,v74,v75
v_cvt_pk_bf16_f32 v74,v76,v77
v_cvt_pk_bf16_f32 v75,v78,v79
v_accvgpr_write_b32 a52,v72
v_accvgpr_write_b32 a53,v73
)"
R"(v_accvgpr_write_b32 a54,v74
v_accvgpr_write_b32 a55,v75
v_accvgpr_read_b32 v80,a240
v_accvgpr_read_b32 v81,a241
v_accvgpr_read_b32 v82,a242
v_accvgpr_read_b32 v83,a243
v_accvgpr_read_b32 v84,a244
v_accvgpr_read_b32 v85,a245
v_accvgpr_read_b32 v86,a246
v_accvgpr_read_b32 v87,a247
v_pk_mul_f32 v[80:81],v[80:81],s[80:81]
v_pk_mul_f32 v[82:83],v[82:83],s[80:81]
v_pk_mul_f32 v[84:85],v[84:85],s[80:81]
v_pk_mul_f32 v[86:87],v[86:87],s[80:81]
v_cvt_pk_bf16_f32 v80,v80,v81
v_cvt_pk_bf16_f32 v81,v82,v83
v_cvt_pk_bf16_f32 v82,v84,v85
v_cvt_pk_bf16_f32 v83,v86,v87
v_accvgpr_write_b32 a56,v80
v_accvgpr_write_b32 a57,v81
v_accvgpr_write_b32 a58,v82
v_accvgpr_write_b32 a59,v83
v_accvgpr_read_b32 v88,a248
v_accvgpr_read_b32 v89,a249
)"
R"(v_accvgpr_read_b32 v90,a250
v_accvgpr_read_b32 v91,a251
v_accvgpr_read_b32 v92,a252
v_accvgpr_read_b32 v93,a253
v_accvgpr_read_b32 v94,a254
v_accvgpr_read_b32 v95,a255
v_pk_mul_f32 v[88:89],v[88:89],s[80:81]
v_pk_mul_f32 v[90:91],v[90:91],s[80:81]
v_pk_mul_f32 v[92:93],v[92:93],s[80:81]
v_pk_mul_f32 v[94:95],v[94:95],s[80:81]
v_cvt_pk_bf16_f32 v88,v88,v89
v_cvt_pk_bf16_f32 v89,v90,v91
v_cvt_pk_bf16_f32 v90,v92,v93
v_cvt_pk_bf16_f32 v91,v94,v95
v_accvgpr_write_b32 a60,v88
v_accvgpr_write_b32 a61,v89
v_accvgpr_write_b32 a62,v90
v_accvgpr_write_b32 a63,v91
s_waitcnt vmcnt(20)
s_barrier
ds_read_b128 a[64:67],v240 offset:0
ds_read_b128 a[68:71],v240 offset:1024
ds_read_b128 a[72:75],v240 offset:2048
ds_read_b128 a[76:79],v240 offset:3072
)"
R"(ds_read_b128 a[80:83],v240 offset:4096
ds_read_b128 a[84:87],v240 offset:5120
ds_read_b128 a[88:91],v240 offset:6144
ds_read_b128 a[92:95],v240 offset:7168
s_mov_b32 s87,0
s_mov_b32 s12,0x41000000
s_mov_b32 s68,0
v_cmp_le_i32 vcc,18,v26
s_nop 1
v_cndmask_b32 v22,v252,v25,vcc
v_cmp_le_i32 vcc,19,v26
s_nop 1
v_cndmask_b32 v23,v252,v25,vcc
s_cmp_lg_u32 s55,0
s_cbranch_scc1 gm_K2_nm0
v_min_f32 v208,v208,v12
v_min_f32 v209,v209,v13
v_min_f32 v210,v210,v16
v_min_f32 v211,v211,v17
v_min_f32 v212,v212,v20
v_min_f32 v213,v213,v21
v_min_f32 v214,v214,v22
v_min_f32 v215,v215,v23
gm_K2_nm0:
)"
R"(s_waitcnt lgkmcnt(0)
v_mfma_f32_16x16x32_bf16 v[128:131],a[64:67],a[0:3],v[208:211]
s_cmp_lt_u32 s70,2
s_cselect_b64 s[92:93],s[26:27],s[8:9]
s_cselect_b64 s[94:95],s[28:29],s[10:11]
s_cselect_b32 s14,13,15
ds_read_b128 a[96:99],v240 offset:8192
v_min_u32 v254,s14,v19
v_mfma_f32_16x16x32_bf16 v[128:131],a[68:71],a[4:7],v[128:131]
v_lshlrev_b32 v254,7,v254
v_mfma_f32_16x16x32_bf16 v[136:139],a[64:67],a[16:19],v[216:219]
s_add_u32 s14,s14,1
ds_read_b128 a[100:103],v240 offset:9216
s_lshl_b32 s14,s14,7
s_bitcmp1_b32 s70,0
s_cselect_b64 s[92:93],s[94:95],s[92:93]
v_mfma_f32_16x16x32_bf16 v[136:139],a[68:71],a[20:23],v[136:139]
s_cmp_eq_u32 s70,2
s_cselect_b32 s83,32,28
s_lshl_b32 s83,s83,10
v_mfma_f32_16x16x32_bf16 v[128:131],a[72:75],a[8:11],v[128:131]
s_mul_i32 s84,%1,s83
ds_read_b128 a[104:107],v240 offset:10240
s_add_u32 s92,s92,s84
)"
R"(s_addc_u32 s93,s93,0
s_and_b32 s93,s93,0xffff
v_mfma_f32_16x16x32_bf16 v[128:131],a[76:79],a[12:15],v[128:131]
s_mov_b32 s94,s83
s_mov_b32 s95,0x20000
s_sub_u32 s13,0,s14
v_mfma_f32_16x16x32_bf16 v[136:139],a[72:75],a[24:27],v[136:139]
s_mov_b32 s60,s58
ds_read_b128 a[108:111],v240 offset:11264
s_add_u32 s58,s58,0x8000
s_cmp_eq_u32 s58,s61
s_cselect_b32 s58,0,s58
s_cselect_b32 s61,-1,s61
v_mfma_f32_16x16x32_bf16 v[136:139],a[76:79],a[28:31],v[136:139]
s_xor_b32 s66,s66,0x38000
s_mov_b32 s82,s63
s_mov_b32 s63,s64
v_mfma_f32_16x16x32_bf16 v[132:135],a[80:83],a[0:3],v[212:215]
s_mov_b32 s64,s65
ds_read_b128 a[112:115],v240 offset:12288
s_mov_b32 s65,s82
s_sub_u32 s62,s64,s63
v_mfma_f32_16x16x32_bf16 v[132:135],a[84:87],a[4:7],v[132:135]
s_add_u32 s13,s13,s14
)"
R"(v_mfma_f32_16x16x32_bf16 v[140:143],a[80:83],a[16:19],v[220:223]
ds_read_b128 a[116:119],v240 offset:13312
v_mfma_f32_16x16x32_bf16 v[140:143],a[84:87],a[20:23],v[140:143]
v_mfma_f32_16x16x32_bf16 v[132:135],a[88:91],a[8:11],v[132:135]
ds_read_b128 a[120:123],v240 offset:14336
v_mfma_f32_16x16x32_bf16 v[132:135],a[92:95],a[12:15],v[132:135]
v_mfma_f32_16x16x32_bf16 v[140:143],a[88:91],a[24:27],v[140:143]
ds_read_b128 a[124:127],v240 offset:15360
v_mfma_f32_16x16x32_bf16 v[140:143],a[92:95],a[28:31],v[140:143]
s_waitcnt lgkmcnt(8)
v_max3_f32 v248,v128,v129,v130
v_max3_f32 v248,v248,v131,v136
v_max3_f32 v248,v248,v137,v138
s_nop 0
v_max3_f32 v248,v248,v139,v132
v_max3_f32 v248,v248,v133,v134
s_nop 0
v_max3_f32 v248,v248,v135,v140
v_max3_f32 v248,v248,v141,v142
v_max_f32 v248,v248,v143
v_cmp_lt_f32 vcc,s12,v248
s_waitcnt lgkmcnt(0)
s_lshl_b32 s15,s55,7
s_lshl_b32 s83,s53,2
)"
R"(s_add_u32 s15,s15,s83
s_lshl_b32 s15,s15,13
s_lshl_b32 s83,s70,10
s_add_u32 s15,s15,s83
s_add_u32 s15,s15,67108864
s_add_u32 s44,s30,s15
s_addc_u32 s45,s31,0
s_and_b32 s45,s45,0xffff
s_mov_b32 s46,32768
s_mov_b32 s47,0x20000
v_max3_f32 v242,v128,v129,v129
v_max3_f32 v242,v242,v130,v131
v_max3_f32 v242,v242,v132,v133
v_max3_f32 v242,v242,v134,v135
v_max3_f32 v243,v136,v137,v137
v_max3_f32 v243,v243,v138,v139
v_max3_f32 v243,v243,v140,v141
v_max3_f32 v243,v243,v142,v143
v_mov_b32 v244,v242
v_mov_b32 v253,v243
s_nop 0
v_permlane16_swap_b32 v242,v244
v_permlane16_swap_b32 v243,v253
v_max_f32 v242,v242,v244
)"
R"(v_max_f32 v243,v243,v253
v_mov_b32 v244,v242
v_mov_b32 v253,v243
s_nop 0
v_permlane32_swap_b32 v242,v244
v_permlane32_swap_b32 v243,v253
v_max_f32 v242,v242,v244
v_max_f32 v243,v243,v253
v_sub_f32 v208,0,v242
v_sub_f32 v209,0,v242
v_sub_f32 v216,0,v243
v_sub_f32 v217,0,v243
v_pk_mov_b32 v[210:211],v[208:209],v[208:209] op_sel:[0,1]
v_pk_mov_b32 v[212:213],v[208:209],v[208:209] op_sel:[0,1]
v_pk_mov_b32 v[214:215],v[208:209],v[208:209] op_sel:[0,1]
v_pk_mov_b32 v[218:219],v[216:217],v[216:217] op_sel:[0,1]
v_pk_mov_b32 v[220:221],v[216:217],v[216:217] op_sel:[0,1]
v_pk_mov_b32 v[222:223],v[216:217],v[216:217] op_sel:[0,1]
v_pk_add_f32 v[128:129],v[128:129],v[208:209]
v_pk_add_f32 v[130:131],v[130:131],v[208:209]
v_pk_add_f32 v[132:133],v[132:133],v[208:209]
v_pk_add_f32 v[134:135],v[134:135],v[208:209]
v_pk_add_f32 v[136:137],v[136:137],v[216:217]
v_pk_add_f32 v[138:139],v[138:139],v[216:217]
)"
R"(v_pk_add_f32 v[140:141],v[140:141],v[216:217]
v_pk_add_f32 v[142:143],v[142:143],v[216:217]
s_cmp_lg_u32 s55,0
s_cbranch_scc1 gm_K2_nm1
v_min_f32 v208,v208,v56
v_min_f32 v209,v209,v57
v_min_f32 v210,v210,v59
v_min_f32 v211,v211,v60
v_min_f32 v212,v212,v61
v_min_f32 v213,v213,v62
v_min_f32 v214,v214,v192
v_min_f32 v215,v215,v193
gm_K2_nm1:
s_nop 1
v_mfma_f32_16x16x32_bf16 v[144:147],a[96:99],a[0:3],v[208:211]
ds_read_b128 a[64:67],v240 offset:16384
v_mfma_f32_16x16x32_bf16 v[144:147],a[100:103],a[4:7],v[144:147]
v_exp_f32 v128,v128
v_mfma_f32_16x16x32_bf16 v[152:155],a[96:99],a[16:19],v[216:219]
s_branch gm_KB_sk14
.p2align 12
gm_KB_pg14:
s_setpc_b64 s[100:101]
gm_KB_sk14:
)"
R"(ds_read_b128 a[68:71],v240 offset:17408
v_mfma_f32_16x16x32_bf16 v[152:155],a[100:103],a[20:23],v[152:155]
v_exp_f32 v129,v129
v_mfma_f32_16x16x32_bf16 v[144:147],a[104:107],a[8:11],v[144:147]
ds_read_b128 a[72:75],v240 offset:18432
v_mfma_f32_16x16x32_bf16 v[144:147],a[108:111],a[12:15],v[144:147]
v_exp_f32 v130,v130
v_mfma_f32_16x16x32_bf16 v[152:155],a[104:107],a[24:27],v[152:155]
ds_read_b128 a[76:79],v240 offset:19456
v_mfma_f32_16x16x32_bf16 v[152:155],a[108:111],a[28:31],v[152:155]
v_exp_f32 v131,v131
v_mfma_f32_16x16x32_bf16 v[148:151],a[112:115],a[0:3],v[212:215]
ds_read_b128 a[80:83],v240 offset:20480
v_mfma_f32_16x16x32_bf16 v[148:151],a[116:119],a[4:7],v[148:151]
v_exp_f32 v132,v132
v_mfma_f32_16x16x32_bf16 v[156:159],a[112:115],a[16:19],v[220:223]
ds_read_b128 a[84:87],v240 offset:21504
v_mfma_f32_16x16x32_bf16 v[156:159],a[116:119],a[20:23],v[156:159]
v_exp_f32 v133,v133
v_mfma_f32_16x16x32_bf16 v[148:151],a[120:123],a[8:11],v[148:151]
ds_read_b128 a[88:91],v240 offset:22528
v_xor_b32 v241,0x38000,v241
v_mfma_f32_16x16x32_bf16 v[148:151],a[124:127],a[12:15],v[148:151]
v_exp_f32 v134,v134
)"
R"(v_mfma_f32_16x16x32_bf16 v[156:159],a[120:123],a[24:27],v[156:159]
ds_read_b128 a[92:95],v240 offset:23552
v_mfma_f32_16x16x32_bf16 v[156:159],a[124:127],a[28:31],v[156:159]
s_waitcnt vmcnt(0) lgkmcnt(8)
s_barrier
s_add_u32 m0,s66,0
ds_read_b128 a[128:131],v241 offset:0
buffer_load_dwordx4 v18,s[48:51],s60 offen offset:0 sc1 lds
v_max3_f32 v248,v144,v145,v146
v_max3_f32 v248,v248,v147,v152
v_exp_f32 v135,v135
ds_read_b128 a[132:135],v241 offset:1024
v_max3_f32 v248,v248,v153,v154
v_exp_f32 v136,v136
ds_read_b128 a[136:139],v241 offset:2048
buffer_load_dwordx4 v18,s[48:51],s60 offen offset:1024 sc1 lds
v_max3_f32 v248,v248,v155,v148
v_exp_f32 v137,v137
ds_read_b128 a[140:143],v241 offset:3072
v_max3_f32 v248,v248,v149,v150
v_exp_f32 v138,v138
ds_read_b128 a[144:147],v241 offset:4096
buffer_load_dwordx4 v18,s[48:51],s60 offen offset:2048 sc1 lds
v_max3_f32 v248,v248,v151,v156
)"
R"(v_exp_f32 v139,v139
ds_read_b128 a[148:151],v241 offset:5120
v_max3_f32 v248,v248,v157,v158
v_exp_f32 v140,v140
ds_read_b128 a[152:155],v241 offset:6144
buffer_load_dwordx4 v18,s[48:51],s60 offen offset:3072 sc1 lds
v_max_f32 v248,v248,v159
s_add_u32 m0,s66,4096
v_exp_f32 v141,v141
s_add_u32 s75,s60,4096
ds_read_b128 a[156:159],v241 offset:7168
v_cmp_lt_f32 vcc,s12,v248
v_exp_f32 v142,v142
v_exp_f32 v143,v143
s_cmp_lg_u32 s55,0
s_cbranch_scc1 gm_K2_nm2
v_min_f32 v216,v216,v194
v_min_f32 v217,v217,v195
v_min_f32 v218,v218,v196
v_min_f32 v219,v219,v197
v_min_f32 v220,v220,v198
v_min_f32 v221,v221,v199
v_min_f32 v222,v222,v224
v_min_f32 v223,v223,v225
)"
R"(v_mov_b32 v208,v252
v_mov_b32 v209,v252
v_mov_b32 v210,v252
v_mov_b32 v211,v252
v_mov_b32 v212,v252
v_mov_b32 v213,v252
v_mov_b32 v214,v252
v_mov_b32 v215,v252
gm_K2_nm2:
s_waitcnt lgkmcnt(8)
s_cbranch_vccnz gm_K2_stub0
gm_K2_ret0:
v_mfma_f32_16x16x32_bf16 v[160:163],a[64:67],a[0:3],v[208:211]
ds_read_b128 a[96:99],v240 offset:24576
buffer_load_dwordx4 v18,s[48:51],s75 offen offset:0 sc1 lds
v_cvt_pk_bf16_f32 v128,v128,v129
v_mfma_f32_16x16x32_bf16 v[160:163],a[68:71],a[4:7],v[160:163]
v_exp_f32 v144,v144
v_mfma_f32_16x16x32_bf16 v[168:171],a[64:67],a[16:19],v[216:219]
ds_read_b128 a[100:103],v240 offset:25600
v_cvt_pk_bf16_f32 v129,v130,v131
v_mfma_f32_16x16x32_bf16 v[168:171],a[68:71],a[20:23],v[168:171]
v_exp_f32 v145,v145
v_mfma_f32_16x16x32_bf16 v[160:163],a[72:75],a[8:11],v[160:163]
)"
R"(ds_read_b128 a[104:107],v240 offset:26624
buffer_load_dwordx4 v18,s[48:51],s75 offen offset:1024 sc1 lds
v_cvt_pk_bf16_f32 v130,v132,v133
v_mfma_f32_16x16x32_bf16 v[160:163],a[76:79],a[12:15],v[160:163]
v_exp_f32 v146,v146
v_mfma_f32_16x16x32_bf16 v[168:171],a[72:75],a[24:27],v[168:171]
ds_read_b128 a[108:111],v240 offset:27648
v_cvt_pk_bf16_f32 v131,v134,v135
v_mfma_f32_16x16x32_bf16 v[168:171],a[76:79],a[28:31],v[168:171]
v_exp_f32 v147,v147
v_mfma_f32_16x16x32_bf16 v[164:167],a[80:83],a[0:3],v[212:215]
ds_read_b128 a[112:115],v240 offset:28672
buffer_load_dwordx4 v18,s[48:51],s75 offen offset:2048 sc1 lds
v_cvt_pk_bf16_f32 v136,v136,v137
v_mfma_f32_16x16x32_bf16 v[164:167],a[84:87],a[4:7],v[164:167]
v_exp_f32 v148,v148
v_mfma_f32_16x16x32_bf16 v[172:175],a[80:83],a[16:19],v[220:223]
ds_read_b128 a[116:119],v240 offset:29696
v_cvt_pk_bf16_f32 v137,v138,v139
v_mfma_f32_16x16x32_bf16 v[172:175],a[84:87],a[20:23],v[172:175]
v_exp_f32 v149,v149
v_mfma_f32_16x16x32_bf16 v[164:167],a[88:91],a[8:11],v[164:167]
ds_read_b128 a[120:123],v240 offset:30720
buffer_load_dwordx4 v18,s[48:51],s75 offen offset:3072 sc1 lds
)"
R"(v_cvt_pk_bf16_f32 v138,v140,v141
v_mfma_f32_16x16x32_bf16 v[164:167],a[92:95],a[12:15],v[164:167]
s_add_u32 m0,s65,0
v_exp_f32 v150,v150
v_mfma_f32_16x16x32_bf16 v[172:175],a[88:91],a[24:27],v[172:175]
ds_read_b128 a[124:127],v240 offset:31744
v_cvt_pk_bf16_f32 v139,v142,v143
v_mfma_f32_16x16x32_bf16 v[172:175],a[92:95],a[28:31],v[172:175]
s_waitcnt lgkmcnt(8)
v_exp_f32 v151,v151
v_mfma_f32_16x16x32_bf16 v[64:67],a[128:131],v[128:131],0
ds_read_b128 a[160:163],v241 offset:8192
buffer_load_dwordx4 v18,s[36:39],s58 offen offset:0 sc1 lds
v_add_u32 v240,s62,v240
v_max3_f32 v248,v160,v161,v162
v_mfma_f32_16x16x32_bf16 v[96:99],a[128:131],v[136:139],0
v_exp_f32 v152,v152
v_mfma_f32_16x16x32_bf16 v[68:71],a[132:135],v[128:131],0
ds_read_b128 a[164:167],v241 offset:9216
v_max3_f32 v248,v248,v163,v168
v_mfma_f32_16x16x32_bf16 v[100:103],a[132:135],v[136:139],0
v_exp_f32 v153,v153
v_mfma_f32_16x16x32_bf16 v[72:75],a[136:139],v[128:131],0
ds_read_b128 a[168:171],v241 offset:10240
)"
R"(buffer_load_dwordx4 v18,s[36:39],s58 offen offset:1024 sc1 lds
v_max3_f32 v248,v248,v169,v170
v_mfma_f32_16x16x32_bf16 v[104:107],a[136:139],v[136:139],0
v_exp_f32 v154,v154
v_mfma_f32_16x16x32_bf16 v[76:79],a[140:143],v[128:131],0
ds_read_b128 a[172:175],v241 offset:11264
v_max3_f32 v248,v248,v171,v164
v_mfma_f32_16x16x32_bf16 v[108:111],a[140:143],v[136:139],0
v_exp_f32 v155,v155
v_mfma_f32_16x16x32_bf16 v[80:83],a[144:147],v[128:131],0
ds_read_b128 a[176:179],v241 offset:12288
buffer_load_dwordx4 v18,s[36:39],s58 offen offset:2048 sc1 lds
v_max3_f32 v248,v248,v165,v166
v_mfma_f32_16x16x32_bf16 v[112:115],a[144:147],v[136:139],0
v_exp_f32 v156,v156
v_mfma_f32_16x16x32_bf16 v[84:87],a[148:151],v[128:131],0
ds_read_b128 a[180:183],v241 offset:13312
v_max3_f32 v248,v248,v167,v172
v_mfma_f32_16x16x32_bf16 v[116:119],a[148:151],v[136:139],0
v_exp_f32 v157,v157
v_mfma_f32_16x16x32_bf16 v[88:91],a[152:155],v[128:131],0
ds_read_b128 a[184:187],v241 offset:14336
buffer_load_dwordx4 v18,s[36:39],s58 offen offset:3072 sc1 lds
v_max3_f32 v248,v248,v173,v174
)"
R"(v_mfma_f32_16x16x32_bf16 v[120:123],a[152:155],v[136:139],0
s_add_u32 m0,s65,4096
v_exp_f32 v158,v158
v_mfma_f32_16x16x32_bf16 v[92:95],a[156:159],v[128:131],0
s_add_u32 s75,s58,4096
ds_read_b128 a[188:191],v241 offset:15360
v_max_f32 v248,v248,v175
v_mfma_f32_16x16x32_bf16 v[124:127],a[156:159],v[136:139],0
v_exp_f32 v159,v159
v_mfma_f32_16x16x32_bf16 v[200:203],v[228:231],v[128:131],0
buffer_load_dwordx4 v18,s[36:39],s75 offen offset:0 sc1 lds
s_cmp_lg_u32 s55,0
s_cbranch_scc1 gm_K2_nm3
v_min_f32 v216,v216,v226
v_min_f32 v217,v217,v227
v_min_f32 v218,v218,v245
v_min_f32 v219,v219,v246
v_min_f32 v220,v220,v247
v_min_f32 v221,v221,v249
v_min_f32 v222,v222,v250
v_min_f32 v223,v223,v251
gm_K2_nm3:
v_cmp_lt_f32 vcc,s12,v248
v_mfma_f32_16x16x32_bf16 v[204:207],v[228:231],v[136:139],0
)"
R"(s_waitcnt lgkmcnt(8)
s_cbranch_vccnz gm_K2_stub1
gm_K2_ret1:
v_mfma_f32_16x16x32_bf16 v[176:179],a[96:99],a[0:3],v[208:211]
ds_read_b128 a[64:67],v240 offset:0
v_cvt_pk_bf16_f32 v144,v144,v145
v_mfma_f32_16x16x32_bf16 v[176:179],a[100:103],a[4:7],v[176:179]
v_exp_f32 v160,v160
v_mfma_f32_16x16x32_bf16 v[184:187],a[96:99],a[16:19],v[216:219]
ds_read_b128 a[68:71],v240 offset:1024
buffer_load_dwordx4 v18,s[36:39],s75 offen offset:1024 sc1 lds
v_cvt_pk_bf16_f32 v145,v146,v147
v_mfma_f32_16x16x32_bf16 v[184:187],a[100:103],a[20:23],v[184:187]
v_exp_f32 v161,v161
v_mfma_f32_16x16x32_bf16 v[176:179],a[104:107],a[8:11],v[176:179]
ds_read_b128 a[72:75],v240 offset:2048
v_cvt_pk_bf16_f32 v146,v148,v149
v_mfma_f32_16x16x32_bf16 v[176:179],a[108:111],a[12:15],v[176:179]
v_exp_f32 v162,v162
v_mfma_f32_16x16x32_bf16 v[184:187],a[104:107],a[24:27],v[184:187]
ds_read_b128 a[76:79],v240 offset:3072
buffer_load_dwordx4 v18,s[36:39],s75 offen offset:2048 sc1 lds
v_cvt_pk_bf16_f32 v147,v150,v151
v_mfma_f32_16x16x32_bf16 v[184:187],a[108:111],a[28:31],v[184:187]
)"
R"(v_exp_f32 v163,v163
v_mfma_f32_16x16x32_bf16 v[180:183],a[112:115],a[0:3],v[212:215]
ds_read_b128 a[80:83],v240 offset:4096
v_cvt_pk_bf16_f32 v152,v152,v153
v_mfma_f32_16x16x32_bf16 v[180:183],a[116:119],a[4:7],v[180:183]
v_exp_f32 v164,v164
v_mfma_f32_16x16x32_bf16 v[188:191],a[112:115],a[16:19],v[220:223]
ds_read_b128 a[84:87],v240 offset:5120
buffer_load_dwordx4 v18,s[36:39],s75 offen offset:3072 sc1 lds
v_cvt_pk_bf16_f32 v153,v154,v155
v_mfma_f32_16x16x32_bf16 v[188:191],a[116:119],a[20:23],v[188:191]
v_exp_f32 v165,v165
v_mfma_f32_16x16x32_bf16 v[180:183],a[120:123],a[8:11],v[180:183]
ds_read_b128 a[88:91],v240 offset:6144
v_cvt_pk_bf16_f32 v154,v156,v157
v_mfma_f32_16x16x32_bf16 v[180:183],a[124:127],a[12:15],v[180:183]
v_exp_f32 v166,v166
v_mfma_f32_16x16x32_bf16 v[188:191],a[120:123],a[24:27],v[188:191]
ds_read_b128 a[92:95],v240 offset:7168
buffer_load_dword v255,v254,s[92:95],s13 offen
v_cvt_pk_bf16_f32 v155,v158,v159
v_mfma_f32_16x16x32_bf16 v[188:191],a[124:127],a[28:31],v[188:191]
s_waitcnt lgkmcnt(8)
v_exp_f32 v167,v167
)"
R"(v_mfma_f32_16x16x32_bf16 v[64:67],a[160:163],v[144:147],v[64:67]
ds_read_b128 a[128:131],v241 offset:16384
v_max3_f32 v248,v176,v177,v178
v_mfma_f32_16x16x32_bf16 v[96:99],a[160:163],v[152:155],v[96:99]
v_exp_f32 v168,v168
v_mfma_f32_16x16x32_bf16 v[68:71],a[164:167],v[144:147],v[68:71]
ds_read_b128 a[132:135],v241 offset:17408
v_max3_f32 v248,v248,v179,v184
v_mfma_f32_16x16x32_bf16 v[100:103],a[164:167],v[152:155],v[100:103]
v_exp_f32 v169,v169
v_mfma_f32_16x16x32_bf16 v[72:75],a[168:171],v[144:147],v[72:75]
ds_read_b128 a[136:139],v241 offset:18432
v_max3_f32 v248,v248,v185,v186
v_mfma_f32_16x16x32_bf16 v[104:107],a[168:171],v[152:155],v[104:107]
v_exp_f32 v170,v170
v_mfma_f32_16x16x32_bf16 v[76:79],a[172:175],v[144:147],v[76:79]
ds_read_b128 a[140:143],v241 offset:19456
v_max3_f32 v248,v248,v187,v180
v_mfma_f32_16x16x32_bf16 v[108:111],a[172:175],v[152:155],v[108:111]
v_exp_f32 v171,v171
v_mfma_f32_16x16x32_bf16 v[80:83],a[176:179],v[144:147],v[80:83]
ds_read_b128 a[144:147],v241 offset:20480
v_max3_f32 v248,v248,v181,v182
v_mfma_f32_16x16x32_bf16 v[112:115],a[176:179],v[152:155],v[112:115]
)"
R"(v_exp_f32 v172,v172
v_mfma_f32_16x16x32_bf16 v[84:87],a[180:183],v[144:147],v[84:87]
ds_read_b128 a[148:151],v241 offset:21504
v_max3_f32 v248,v248,v183,v188
v_mfma_f32_16x16x32_bf16 v[116:119],a[180:183],v[152:155],v[116:119]
v_exp_f32 v173,v173
v_mfma_f32_16x16x32_bf16 v[88:91],a[184:187],v[144:147],v[88:91]
ds_read_b128 a[152:155],v241 offset:22528
v_max3_f32 v248,v248,v189,v190
v_mfma_f32_16x16x32_bf16 v[120:123],a[184:187],v[152:155],v[120:123]
v_exp_f32 v174,v174
v_mfma_f32_16x16x32_bf16 v[92:95],a[188:191],v[144:147],v[92:95]
ds_read_b128 a[156:159],v241 offset:23552
v_max_f32 v248,v248,v191
v_mfma_f32_16x16x32_bf16 v[124:127],a[188:191],v[152:155],v[124:127]
v_exp_f32 v175,v175
v_mfma_f32_16x16x32_bf16 v[200:203],v[228:231],v[144:147],v[200:203]
v_cmp_lt_f32 vcc,s12,v248
v_mfma_f32_16x16x32_bf16 v[204:207],v[228:231],v[152:155],v[204:207]
s_waitcnt lgkmcnt(8)
s_cbranch_vccnz gm_K2_stub2
gm_K2_ret2:
s_cmp_eq_u32 s55,0
s_cbranch_scc1 gm_K2_seam0
)"
R"(s_sub_u32 s67,s55,1
gm_K2_body:
s_cmp_eq_u32 s67,0
s_cbranch_scc1 gm_K2_diag
s_branch gm_KB_sk15
.p2align 12
gm_KB_pg15:
s_setpc_b64 s[100:101]
s_nop 0
gm_KB_sk15:
gm_K2_loop:
v_mfma_f32_16x16x32_bf16 v[128:131],a[64:67],a[0:3],v[208:211]
s_mov_b32 s60,s58
ds_read_b128 a[96:99],v240 offset:8192
v_cvt_pk_bf16_f32 v160,v160,v161
v_mfma_f32_16x16x32_bf16 v[128:131],a[68:71],a[4:7],v[128:131]
s_add_u32 s58,s58,0x8000
v_exp_f32 v176,v176
v_mfma_f32_16x16x32_bf16 v[136:139],a[64:67],a[16:19],v[216:219]
s_cmp_eq_u32 s58,s61
s_cselect_b32 s58,0,s58
s_cselect_b32 s61,-1,s61
ds_read_b128 a[100:103],v240 offset:9216
v_cvt_pk_bf16_f32 v161,v162,v163
)"
R"(v_mfma_f32_16x16x32_bf16 v[136:139],a[68:71],a[20:23],v[136:139]
s_xor_b32 s66,s66,0x38000
v_exp_f32 v177,v177
v_mfma_f32_16x16x32_bf16 v[128:131],a[72:75],a[8:11],v[128:131]
s_mov_b32 s82,s63
ds_read_b128 a[104:107],v240 offset:10240
v_cvt_pk_bf16_f32 v162,v164,v165
v_mfma_f32_16x16x32_bf16 v[128:131],a[76:79],a[12:15],v[128:131]
s_mov_b32 s63,s64
v_exp_f32 v178,v178
v_mfma_f32_16x16x32_bf16 v[136:139],a[72:75],a[24:27],v[136:139]
s_mov_b32 s64,s65
ds_read_b128 a[108:111],v240 offset:11264
v_cvt_pk_bf16_f32 v163,v166,v167
v_mfma_f32_16x16x32_bf16 v[136:139],a[76:79],a[28:31],v[136:139]
s_mov_b32 s65,s82
v_exp_f32 v179,v179
v_mfma_f32_16x16x32_bf16 v[132:135],a[80:83],a[0:3],v[212:215]
s_sub_u32 s62,s64,s63
ds_read_b128 a[112:115],v240 offset:12288
v_cvt_pk_bf16_f32 v168,v168,v169
v_mfma_f32_16x16x32_bf16 v[132:135],a[84:87],a[4:7],v[132:135]
s_add_u32 s13,s13,s14
v_exp_f32 v180,v180
)"
R"(v_mfma_f32_16x16x32_bf16 v[140:143],a[80:83],a[16:19],v[220:223]
s_sub_u32 s67,s67,1
ds_read_b128 a[116:119],v240 offset:13312
v_cvt_pk_bf16_f32 v169,v170,v171
v_mfma_f32_16x16x32_bf16 v[140:143],a[84:87],a[20:23],v[140:143]
v_exp_f32 v181,v181
v_mfma_f32_16x16x32_bf16 v[132:135],a[88:91],a[8:11],v[132:135]
ds_read_b128 a[120:123],v240 offset:14336
v_cvt_pk_bf16_f32 v170,v172,v173
v_mfma_f32_16x16x32_bf16 v[132:135],a[92:95],a[12:15],v[132:135]
v_exp_f32 v182,v182
v_mfma_f32_16x16x32_bf16 v[140:143],a[88:91],a[24:27],v[140:143]
ds_read_b128 a[124:127],v240 offset:15360
v_cvt_pk_bf16_f32 v171,v174,v175
v_mfma_f32_16x16x32_bf16 v[140:143],a[92:95],a[28:31],v[140:143]
s_waitcnt lgkmcnt(8)
v_exp_f32 v183,v183
v_mfma_f32_16x16x32_bf16 v[64:67],a[128:131],v[160:163],v[64:67]
ds_read_b128 a[160:163],v241 offset:24576
v_max3_f32 v248,v128,v129,v130
v_mfma_f32_16x16x32_bf16 v[96:99],a[128:131],v[168:171],v[96:99]
v_exp_f32 v184,v184
v_mfma_f32_16x16x32_bf16 v[68:71],a[132:135],v[160:163],v[68:71]
ds_read_b128 a[164:167],v241 offset:25600
)"
R"(v_max3_f32 v248,v248,v131,v136
v_mfma_f32_16x16x32_bf16 v[100:103],a[132:135],v[168:171],v[100:103]
v_exp_f32 v185,v185
v_mfma_f32_16x16x32_bf16 v[72:75],a[136:139],v[160:163],v[72:75]
ds_read_b128 a[168:171],v241 offset:26624
v_max3_f32 v248,v248,v137,v138
v_mfma_f32_16x16x32_bf16 v[104:107],a[136:139],v[168:171],v[104:107]
v_exp_f32 v186,v186
v_mfma_f32_16x16x32_bf16 v[76:79],a[140:143],v[160:163],v[76:79]
ds_read_b128 a[172:175],v241 offset:27648
v_max3_f32 v248,v248,v139,v132
v_mfma_f32_16x16x32_bf16 v[108:111],a[140:143],v[168:171],v[108:111]
v_exp_f32 v187,v187
v_mfma_f32_16x16x32_bf16 v[80:83],a[144:147],v[160:163],v[80:83]
ds_read_b128 a[176:179],v241 offset:28672
v_max3_f32 v248,v248,v133,v134
v_mfma_f32_16x16x32_bf16 v[112:115],a[144:147],v[168:171],v[112:115]
v_exp_f32 v188,v188
v_mfma_f32_16x16x32_bf16 v[84:87],a[148:151],v[160:163],v[84:87]
ds_read_b128 a[180:183],v241 offset:29696
v_max3_f32 v248,v248,v135,v140
v_mfma_f32_16x16x32_bf16 v[116:119],a[148:151],v[168:171],v[116:119]
v_exp_f32 v189,v189
v_mfma_f32_16x16x32_bf16 v[88:91],a[152:155],v[160:163],v[88:91]
)"
R"(ds_read_b128 a[184:187],v241 offset:30720
v_max3_f32 v248,v248,v141,v142
v_mfma_f32_16x16x32_bf16 v[120:123],a[152:155],v[168:171],v[120:123]
v_exp_f32 v190,v190
v_mfma_f32_16x16x32_bf16 v[92:95],a[156:159],v[160:163],v[92:95]
ds_read_b128 a[188:191],v241 offset:31744
v_max_f32 v248,v248,v143
v_mfma_f32_16x16x32_bf16 v[124:127],a[156:159],v[168:171],v[124:127]
v_exp_f32 v191,v191
v_mfma_f32_16x16x32_bf16 v[200:203],v[228:231],v[160:163],v[200:203]
v_cmp_lt_f32 vcc,s12,v248
v_mfma_f32_16x16x32_bf16 v[204:207],v[228:231],v[168:171],v[204:207]
s_waitcnt lgkmcnt(8)
s_cbranch_vccnz gm_K2_stub3
gm_K2_ret3:
v_mfma_f32_16x16x32_bf16 v[144:147],a[96:99],a[0:3],v[208:211]
ds_read_b128 a[64:67],v240 offset:16384
v_cvt_pk_bf16_f32 v176,v176,v177
v_mfma_f32_16x16x32_bf16 v[144:147],a[100:103],a[4:7],v[144:147]
v_exp_f32 v128,v128
v_mfma_f32_16x16x32_bf16 v[152:155],a[96:99],a[16:19],v[216:219]
ds_read_b128 a[68:71],v240 offset:17408
v_cvt_pk_bf16_f32 v177,v178,v179
v_mfma_f32_16x16x32_bf16 v[152:155],a[100:103],a[20:23],v[152:155]
)"
R"(v_exp_f32 v129,v129
v_mfma_f32_16x16x32_bf16 v[144:147],a[104:107],a[8:11],v[144:147]
ds_read_b128 a[72:75],v240 offset:18432
v_cvt_pk_bf16_f32 v178,v180,v181
v_mfma_f32_16x16x32_bf16 v[144:147],a[108:111],a[12:15],v[144:147]
v_exp_f32 v130,v130
v_mfma_f32_16x16x32_bf16 v[152:155],a[104:107],a[24:27],v[152:155]
ds_read_b128 a[76:79],v240 offset:19456
v_cvt_pk_bf16_f32 v179,v182,v183
v_mfma_f32_16x16x32_bf16 v[152:155],a[108:111],a[28:31],v[152:155]
v_exp_f32 v131,v131
v_mfma_f32_16x16x32_bf16 v[148:151],a[112:115],a[0:3],v[212:215]
ds_read_b128 a[80:83],v240 offset:20480
v_cvt_pk_bf16_f32 v184,v184,v185
v_mfma_f32_16x16x32_bf16 v[148:151],a[116:119],a[4:7],v[148:151]
v_exp_f32 v132,v132
v_mfma_f32_16x16x32_bf16 v[156:159],a[112:115],a[16:19],v[220:223]
ds_read_b128 a[84:87],v240 offset:21504
v_cvt_pk_bf16_f32 v185,v186,v187
v_mfma_f32_16x16x32_bf16 v[156:159],a[116:119],a[20:23],v[156:159]
v_exp_f32 v133,v133
v_mfma_f32_16x16x32_bf16 v[148:151],a[120:123],a[8:11],v[148:151]
ds_read_b128 a[88:91],v240 offset:22528
v_xor_b32 v241,0x38000,v241
)"
R"(v_cvt_pk_bf16_f32 v186,v188,v189
v_mfma_f32_16x16x32_bf16 v[148:151],a[124:127],a[12:15],v[148:151]
v_exp_f32 v134,v134
v_mfma_f32_16x16x32_bf16 v[156:159],a[120:123],a[24:27],v[156:159]
ds_read_b128 a[92:95],v240 offset:23552
v_cvt_pk_bf16_f32 v187,v190,v191
v_mfma_f32_16x16x32_bf16 v[156:159],a[124:127],a[28:31],v[156:159]
s_waitcnt vmcnt(1) lgkmcnt(8)
s_barrier
v_mfma_f32_16x16x32_bf16 v[64:67],a[160:163],v[176:179],v[64:67]
s_add_u32 m0,s66,0
ds_read_b128 a[128:131],v241 offset:0
buffer_load_dwordx4 v18,s[48:51],s60 offen offset:0 sc1 lds
v_max3_f32 v248,v144,v145,v146
v_max3_f32 v248,v248,v147,v152
v_mfma_f32_16x16x32_bf16 v[96:99],a[160:163],v[184:187],v[96:99]
v_exp_f32 v135,v135
v_mfma_f32_16x16x32_bf16 v[68:71],a[164:167],v[176:179],v[68:71]
ds_read_b128 a[132:135],v241 offset:1024
v_max3_f32 v248,v248,v153,v154
v_mfma_f32_16x16x32_bf16 v[100:103],a[164:167],v[184:187],v[100:103]
v_exp_f32 v136,v136
v_mfma_f32_16x16x32_bf16 v[72:75],a[168:171],v[176:179],v[72:75]
ds_read_b128 a[136:139],v241 offset:2048
)"
R"(buffer_load_dwordx4 v18,s[48:51],s60 offen offset:1024 sc1 lds
v_max3_f32 v248,v248,v155,v148
v_mfma_f32_16x16x32_bf16 v[104:107],a[168:171],v[184:187],v[104:107]
v_exp_f32 v137,v137
v_mfma_f32_16x16x32_bf16 v[76:79],a[172:175],v[176:179],v[76:79]
ds_read_b128 a[140:143],v241 offset:3072
v_max3_f32 v248,v248,v149,v150
v_mfma_f32_16x16x32_bf16 v[108:111],a[172:175],v[184:187],v[108:111]
v_exp_f32 v138,v138
v_mfma_f32_16x16x32_bf16 v[80:83],a[176:179],v[176:179],v[80:83]
ds_read_b128 a[144:147],v241 offset:4096
buffer_load_dwordx4 v18,s[48:51],s60 offen offset:2048 sc1 lds
v_max3_f32 v248,v248,v151,v156
v_mfma_f32_16x16x32_bf16 v[112:115],a[176:179],v[184:187],v[112:115]
v_exp_f32 v139,v139
v_mfma_f32_16x16x32_bf16 v[84:87],a[180:183],v[176:179],v[84:87]
ds_read_b128 a[148:151],v241 offset:5120
v_max3_f32 v248,v248,v157,v158
v_mfma_f32_16x16x32_bf16 v[116:119],a[180:183],v[184:187],v[116:119]
v_exp_f32 v140,v140
v_mfma_f32_16x16x32_bf16 v[88:91],a[184:187],v[176:179],v[88:91]
ds_read_b128 a[152:155],v241 offset:6144
buffer_load_dwordx4 v18,s[48:51],s60 offen offset:3072 sc1 lds
v_max_f32 v248,v248,v159
)"
R"(v_mfma_f32_16x16x32_bf16 v[120:123],a[184:187],v[184:187],v[120:123]
s_add_u32 m0,s66,4096
v_exp_f32 v141,v141
v_mfma_f32_16x16x32_bf16 v[92:95],a[188:191],v[176:179],v[92:95]
s_add_u32 s75,s60,4096
ds_read_b128 a[156:159],v241 offset:7168
v_cmp_lt_f32 vcc,s12,v248
v_mfma_f32_16x16x32_bf16 v[124:127],a[188:191],v[184:187],v[124:127]
v_exp_f32 v142,v142
v_mfma_f32_16x16x32_bf16 v[200:203],v[228:231],v[176:179],v[200:203]
v_exp_f32 v143,v143
v_mfma_f32_16x16x32_bf16 v[204:207],v[228:231],v[184:187],v[204:207]
s_waitcnt lgkmcnt(8)
s_cbranch_vccnz gm_K2_stub4
gm_K2_ret4:
v_mfma_f32_16x16x32_bf16 v[160:163],a[64:67],a[0:3],v[208:211]
ds_read_b128 a[96:99],v240 offset:24576
buffer_load_dwordx4 v18,s[48:51],s75 offen offset:0 sc1 lds
v_cvt_pk_bf16_f32 v128,v128,v129
v_mfma_f32_16x16x32_bf16 v[160:163],a[68:71],a[4:7],v[160:163]
v_exp_f32 v144,v144
v_mfma_f32_16x16x32_bf16 v[168:171],a[64:67],a[16:19],v[216:219]
ds_read_b128 a[100:103],v240 offset:25600
v_cvt_pk_bf16_f32 v129,v130,v131
)"
R"(v_mfma_f32_16x16x32_bf16 v[168:171],a[68:71],a[20:23],v[168:171]
v_exp_f32 v145,v145
v_mfma_f32_16x16x32_bf16 v[160:163],a[72:75],a[8:11],v[160:163]
ds_read_b128 a[104:107],v240 offset:26624
buffer_load_dwordx4 v18,s[48:51],s75 offen offset:1024 sc1 lds
v_cvt_pk_bf16_f32 v130,v132,v133
v_mfma_f32_16x16x32_bf16 v[160:163],a[76:79],a[12:15],v[160:163]
v_exp_f32 v146,v146
v_mfma_f32_16x16x32_bf16 v[168:171],a[72:75],a[24:27],v[168:171]
ds_read_b128 a[108:111],v240 offset:27648
v_cvt_pk_bf16_f32 v131,v134,v135
v_mfma_f32_16x16x32_bf16 v[168:171],a[76:79],a[28:31],v[168:171]
v_exp_f32 v147,v147
v_mfma_f32_16x16x32_bf16 v[164:167],a[80:83],a[0:3],v[212:215]
ds_read_b128 a[112:115],v240 offset:28672
buffer_load_dwordx4 v18,s[48:51],s75 offen offset:2048 sc1 lds
v_cvt_pk_bf16_f32 v136,v136,v137
v_mfma_f32_16x16x32_bf16 v[164:167],a[84:87],a[4:7],v[164:167]
v_exp_f32 v148,v148
v_mfma_f32_16x16x32_bf16 v[172:175],a[80:83],a[16:19],v[220:223]
ds_read_b128 a[116:119],v240 offset:29696
v_cvt_pk_bf16_f32 v137,v138,v139
v_mfma_f32_16x16x32_bf16 v[172:175],a[84:87],a[20:23],v[172:175]
v_exp_f32 v149,v149
)"
R"(v_mfma_f32_16x16x32_bf16 v[164:167],a[88:91],a[8:11],v[164:167]
ds_read_b128 a[120:123],v240 offset:30720
buffer_load_dwordx4 v18,s[48:51],s75 offen offset:3072 sc1 lds
v_cvt_pk_bf16_f32 v138,v140,v141
v_mfma_f32_16x16x32_bf16 v[164:167],a[92:95],a[12:15],v[164:167]
s_add_u32 m0,s65,0
v_exp_f32 v150,v150
v_mfma_f32_16x16x32_bf16 v[172:175],a[88:91],a[24:27],v[172:175]
ds_read_b128 a[124:127],v240 offset:31744
v_cvt_pk_bf16_f32 v139,v142,v143
v_mfma_f32_16x16x32_bf16 v[172:175],a[92:95],a[28:31],v[172:175]
s_waitcnt lgkmcnt(8)
v_exp_f32 v151,v151
v_mfma_f32_16x16x32_bf16 v[64:67],a[128:131],v[128:131],v[64:67]
ds_read_b128 a[160:163],v241 offset:8192
buffer_load_dwordx4 v18,s[36:39],s58 offen offset:0 sc1 lds
v_add_u32 v240,s62,v240
v_max3_f32 v248,v160,v161,v162
v_mfma_f32_16x16x32_bf16 v[96:99],a[128:131],v[136:139],v[96:99]
v_exp_f32 v152,v152
v_mfma_f32_16x16x32_bf16 v[68:71],a[132:135],v[128:131],v[68:71]
ds_read_b128 a[164:167],v241 offset:9216
v_max3_f32 v248,v248,v163,v168
v_mfma_f32_16x16x32_bf16 v[100:103],a[132:135],v[136:139],v[100:103]
)"
R"(v_exp_f32 v153,v153
v_mfma_f32_16x16x32_bf16 v[72:75],a[136:139],v[128:131],v[72:75]
ds_read_b128 a[168:171],v241 offset:10240
buffer_load_dwordx4 v18,s[36:39],s58 offen offset:1024 sc1 lds
v_max3_f32 v248,v248,v169,v170
v_mfma_f32_16x16x32_bf16 v[104:107],a[136:139],v[136:139],v[104:107]
v_exp_f32 v154,v154
v_mfma_f32_16x16x32_bf16 v[76:79],a[140:143],v[128:131],v[76:79]
ds_read_b128 a[172:175],v241 offset:11264
v_max3_f32 v248,v248,v171,v164
v_mfma_f32_16x16x32_bf16 v[108:111],a[140:143],v[136:139],v[108:111]
v_exp_f32 v155,v155
v_mfma_f32_16x16x32_bf16 v[80:83],a[144:147],v[128:131],v[80:83]
ds_read_b128 a[176:179],v241 offset:12288
buffer_load_dwordx4 v18,s[36:39],s58 offen offset:2048 sc1 lds
v_max3_f32 v248,v248,v165,v166
v_mfma_f32_16x16x32_bf16 v[112:115],a[144:147],v[136:139],v[112:115]
v_exp_f32 v156,v156
v_mfma_f32_16x16x32_bf16 v[84:87],a[148:151],v[128:131],v[84:87]
ds_read_b128 a[180:183],v241 offset:13312
v_max3_f32 v248,v248,v167,v172
v_mfma_f32_16x16x32_bf16 v[116:119],a[148:151],v[136:139],v[116:119]
v_exp_f32 v157,v157
v_mfma_f32_16x16x32_bf16 v[88:91],a[152:155],v[128:131],v[88:91]
)"
R"(ds_read_b128 a[184:187],v241 offset:14336
buffer_load_dwordx4 v18,s[36:39],s58 offen offset:3072 sc1 lds
v_max3_f32 v248,v248,v173,v174
v_mfma_f32_16x16x32_bf16 v[120:123],a[152:155],v[136:139],v[120:123]
s_add_u32 m0,s65,4096
v_exp_f32 v158,v158
v_mfma_f32_16x16x32_bf16 v[92:95],a[156:159],v[128:131],v[92:95]
s_add_u32 s75,s58,4096
ds_read_b128 a[188:191],v241 offset:15360
v_max_f32 v248,v248,v175
v_mfma_f32_16x16x32_bf16 v[124:127],a[156:159],v[136:139],v[124:127]
v_exp_f32 v159,v159
v_mfma_f32_16x16x32_bf16 v[200:203],v[228:231],v[128:131],v[200:203]
buffer_load_dwordx4 v18,s[36:39],s75 offen offset:0 sc1 lds
v_cmp_lt_f32 vcc,s12,v248
v_mfma_f32_16x16x32_bf16 v[204:207],v[228:231],v[136:139],v[204:207]
s_waitcnt lgkmcnt(8)
s_cbranch_vccnz gm_K2_stub5
gm_K2_ret5:
v_mfma_f32_16x16x32_bf16 v[176:179],a[96:99],a[0:3],v[208:211]
ds_read_b128 a[64:67],v240 offset:0
v_cvt_pk_bf16_f32 v144,v144,v145
v_mfma_f32_16x16x32_bf16 v[176:179],a[100:103],a[4:7],v[176:179]
v_exp_f32 v160,v160
)"
R"(v_mfma_f32_16x16x32_bf16 v[184:187],a[96:99],a[16:19],v[216:219]
ds_read_b128 a[68:71],v240 offset:1024
buffer_load_dwordx4 v18,s[36:39],s75 offen offset:1024 sc1 lds
v_cvt_pk_bf16_f32 v145,v146,v147
v_mfma_f32_16x16x32_bf16 v[184:187],a[100:103],a[20:23],v[184:187]
v_exp_f32 v161,v161
v_mfma_f32_16x16x32_bf16 v[176:179],a[104:107],a[8:11],v[176:179]
ds_read_b128 a[72:75],v240 offset:2048
v_cvt_pk_bf16_f32 v146,v148,v149
v_mfma_f32_16x16x32_bf16 v[176:179],a[108:111],a[12:15],v[176:179]
v_exp_f32 v162,v162
v_mfma_f32_16x16x32_bf16 v[184:187],a[104:107],a[24:27],v[184:187]
ds_read_b128 a[76:79],v240 offset:3072
buffer_load_dwordx4 v18,s[36:39],s75 offen offset:2048 sc1 lds
v_cvt_pk_bf16_f32 v147,v150,v151
v_mfma_f32_16x16x32_bf16 v[184:187],a[108:111],a[28:31],v[184:187]
v_exp_f32 v163,v163
v_mfma_f32_16x16x32_bf16 v[180:183],a[112:115],a[0:3],v[212:215]
ds_read_b128 a[80:83],v240 offset:4096
v_cvt_pk_bf16_f32 v152,v152,v153
v_mfma_f32_16x16x32_bf16 v[180:183],a[116:119],a[4:7],v[180:183]
v_exp_f32 v164,v164
v_mfma_f32_16x16x32_bf16 v[188:191],a[112:115],a[16:19],v[220:223]
ds_read_b128 a[84:87],v240 offset:5120
)"
R"(buffer_load_dwordx4 v18,s[36:39],s75 offen offset:3072 sc1 lds
v_cvt_pk_bf16_f32 v153,v154,v155
v_mfma_f32_16x16x32_bf16 v[188:191],a[116:119],a[20:23],v[188:191]
v_exp_f32 v165,v165
v_mfma_f32_16x16x32_bf16 v[180:183],a[120:123],a[8:11],v[180:183]
ds_read_b128 a[88:91],v240 offset:6144
v_cvt_pk_bf16_f32 v154,v156,v157
v_mfma_f32_16x16x32_bf16 v[180:183],a[124:127],a[12:15],v[180:183]
v_exp_f32 v166,v166
v_mfma_f32_16x16x32_bf16 v[188:191],a[120:123],a[24:27],v[188:191]
ds_read_b128 a[92:95],v240 offset:7168
buffer_load_dword v255,v254,s[92:95],s13 offen
v_cvt_pk_bf16_f32 v155,v158,v159
v_mfma_f32_16x16x32_bf16 v[188:191],a[124:127],a[28:31],v[188:191]
s_waitcnt lgkmcnt(8)
v_exp_f32 v167,v167
v_mfma_f32_16x16x32_bf16 v[64:67],a[160:163],v[144:147],v[64:67]
ds_read_b128 a[128:131],v241 offset:16384
v_max3_f32 v248,v176,v177,v178
v_mfma_f32_16x16x32_bf16 v[96:99],a[160:163],v[152:155],v[96:99]
v_exp_f32 v168,v168
v_mfma_f32_16x16x32_bf16 v[68:71],a[164:167],v[144:147],v[68:71]
ds_read_b128 a[132:135],v241 offset:17408
v_max3_f32 v248,v248,v179,v184
)"
R"(v_mfma_f32_16x16x32_bf16 v[100:103],a[164:167],v[152:155],v[100:103]
v_exp_f32 v169,v169
v_mfma_f32_16x16x32_bf16 v[72:75],a[168:171],v[144:147],v[72:75]
ds_read_b128 a[136:139],v241 offset:18432
v_max3_f32 v248,v248,v185,v186
v_mfma_f32_16x16x32_bf16 v[104:107],a[168:171],v[152:155],v[104:107]
v_exp_f32 v170,v170
v_mfma_f32_16x16x32_bf16 v[76:79],a[172:175],v[144:147],v[76:79]
ds_read_b128 a[140:143],v241 offset:19456
v_max3_f32 v248,v248,v187,v180
v_mfma_f32_16x16x32_bf16 v[108:111],a[172:175],v[152:155],v[108:111]
v_exp_f32 v171,v171
v_mfma_f32_16x16x32_bf16 v[80:83],a[176:179],v[144:147],v[80:83]
ds_read_b128 a[144:147],v241 offset:20480
v_max3_f32 v248,v248,v181,v182
v_mfma_f32_16x16x32_bf16 v[112:115],a[176:179],v[152:155],v[112:115]
v_exp_f32 v172,v172
v_mfma_f32_16x16x32_bf16 v[84:87],a[180:183],v[144:147],v[84:87]
ds_read_b128 a[148:151],v241 offset:21504
v_max3_f32 v248,v248,v183,v188
v_mfma_f32_16x16x32_bf16 v[116:119],a[180:183],v[152:155],v[116:119]
v_exp_f32 v173,v173
v_mfma_f32_16x16x32_bf16 v[88:91],a[184:187],v[144:147],v[88:91]
ds_read_b128 a[152:155],v241 offset:22528
)"
R"(v_max3_f32 v248,v248,v189,v190
v_mfma_f32_16x16x32_bf16 v[120:123],a[184:187],v[152:155],v[120:123]
v_exp_f32 v174,v174
v_mfma_f32_16x16x32_bf16 v[92:95],a[188:191],v[144:147],v[92:95]
ds_read_b128 a[156:159],v241 offset:23552
v_max_f32 v248,v248,v191
v_mfma_f32_16x16x32_bf16 v[124:127],a[188:191],v[152:155],v[124:127]
v_exp_f32 v175,v175
v_mfma_f32_16x16x32_bf16 v[200:203],v[228:231],v[144:147],v[200:203]
v_cmp_lt_f32 vcc,s12,v248
v_mfma_f32_16x16x32_bf16 v[204:207],v[228:231],v[152:155],v[204:207]
s_waitcnt lgkmcnt(8)
s_cbranch_vccnz gm_K2_stub6
gm_K2_ret6:
s_cmp_lg_u32 s67,0
s_cbranch_scc1 gm_K2_loop
gm_K2_diag:
s_cmp_lg_u32 s68,0
s_cbranch_scc1 gm_K2_last
v_min_f32 v208,v208,v12
v_min_f32 v209,v209,v13
v_min_f32 v210,v210,v16
v_min_f32 v211,v211,v17
v_min_f32 v212,v212,v20
)"
R"(v_min_f32 v213,v213,v21
v_min_f32 v214,v214,v22
v_min_f32 v215,v215,v23
v_mfma_f32_16x16x32_bf16 v[128:131],a[64:67],a[0:3],v[208:211]
s_mov_b32 s60,s58
ds_read_b128 a[96:99],v240 offset:8192
v_cvt_pk_bf16_f32 v160,v160,v161
v_mfma_f32_16x16x32_bf16 v[128:131],a[68:71],a[4:7],v[128:131]
s_add_u32 s58,s58,0x8000
v_exp_f32 v176,v176
v_mfma_f32_16x16x32_bf16 v[136:139],a[64:67],a[16:19],v[216:219]
s_cmp_eq_u32 s58,s61
s_cselect_b32 s58,0,s58
s_cselect_b32 s61,-1,s61
ds_read_b128 a[100:103],v240 offset:9216
v_cvt_pk_bf16_f32 v161,v162,v163
v_mfma_f32_16x16x32_bf16 v[136:139],a[68:71],a[20:23],v[136:139]
s_xor_b32 s66,s66,0x38000
v_exp_f32 v177,v177
v_mfma_f32_16x16x32_bf16 v[128:131],a[72:75],a[8:11],v[128:131]
s_mov_b32 s82,s63
ds_read_b128 a[104:107],v240 offset:10240
v_cvt_pk_bf16_f32 v162,v164,v165
v_mfma_f32_16x16x32_bf16 v[128:131],a[76:79],a[12:15],v[128:131]
)"
R"(s_mov_b32 s63,s64
v_exp_f32 v178,v178
v_mfma_f32_16x16x32_bf16 v[136:139],a[72:75],a[24:27],v[136:139]
s_mov_b32 s64,s65
ds_read_b128 a[108:111],v240 offset:11264
v_cvt_pk_bf16_f32 v163,v166,v167
v_mfma_f32_16x16x32_bf16 v[136:139],a[76:79],a[28:31],v[136:139]
s_mov_b32 s65,s82
v_exp_f32 v179,v179
v_mfma_f32_16x16x32_bf16 v[132:135],a[80:83],a[0:3],v[212:215]
s_sub_u32 s62,s64,s63
ds_read_b128 a[112:115],v240 offset:12288
v_cvt_pk_bf16_f32 v168,v168,v169
v_mfma_f32_16x16x32_bf16 v[132:135],a[84:87],a[4:7],v[132:135]
s_add_u32 s13,s13,s14
v_exp_f32 v180,v180
v_mfma_f32_16x16x32_bf16 v[140:143],a[80:83],a[16:19],v[220:223]
ds_read_b128 a[116:119],v240 offset:13312
v_cvt_pk_bf16_f32 v169,v170,v171
v_mfma_f32_16x16x32_bf16 v[140:143],a[84:87],a[20:23],v[140:143]
v_exp_f32 v181,v181
v_mfma_f32_16x16x32_bf16 v[132:135],a[88:91],a[8:11],v[132:135]
ds_read_b128 a[120:123],v240 offset:14336
v_cvt_pk_bf16_f32 v170,v172,v173
)"
R"(v_mfma_f32_16x16x32_bf16 v[132:135],a[92:95],a[12:15],v[132:135]
v_exp_f32 v182,v182
v_mfma_f32_16x16x32_bf16 v[140:143],a[88:91],a[24:27],v[140:143]
ds_read_b128 a[124:127],v240 offset:15360
v_cvt_pk_bf16_f32 v171,v174,v175
v_mfma_f32_16x16x32_bf16 v[140:143],a[92:95],a[28:31],v[140:143]
s_waitcnt lgkmcnt(8)
v_exp_f32 v183,v183
v_mfma_f32_16x16x32_bf16 v[64:67],a[128:131],v[160:163],v[64:67]
ds_read_b128 a[160:163],v241 offset:24576
v_min_f32 v208,v208,v56
v_max3_f32 v248,v128,v129,v130
v_mfma_f32_16x16x32_bf16 v[96:99],a[128:131],v[168:171],v[96:99]
v_exp_f32 v184,v184
v_mfma_f32_16x16x32_bf16 v[68:71],a[132:135],v[160:163],v[68:71]
ds_read_b128 a[164:167],v241 offset:25600
v_min_f32 v209,v209,v57
v_max3_f32 v248,v248,v131,v136
v_mfma_f32_16x16x32_bf16 v[100:103],a[132:135],v[168:171],v[100:103]
v_exp_f32 v185,v185
v_mfma_f32_16x16x32_bf16 v[72:75],a[136:139],v[160:163],v[72:75]
ds_read_b128 a[168:171],v241 offset:26624
v_min_f32 v210,v210,v59
v_max3_f32 v248,v248,v137,v138
)"
R"(v_mfma_f32_16x16x32_bf16 v[104:107],a[136:139],v[168:171],v[104:107]
v_exp_f32 v186,v186
v_mfma_f32_16x16x32_bf16 v[76:79],a[140:143],v[160:163],v[76:79]
ds_read_b128 a[172:175],v241 offset:27648
v_min_f32 v211,v211,v60
v_max3_f32 v248,v248,v139,v132
v_mfma_f32_16x16x32_bf16 v[108:111],a[140:143],v[168:171],v[108:111]
v_exp_f32 v187,v187
v_mfma_f32_16x16x32_bf16 v[80:83],a[144:147],v[160:163],v[80:83]
ds_read_b128 a[176:179],v241 offset:28672
v_min_f32 v212,v212,v61
v_max3_f32 v248,v248,v133,v134
v_mfma_f32_16x16x32_bf16 v[112:115],a[144:147],v[168:171],v[112:115]
v_exp_f32 v188,v188
v_mfma_f32_16x16x32_bf16 v[84:87],a[148:151],v[160:163],v[84:87]
ds_read_b128 a[180:183],v241 offset:29696
v_min_f32 v213,v213,v62
v_max3_f32 v248,v248,v135,v140
v_mfma_f32_16x16x32_bf16 v[116:119],a[148:151],v[168:171],v[116:119]
v_exp_f32 v189,v189
v_mfma_f32_16x16x32_bf16 v[88:91],a[152:155],v[160:163],v[88:91]
ds_read_b128 a[184:187],v241 offset:30720
v_min_f32 v214,v214,v192
v_max3_f32 v248,v248,v141,v142
)"
R"(v_mfma_f32_16x16x32_bf16 v[120:123],a[152:155],v[168:171],v[120:123]
v_exp_f32 v190,v190
v_mfma_f32_16x16x32_bf16 v[92:95],a[156:159],v[160:163],v[92:95]
ds_read_b128 a[188:191],v241 offset:31744
v_min_f32 v215,v215,v193
v_max_f32 v248,v248,v143
v_mfma_f32_16x16x32_bf16 v[124:127],a[156:159],v[168:171],v[124:127]
v_exp_f32 v191,v191
v_mfma_f32_16x16x32_bf16 v[200:203],v[228:231],v[160:163],v[200:203]
v_cmp_lt_f32 vcc,s12,v248
v_mfma_f32_16x16x32_bf16 v[204:207],v[228:231],v[168:171],v[204:207]
s_waitcnt lgkmcnt(8)
s_cbranch_vccnz gm_K2_stub7
gm_K2_ret7:
v_mfma_f32_16x16x32_bf16 v[144:147],a[96:99],a[0:3],v[208:211]
ds_read_b128 a[64:67],v240 offset:16384
v_cvt_pk_bf16_f32 v176,v176,v177
v_mfma_f32_16x16x32_bf16 v[144:147],a[100:103],a[4:7],v[144:147]
v_exp_f32 v128,v128
v_mfma_f32_16x16x32_bf16 v[152:155],a[96:99],a[16:19],v[216:219]
ds_read_b128 a[68:71],v240 offset:17408
v_cvt_pk_bf16_f32 v177,v178,v179
v_mfma_f32_16x16x32_bf16 v[152:155],a[100:103],a[20:23],v[152:155]
v_exp_f32 v129,v129
)"
R"(v_mfma_f32_16x16x32_bf16 v[144:147],a[104:107],a[8:11],v[144:147]
ds_read_b128 a[72:75],v240 offset:18432
v_cvt_pk_bf16_f32 v178,v180,v181
v_mfma_f32_16x16x32_bf16 v[144:147],a[108:111],a[12:15],v[144:147]
v_exp_f32 v130,v130
v_mfma_f32_16x16x32_bf16 v[152:155],a[104:107],a[24:27],v[152:155]
ds_read_b128 a[76:79],v240 offset:19456
v_cvt_pk_bf16_f32 v179,v182,v183
v_mfma_f32_16x16x32_bf16 v[152:155],a[108:111],a[28:31],v[152:155]
v_exp_f32 v131,v131
v_mfma_f32_16x16x32_bf16 v[148:151],a[112:115],a[0:3],v[212:215]
ds_read_b128 a[80:83],v240 offset:20480
v_cvt_pk_bf16_f32 v184,v184,v185
v_mfma_f32_16x16x32_bf16 v[148:151],a[116:119],a[4:7],v[148:151]
v_exp_f32 v132,v132
v_mfma_f32_16x16x32_bf16 v[156:159],a[112:115],a[16:19],v[220:223]
ds_read_b128 a[84:87],v240 offset:21504
v_cvt_pk_bf16_f32 v185,v186,v187
v_mfma_f32_16x16x32_bf16 v[156:159],a[116:119],a[20:23],v[156:159]
v_exp_f32 v133,v133
v_mfma_f32_16x16x32_bf16 v[148:151],a[120:123],a[8:11],v[148:151]
ds_read_b128 a[88:91],v240 offset:22528
v_xor_b32 v241,0x38000,v241
v_cvt_pk_bf16_f32 v186,v188,v189
)"
R"(v_mfma_f32_16x16x32_bf16 v[148:151],a[124:127],a[12:15],v[148:151]
v_exp_f32 v134,v134
v_mfma_f32_16x16x32_bf16 v[156:159],a[120:123],a[24:27],v[156:159]
ds_read_b128 a[92:95],v240 offset:23552
v_cvt_pk_bf16_f32 v187,v190,v191
v_mfma_f32_16x16x32_bf16 v[156:159],a[124:127],a[28:31],v[156:159]
s_waitcnt vmcnt(1) lgkmcnt(8)
s_barrier
v_mfma_f32_16x16x32_bf16 v[64:67],a[160:163],v[176:179],v[64:67]
s_add_u32 m0,s66,0
ds_read_b128 a[128:131],v241 offset:0
buffer_load_dwordx4 v18,s[48:51],s60 offen offset:0 sc1 lds
v_min_f32 v216,v216,v194
v_max3_f32 v248,v144,v145,v146
v_max3_f32 v248,v248,v147,v152
v_mfma_f32_16x16x32_bf16 v[96:99],a[160:163],v[184:187],v[96:99]
v_exp_f32 v135,v135
v_mfma_f32_16x16x32_bf16 v[68:71],a[164:167],v[176:179],v[68:71]
ds_read_b128 a[132:135],v241 offset:1024
v_min_f32 v217,v217,v195
v_max3_f32 v248,v248,v153,v154
v_mfma_f32_16x16x32_bf16 v[100:103],a[164:167],v[184:187],v[100:103]
v_exp_f32 v136,v136
v_mfma_f32_16x16x32_bf16 v[72:75],a[168:171],v[176:179],v[72:75]
)"
R"(ds_read_b128 a[136:139],v241 offset:2048
buffer_load_dwordx4 v18,s[48:51],s60 offen offset:1024 sc1 lds
v_min_f32 v218,v218,v196
v_max3_f32 v248,v248,v155,v148
v_mfma_f32_16x16x32_bf16 v[104:107],a[168:171],v[184:187],v[104:107]
v_exp_f32 v137,v137
v_mfma_f32_16x16x32_bf16 v[76:79],a[172:175],v[176:179],v[76:79]
ds_read_b128 a[140:143],v241 offset:3072
v_min_f32 v219,v219,v197
v_max3_f32 v248,v248,v149,v150
v_mfma_f32_16x16x32_bf16 v[108:111],a[172:175],v[184:187],v[108:111]
v_exp_f32 v138,v138
v_mfma_f32_16x16x32_bf16 v[80:83],a[176:179],v[176:179],v[80:83]
ds_read_b128 a[144:147],v241 offset:4096
buffer_load_dwordx4 v18,s[48:51],s60 offen offset:2048 sc1 lds
v_min_f32 v220,v220,v198
v_max3_f32 v248,v248,v151,v156
v_mfma_f32_16x16x32_bf16 v[112:115],a[176:179],v[184:187],v[112:115]
v_exp_f32 v139,v139
v_mfma_f32_16x16x32_bf16 v[84:87],a[180:183],v[176:179],v[84:87]
ds_read_b128 a[148:151],v241 offset:5120
v_min_f32 v221,v221,v199
v_max3_f32 v248,v248,v157,v158
v_mfma_f32_16x16x32_bf16 v[116:119],a[180:183],v[184:187],v[116:119]
)"
R"(v_exp_f32 v140,v140
v_mfma_f32_16x16x32_bf16 v[88:91],a[184:187],v[176:179],v[88:91]
ds_read_b128 a[152:155],v241 offset:6144
buffer_load_dwordx4 v18,s[48:51],s60 offen offset:3072 sc1 lds
v_min_f32 v222,v222,v224
v_max_f32 v248,v248,v159
v_mfma_f32_16x16x32_bf16 v[120:123],a[184:187],v[184:187],v[120:123]
s_add_u32 m0,s66,4096
v_exp_f32 v141,v141
v_mfma_f32_16x16x32_bf16 v[92:95],a[188:191],v[176:179],v[92:95]
s_add_u32 s75,s60,4096
ds_read_b128 a[156:159],v241 offset:7168
v_min_f32 v223,v223,v225
v_cmp_lt_f32 vcc,s12,v248
v_mfma_f32_16x16x32_bf16 v[124:127],a[188:191],v[184:187],v[124:127]
s_branch gm_KB_sk16
.p2align 12
gm_KB_pg16:
s_setpc_b64 s[100:101]
gm_KB_sk16:
v_exp_f32 v142,v142
v_mfma_f32_16x16x32_bf16 v[200:203],v[228:231],v[176:179],v[200:203]
v_exp_f32 v143,v143
v_mfma_f32_16x16x32_bf16 v[204:207],v[228:231],v[184:187],v[204:207]
)"
R"(s_waitcnt lgkmcnt(8)
s_cbranch_vccnz gm_K2_stub8
gm_K2_ret8:
ds_read_b128 a[96:99],v240 offset:24576
buffer_load_dwordx4 v18,s[48:51],s75 offen offset:0 sc1 lds
v_cvt_pk_bf16_f32 v128,v128,v129
v_exp_f32 v144,v144
v_mfma_f32_16x16x32_bf16 v[168:171],a[64:67],a[16:19],v[216:219]
ds_read_b128 a[100:103],v240 offset:25600
v_cvt_pk_bf16_f32 v129,v130,v131
v_mfma_f32_16x16x32_bf16 v[168:171],a[68:71],a[20:23],v[168:171]
ds_read_b128 a[104:107],v240 offset:26624
buffer_load_dwordx4 v18,s[48:51],s75 offen offset:1024 sc1 lds
v_cvt_pk_bf16_f32 v130,v132,v133
v_exp_f32 v145,v145
v_exp_f32 v146,v146
v_mfma_f32_16x16x32_bf16 v[168:171],a[72:75],a[24:27],v[168:171]
ds_read_b128 a[108:111],v240 offset:27648
v_cvt_pk_bf16_f32 v131,v134,v135
v_mfma_f32_16x16x32_bf16 v[168:171],a[76:79],a[28:31],v[168:171]
ds_read_b128 a[112:115],v240 offset:28672
buffer_load_dwordx4 v18,s[48:51],s75 offen offset:2048 sc1 lds
v_cvt_pk_bf16_f32 v136,v136,v137
v_exp_f32 v147,v147
)"
R"(v_exp_f32 v148,v148
v_mfma_f32_16x16x32_bf16 v[172:175],a[80:83],a[16:19],v[220:223]
ds_read_b128 a[116:119],v240 offset:29696
v_cvt_pk_bf16_f32 v137,v138,v139
v_mfma_f32_16x16x32_bf16 v[172:175],a[84:87],a[20:23],v[172:175]
ds_read_b128 a[120:123],v240 offset:30720
buffer_load_dwordx4 v18,s[48:51],s75 offen offset:3072 sc1 lds
s_add_u32 m0,s65,0
v_cvt_pk_bf16_f32 v138,v140,v141
v_exp_f32 v149,v149
v_exp_f32 v150,v150
v_mfma_f32_16x16x32_bf16 v[172:175],a[88:91],a[24:27],v[172:175]
ds_read_b128 a[124:127],v240 offset:31744
v_cvt_pk_bf16_f32 v139,v142,v143
v_mfma_f32_16x16x32_bf16 v[172:175],a[92:95],a[28:31],v[172:175]
s_waitcnt lgkmcnt(8)
v_exp_f32 v151,v151
v_mfma_f32_16x16x32_bf16 v[64:67],a[128:131],v[128:131],v[64:67]
ds_read_b128 a[160:163],v241 offset:8192
buffer_load_dwordx4 v18,s[36:39],s58 offen offset:0 sc1 lds
v_add_u32 v240,s62,v240
v_min_f32 v216,v216,v226
v_mfma_f32_16x16x32_bf16 v[96:99],a[128:131],v[136:139],v[96:99]
v_exp_f32 v152,v152
)"
R"(v_mfma_f32_16x16x32_bf16 v[68:71],a[132:135],v[128:131],v[68:71]
ds_read_b128 a[164:167],v241 offset:9216
v_min_f32 v217,v217,v227
v_mfma_f32_16x16x32_bf16 v[100:103],a[132:135],v[136:139],v[100:103]
v_exp_f32 v153,v153
v_mfma_f32_16x16x32_bf16 v[72:75],a[136:139],v[128:131],v[72:75]
ds_read_b128 a[168:171],v241 offset:10240
buffer_load_dwordx4 v18,s[36:39],s58 offen offset:1024 sc1 lds
v_min_f32 v218,v218,v245
v_mfma_f32_16x16x32_bf16 v[104:107],a[136:139],v[136:139],v[104:107]
v_exp_f32 v154,v154
v_mfma_f32_16x16x32_bf16 v[76:79],a[140:143],v[128:131],v[76:79]
ds_read_b128 a[172:175],v241 offset:11264
v_min_f32 v219,v219,v246
v_mfma_f32_16x16x32_bf16 v[108:111],a[140:143],v[136:139],v[108:111]
v_exp_f32 v155,v155
v_mfma_f32_16x16x32_bf16 v[80:83],a[144:147],v[128:131],v[80:83]
ds_read_b128 a[176:179],v241 offset:12288
buffer_load_dwordx4 v18,s[36:39],s58 offen offset:2048 sc1 lds
v_min_f32 v220,v220,v247
v_max3_f32 v248,v168,v169,v170
v_mfma_f32_16x16x32_bf16 v[112:115],a[144:147],v[136:139],v[112:115]
v_exp_f32 v156,v156
v_mfma_f32_16x16x32_bf16 v[84:87],a[148:151],v[128:131],v[84:87]
)"
R"(ds_read_b128 a[180:183],v241 offset:13312
v_min_f32 v221,v221,v249
v_max3_f32 v248,v248,v171,v172
v_mfma_f32_16x16x32_bf16 v[116:119],a[148:151],v[136:139],v[116:119]
v_exp_f32 v157,v157
v_mfma_f32_16x16x32_bf16 v[88:91],a[152:155],v[128:131],v[88:91]
ds_read_b128 a[184:187],v241 offset:14336
buffer_load_dwordx4 v18,s[36:39],s58 offen offset:3072 sc1 lds
v_min_f32 v222,v222,v250
v_max3_f32 v248,v248,v173,v174
v_mfma_f32_16x16x32_bf16 v[120:123],a[152:155],v[136:139],v[120:123]
s_add_u32 m0,s65,4096
v_exp_f32 v158,v158
v_mfma_f32_16x16x32_bf16 v[92:95],a[156:159],v[128:131],v[92:95]
s_add_u32 s75,s58,4096
ds_read_b128 a[188:191],v241 offset:15360
v_min_f32 v223,v223,v251
v_max_f32 v248,v248,v175
v_mfma_f32_16x16x32_bf16 v[124:127],a[156:159],v[136:139],v[124:127]
v_exp_f32 v159,v159
v_mfma_f32_16x16x32_bf16 v[200:203],v[228:231],v[128:131],v[200:203]
buffer_load_dwordx4 v18,s[36:39],s75 offen offset:0 sc1 lds
v_cmp_lt_f32 vcc,s12,v248
v_mfma_f32_16x16x32_bf16 v[204:207],v[228:231],v[136:139],v[204:207]
)"
R"(s_waitcnt lgkmcnt(8)
s_cbranch_vccnz gm_K2_stub9
gm_K2_ret9:
ds_read_b128 a[64:67],v240 offset:0
v_cvt_pk_bf16_f32 v144,v144,v145
v_cvt_pk_bf16_f32 v145,v146,v147
v_mfma_f32_16x16x32_bf16 v[184:187],a[96:99],a[16:19],v[216:219]
ds_read_b128 a[68:71],v240 offset:1024
buffer_load_dwordx4 v18,s[36:39],s75 offen offset:1024 sc1 lds
v_mfma_f32_16x16x32_bf16 v[184:187],a[100:103],a[20:23],v[184:187]
ds_read_b128 a[72:75],v240 offset:2048
v_cvt_pk_bf16_f32 v146,v148,v149
v_cvt_pk_bf16_f32 v147,v150,v151
v_exp_f32 v168,v168
v_mfma_f32_16x16x32_bf16 v[184:187],a[104:107],a[24:27],v[184:187]
ds_read_b128 a[76:79],v240 offset:3072
buffer_load_dwordx4 v18,s[36:39],s75 offen offset:2048 sc1 lds
v_mfma_f32_16x16x32_bf16 v[184:187],a[108:111],a[28:31],v[184:187]
ds_read_b128 a[80:83],v240 offset:4096
v_cvt_pk_bf16_f32 v152,v152,v153
v_cvt_pk_bf16_f32 v153,v154,v155
v_exp_f32 v169,v169
v_mfma_f32_16x16x32_bf16 v[188:191],a[112:115],a[16:19],v[220:223]
ds_read_b128 a[84:87],v240 offset:5120
)"
R"(buffer_load_dwordx4 v18,s[36:39],s75 offen offset:3072 sc1 lds
v_mfma_f32_16x16x32_bf16 v[188:191],a[116:119],a[20:23],v[188:191]
ds_read_b128 a[88:91],v240 offset:6144
v_cvt_pk_bf16_f32 v154,v156,v157
v_cvt_pk_bf16_f32 v155,v158,v159
v_exp_f32 v170,v170
v_mfma_f32_16x16x32_bf16 v[188:191],a[120:123],a[24:27],v[188:191]
ds_read_b128 a[92:95],v240 offset:7168
buffer_load_dword v255,v254,s[92:95],s13 offen
v_mfma_f32_16x16x32_bf16 v[188:191],a[124:127],a[28:31],v[188:191]
s_waitcnt lgkmcnt(8)
v_exp_f32 v171,v171
v_mfma_f32_16x16x32_bf16 v[64:67],a[160:163],v[144:147],v[64:67]
ds_read_b128 a[128:131],v241 offset:16384
v_max3_f32 v248,v184,v185,v186
v_mfma_f32_16x16x32_bf16 v[96:99],a[160:163],v[152:155],v[96:99]
v_accvgpr_mov_b32 a0,a32
v_accvgpr_mov_b32 a1,a33
v_accvgpr_mov_b32 a2,a34
v_accvgpr_mov_b32 a3,a35
v_mfma_f32_16x16x32_bf16 v[68:71],a[164:167],v[144:147],v[68:71]
ds_read_b128 a[132:135],v241 offset:17408
v_accvgpr_mov_b32 a4,a36
v_accvgpr_mov_b32 a5,a37
)"
R"(v_accvgpr_mov_b32 a6,a38
v_accvgpr_mov_b32 a7,a39
v_mfma_f32_16x16x32_bf16 v[100:103],a[164:167],v[152:155],v[100:103]
v_exp_f32 v172,v172
v_mfma_f32_16x16x32_bf16 v[72:75],a[168:171],v[144:147],v[72:75]
ds_read_b128 a[136:139],v241 offset:18432
v_max3_f32 v248,v248,v187,v188
v_accvgpr_mov_b32 a8,a40
v_accvgpr_mov_b32 a9,a41
v_accvgpr_mov_b32 a10,a42
v_accvgpr_mov_b32 a11,a43
v_mfma_f32_16x16x32_bf16 v[104:107],a[168:171],v[152:155],v[104:107]
v_max3_f32 v248,v248,v189,v190
v_accvgpr_mov_b32 a12,a44
v_accvgpr_mov_b32 a13,a45
v_accvgpr_mov_b32 a14,a46
v_accvgpr_mov_b32 a15,a47
v_mfma_f32_16x16x32_bf16 v[76:79],a[172:175],v[144:147],v[76:79]
ds_read_b128 a[140:143],v241 offset:19456
v_max_f32 v248,v248,v191
v_accvgpr_mov_b32 a16,a48
v_accvgpr_mov_b32 a17,a49
v_accvgpr_mov_b32 a18,a50
v_accvgpr_mov_b32 a19,a51
)"
R"(v_mfma_f32_16x16x32_bf16 v[108:111],a[172:175],v[152:155],v[108:111]
v_exp_f32 v173,v173
v_mfma_f32_16x16x32_bf16 v[80:83],a[176:179],v[144:147],v[80:83]
ds_read_b128 a[144:147],v241 offset:20480
v_cmp_lt_f32 vcc,s12,v248
v_accvgpr_mov_b32 a20,a52
v_accvgpr_mov_b32 a21,a53
v_accvgpr_mov_b32 a22,a54
v_accvgpr_mov_b32 a23,a55
v_mfma_f32_16x16x32_bf16 v[112:115],a[176:179],v[152:155],v[112:115]
v_accvgpr_mov_b32 a24,a56
v_accvgpr_mov_b32 a25,a57
v_accvgpr_mov_b32 a26,a58
v_accvgpr_mov_b32 a27,a59
v_mfma_f32_16x16x32_bf16 v[84:87],a[180:183],v[144:147],v[84:87]
ds_read_b128 a[148:151],v241 offset:21504
v_accvgpr_mov_b32 a28,a60
v_accvgpr_mov_b32 a29,a61
v_accvgpr_mov_b32 a30,a62
v_accvgpr_mov_b32 a31,a63
v_mfma_f32_16x16x32_bf16 v[116:119],a[180:183],v[152:155],v[116:119]
v_exp_f32 v174,v174
v_mfma_f32_16x16x32_bf16 v[88:91],a[184:187],v[144:147],v[88:91]
ds_read_b128 a[152:155],v241 offset:22528
)"
R"(v_mfma_f32_16x16x32_bf16 v[120:123],a[184:187],v[152:155],v[120:123]
v_mfma_f32_16x16x32_bf16 v[92:95],a[188:191],v[144:147],v[92:95]
ds_read_b128 a[156:159],v241 offset:23552
v_mfma_f32_16x16x32_bf16 v[124:127],a[188:191],v[152:155],v[124:127]
v_exp_f32 v175,v175
v_mfma_f32_16x16x32_bf16 v[200:203],v[228:231],v[144:147],v[200:203]
v_mfma_f32_16x16x32_bf16 v[204:207],v[228:231],v[152:155],v[204:207]
s_waitcnt lgkmcnt(8)
s_cbranch_vccnz gm_K2_stub10
gm_K2_ret10:
gm_K2_seam:
v_mfma_f32_16x16x32_bf16 v[128:131],a[64:67],a[0:3],0
s_mov_b32 s60,s58
ds_read_b128 a[96:99],v240 offset:8192
v_cvt_pk_bf16_f32 v168,v168,v169
v_mfma_f32_16x16x32_bf16 v[128:131],a[68:71],a[4:7],v[128:131]
s_add_u32 s58,s58,0x8000
v_cvt_pk_bf16_f32 v169,v170,v171
v_mfma_f32_16x16x32_bf16 v[136:139],a[64:67],a[16:19],0
s_cmp_eq_u32 s58,s61
s_cselect_b32 s58,0,s58
s_cselect_b32 s61,-1,s61
ds_read_b128 a[100:103],v240 offset:9216
v_mfma_f32_16x16x32_bf16 v[136:139],a[68:71],a[20:23],v[136:139]
)"
R"(s_xor_b32 s66,s66,0x38000
v_exp_f32 v184,v184
v_mfma_f32_16x16x32_bf16 v[128:131],a[72:75],a[8:11],v[128:131]
s_mov_b32 s82,s63
ds_read_b128 a[104:107],v240 offset:10240
v_cvt_pk_bf16_f32 v170,v172,v173
v_mfma_f32_16x16x32_bf16 v[128:131],a[76:79],a[12:15],v[128:131]
s_mov_b32 s63,s64
v_cvt_pk_bf16_f32 v171,v174,v175
v_mfma_f32_16x16x32_bf16 v[136:139],a[72:75],a[24:27],v[136:139]
s_mov_b32 s64,s65
ds_read_b128 a[108:111],v240 offset:11264
v_mfma_f32_16x16x32_bf16 v[136:139],a[76:79],a[28:31],v[136:139]
s_mov_b32 s65,s82
v_exp_f32 v185,v185
v_mfma_f32_16x16x32_bf16 v[132:135],a[80:83],a[0:3],0
s_sub_u32 s62,s64,s63
ds_read_b128 a[112:115],v240 offset:12288
v_mfma_f32_16x16x32_bf16 v[132:135],a[84:87],a[4:7],v[132:135]
s_add_u32 s13,s13,s14
v_mfma_f32_16x16x32_bf16 v[140:143],a[80:83],a[16:19],0
ds_read_b128 a[116:119],v240 offset:13312
v_mfma_f32_16x16x32_bf16 v[140:143],a[84:87],a[20:23],v[140:143]
v_exp_f32 v186,v186
)"
R"(v_mfma_f32_16x16x32_bf16 v[132:135],a[88:91],a[8:11],v[132:135]
ds_read_b128 a[120:123],v240 offset:14336
v_mfma_f32_16x16x32_bf16 v[132:135],a[92:95],a[12:15],v[132:135]
v_mfma_f32_16x16x32_bf16 v[140:143],a[88:91],a[24:27],v[140:143]
ds_read_b128 a[124:127],v240 offset:15360
v_mfma_f32_16x16x32_bf16 v[140:143],a[92:95],a[28:31],v[140:143]
s_waitcnt lgkmcnt(8)
ds_read_b128 a[160:163],v241 offset:24576
v_max3_f32 v248,v128,v129,v130
v_exp_f32 v187,v187
v_mfma_f32_16x16x32_bf16 v[96:99],a[128:131],v[168:171],v[96:99]
ds_read_b128 a[164:167],v241 offset:25600
v_max3_f32 v248,v248,v131,v136
v_max3_f32 v248,v248,v137,v138
v_mfma_f32_16x16x32_bf16 v[100:103],a[132:135],v[168:171],v[100:103]
ds_read_b128 a[168:171],v241 offset:26624
v_max3_f32 v248,v248,v139,v132
v_exp_f32 v188,v188
v_mfma_f32_16x16x32_bf16 v[104:107],a[136:139],v[168:171],v[104:107]
ds_read_b128 a[172:175],v241 offset:27648
v_max3_f32 v248,v248,v133,v134
v_max3_f32 v248,v248,v135,v140
v_mfma_f32_16x16x32_bf16 v[108:111],a[140:143],v[168:171],v[108:111]
ds_read_b128 a[176:179],v241 offset:28672
)"
R"(v_max3_f32 v248,v248,v141,v142
v_exp_f32 v189,v189
v_mfma_f32_16x16x32_bf16 v[112:115],a[144:147],v[168:171],v[112:115]
ds_read_b128 a[180:183],v241 offset:29696
v_max_f32 v248,v248,v143
v_cmp_lt_f32 vcc,s12,v248
v_mfma_f32_16x16x32_bf16 v[116:119],a[148:151],v[168:171],v[116:119]
ds_read_b128 a[184:187],v241 offset:30720
v_exp_f32 v190,v190
v_mfma_f32_16x16x32_bf16 v[120:123],a[152:155],v[168:171],v[120:123]
ds_read_b128 a[188:191],v241 offset:31744
v_mfma_f32_16x16x32_bf16 v[124:127],a[156:159],v[168:171],v[124:127]
v_exp_f32 v191,v191
v_mfma_f32_16x16x32_bf16 v[204:207],v[228:231],v[168:171],v[204:207]
s_waitcnt lgkmcnt(8)
s_cmp_lg_u32 s87,0
s_cbranch_scc1 gm_K2_stub11
gm_K2_ret11:
v_max3_f32 v242,v128,v129,v129
v_max3_f32 v242,v242,v130,v131
v_max3_f32 v242,v242,v132,v133
v_max3_f32 v242,v242,v134,v135
v_max3_f32 v243,v136,v137,v137
v_max3_f32 v243,v243,v138,v139
)"
R"(v_max3_f32 v243,v243,v140,v141
v_max3_f32 v243,v243,v142,v143
v_mov_b32 v244,v242
v_mov_b32 v253,v243
s_nop 0
v_permlane16_swap_b32 v242,v244
v_permlane16_swap_b32 v243,v253
v_max_f32 v242,v242,v244
v_max_f32 v243,v243,v253
v_mov_b32 v244,v242
v_mov_b32 v253,v243
s_nop 0
v_permlane32_swap_b32 v242,v244
v_permlane32_swap_b32 v243,v253
v_max_f32 v242,v242,v244
v_max_f32 v243,v243,v253
v_sub_f32 v208,0,v242
v_sub_f32 v209,0,v242
v_sub_f32 v216,0,v243
v_sub_f32 v217,0,v243
v_pk_mov_b32 v[210:211],v[208:209],v[208:209] op_sel:[0,1]
v_pk_mov_b32 v[212:213],v[208:209],v[208:209] op_sel:[0,1]
v_pk_mov_b32 v[214:215],v[208:209],v[208:209] op_sel:[0,1]
v_pk_mov_b32 v[218:219],v[216:217],v[216:217] op_sel:[0,1]
)"
R"(v_pk_mov_b32 v[220:221],v[216:217],v[216:217] op_sel:[0,1]
v_pk_mov_b32 v[222:223],v[216:217],v[216:217] op_sel:[0,1]
v_pk_add_f32 v[128:129],v[128:129],v[208:209]
v_pk_add_f32 v[130:131],v[130:131],v[208:209]
v_pk_add_f32 v[132:133],v[132:133],v[208:209]
v_pk_add_f32 v[134:135],v[134:135],v[208:209]
v_pk_add_f32 v[136:137],v[136:137],v[216:217]
v_pk_add_f32 v[138:139],v[138:139],v[216:217]
v_pk_add_f32 v[140:141],v[140:141],v[216:217]
v_pk_add_f32 v[142:143],v[142:143],v[216:217]
v_mfma_f32_16x16x32_bf16 v[144:147],a[96:99],a[0:3],v[208:211]
ds_read_b128 a[64:67],v240 offset:16384
v_cvt_pk_bf16_f32 v184,v184,v185
v_mfma_f32_16x16x32_bf16 v[144:147],a[100:103],a[4:7],v[144:147]
v_exp_f32 v128,v128
v_mfma_f32_16x16x32_bf16 v[152:155],a[96:99],a[16:19],v[216:219]
ds_read_b128 a[68:71],v240 offset:17408
v_cvt_pk_bf16_f32 v185,v186,v187
v_mfma_f32_16x16x32_bf16 v[152:155],a[100:103],a[20:23],v[152:155]
v_exp_f32 v129,v129
v_mfma_f32_16x16x32_bf16 v[144:147],a[104:107],a[8:11],v[144:147]
ds_read_b128 a[72:75],v240 offset:18432
v_cvt_pk_bf16_f32 v186,v188,v189
v_mfma_f32_16x16x32_bf16 v[144:147],a[108:111],a[12:15],v[144:147]
)"
R"(v_exp_f32 v130,v130
v_mfma_f32_16x16x32_bf16 v[152:155],a[104:107],a[24:27],v[152:155]
ds_read_b128 a[76:79],v240 offset:19456
v_cvt_pk_bf16_f32 v187,v190,v191
v_mfma_f32_16x16x32_bf16 v[152:155],a[108:111],a[28:31],v[152:155]
v_exp_f32 v131,v131
v_mfma_f32_16x16x32_bf16 v[148:151],a[112:115],a[0:3],v[212:215]
ds_read_b128 a[80:83],v240 offset:20480
v_mfma_f32_16x16x32_bf16 v[148:151],a[116:119],a[4:7],v[148:151]
v_exp_f32 v132,v132
v_mfma_f32_16x16x32_bf16 v[156:159],a[112:115],a[16:19],v[220:223]
ds_read_b128 a[84:87],v240 offset:21504
v_mfma_f32_16x16x32_bf16 v[156:159],a[116:119],a[20:23],v[156:159]
v_exp_f32 v133,v133
v_mfma_f32_16x16x32_bf16 v[148:151],a[120:123],a[8:11],v[148:151]
ds_read_b128 a[88:91],v240 offset:22528
v_xor_b32 v241,0x38000,v241
v_mfma_f32_16x16x32_bf16 v[148:151],a[124:127],a[12:15],v[148:151]
v_exp_f32 v134,v134
v_mfma_f32_16x16x32_bf16 v[156:159],a[120:123],a[24:27],v[156:159]
ds_read_b128 a[92:95],v240 offset:23552
v_mfma_f32_16x16x32_bf16 v[156:159],a[124:127],a[28:31],v[156:159]
s_waitcnt vmcnt(1) lgkmcnt(8)
s_barrier
)"
R"(s_add_u32 m0,s66,0
ds_read_b128 a[128:131],v241 offset:0
buffer_load_dwordx4 v18,s[48:51],s60 offen offset:0 sc1 lds
v_max3_f32 v248,v144,v145,v146
v_max3_f32 v248,v248,v147,v152
v_mfma_f32_16x16x32_bf16 v[96:99],a[160:163],v[184:187],v[96:99]
ds_read_b128 a[132:135],v241 offset:1024
v_max3_f32 v248,v248,v153,v154
v_exp_f32 v135,v135
v_mfma_f32_16x16x32_bf16 v[100:103],a[164:167],v[184:187],v[100:103]
ds_read_b128 a[136:139],v241 offset:2048
buffer_load_dwordx4 v18,s[48:51],s60 offen offset:1024 sc1 lds
v_max3_f32 v248,v248,v155,v148
v_exp_f32 v136,v136
v_mfma_f32_16x16x32_bf16 v[104:107],a[168:171],v[184:187],v[104:107]
ds_read_b128 a[140:143],v241 offset:3072
v_max3_f32 v248,v248,v149,v150
v_exp_f32 v137,v137
v_mfma_f32_16x16x32_bf16 v[108:111],a[172:175],v[184:187],v[108:111]
ds_read_b128 a[144:147],v241 offset:4096
buffer_load_dwordx4 v18,s[48:51],s60 offen offset:2048 sc1 lds
v_max3_f32 v248,v248,v151,v156
v_exp_f32 v138,v138
v_mfma_f32_16x16x32_bf16 v[112:115],a[176:179],v[184:187],v[112:115]
)"
R"(ds_read_b128 a[148:151],v241 offset:5120
v_max3_f32 v248,v248,v157,v158
v_exp_f32 v139,v139
v_mfma_f32_16x16x32_bf16 v[116:119],a[180:183],v[184:187],v[116:119]
ds_read_b128 a[152:155],v241 offset:6144
buffer_load_dwordx4 v18,s[48:51],s60 offen offset:3072 sc1 lds
v_max_f32 v248,v248,v159
v_exp_f32 v140,v140
v_mfma_f32_16x16x32_bf16 v[120:123],a[184:187],v[184:187],v[120:123]
s_add_u32 m0,s66,4096
s_add_u32 s75,s60,4096
ds_read_b128 a[156:159],v241 offset:7168
v_cmp_lt_f32 vcc,s12,v248
v_exp_f32 v141,v141
v_mfma_f32_16x16x32_bf16 v[124:127],a[188:191],v[184:187],v[124:127]
v_exp_f32 v142,v142
v_exp_f32 v143,v143
v_mfma_f32_16x16x32_bf16 v[204:207],v[228:231],v[184:187],v[204:207]
s_waitcnt lgkmcnt(8)
s_cbranch_vccnz gm_K2_stub12
gm_K2_ret12:
v_rcp_f32 v236,v200
s_nop 4
v_rcp_f32 v237,v204
)"
R"(s_nop 0
v_pk_mul_f32 v[236:237],v[236:237],v[232:233]
v_pk_mul_f32 v[64:65],v[64:65],v[236:237] op_sel:[0,0] op_sel_hi:[1,0]
v_pk_mul_f32 v[66:67],v[66:67],v[236:237] op_sel:[0,0] op_sel_hi:[1,0]
v_pk_mul_f32 v[68:69],v[68:69],v[236:237] op_sel:[0,0] op_sel_hi:[1,0]
v_pk_mul_f32 v[70:71],v[70:71],v[236:237] op_sel:[0,0] op_sel_hi:[1,0]
v_cvt_pk_bf16_f32 v24,v64,v65
v_cvt_pk_bf16_f32 v25,v66,v67
v_cvt_pk_bf16_f32 v26,v68,v69
v_cvt_pk_bf16_f32 v27,v70,v71
v_pk_mul_f32 v[96:97],v[96:97],v[236:237] op_sel:[0,1] op_sel_hi:[1,1]
v_pk_mul_f32 v[98:99],v[98:99],v[236:237] op_sel:[0,1] op_sel_hi:[1,1]
v_pk_mul_f32 v[100:101],v[100:101],v[236:237] op_sel:[0,1] op_sel_hi:[1,1]
v_pk_mul_f32 v[102:103],v[102:103],v[236:237] op_sel:[0,1] op_sel_hi:[1,1]
v_cvt_pk_bf16_f32 v28,v96,v97
v_cvt_pk_bf16_f32 v29,v98,v99
v_cvt_pk_bf16_f32 v30,v100,v101
v_cvt_pk_bf16_f32 v31,v102,v103
v_pk_mul_f32 v[72:73],v[72:73],v[236:237] op_sel:[0,0] op_sel_hi:[1,0]
v_pk_mul_f32 v[74:75],v[74:75],v[236:237] op_sel:[0,0] op_sel_hi:[1,0]
v_pk_mul_f32 v[76:77],v[76:77],v[236:237] op_sel:[0,0] op_sel_hi:[1,0]
v_pk_mul_f32 v[78:79],v[78:79],v[236:237] op_sel:[0,0] op_sel_hi:[1,0]
v_cvt_pk_bf16_f32 v32,v72,v73
v_cvt_pk_bf16_f32 v33,v74,v75
)"
R"(v_cvt_pk_bf16_f32 v34,v76,v77
v_cvt_pk_bf16_f32 v35,v78,v79
v_pk_mul_f32 v[104:105],v[104:105],v[236:237] op_sel:[0,1] op_sel_hi:[1,1]
v_pk_mul_f32 v[106:107],v[106:107],v[236:237] op_sel:[0,1] op_sel_hi:[1,1]
v_pk_mul_f32 v[108:109],v[108:109],v[236:237] op_sel:[0,1] op_sel_hi:[1,1]
v_pk_mul_f32 v[110:111],v[110:111],v[236:237] op_sel:[0,1] op_sel_hi:[1,1]
v_cvt_pk_bf16_f32 v36,v104,v105
v_cvt_pk_bf16_f32 v37,v106,v107
v_cvt_pk_bf16_f32 v38,v108,v109
v_cvt_pk_bf16_f32 v39,v110,v111
v_pk_mul_f32 v[80:81],v[80:81],v[236:237] op_sel:[0,0] op_sel_hi:[1,0]
v_pk_mul_f32 v[82:83],v[82:83],v[236:237] op_sel:[0,0] op_sel_hi:[1,0]
v_pk_mul_f32 v[84:85],v[84:85],v[236:237] op_sel:[0,0] op_sel_hi:[1,0]
v_pk_mul_f32 v[86:87],v[86:87],v[236:237] op_sel:[0,0] op_sel_hi:[1,0]
v_cvt_pk_bf16_f32 v40,v80,v81
v_cvt_pk_bf16_f32 v41,v82,v83
v_cvt_pk_bf16_f32 v42,v84,v85
v_cvt_pk_bf16_f32 v43,v86,v87
v_pk_mul_f32 v[112:113],v[112:113],v[236:237] op_sel:[0,1] op_sel_hi:[1,1]
v_pk_mul_f32 v[114:115],v[114:115],v[236:237] op_sel:[0,1] op_sel_hi:[1,1]
v_pk_mul_f32 v[116:117],v[116:117],v[236:237] op_sel:[0,1] op_sel_hi:[1,1]
v_pk_mul_f32 v[118:119],v[118:119],v[236:237] op_sel:[0,1] op_sel_hi:[1,1]
v_cvt_pk_bf16_f32 v44,v112,v113
v_cvt_pk_bf16_f32 v45,v114,v115
)"
R"(v_cvt_pk_bf16_f32 v46,v116,v117
v_cvt_pk_bf16_f32 v47,v118,v119
v_pk_mul_f32 v[88:89],v[88:89],v[236:237] op_sel:[0,0] op_sel_hi:[1,0]
v_pk_mul_f32 v[90:91],v[90:91],v[236:237] op_sel:[0,0] op_sel_hi:[1,0]
v_pk_mul_f32 v[92:93],v[92:93],v[236:237] op_sel:[0,0] op_sel_hi:[1,0]
v_pk_mul_f32 v[94:95],v[94:95],v[236:237] op_sel:[0,0] op_sel_hi:[1,0]
v_cvt_pk_bf16_f32 v48,v88,v89
v_cvt_pk_bf16_f32 v49,v90,v91
v_cvt_pk_bf16_f32 v50,v92,v93
v_cvt_pk_bf16_f32 v51,v94,v95
v_pk_mul_f32 v[120:121],v[120:121],v[236:237] op_sel:[0,1] op_sel_hi:[1,1]
v_pk_mul_f32 v[122:123],v[122:123],v[236:237] op_sel:[0,1] op_sel_hi:[1,1]
v_pk_mul_f32 v[124:125],v[124:125],v[236:237] op_sel:[0,1] op_sel_hi:[1,1]
v_pk_mul_f32 v[126:127],v[126:127],v[236:237] op_sel:[0,1] op_sel_hi:[1,1]
v_cvt_pk_bf16_f32 v52,v120,v121
v_cvt_pk_bf16_f32 v53,v122,v123
v_cvt_pk_bf16_f32 v54,v124,v125
v_cvt_pk_bf16_f32 v55,v126,v127
v_mfma_f32_16x16x32_bf16 v[160:163],a[64:67],a[0:3],v[208:211]
ds_read_b128 a[96:99],v240 offset:24576
buffer_load_dwordx4 v18,s[48:51],s75 offen offset:0 sc1 lds
v_cvt_pk_bf16_f32 v128,v128,v129
v_mfma_f32_16x16x32_bf16 v[160:163],a[68:71],a[4:7],v[160:163]
v_exp_f32 v144,v144
)"
R"(v_mfma_f32_16x16x32_bf16 v[168:171],a[64:67],a[16:19],v[216:219]
ds_read_b128 a[100:103],v240 offset:25600
v_cvt_pk_bf16_f32 v129,v130,v131
v_mfma_f32_16x16x32_bf16 v[168:171],a[68:71],a[20:23],v[168:171]
buffer_load_dwordx4 v18,s[48:51],s75 offen offset:1024 sc1 lds
v_exp_f32 v145,v145
v_mfma_f32_16x16x32_bf16 v[160:163],a[72:75],a[8:11],v[160:163]
ds_read_b128 a[104:107],v240 offset:26624
v_cvt_pk_bf16_f32 v130,v132,v133
v_mfma_f32_16x16x32_bf16 v[160:163],a[76:79],a[12:15],v[160:163]
v_exp_f32 v146,v146
v_mfma_f32_16x16x32_bf16 v[168:171],a[72:75],a[24:27],v[168:171]
ds_read_b128 a[108:111],v240 offset:27648
buffer_load_dwordx4 v18,s[48:51],s75 offen offset:2048 sc1 lds
v_cvt_pk_bf16_f32 v131,v134,v135
v_mfma_f32_16x16x32_bf16 v[168:171],a[76:79],a[28:31],v[168:171]
v_exp_f32 v147,v147
v_mfma_f32_16x16x32_bf16 v[164:167],a[80:83],a[0:3],v[212:215]
ds_read_b128 a[112:115],v240 offset:28672
v_cvt_pk_bf16_f32 v136,v136,v137
v_mfma_f32_16x16x32_bf16 v[164:167],a[84:87],a[4:7],v[164:167]
buffer_load_dwordx4 v18,s[48:51],s75 offen offset:3072 sc1 lds
v_exp_f32 v148,v148
v_mfma_f32_16x16x32_bf16 v[172:175],a[80:83],a[16:19],v[220:223]
)"
R"(s_add_u32 m0,s65,0
ds_read_b128 a[116:119],v240 offset:29696
v_cvt_pk_bf16_f32 v137,v138,v139
v_mfma_f32_16x16x32_bf16 v[172:175],a[84:87],a[20:23],v[172:175]
v_exp_f32 v149,v149
v_mfma_f32_16x16x32_bf16 v[164:167],a[88:91],a[8:11],v[164:167]
ds_read_b128 a[120:123],v240 offset:30720
buffer_load_dwordx4 v18,s[36:39],s58 offen offset:0 sc1 lds
v_cvt_pk_bf16_f32 v138,v140,v141
v_mfma_f32_16x16x32_bf16 v[164:167],a[92:95],a[12:15],v[164:167]
v_exp_f32 v150,v150
v_mfma_f32_16x16x32_bf16 v[172:175],a[88:91],a[24:27],v[172:175]
ds_read_b128 a[124:127],v240 offset:31744
v_cvt_pk_bf16_f32 v139,v142,v143
v_mfma_f32_16x16x32_bf16 v[172:175],a[92:95],a[28:31],v[172:175]
s_waitcnt lgkmcnt(8)
v_exp_f32 v151,v151
v_mfma_f32_16x16x32_bf16 v[64:67],a[128:131],v[128:131],0
ds_read_b128 a[160:163],v241 offset:8192
buffer_load_dwordx4 v18,s[36:39],s58 offen offset:1024 sc1 lds
v_add_u32 v240,s62,v240
v_max3_f32 v248,v160,v161,v162
v_mfma_f32_16x16x32_bf16 v[96:99],a[128:131],v[136:139],0
v_exp_f32 v152,v152
)"
R"(v_mfma_f32_16x16x32_bf16 v[68:71],a[132:135],v[128:131],0
ds_read_b128 a[164:167],v241 offset:9216
v_max3_f32 v248,v248,v163,v168
v_mfma_f32_16x16x32_bf16 v[100:103],a[132:135],v[136:139],0
buffer_load_dwordx4 v18,s[36:39],s58 offen offset:2048 sc1 lds
v_exp_f32 v153,v153
v_mfma_f32_16x16x32_bf16 v[72:75],a[136:139],v[128:131],0
ds_read_b128 a[168:171],v241 offset:10240
v_max3_f32 v248,v248,v169,v170
v_mfma_f32_16x16x32_bf16 v[104:107],a[136:139],v[136:139],0
v_exp_f32 v154,v154
v_mfma_f32_16x16x32_bf16 v[76:79],a[140:143],v[128:131],0
ds_read_b128 a[172:175],v241 offset:11264
buffer_load_dwordx4 v18,s[36:39],s58 offen offset:3072 sc1 lds
v_max3_f32 v248,v248,v171,v164
v_mfma_f32_16x16x32_bf16 v[108:111],a[140:143],v[136:139],0
s_add_u32 m0,s65,4096
v_exp_f32 v155,v155
v_mfma_f32_16x16x32_bf16 v[80:83],a[144:147],v[128:131],0
s_add_u32 s75,s58,4096
ds_read_b128 a[176:179],v241 offset:12288
v_max3_f32 v248,v248,v165,v166
v_mfma_f32_16x16x32_bf16 v[112:115],a[144:147],v[136:139],0
buffer_load_dwordx4 v18,s[36:39],s75 offen offset:0 sc1 lds
)"
R"(v_exp_f32 v156,v156
v_mfma_f32_16x16x32_bf16 v[84:87],a[148:151],v[128:131],0
ds_read_b128 a[180:183],v241 offset:13312
v_max3_f32 v248,v248,v167,v172
v_mfma_f32_16x16x32_bf16 v[116:119],a[148:151],v[136:139],0
v_exp_f32 v157,v157
v_mfma_f32_16x16x32_bf16 v[88:91],a[152:155],v[128:131],0
ds_read_b128 a[184:187],v241 offset:14336
buffer_load_dwordx4 v18,s[36:39],s75 offen offset:1024 sc1 lds
v_max3_f32 v248,v248,v173,v174
v_mfma_f32_16x16x32_bf16 v[120:123],a[152:155],v[136:139],0
v_exp_f32 v158,v158
v_mfma_f32_16x16x32_bf16 v[92:95],a[156:159],v[128:131],0
ds_read_b128 a[188:191],v241 offset:15360
v_max_f32 v248,v248,v175
v_mfma_f32_16x16x32_bf16 v[124:127],a[156:159],v[136:139],0
buffer_load_dwordx4 v18,s[36:39],s75 offen offset:2048 sc1 lds
v_exp_f32 v159,v159
v_mfma_f32_16x16x32_bf16 v[200:203],v[228:231],v[128:131],0
v_cmp_lt_f32 vcc,s12,v248
v_mfma_f32_16x16x32_bf16 v[204:207],v[228:231],v[136:139],0
s_waitcnt lgkmcnt(8)
s_cbranch_vccnz gm_K2_stub13
gm_K2_ret13:
)"
R"(v_mfma_f32_16x16x32_bf16 v[176:179],a[96:99],a[0:3],v[208:211]
ds_read_b128 a[64:67],v240 offset:0
buffer_load_dwordx4 v18,s[36:39],s75 offen offset:3072 sc1 lds
v_cvt_pk_bf16_f32 v144,v144,v145
v_mfma_f32_16x16x32_bf16 v[176:179],a[100:103],a[4:7],v[176:179]
s_mov_b32 s15,0
v_exp_f32 v160,v160
v_mfma_f32_16x16x32_bf16 v[184:187],a[96:99],a[16:19],v[216:219]
ds_read_b128 a[68:71],v240 offset:1024
v_cvt_pk_bf16_f32 v145,v146,v147
v_mfma_f32_16x16x32_bf16 v[184:187],a[100:103],a[20:23],v[184:187]
buffer_store_dwordx4 v[24:27],v18,s[44:47],s15 offen sc1
v_exp_f32 v161,v161
v_mfma_f32_16x16x32_bf16 v[176:179],a[104:107],a[8:11],v[176:179]
ds_read_b128 a[72:75],v240 offset:2048
v_cvt_pk_bf16_f32 v146,v148,v149
v_mfma_f32_16x16x32_bf16 v[176:179],a[108:111],a[12:15],v[176:179]
v_exp_f32 v162,v162
v_mfma_f32_16x16x32_bf16 v[184:187],a[104:107],a[24:27],v[184:187]
ds_read_b128 a[76:79],v240 offset:3072
buffer_store_dwordx4 v[28:31],v58,s[44:47],s15 offen sc1
v_cvt_pk_bf16_f32 v147,v150,v151
v_mfma_f32_16x16x32_bf16 v[184:187],a[108:111],a[28:31],v[184:187]
s_mov_b32 s15,8192
)"
R"(v_exp_f32 v163,v163
s_branch gm_KB_sk17
.p2align 12
gm_KB_pg17:
s_setpc_b64 s[100:101]
gm_KB_sk17:
v_mfma_f32_16x16x32_bf16 v[180:183],a[112:115],a[0:3],v[212:215]
ds_read_b128 a[80:83],v240 offset:4096
v_cvt_pk_bf16_f32 v152,v152,v153
v_mfma_f32_16x16x32_bf16 v[180:183],a[116:119],a[4:7],v[180:183]
buffer_store_dwordx4 v[32:35],v18,s[44:47],s15 offen sc1
v_exp_f32 v164,v164
v_mfma_f32_16x16x32_bf16 v[188:191],a[112:115],a[16:19],v[220:223]
ds_read_b128 a[84:87],v240 offset:5120
v_cvt_pk_bf16_f32 v153,v154,v155
v_mfma_f32_16x16x32_bf16 v[188:191],a[116:119],a[20:23],v[188:191]
v_exp_f32 v165,v165
v_mfma_f32_16x16x32_bf16 v[180:183],a[120:123],a[8:11],v[180:183]
ds_read_b128 a[88:91],v240 offset:6144
buffer_store_dwordx4 v[36:39],v58,s[44:47],s15 offen sc1
v_cvt_pk_bf16_f32 v154,v156,v157
v_mfma_f32_16x16x32_bf16 v[180:183],a[124:127],a[12:15],v[180:183]
s_mov_b32 s15,16384
v_exp_f32 v166,v166
)"
R"(v_mfma_f32_16x16x32_bf16 v[188:191],a[120:123],a[24:27],v[188:191]
ds_read_b128 a[92:95],v240 offset:7168
v_cvt_pk_bf16_f32 v155,v158,v159
v_mfma_f32_16x16x32_bf16 v[188:191],a[124:127],a[28:31],v[188:191]
s_waitcnt lgkmcnt(8)
v_exp_f32 v167,v167
v_mfma_f32_16x16x32_bf16 v[64:67],a[160:163],v[144:147],v[64:67]
ds_read_b128 a[128:131],v241 offset:16384
buffer_store_dwordx4 v[40:43],v18,s[44:47],s15 offen sc1
v_max3_f32 v248,v176,v177,v178
v_mfma_f32_16x16x32_bf16 v[96:99],a[160:163],v[152:155],v[96:99]
v_exp_f32 v168,v168
v_mfma_f32_16x16x32_bf16 v[68:71],a[164:167],v[144:147],v[68:71]
ds_read_b128 a[132:135],v241 offset:17408
v_max3_f32 v248,v248,v179,v184
v_mfma_f32_16x16x32_bf16 v[100:103],a[164:167],v[152:155],v[100:103]
buffer_store_dwordx4 v[44:47],v58,s[44:47],s15 offen sc1
v_exp_f32 v169,v169
v_mfma_f32_16x16x32_bf16 v[72:75],a[168:171],v[144:147],v[72:75]
s_mov_b32 s15,24576
ds_read_b128 a[136:139],v241 offset:18432
v_max3_f32 v248,v248,v185,v186
v_mfma_f32_16x16x32_bf16 v[104:107],a[168:171],v[152:155],v[104:107]
v_exp_f32 v170,v170
)"
R"(v_mfma_f32_16x16x32_bf16 v[76:79],a[172:175],v[144:147],v[76:79]
ds_read_b128 a[140:143],v241 offset:19456
buffer_store_dwordx4 v[48:51],v18,s[44:47],s15 offen sc1
v_max3_f32 v248,v248,v187,v180
v_mfma_f32_16x16x32_bf16 v[108:111],a[172:175],v[152:155],v[108:111]
v_exp_f32 v171,v171
v_mfma_f32_16x16x32_bf16 v[80:83],a[176:179],v[144:147],v[80:83]
ds_read_b128 a[144:147],v241 offset:20480
v_max3_f32 v248,v248,v181,v182
v_mfma_f32_16x16x32_bf16 v[112:115],a[176:179],v[152:155],v[112:115]
buffer_store_dwordx4 v[52:55],v58,s[44:47],s15 offen sc1
v_exp_f32 v172,v172
v_mfma_f32_16x16x32_bf16 v[84:87],a[180:183],v[144:147],v[84:87]
ds_read_b128 a[148:151],v241 offset:21504
v_max3_f32 v248,v248,v183,v188
v_mfma_f32_16x16x32_bf16 v[116:119],a[180:183],v[152:155],v[116:119]
v_exp_f32 v173,v173
v_mfma_f32_16x16x32_bf16 v[88:91],a[184:187],v[144:147],v[88:91]
ds_read_b128 a[152:155],v241 offset:22528
buffer_load_dword v255,v254,s[92:95],s13 offen
v_max3_f32 v248,v248,v189,v190
v_mfma_f32_16x16x32_bf16 v[120:123],a[184:187],v[152:155],v[120:123]
v_exp_f32 v174,v174
v_mfma_f32_16x16x32_bf16 v[92:95],a[188:191],v[144:147],v[92:95]
)"
R"(ds_read_b128 a[156:159],v241 offset:23552
v_max_f32 v248,v248,v191
v_mfma_f32_16x16x32_bf16 v[124:127],a[188:191],v[152:155],v[124:127]
v_exp_f32 v175,v175
v_mfma_f32_16x16x32_bf16 v[200:203],v[228:231],v[144:147],v[200:203]
v_cmp_lt_f32 vcc,s12,v248
v_mfma_f32_16x16x32_bf16 v[204:207],v[228:231],v[152:155],v[204:207]
s_waitcnt lgkmcnt(8)
s_cbranch_vccnz gm_K2_stub14
gm_K2_ret14:
s_mov_b32 s68,1
s_sub_u32 s67,14,s55
s_branch gm_K2_body
gm_KB_tp_last:
s_setpc_b64 s[100:101]
gm_K2_last:
s_sub_u32 s85,15,s55
s_lshl_b32 s15,s85,7
s_lshl_b32 s83,s53,2
s_add_u32 s15,s15,s83
s_lshl_b32 s15,s15,13
s_lshl_b32 s83,s70,10
s_add_u32 s15,s15,s83
s_add_u32 s15,s15,67108864
)"
R"(s_add_u32 s44,s30,s15
s_addc_u32 s45,s31,0
s_and_b32 s45,s45,0xffff
s_mov_b32 s46,32768
s_mov_b32 s47,0x20000
s_lshl_b32 s82,%2,13
s_add_u32 s82,s82,73728
s_getpc_b64 s[88:89]
gm_CPF6:
s_add_u32 s88,s88,gm_KB_start-gm_CPF6
s_addc_u32 s89,s89,(gm_KB_start-gm_CPF6)>>32
s_and_b32 s89,s89,0xffff
s_mov_b32 s90,gm_KB_end-gm_KB_start
s_mov_b32 s91,0x20000
v_mbcnt_lo_u32_b32 v254,-1,0
v_mbcnt_hi_u32_b32 v254,-1,v254
v_lshlrev_b32 v254,7,v254
buffer_load_dword v255,v254,s[88:91],s82 offen
v_min_f32 v208,v208,v12
v_min_f32 v209,v209,v13
v_min_f32 v210,v210,v16
v_min_f32 v211,v211,v17
v_min_f32 v212,v212,v20
v_min_f32 v213,v213,v21
)"
R"(v_min_f32 v214,v214,v22
v_min_f32 v215,v215,v23
v_mfma_f32_16x16x32_bf16 v[128:131],a[64:67],a[0:3],v[208:211]
ds_read_b128 a[96:99],v240 offset:8192
v_cvt_pk_bf16_f32 v160,v160,v161
v_mfma_f32_16x16x32_bf16 v[128:131],a[68:71],a[4:7],v[128:131]
v_exp_f32 v176,v176
v_mfma_f32_16x16x32_bf16 v[136:139],a[64:67],a[16:19],v[216:219]
ds_read_b128 a[100:103],v240 offset:9216
v_cvt_pk_bf16_f32 v161,v162,v163
v_mfma_f32_16x16x32_bf16 v[136:139],a[68:71],a[20:23],v[136:139]
v_exp_f32 v177,v177
v_mfma_f32_16x16x32_bf16 v[128:131],a[72:75],a[8:11],v[128:131]
ds_read_b128 a[104:107],v240 offset:10240
v_cvt_pk_bf16_f32 v162,v164,v165
v_mfma_f32_16x16x32_bf16 v[128:131],a[76:79],a[12:15],v[128:131]
v_exp_f32 v178,v178
v_mfma_f32_16x16x32_bf16 v[136:139],a[72:75],a[24:27],v[136:139]
ds_read_b128 a[108:111],v240 offset:11264
v_cvt_pk_bf16_f32 v163,v166,v167
v_mfma_f32_16x16x32_bf16 v[136:139],a[76:79],a[28:31],v[136:139]
v_exp_f32 v179,v179
v_mfma_f32_16x16x32_bf16 v[132:135],a[80:83],a[0:3],v[212:215]
ds_read_b128 a[112:115],v240 offset:12288
)"
R"(v_cvt_pk_bf16_f32 v168,v168,v169
v_mfma_f32_16x16x32_bf16 v[132:135],a[84:87],a[4:7],v[132:135]
v_exp_f32 v180,v180
v_mfma_f32_16x16x32_bf16 v[140:143],a[80:83],a[16:19],v[220:223]
ds_read_b128 a[116:119],v240 offset:13312
v_cvt_pk_bf16_f32 v169,v170,v171
v_mfma_f32_16x16x32_bf16 v[140:143],a[84:87],a[20:23],v[140:143]
v_exp_f32 v181,v181
v_mfma_f32_16x16x32_bf16 v[132:135],a[88:91],a[8:11],v[132:135]
ds_read_b128 a[120:123],v240 offset:14336
v_cvt_pk_bf16_f32 v170,v172,v173
v_mfma_f32_16x16x32_bf16 v[132:135],a[92:95],a[12:15],v[132:135]
v_exp_f32 v182,v182
v_mfma_f32_16x16x32_bf16 v[140:143],a[88:91],a[24:27],v[140:143]
ds_read_b128 a[124:127],v240 offset:15360
v_cvt_pk_bf16_f32 v171,v174,v175
v_mfma_f32_16x16x32_bf16 v[140:143],a[92:95],a[28:31],v[140:143]
s_waitcnt lgkmcnt(8)
v_exp_f32 v183,v183
v_mfma_f32_16x16x32_bf16 v[64:67],a[128:131],v[160:163],v[64:67]
ds_read_b128 a[160:163],v241 offset:24576
v_min_f32 v208,v208,v56
v_max3_f32 v248,v128,v129,v130
v_mfma_f32_16x16x32_bf16 v[96:99],a[128:131],v[168:171],v[96:99]
)"
R"(v_exp_f32 v184,v184
v_mfma_f32_16x16x32_bf16 v[68:71],a[132:135],v[160:163],v[68:71]
ds_read_b128 a[164:167],v241 offset:25600
v_min_f32 v209,v209,v57
v_max3_f32 v248,v248,v131,v136
v_mfma_f32_16x16x32_bf16 v[100:103],a[132:135],v[168:171],v[100:103]
v_exp_f32 v185,v185
v_mfma_f32_16x16x32_bf16 v[72:75],a[136:139],v[160:163],v[72:75]
ds_read_b128 a[168:171],v241 offset:26624
v_min_f32 v210,v210,v59
v_max3_f32 v248,v248,v137,v138
v_mfma_f32_16x16x32_bf16 v[104:107],a[136:139],v[168:171],v[104:107]
v_exp_f32 v186,v186
v_mfma_f32_16x16x32_bf16 v[76:79],a[140:143],v[160:163],v[76:79]
ds_read_b128 a[172:175],v241 offset:27648
v_min_f32 v211,v211,v60
v_max3_f32 v248,v248,v139,v132
v_mfma_f32_16x16x32_bf16 v[108:111],a[140:143],v[168:171],v[108:111]
v_exp_f32 v187,v187
v_mfma_f32_16x16x32_bf16 v[80:83],a[144:147],v[160:163],v[80:83]
ds_read_b128 a[176:179],v241 offset:28672
v_min_f32 v212,v212,v61
v_max3_f32 v248,v248,v133,v134
v_mfma_f32_16x16x32_bf16 v[112:115],a[144:147],v[168:171],v[112:115]
)"
R"(v_exp_f32 v188,v188
v_mfma_f32_16x16x32_bf16 v[84:87],a[148:151],v[160:163],v[84:87]
ds_read_b128 a[180:183],v241 offset:29696
v_min_f32 v213,v213,v62
v_max3_f32 v248,v248,v135,v140
v_mfma_f32_16x16x32_bf16 v[116:119],a[148:151],v[168:171],v[116:119]
v_exp_f32 v189,v189
v_mfma_f32_16x16x32_bf16 v[88:91],a[152:155],v[160:163],v[88:91]
ds_read_b128 a[184:187],v241 offset:30720
v_min_f32 v214,v214,v192
v_max3_f32 v248,v248,v141,v142
v_mfma_f32_16x16x32_bf16 v[120:123],a[152:155],v[168:171],v[120:123]
v_exp_f32 v190,v190
v_mfma_f32_16x16x32_bf16 v[92:95],a[156:159],v[160:163],v[92:95]
ds_read_b128 a[188:191],v241 offset:31744
v_min_f32 v215,v215,v193
v_max_f32 v248,v248,v143
v_mfma_f32_16x16x32_bf16 v[124:127],a[156:159],v[168:171],v[124:127]
v_exp_f32 v191,v191
v_mfma_f32_16x16x32_bf16 v[200:203],v[228:231],v[160:163],v[200:203]
v_cmp_lt_f32 vcc,s12,v248
v_mfma_f32_16x16x32_bf16 v[204:207],v[228:231],v[168:171],v[204:207]
s_waitcnt lgkmcnt(8)
s_cbranch_vccnz gm_K2_stub15
)"
R"(gm_K2_ret15:
v_mfma_f32_16x16x32_bf16 v[144:147],a[96:99],a[0:3],v[208:211]
ds_read_b128 a[64:67],v240 offset:16384
v_cvt_pk_bf16_f32 v176,v176,v177
v_mfma_f32_16x16x32_bf16 v[144:147],a[100:103],a[4:7],v[144:147]
v_exp_f32 v128,v128
v_mfma_f32_16x16x32_bf16 v[152:155],a[96:99],a[16:19],v[216:219]
ds_read_b128 a[68:71],v240 offset:17408
v_cvt_pk_bf16_f32 v177,v178,v179
v_mfma_f32_16x16x32_bf16 v[152:155],a[100:103],a[20:23],v[152:155]
v_exp_f32 v129,v129
v_mfma_f32_16x16x32_bf16 v[144:147],a[104:107],a[8:11],v[144:147]
ds_read_b128 a[72:75],v240 offset:18432
v_cvt_pk_bf16_f32 v178,v180,v181
v_mfma_f32_16x16x32_bf16 v[144:147],a[108:111],a[12:15],v[144:147]
v_exp_f32 v130,v130
v_mfma_f32_16x16x32_bf16 v[152:155],a[104:107],a[24:27],v[152:155]
ds_read_b128 a[76:79],v240 offset:19456
v_cvt_pk_bf16_f32 v179,v182,v183
v_mfma_f32_16x16x32_bf16 v[152:155],a[108:111],a[28:31],v[152:155]
v_exp_f32 v131,v131
v_mfma_f32_16x16x32_bf16 v[148:151],a[112:115],a[0:3],v[212:215]
ds_read_b128 a[80:83],v240 offset:20480
v_cvt_pk_bf16_f32 v184,v184,v185
)"
R"(v_mfma_f32_16x16x32_bf16 v[148:151],a[116:119],a[4:7],v[148:151]
v_exp_f32 v132,v132
v_mfma_f32_16x16x32_bf16 v[156:159],a[112:115],a[16:19],v[220:223]
ds_read_b128 a[84:87],v240 offset:21504
v_cvt_pk_bf16_f32 v185,v186,v187
v_mfma_f32_16x16x32_bf16 v[156:159],a[116:119],a[20:23],v[156:159]
v_exp_f32 v133,v133
v_mfma_f32_16x16x32_bf16 v[148:151],a[120:123],a[8:11],v[148:151]
ds_read_b128 a[88:91],v240 offset:22528
v_xor_b32 v241,0x38000,v241
v_cvt_pk_bf16_f32 v186,v188,v189
v_mfma_f32_16x16x32_bf16 v[148:151],a[124:127],a[12:15],v[148:151]
v_exp_f32 v134,v134
v_mfma_f32_16x16x32_bf16 v[156:159],a[120:123],a[24:27],v[156:159]
ds_read_b128 a[92:95],v240 offset:23552
v_cvt_pk_bf16_f32 v187,v190,v191
v_mfma_f32_16x16x32_bf16 v[156:159],a[124:127],a[28:31],v[156:159]
s_waitcnt vmcnt(0) lgkmcnt(8)
s_barrier
v_mfma_f32_16x16x32_bf16 v[64:67],a[160:163],v[176:179],v[64:67]
s_lshl_b32 s56,s70,13
ds_read_b128 a[128:131],v241 offset:0
s_add_u32 s57,s56,0x20000
v_min_f32 v216,v216,v194
)"
R"(v_max3_f32 v248,v144,v145,v146
v_max3_f32 v248,v248,v147,v152
v_mfma_f32_16x16x32_bf16 v[96:99],a[160:163],v[184:187],v[96:99]
s_add_u32 s56,s56,0x8000
v_exp_f32 v135,v135
v_mfma_f32_16x16x32_bf16 v[68:71],a[164:167],v[176:179],v[68:71]
s_mov_b32 s59,0x10000
ds_read_b128 a[132:135],v241 offset:1024
v_xor_b32 v0,0x8000,v0
v_min_f32 v217,v217,v195
v_max3_f32 v248,v248,v153,v154
v_mfma_f32_16x16x32_bf16 v[100:103],a[164:167],v[184:187],v[100:103]
v_exp_f32 v136,v136
v_mfma_f32_16x16x32_bf16 v[72:75],a[168:171],v[176:179],v[72:75]
ds_read_b128 a[136:139],v241 offset:2048
v_min_f32 v218,v218,v196
v_max3_f32 v248,v248,v155,v148
v_mfma_f32_16x16x32_bf16 v[104:107],a[168:171],v[184:187],v[104:107]
v_exp_f32 v137,v137
v_mfma_f32_16x16x32_bf16 v[76:79],a[172:175],v[176:179],v[76:79]
ds_read_b128 a[140:143],v241 offset:3072
v_xor_b32 v1,0x8000,v1
v_min_f32 v219,v219,v197
v_max3_f32 v248,v248,v149,v150
)"
R"(v_mfma_f32_16x16x32_bf16 v[108:111],a[172:175],v[184:187],v[108:111]
s_add_u32 m0,s57,0
v_exp_f32 v138,v138
v_mfma_f32_16x16x32_bf16 v[80:83],a[176:179],v[176:179],v[80:83]
s_add_u32 s63,s54,0
ds_read_b128 a[144:147],v241 offset:4096
v_min_f32 v220,v220,v198
v_max3_f32 v248,v248,v151,v156
v_mfma_f32_16x16x32_bf16 v[112:115],a[176:179],v[184:187],v[112:115]
v_exp_f32 v139,v139
v_mfma_f32_16x16x32_bf16 v[84:87],a[180:183],v[176:179],v[84:87]
ds_read_b128 a[148:151],v241 offset:5120
buffer_load_dwordx4 v14,s[40:43],s63 offen lds
v_min_f32 v221,v221,v199
v_max3_f32 v248,v248,v157,v158
v_mfma_f32_16x16x32_bf16 v[116:119],a[180:183],v[184:187],v[116:119]
s_add_u32 m0,s57,1024
v_exp_f32 v140,v140
v_mfma_f32_16x16x32_bf16 v[88:91],a[184:187],v[176:179],v[88:91]
s_add_u32 s64,s54,28672
ds_read_b128 a[152:155],v241 offset:6144
v_min_f32 v222,v222,v224
v_max_f32 v248,v248,v159
v_mfma_f32_16x16x32_bf16 v[120:123],a[184:187],v[184:187],v[120:123]
)"
R"(v_exp_f32 v141,v141
v_mfma_f32_16x16x32_bf16 v[92:95],a[188:191],v[176:179],v[92:95]
ds_read_b128 a[156:159],v241 offset:7168
buffer_load_dwordx4 v14,s[40:43],s64 offen lds
v_min_f32 v223,v223,v225
v_cmp_lt_f32 vcc,s12,v248
v_mfma_f32_16x16x32_bf16 v[124:127],a[188:191],v[184:187],v[124:127]
s_add_u32 m0,s57,2048
v_exp_f32 v142,v142
v_mfma_f32_16x16x32_bf16 v[200:203],v[228:231],v[176:179],v[200:203]
s_add_u32 s63,s54,57344
v_exp_f32 v143,v143
v_mfma_f32_16x16x32_bf16 v[204:207],v[228:231],v[184:187],v[204:207]
s_waitcnt lgkmcnt(8)
s_cbranch_vccnz gm_K2_stub16
gm_K2_ret16:
ds_read_b128 a[96:99],v240 offset:24576
buffer_load_dwordx4 v15,s[40:43],s63 offen lds
s_add_u32 m0,s57,3072
v_cvt_pk_bf16_f32 v128,v128,v129
v_exp_f32 v144,v144
v_mfma_f32_16x16x32_bf16 v[168:171],a[64:67],a[16:19],v[216:219]
s_add_u32 s64,s54,86016
ds_read_b128 a[100:103],v240 offset:25600
)"
R"(v_cvt_pk_bf16_f32 v129,v130,v131
v_mfma_f32_16x16x32_bf16 v[168:171],a[68:71],a[20:23],v[168:171]
ds_read_b128 a[104:107],v240 offset:26624
buffer_load_dwordx4 v15,s[40:43],s64 offen lds
s_add_u32 m0,s57,4096
v_cvt_pk_bf16_f32 v130,v132,v133
v_exp_f32 v145,v145
v_exp_f32 v146,v146
v_mfma_f32_16x16x32_bf16 v[168:171],a[72:75],a[24:27],v[168:171]
s_add_u32 s63,s54,114688
ds_read_b128 a[108:111],v240 offset:27648
v_cvt_pk_bf16_f32 v131,v134,v135
v_mfma_f32_16x16x32_bf16 v[168:171],a[76:79],a[28:31],v[168:171]
ds_read_b128 a[112:115],v240 offset:28672
buffer_load_dwordx4 v14,s[40:43],s63 offen lds
s_add_u32 m0,s57,5120
v_cvt_pk_bf16_f32 v136,v136,v137
v_exp_f32 v147,v147
v_exp_f32 v148,v148
v_mfma_f32_16x16x32_bf16 v[172:175],a[80:83],a[16:19],v[220:223]
s_add_u32 s64,s54,143360
ds_read_b128 a[116:119],v240 offset:29696
v_cvt_pk_bf16_f32 v137,v138,v139
v_mfma_f32_16x16x32_bf16 v[172:175],a[84:87],a[20:23],v[172:175]
)"
R"(ds_read_b128 a[120:123],v240 offset:30720
buffer_load_dwordx4 v14,s[40:43],s64 offen lds
s_add_u32 m0,s57,6144
v_cvt_pk_bf16_f32 v138,v140,v141
v_exp_f32 v149,v149
v_exp_f32 v150,v150
v_mfma_f32_16x16x32_bf16 v[172:175],a[88:91],a[24:27],v[172:175]
s_add_u32 s63,s54,172032
ds_read_b128 a[124:127],v240 offset:31744
v_cvt_pk_bf16_f32 v139,v142,v143
v_mfma_f32_16x16x32_bf16 v[172:175],a[92:95],a[28:31],v[172:175]
s_waitcnt lgkmcnt(8)
v_exp_f32 v151,v151
v_mfma_f32_16x16x32_bf16 v[64:67],a[128:131],v[128:131],v[64:67]
ds_read_b128 a[160:163],v241 offset:8192
buffer_load_dwordx4 v15,s[40:43],s63 offen lds
v_min_f32 v216,v216,v226
v_mfma_f32_16x16x32_bf16 v[96:99],a[128:131],v[136:139],v[96:99]
s_add_u32 m0,s57,7168
v_exp_f32 v152,v152
v_mfma_f32_16x16x32_bf16 v[68:71],a[132:135],v[128:131],v[68:71]
s_add_u32 s64,s54,200704
ds_read_b128 a[164:167],v241 offset:9216
v_min_f32 v217,v217,v227
)"
R"(v_mfma_f32_16x16x32_bf16 v[100:103],a[132:135],v[136:139],v[100:103]
v_exp_f32 v153,v153
v_mfma_f32_16x16x32_bf16 v[72:75],a[136:139],v[128:131],v[72:75]
ds_read_b128 a[168:171],v241 offset:10240
buffer_load_dwordx4 v15,s[40:43],s64 offen lds
v_min_f32 v218,v218,v245
v_mfma_f32_16x16x32_bf16 v[104:107],a[136:139],v[136:139],v[104:107]
s_add_u32 s57,s57,0x8000
v_exp_f32 v154,v154
v_mfma_f32_16x16x32_bf16 v[76:79],a[140:143],v[128:131],v[76:79]
s_cmp_ge_u32 s57,0x28000
s_cselect_b32 s65,0x18000,0
ds_read_b128 a[172:175],v241 offset:11264
v_min_f32 v219,v219,v246
v_mfma_f32_16x16x32_bf16 v[108:111],a[140:143],v[136:139],v[108:111]
s_sub_u32 s57,s57,s65
v_exp_f32 v155,v155
v_mfma_f32_16x16x32_bf16 v[80:83],a[144:147],v[128:131],v[80:83]
s_add_u32 s54,s54,458752
ds_read_b128 a[176:179],v241 offset:12288
s_cmp_eq_u32 s54,7340032
s_cselect_b32 s54,0,s54
v_min_f32 v220,v220,v247
v_max3_f32 v248,v168,v169,v170
)"
R"(v_mfma_f32_16x16x32_bf16 v[112:115],a[144:147],v[136:139],v[112:115]
s_add_u32 m0,s56,0
v_exp_f32 v156,v156
v_mfma_f32_16x16x32_bf16 v[84:87],a[148:151],v[128:131],v[84:87]
s_add_u32 s61,s52,0
ds_read_b128 a[180:183],v241 offset:13312
buffer_load_dwordx4 v10,s[32:35],s61 offen lds
v_min_f32 v221,v221,v249
v_max3_f32 v248,v248,v171,v172
v_mfma_f32_16x16x32_bf16 v[116:119],a[148:151],v[136:139],v[116:119]
s_add_u32 m0,s56,1024
v_exp_f32 v157,v157
v_mfma_f32_16x16x32_bf16 v[88:91],a[152:155],v[128:131],v[88:91]
s_add_u32 s62,s52,16384
ds_read_b128 a[184:187],v241 offset:14336
v_min_f32 v222,v222,v250
v_max3_f32 v248,v248,v173,v174
v_mfma_f32_16x16x32_bf16 v[120:123],a[152:155],v[136:139],v[120:123]
v_exp_f32 v158,v158
v_mfma_f32_16x16x32_bf16 v[92:95],a[156:159],v[128:131],v[92:95]
ds_read_b128 a[188:191],v241 offset:15360
buffer_load_dwordx4 v11,s[32:35],s62 offen lds
v_min_f32 v223,v223,v251
v_max_f32 v248,v248,v175
)"
R"(v_mfma_f32_16x16x32_bf16 v[124:127],a[156:159],v[136:139],v[124:127]
s_add_u32 m0,s56,2048
v_exp_f32 v159,v159
v_mfma_f32_16x16x32_bf16 v[200:203],v[228:231],v[128:131],v[200:203]
s_add_u32 s61,s52,32768
v_cmp_lt_f32 vcc,s12,v248
v_mfma_f32_16x16x32_bf16 v[204:207],v[228:231],v[136:139],v[204:207]
s_waitcnt lgkmcnt(8)
s_cbranch_vccnz gm_K2_stub17
gm_K2_ret17:
buffer_load_dwordx4 v10,s[32:35],s61 offen lds
s_add_u32 m0,s56,3072
v_cvt_pk_bf16_f32 v144,v144,v145
v_cvt_pk_bf16_f32 v145,v146,v147
v_mfma_f32_16x16x32_bf16 v[184:187],a[96:99],a[16:19],v[216:219]
s_add_u32 s62,s52,49152
v_cvt_pk_bf16_f32 v146,v148,v149
v_mfma_f32_16x16x32_bf16 v[184:187],a[100:103],a[20:23],v[184:187]
buffer_load_dwordx4 v11,s[32:35],s62 offen lds
s_add_u32 m0,s56,4096
v_cvt_pk_bf16_f32 v147,v150,v151
v_cvt_pk_bf16_f32 v152,v152,v153
v_exp_f32 v168,v168
v_mfma_f32_16x16x32_bf16 v[184:187],a[104:107],a[24:27],v[184:187]
)"
R"(s_add_u32 s61,s52,65536
v_cvt_pk_bf16_f32 v153,v154,v155
v_mfma_f32_16x16x32_bf16 v[184:187],a[108:111],a[28:31],v[184:187]
buffer_load_dwordx4 v10,s[32:35],s61 offen lds
s_add_u32 m0,s56,5120
v_cvt_pk_bf16_f32 v154,v156,v157
v_cvt_pk_bf16_f32 v155,v158,v159
v_exp_f32 v169,v169
v_mfma_f32_16x16x32_bf16 v[188:191],a[112:115],a[16:19],v[220:223]
s_add_u32 s62,s52,81920
v_mfma_f32_16x16x32_bf16 v[188:191],a[116:119],a[20:23],v[188:191]
buffer_load_dwordx4 v11,s[32:35],s62 offen lds
s_add_u32 m0,s56,6144
v_exp_f32 v170,v170
v_mfma_f32_16x16x32_bf16 v[188:191],a[120:123],a[24:27],v[188:191]
s_add_u32 s61,s52,98304
v_mfma_f32_16x16x32_bf16 v[188:191],a[124:127],a[28:31],v[188:191]
s_waitcnt lgkmcnt(0)
v_exp_f32 v171,v171
v_mfma_f32_16x16x32_bf16 v[64:67],a[160:163],v[144:147],v[64:67]
ds_read_b128 a[128:131],v241 offset:16384
buffer_load_dwordx4 v10,s[32:35],s61 offen lds
v_mfma_f32_16x16x32_bf16 v[96:99],a[160:163],v[152:155],v[96:99]
s_add_u32 m0,s56,7168
)"
R"(v_max3_f32 v248,v184,v185,v186
v_mfma_f32_16x16x32_bf16 v[68:71],a[164:167],v[144:147],v[68:71]
s_add_u32 s62,s52,114688
ds_read_b128 a[132:135],v241 offset:17408
v_mfma_f32_16x16x32_bf16 v[100:103],a[164:167],v[152:155],v[100:103]
v_exp_f32 v172,v172
v_mfma_f32_16x16x32_bf16 v[72:75],a[168:171],v[144:147],v[72:75]
ds_read_b128 a[136:139],v241 offset:18432
buffer_load_dwordx4 v11,s[32:35],s62 offen lds
v_mfma_f32_16x16x32_bf16 v[104:107],a[168:171],v[152:155],v[104:107]
s_xor_b32 s56,s56,0x8000
v_max3_f32 v248,v248,v187,v188
v_mfma_f32_16x16x32_bf16 v[76:79],a[172:175],v[144:147],v[76:79]
s_add_u32 s52,s52,128
ds_read_b128 a[140:143],v241 offset:19456
v_max3_f32 v248,v248,v189,v190
v_mfma_f32_16x16x32_bf16 v[108:111],a[172:175],v[152:155],v[108:111]
v_exp_f32 v173,v173
v_mfma_f32_16x16x32_bf16 v[80:83],a[176:179],v[144:147],v[80:83]
s_cmp_eq_u32 s52,2048
s_cselect_b32 s52,0,s52
ds_read_b128 a[144:147],v241 offset:20480
s_add_u32 m0,s57,0
v_mfma_f32_16x16x32_bf16 v[112:115],a[176:179],v[152:155],v[112:115]
)"
R"(s_add_u32 s63,s54,0
buffer_load_dwordx4 v14,s[40:43],s63 offen lds
v_max_f32 v248,v248,v191
v_mfma_f32_16x16x32_bf16 v[84:87],a[180:183],v[144:147],v[84:87]
s_add_u32 m0,s57,1024
ds_read_b128 a[148:151],v241 offset:21504
v_cmp_lt_f32 vcc,s12,v248
v_mfma_f32_16x16x32_bf16 v[116:119],a[180:183],v[152:155],v[116:119]
s_add_u32 s64,s54,28672
v_exp_f32 v174,v174
v_mfma_f32_16x16x32_bf16 v[88:91],a[184:187],v[144:147],v[88:91]
ds_read_b128 a[152:155],v241 offset:22528
v_mfma_f32_16x16x32_bf16 v[120:123],a[184:187],v[152:155],v[120:123]
buffer_load_dwordx4 v14,s[40:43],s64 offen lds
v_mfma_f32_16x16x32_bf16 v[92:95],a[188:191],v[144:147],v[92:95]
s_add_u32 m0,s57,2048
ds_read_b128 a[156:159],v241 offset:23552
v_mfma_f32_16x16x32_bf16 v[124:127],a[188:191],v[152:155],v[124:127]
s_add_u32 s63,s54,57344
v_exp_f32 v175,v175
v_mfma_f32_16x16x32_bf16 v[200:203],v[228:231],v[144:147],v[200:203]
v_mfma_f32_16x16x32_bf16 v[204:207],v[228:231],v[152:155],v[204:207]
s_waitcnt lgkmcnt(8)
s_cbranch_vccnz gm_K2_stub18
)"
R"(gm_K2_ret18:
buffer_load_dwordx4 v15,s[40:43],s63 offen lds
s_add_u32 m0,s57,3072
s_add_u32 s64,s54,86016
buffer_load_dwordx4 v15,s[40:43],s64 offen lds
s_add_u32 m0,s57,4096
s_add_u32 s63,s54,114688
buffer_load_dwordx4 v14,s[40:43],s63 offen lds
s_add_u32 m0,s57,5120
s_add_u32 s64,s54,143360
buffer_load_dwordx4 v14,s[40:43],s64 offen lds
s_add_u32 m0,s57,6144
s_add_u32 s63,s54,172032
v_cvt_pk_bf16_f32 v168,v168,v169
v_cvt_pk_bf16_f32 v169,v170,v171
v_cvt_pk_bf16_f32 v170,v172,v173
v_cvt_pk_bf16_f32 v171,v174,v175
v_exp_f32 v184,v184
v_exp_f32 v185,v185
v_exp_f32 v186,v186
s_waitcnt lgkmcnt(0)
ds_read_b128 a[160:163],v241 offset:24576
buffer_load_dwordx4 v15,s[40:43],s63 offen lds
v_exp_f32 v187,v187
)"
R"(v_mfma_f32_16x16x32_bf16 v[96:99],a[128:131],v[168:171],v[96:99]
s_add_u32 m0,s57,7168
s_add_u32 s64,s54,200704
ds_read_b128 a[164:167],v241 offset:25600
v_mfma_f32_16x16x32_bf16 v[100:103],a[132:135],v[168:171],v[100:103]
ds_read_b128 a[168:171],v241 offset:26624
buffer_load_dwordx4 v15,s[40:43],s64 offen lds
v_exp_f32 v188,v188
v_mfma_f32_16x16x32_bf16 v[104:107],a[136:139],v[168:171],v[104:107]
s_add_u32 s57,s57,0x8000
s_cmp_ge_u32 s57,0x28000
s_cselect_b32 s65,0x18000,0
ds_read_b128 a[172:175],v241 offset:27648
v_mfma_f32_16x16x32_bf16 v[108:111],a[140:143],v[168:171],v[108:111]
s_sub_u32 s57,s57,s65
s_add_u32 s54,s54,458752
ds_read_b128 a[176:179],v241 offset:28672
s_cmp_eq_u32 s54,7340032
s_cselect_b32 s54,0,s54
v_exp_f32 v189,v189
v_mfma_f32_16x16x32_bf16 v[112:115],a[144:147],v[168:171],v[112:115]
ds_read_b128 a[180:183],v241 offset:29696
v_mfma_f32_16x16x32_bf16 v[116:119],a[148:151],v[168:171],v[116:119]
ds_read_b128 a[184:187],v241 offset:30720
)"
R"(v_exp_f32 v190,v190
v_mfma_f32_16x16x32_bf16 v[120:123],a[152:155],v[168:171],v[120:123]
ds_read_b128 a[188:191],v241 offset:31744
v_mfma_f32_16x16x32_bf16 v[124:127],a[156:159],v[168:171],v[124:127]
v_exp_f32 v191,v191
v_mfma_f32_16x16x32_bf16 v[204:207],v[228:231],v[168:171],v[204:207]
s_waitcnt lgkmcnt(8)
s_cmp_lg_u32 s87,0
s_cbranch_scc1 gm_K2_stub19
gm_K2_ret19:
s_waitcnt vmcnt(8) lgkmcnt(0)
s_barrier
s_add_u32 m0,s56,0
s_add_u32 s61,s52,0
buffer_load_dwordx4 v10,s[32:35],s61 offen lds
s_add_u32 m0,s56,1024
s_add_u32 s62,s52,16384
buffer_load_dwordx4 v11,s[32:35],s62 offen lds
s_add_u32 m0,s56,2048
s_add_u32 s61,s52,32768
buffer_load_dwordx4 v10,s[32:35],s61 offen lds
s_add_u32 m0,s56,3072
s_add_u32 s62,s52,49152
buffer_load_dwordx4 v11,s[32:35],s62 offen lds
)"
R"(s_add_u32 m0,s56,4096
s_add_u32 s61,s52,65536
buffer_load_dwordx4 v10,s[32:35],s61 offen lds
s_add_u32 m0,s56,5120
s_add_u32 s62,s52,81920
buffer_load_dwordx4 v11,s[32:35],s62 offen lds
s_add_u32 m0,s56,6144
s_add_u32 s61,s52,98304
buffer_load_dwordx4 v10,s[32:35],s61 offen lds
s_add_u32 m0,s56,7168
s_branch gm_KB_sk18
.p2align 12
gm_KB_pg18:
s_setpc_b64 s[100:101]
gm_KB_sk18:
s_add_u32 s62,s52,114688
buffer_load_dwordx4 v11,s[32:35],s62 offen lds
s_xor_b32 s56,s56,0x8000
s_add_u32 s52,s52,128
s_cmp_eq_u32 s52,2048
s_cselect_b32 s52,0,s52
v_cvt_pk_bf16_f32 v184,v184,v185
v_cvt_pk_bf16_f32 v185,v186,v187
v_cvt_pk_bf16_f32 v186,v188,v189
)"
R"(v_cvt_pk_bf16_f32 v187,v190,v191
s_waitcnt lgkmcnt(0)
s_nop 0
v_mfma_f32_16x16x32_bf16 v[96:99],a[160:163],v[184:187],v[96:99]
v_mfma_f32_16x16x32_bf16 v[100:103],a[164:167],v[184:187],v[100:103]
v_mfma_f32_16x16x32_bf16 v[104:107],a[168:171],v[184:187],v[104:107]
v_mfma_f32_16x16x32_bf16 v[108:111],a[172:175],v[184:187],v[108:111]
v_mfma_f32_16x16x32_bf16 v[112:115],a[176:179],v[184:187],v[112:115]
v_mfma_f32_16x16x32_bf16 v[116:119],a[180:183],v[184:187],v[116:119]
v_mfma_f32_16x16x32_bf16 v[120:123],a[184:187],v[184:187],v[120:123]
v_mfma_f32_16x16x32_bf16 v[124:127],a[188:191],v[184:187],v[124:127]
v_mfma_f32_16x16x32_bf16 v[204:207],v[228:231],v[184:187],v[204:207]
s_waitcnt lgkmcnt(0)
v_rcp_f32 v236,v200
s_nop 5
v_rcp_f32 v237,v204
s_nop 0
v_pk_mul_f32 v[236:237],v[236:237],v[234:235]
v_pk_mul_f32 v[64:65],v[64:65],v[236:237] op_sel:[0,0] op_sel_hi:[1,0]
v_pk_mul_f32 v[66:67],v[66:67],v[236:237] op_sel:[0,0] op_sel_hi:[1,0]
v_pk_mul_f32 v[68:69],v[68:69],v[236:237] op_sel:[0,0] op_sel_hi:[1,0]
v_pk_mul_f32 v[70:71],v[70:71],v[236:237] op_sel:[0,0] op_sel_hi:[1,0]
v_cvt_pk_bf16_f32 v24,v64,v65
v_cvt_pk_bf16_f32 v25,v66,v67
)"
R"(v_cvt_pk_bf16_f32 v26,v68,v69
v_cvt_pk_bf16_f32 v27,v70,v71
v_pk_mul_f32 v[96:97],v[96:97],v[236:237] op_sel:[0,1] op_sel_hi:[1,1]
v_pk_mul_f32 v[98:99],v[98:99],v[236:237] op_sel:[0,1] op_sel_hi:[1,1]
v_pk_mul_f32 v[100:101],v[100:101],v[236:237] op_sel:[0,1] op_sel_hi:[1,1]
v_pk_mul_f32 v[102:103],v[102:103],v[236:237] op_sel:[0,1] op_sel_hi:[1,1]
v_cvt_pk_bf16_f32 v28,v96,v97
v_cvt_pk_bf16_f32 v29,v98,v99
v_cvt_pk_bf16_f32 v30,v100,v101
v_cvt_pk_bf16_f32 v31,v102,v103
v_pk_mul_f32 v[72:73],v[72:73],v[236:237] op_sel:[0,0] op_sel_hi:[1,0]
v_pk_mul_f32 v[74:75],v[74:75],v[236:237] op_sel:[0,0] op_sel_hi:[1,0]
v_pk_mul_f32 v[76:77],v[76:77],v[236:237] op_sel:[0,0] op_sel_hi:[1,0]
v_pk_mul_f32 v[78:79],v[78:79],v[236:237] op_sel:[0,0] op_sel_hi:[1,0]
v_cvt_pk_bf16_f32 v32,v72,v73
v_cvt_pk_bf16_f32 v33,v74,v75
v_cvt_pk_bf16_f32 v34,v76,v77
v_cvt_pk_bf16_f32 v35,v78,v79
v_pk_mul_f32 v[104:105],v[104:105],v[236:237] op_sel:[0,1] op_sel_hi:[1,1]
v_pk_mul_f32 v[106:107],v[106:107],v[236:237] op_sel:[0,1] op_sel_hi:[1,1]
v_pk_mul_f32 v[108:109],v[108:109],v[236:237] op_sel:[0,1] op_sel_hi:[1,1]
v_pk_mul_f32 v[110:111],v[110:111],v[236:237] op_sel:[0,1] op_sel_hi:[1,1]
v_cvt_pk_bf16_f32 v36,v104,v105
v_cvt_pk_bf16_f32 v37,v106,v107
)"
R"(v_cvt_pk_bf16_f32 v38,v108,v109
v_cvt_pk_bf16_f32 v39,v110,v111
v_pk_mul_f32 v[80:81],v[80:81],v[236:237] op_sel:[0,0] op_sel_hi:[1,0]
v_pk_mul_f32 v[82:83],v[82:83],v[236:237] op_sel:[0,0] op_sel_hi:[1,0]
v_pk_mul_f32 v[84:85],v[84:85],v[236:237] op_sel:[0,0] op_sel_hi:[1,0]
v_pk_mul_f32 v[86:87],v[86:87],v[236:237] op_sel:[0,0] op_sel_hi:[1,0]
v_cvt_pk_bf16_f32 v40,v80,v81
v_cvt_pk_bf16_f32 v41,v82,v83
v_cvt_pk_bf16_f32 v42,v84,v85
v_cvt_pk_bf16_f32 v43,v86,v87
v_pk_mul_f32 v[112:113],v[112:113],v[236:237] op_sel:[0,1] op_sel_hi:[1,1]
v_pk_mul_f32 v[114:115],v[114:115],v[236:237] op_sel:[0,1] op_sel_hi:[1,1]
v_pk_mul_f32 v[116:117],v[116:117],v[236:237] op_sel:[0,1] op_sel_hi:[1,1]
v_pk_mul_f32 v[118:119],v[118:119],v[236:237] op_sel:[0,1] op_sel_hi:[1,1]
v_cvt_pk_bf16_f32 v44,v112,v113
v_cvt_pk_bf16_f32 v45,v114,v115
v_cvt_pk_bf16_f32 v46,v116,v117
v_cvt_pk_bf16_f32 v47,v118,v119
v_pk_mul_f32 v[88:89],v[88:89],v[236:237] op_sel:[0,0] op_sel_hi:[1,0]
v_pk_mul_f32 v[90:91],v[90:91],v[236:237] op_sel:[0,0] op_sel_hi:[1,0]
v_pk_mul_f32 v[92:93],v[92:93],v[236:237] op_sel:[0,0] op_sel_hi:[1,0]
v_pk_mul_f32 v[94:95],v[94:95],v[236:237] op_sel:[0,0] op_sel_hi:[1,0]
v_cvt_pk_bf16_f32 v48,v88,v89
v_cvt_pk_bf16_f32 v49,v90,v91
)"
R"(v_cvt_pk_bf16_f32 v50,v92,v93
v_cvt_pk_bf16_f32 v51,v94,v95
v_pk_mul_f32 v[120:121],v[120:121],v[236:237] op_sel:[0,1] op_sel_hi:[1,1]
v_pk_mul_f32 v[122:123],v[122:123],v[236:237] op_sel:[0,1] op_sel_hi:[1,1]
v_pk_mul_f32 v[124:125],v[124:125],v[236:237] op_sel:[0,1] op_sel_hi:[1,1]
v_pk_mul_f32 v[126:127],v[126:127],v[236:237] op_sel:[0,1] op_sel_hi:[1,1]
v_cvt_pk_bf16_f32 v52,v120,v121
v_cvt_pk_bf16_f32 v53,v122,v123
v_cvt_pk_bf16_f32 v54,v124,v125
v_cvt_pk_bf16_f32 v55,v126,v127
s_cmp_eq_u32 %2,1
s_cbranch_scc1 gm_KB_wk1_1
s_cmp_eq_u32 %2,2
s_cbranch_scc1 gm_KB_wk1_2
s_cmp_eq_u32 %2,3
s_cbranch_scc1 gm_KB_wk1_3
s_getpc_b64 s[100:101]
s_add_u32 s100,s100,12
s_addc_u32 s101,s101,0
s_branch gm_KB_pg19
s_getpc_b64 s[100:101]
s_add_u32 s100,s100,12
s_addc_u32 s101,s101,0
s_branch gm_KB_pg23
)"
R"(s_getpc_b64 s[100:101]
s_add_u32 s100,s100,12
s_addc_u32 s101,s101,0
s_branch gm_KB_pg27
s_branch gm_KB_wk1_e
gm_KB_wk1_1:
s_getpc_b64 s[100:101]
s_add_u32 s100,s100,12
s_addc_u32 s101,s101,0
s_branch gm_KB_pg20
s_getpc_b64 s[100:101]
s_add_u32 s100,s100,12
s_addc_u32 s101,s101,0
s_branch gm_KB_pg24
s_branch gm_KB_wk1_e
gm_KB_wk1_2:
s_getpc_b64 s[100:101]
s_add_u32 s100,s100,12
s_addc_u32 s101,s101,0
s_branch gm_KB_pg21
s_getpc_b64 s[100:101]
s_add_u32 s100,s100,12
s_addc_u32 s101,s101,0
s_branch gm_KB_pg25
)"
R"(s_branch gm_KB_wk1_e
gm_KB_wk1_3:
s_getpc_b64 s[100:101]
s_add_u32 s100,s100,12
s_addc_u32 s101,s101,0
s_branch gm_KB_pg22
s_getpc_b64 s[100:101]
s_add_u32 s100,s100,12
s_addc_u32 s101,s101,0
s_branch gm_KB_pg26
gm_KB_wk1_e:
ds_read_b64_tr_b16 a[64:65],v2 offset:0
ds_read_b64_tr_b16 a[66:67],v2 offset:1024
ds_read_b64_tr_b16 a[92:93],v2 offset:8192
ds_read_b64_tr_b16 a[94:95],v2 offset:9216
ds_read_b128 a[0:3],v0 offset:0
ds_read_b64_tr_b16 a[68:69],v3 offset:0
ds_read_b64_tr_b16 a[70:71],v3 offset:1024
ds_read_b64_tr_b16 a[96:97],v3 offset:8192
ds_read_b64_tr_b16 a[98:99],v3 offset:9216
ds_read_b128 a[16:19],v1 offset:0
ds_read_b64_tr_b16 a[72:73],v4 offset:0
ds_read_b64_tr_b16 a[74:75],v4 offset:1024
ds_read_b64_tr_b16 a[100:101],v4 offset:8192
)"
R"(ds_read_b64_tr_b16 a[102:103],v4 offset:9216
ds_read_b128 a[4:7],v0 offset:2048
ds_read_b64_tr_b16 a[76:77],v5 offset:0
ds_read_b64_tr_b16 a[78:79],v5 offset:1024
ds_read_b64_tr_b16 a[104:105],v5 offset:8192
ds_read_b64_tr_b16 a[106:107],v5 offset:9216
ds_read_b128 a[20:23],v1 offset:2048
ds_read_b64_tr_b16 a[80:81],v6 offset:0
ds_read_b64_tr_b16 a[82:83],v6 offset:1024
ds_read_b64_tr_b16 a[108:109],v6 offset:8192
ds_read_b64_tr_b16 a[110:111],v6 offset:9216
ds_read_b128 a[8:11],v0 offset:4096
ds_read_b64_tr_b16 a[84:85],v7 offset:0
ds_read_b64_tr_b16 a[86:87],v7 offset:1024
ds_read_b64_tr_b16 a[112:113],v7 offset:8192
ds_read_b64_tr_b16 a[114:115],v7 offset:9216
ds_read_b128 a[24:27],v1 offset:4096
ds_read_b64_tr_b16 a[88:89],v8 offset:0
ds_read_b64_tr_b16 a[90:91],v8 offset:1024
ds_read_b64_tr_b16 a[116:117],v8 offset:8192
ds_read_b64_tr_b16 a[118:119],v8 offset:9216
ds_read_b128 a[12:15],v0 offset:6144
ds_read_b128 a[28:31],v1 offset:6144
s_waitcnt lgkmcnt(0)
)"
R"(v_mfma_f32_16x16x32_bf16 v[64:67],a[64:67],a[0:3],0
s_add_u32 m0,s57,0
s_add_u32 s63,s54,0
ds_read_b64_tr_b16 a[120:121],v2 offset:16384
v_mfma_f32_16x16x32_bf16 v[64:67],a[92:95],a[16:19],v[64:67]
buffer_load_dwordx4 v14,s[40:43],s63 offen lds
ds_read_b64_tr_b16 a[122:123],v2 offset:17408
v_mfma_f32_16x16x32_bf16 v[68:71],a[68:71],a[0:3],0
s_add_u32 s66,s59,0x8000
ds_read_b64_tr_b16 a[148:149],v2 offset:24576
v_mfma_f32_16x16x32_bf16 v[68:71],a[96:99],a[16:19],v[68:71]
s_cmp_eq_u32 s66,0x18000
ds_read_b64_tr_b16 a[150:151],v2 offset:25600
v_mfma_f32_16x16x32_bf16 v[72:75],a[72:75],a[0:3],0
s_cselect_b32 s66,0,s66
s_sub_u32 s58,s66,s59
ds_read_b64_tr_b16 a[124:125],v3 offset:16384
v_mfma_f32_16x16x32_bf16 v[72:75],a[100:103],a[16:19],v[72:75]
s_mov_b32 s59,s66
v_mfma_f32_16x16x32_bf16 v[76:79],a[76:79],a[0:3],0
s_add_u32 m0,s57,1024
ds_read_b64_tr_b16 a[126:127],v3 offset:17408
v_mfma_f32_16x16x32_bf16 v[76:79],a[104:107],a[16:19],v[76:79]
s_add_u32 s64,s54,28672
)"
R"(ds_read_b64_tr_b16 a[152:153],v3 offset:24576
v_mfma_f32_16x16x32_bf16 v[80:83],a[80:83],a[0:3],0
buffer_load_dwordx4 v14,s[40:43],s64 offen lds
ds_read_b64_tr_b16 a[154:155],v3 offset:25600
v_mfma_f32_16x16x32_bf16 v[80:83],a[108:111],a[16:19],v[80:83]
s_add_u32 m0,s57,2048
s_add_u32 s63,s54,57344
ds_read_b64_tr_b16 a[128:129],v4 offset:16384
v_mfma_f32_16x16x32_bf16 v[84:87],a[84:87],a[0:3],0
buffer_load_dwordx4 v15,s[40:43],s63 offen lds
ds_read_b64_tr_b16 a[130:131],v4 offset:17408
v_mfma_f32_16x16x32_bf16 v[84:87],a[112:115],a[16:19],v[84:87]
s_add_u32 m0,s57,3072
v_mfma_f32_16x16x32_bf16 v[88:91],a[88:91],a[0:3],0
s_add_u32 s64,s54,86016
buffer_load_dwordx4 v15,s[40:43],s64 offen lds
ds_read_b64_tr_b16 a[156:157],v4 offset:24576
v_mfma_f32_16x16x32_bf16 v[88:91],a[116:119],a[16:19],v[88:91]
s_add_u32 m0,s57,4096
ds_read_b64_tr_b16 a[158:159],v4 offset:25600
v_mfma_f32_16x16x32_bf16 v[144:147],a[88:91],a[4:7],0
s_add_u32 s63,s54,114688
ds_read_b64_tr_b16 a[132:133],v5 offset:16384
v_mfma_f32_16x16x32_bf16 v[144:147],a[116:119],a[20:23],v[144:147]
)"
R"(buffer_load_dwordx4 v14,s[40:43],s63 offen lds
ds_read_b64_tr_b16 a[134:135],v5 offset:17408
v_mfma_f32_16x16x32_bf16 v[140:143],a[84:87],a[4:7],0
s_add_u32 m0,s57,5120
s_add_u32 s64,s54,143360
ds_read_b64_tr_b16 a[160:161],v5 offset:24576
v_mfma_f32_16x16x32_bf16 v[140:143],a[112:115],a[20:23],v[140:143]
buffer_load_dwordx4 v14,s[40:43],s64 offen lds
v_mfma_f32_16x16x32_bf16 v[136:139],a[80:83],a[4:7],0
s_add_u32 m0,s57,6144
ds_read_b64_tr_b16 a[162:163],v5 offset:25600
v_mfma_f32_16x16x32_bf16 v[136:139],a[108:111],a[20:23],v[136:139]
s_add_u32 s63,s54,172032
ds_read_b64_tr_b16 a[136:137],v6 offset:16384
v_mfma_f32_16x16x32_bf16 v[132:135],a[76:79],a[4:7],0
buffer_load_dwordx4 v15,s[40:43],s63 offen lds
ds_read_b64_tr_b16 a[138:139],v6 offset:17408
v_mfma_f32_16x16x32_bf16 v[132:135],a[104:107],a[20:23],v[132:135]
s_add_u32 m0,s57,7168
s_add_u32 s64,s54,200704
ds_read_b64_tr_b16 a[164:165],v6 offset:24576
v_mfma_f32_16x16x32_bf16 v[128:131],a[72:75],a[4:7],0
buffer_load_dwordx4 v15,s[40:43],s64 offen lds
ds_read_b64_tr_b16 a[166:167],v6 offset:25600
)"
R"(v_mfma_f32_16x16x32_bf16 v[128:131],a[100:103],a[20:23],v[128:131]
s_add_u32 s57,s57,0x8000
v_mfma_f32_16x16x32_bf16 v[124:127],a[68:71],a[4:7],0
s_cmp_ge_u32 s57,0x28000
ds_read_b64_tr_b16 a[140:141],v7 offset:16384
v_mfma_f32_16x16x32_bf16 v[124:127],a[96:99],a[20:23],v[124:127]
s_cselect_b32 s65,0x18000,0
s_sub_u32 s57,s57,s65
ds_read_b64_tr_b16 a[142:143],v7 offset:17408
v_mfma_f32_16x16x32_bf16 v[120:123],a[64:67],a[4:7],0
s_add_u32 s54,s54,458752
ds_read_b64_tr_b16 a[168:169],v7 offset:24576
v_mfma_f32_16x16x32_bf16 v[120:123],a[92:95],a[20:23],v[120:123]
s_cmp_eq_u32 s54,7340032
ds_read_b64_tr_b16 a[170:171],v7 offset:25600
v_mfma_f32_16x16x32_bf16 v[176:179],a[64:67],a[8:11],0
s_cselect_b32 s54,0,s54
ds_read_b64_tr_b16 a[144:145],v8 offset:16384
v_mfma_f32_16x16x32_bf16 v[176:179],a[92:95],a[24:27],v[176:179]
s_mov_b32 s69,0
buffer_store_dwordx4 v[24:27],v18,s[44:47],s69 offen sc1
v_mfma_f32_16x16x32_bf16 v[180:183],a[68:71],a[8:11],0
s_and_b32 s53,s73,3
ds_read_b64_tr_b16 a[146:147],v8 offset:17408
)"
R"(v_mfma_f32_16x16x32_bf16 v[180:183],a[96:99],a[24:27],v[180:183]
s_lshr_b32 s12,%1,3
ds_read_b64_tr_b16 a[172:173],v8 offset:24576
v_mfma_f32_16x16x32_bf16 v[184:187],a[72:75],a[8:11],0
s_lshr_b32 s27,s12,2
ds_read_b64_tr_b16 a[174:175],v8 offset:25600
v_mfma_f32_16x16x32_bf16 v[184:187],a[100:103],a[24:27],v[184:187]
s_and_b32 s12,s12,3
s_and_b32 s26,s73,4
v_mfma_f32_16x16x32_bf16 v[188:191],a[76:79],a[8:11],0
s_add_u32 s26,s26,s12
v_mfma_f32_16x16x32_bf16 v[188:191],a[104:107],a[24:27],v[188:191]
s_lshr_b32 s28,s53,1
v_mfma_f32_16x16x32_bf16 v[192:195],a[80:83],a[8:11],0
s_mul_i32 s28,s28,28
v_xor_b32 v0,0x8000,v0
v_mfma_f32_16x16x32_bf16 v[192:195],a[108:111],a[24:27],v[192:195]
s_and_b32 s12,s53,1
s_lshl_b32 s15,s12,4
v_xor_b32 v1,0x8000,v1
v_mfma_f32_16x16x32_bf16 v[196:199],a[84:87],a[8:11],0
s_add_u32 s28,s28,s15
v_add_u32 v2,s58,v2
v_mfma_f32_16x16x32_bf16 v[196:199],a[112:115],a[24:27],v[196:199]
)"
R"(s_mov_b32 s69,0
v_add_u32 v3,s58,v3
v_mfma_f32_16x16x32_bf16 v[200:203],a[88:91],a[8:11],0
buffer_store_dwordx4 v[28:31],v58,s[44:47],s69 offen sc1
v_add_u32 v4,s58,v4
v_mfma_f32_16x16x32_bf16 v[200:203],a[116:119],a[24:27],v[200:203]
s_sub_u32 s29,7,s12
s_lshr_b32 s12,s70,1
v_add_u32 v5,s58,v5
v_mfma_f32_16x16x32_bf16 a[192:195],a[88:91],a[12:15],0
s_lshl_b32 s15,s26,1
v_add_u32 v6,s58,v6
v_mfma_f32_16x16x32_bf16 a[192:195],a[116:119],a[28:31],a[192:195]
s_add_u32 s98,s12,s15
v_add_u32 v7,s58,v7
v_mfma_f32_16x16x32_bf16 v[252:255],a[84:87],a[12:15],0
s_mul_i32 s12,s98,256
v_add_u32 v8,s58,v8
v_mfma_f32_16x16x32_bf16 v[252:255],a[112:115],a[28:31],v[252:255]
s_lshl_b32 s15,s53,6
s_add_u32 s12,s12,s15
v_add_u32 v9,s58,v9
v_mfma_f32_16x16x32_bf16 v[248:251],a[80:83],a[12:15],0
s_and_b32 s15,s70,1
)"
R"(v_mfma_f32_16x16x32_bf16 v[248:251],a[108:111],a[28:31],v[248:251]
s_add_u32 s12,s12,s15
v_mfma_f32_16x16x32_bf16 v[244:247],a[76:79],a[12:15],0
s_lshl_b32 s12,s12,12
v_mfma_f32_16x16x32_bf16 v[244:247],a[104:107],a[28:31],v[244:247]
s_add_u32 s12,s12,67108864
v_mfma_f32_16x16x32_bf16 v[240:243],a[72:75],a[12:15],0
v_mfma_f32_16x16x32_bf16 v[240:243],a[100:103],a[28:31],v[240:243]
v_mfma_f32_16x16x32_bf16 v[236:239],a[68:71],a[12:15],0
v_mfma_f32_16x16x32_bf16 v[236:239],a[96:99],a[28:31],v[236:239]
v_mfma_f32_16x16x32_bf16 v[232:235],a[64:67],a[12:15],0
s_waitcnt vmcnt(10) lgkmcnt(0)
v_mfma_f32_16x16x32_bf16 v[232:235],a[92:95],a[28:31],v[232:235]
s_barrier
v_mfma_f32_16x16x32_bf16 v[92:95],a[120:123],a[0:3],0
s_add_u32 m0,s56,0
s_add_u32 s61,s52,0
v_mfma_f32_16x16x32_bf16 v[92:95],a[148:151],a[16:19],v[92:95]
buffer_load_dwordx4 v10,s[32:35],s61 offen lds
v_mfma_f32_16x16x32_bf16 v[96:99],a[124:127],a[0:3],0
s_add_u32 m0,s56,1024
ds_read_b64_tr_b16 a[64:65],v2 offset:0
v_mfma_f32_16x16x32_bf16 v[96:99],a[152:155],a[16:19],v[96:99]
s_add_u32 s62,s52,16384
)"
R"(ds_read_b64_tr_b16 a[66:67],v2 offset:1024
v_mfma_f32_16x16x32_bf16 v[100:103],a[128:131],a[0:3],0
buffer_load_dwordx4 v11,s[32:35],s62 offen lds
v_mfma_f32_16x16x32_bf16 v[100:103],a[156:159],a[16:19],v[100:103]
s_add_u32 m0,s56,2048
ds_read_b64_tr_b16 a[92:93],v2 offset:8192
v_mfma_f32_16x16x32_bf16 v[104:107],a[132:135],a[0:3],0
s_add_u32 s61,s52,32768
ds_read_b64_tr_b16 a[94:95],v2 offset:9216
v_mfma_f32_16x16x32_bf16 v[104:107],a[160:163],a[16:19],v[104:107]
buffer_load_dwordx4 v10,s[32:35],s61 offen lds
v_mfma_f32_16x16x32_bf16 v[108:111],a[136:139],a[0:3],0
s_add_u32 m0,s56,3072
ds_read_b128 a[32:35],v0 offset:0
v_mfma_f32_16x16x32_bf16 v[108:111],a[164:167],a[16:19],v[108:111]
s_add_u32 s62,s52,49152
ds_read_b64_tr_b16 a[68:69],v3 offset:0
v_mfma_f32_16x16x32_bf16 v[112:115],a[140:143],a[0:3],0
buffer_load_dwordx4 v11,s[32:35],s62 offen lds
v_mfma_f32_16x16x32_bf16 v[112:115],a[168:171],a[16:19],v[112:115]
s_add_u32 m0,s56,4096
ds_read_b64_tr_b16 a[70:71],v3 offset:1024
v_mfma_f32_16x16x32_bf16 v[116:119],a[144:147],a[0:3],0
s_add_u32 s61,s52,65536
)"
R"(ds_read_b64_tr_b16 a[96:97],v3 offset:8192
v_mfma_f32_16x16x32_bf16 v[116:119],a[172:175],a[16:19],v[116:119]
buffer_load_dwordx4 v10,s[32:35],s61 offen lds
v_mfma_f32_16x16x32_bf16 v[172:175],a[144:147],a[4:7],0
s_add_u32 m0,s56,5120
ds_read_b64_tr_b16 a[98:99],v3 offset:9216
v_mfma_f32_16x16x32_bf16 v[172:175],a[172:175],a[20:23],v[172:175]
s_add_u32 s62,s52,81920
ds_read_b128 a[48:51],v1 offset:0
v_mfma_f32_16x16x32_bf16 v[168:171],a[140:143],a[4:7],0
buffer_load_dwordx4 v11,s[32:35],s62 offen lds
v_mfma_f32_16x16x32_bf16 v[168:171],a[168:171],a[20:23],v[168:171]
s_add_u32 m0,s56,6144
ds_read_b64_tr_b16 a[72:73],v4 offset:0
v_mfma_f32_16x16x32_bf16 v[164:167],a[136:139],a[4:7],0
s_add_u32 s61,s52,98304
ds_read_b64_tr_b16 a[74:75],v4 offset:1024
v_mfma_f32_16x16x32_bf16 v[164:167],a[164:167],a[20:23],v[164:167]
buffer_load_dwordx4 v10,s[32:35],s61 offen lds
v_mfma_f32_16x16x32_bf16 v[160:163],a[132:135],a[4:7],0
s_add_u32 m0,s56,7168
ds_read_b64_tr_b16 a[100:101],v4 offset:8192
v_mfma_f32_16x16x32_bf16 v[160:163],a[160:163],a[20:23],v[160:163]
s_add_u32 s62,s52,114688
)"
R"(ds_read_b64_tr_b16 a[102:103],v4 offset:9216
v_mfma_f32_16x16x32_bf16 v[156:159],a[128:131],a[4:7],0
buffer_load_dwordx4 v11,s[32:35],s62 offen lds
v_mfma_f32_16x16x32_bf16 v[156:159],a[156:159],a[20:23],v[156:159]
s_mov_b32 s69,8192
ds_read_b128 a[36:39],v0 offset:2048
v_mfma_f32_16x16x32_bf16 v[152:155],a[124:127],a[4:7],0
buffer_store_dwordx4 v[32:35],v18,s[44:47],s69 offen sc1
v_mfma_f32_16x16x32_bf16 v[152:155],a[152:155],a[20:23],v[152:155]
s_add_u32 s36,s30,s12
s_addc_u32 s37,s31,0
v_mfma_f32_16x16x32_bf16 v[148:151],a[120:123],a[4:7],0
s_and_b32 s37,s37,0xffff
s_mov_b32 s38,258048
v_mfma_f32_16x16x32_bf16 v[148:151],a[148:151],a[20:23],v[148:151]
s_mov_b32 s39,0x20000
ds_read_b64_tr_b16 a[76:77],v5 offset:0
v_mfma_f32_16x16x32_bf16 v[204:207],a[120:123],a[8:11],0
s_mul_i32 s12,s98,224
ds_read_b64_tr_b16 a[78:79],v5 offset:1024
v_mfma_f32_16x16x32_bf16 v[204:207],a[148:151],a[24:27],v[204:207]
s_lshl_b32 s15,s28,2
ds_read_b64_tr_b16 a[104:105],v5 offset:8192
v_mfma_f32_16x16x32_bf16 v[208:211],a[124:127],a[8:11],0
)"
R"(s_add_u32 s12,s12,s15
ds_read_b64_tr_b16 a[106:107],v5 offset:9216
v_mfma_f32_16x16x32_bf16 v[208:211],a[152:155],a[24:27],v[208:211]
s_and_b32 s15,s70,1
ds_read_b128 a[52:55],v1 offset:2048
v_mfma_f32_16x16x32_bf16 v[212:215],a[128:131],a[8:11],0
s_add_u32 s12,s12,s15
ds_read_b64_tr_b16 a[80:81],v6 offset:0
v_mfma_f32_16x16x32_bf16 v[212:215],a[156:159],a[24:27],v[212:215]
s_lshl_b32 s12,s12,12
ds_read_b64_tr_b16 a[82:83],v6 offset:1024
v_mfma_f32_16x16x32_bf16 v[216:219],a[132:135],a[8:11],0
s_add_u32 s12,s12,50331648
ds_read_b64_tr_b16 a[108:109],v6 offset:8192
v_mfma_f32_16x16x32_bf16 v[216:219],a[160:163],a[24:27],v[216:219]
s_mov_b32 s69,8192
ds_read_b64_tr_b16 a[110:111],v6 offset:9216
v_mfma_f32_16x16x32_bf16 v[220:223],a[136:139],a[8:11],0
buffer_store_dwordx4 v[36:39],v58,s[44:47],s69 offen sc1
v_mfma_f32_16x16x32_bf16 v[220:223],a[164:167],a[24:27],v[220:223]
s_add_u32 s88,s30,s12
s_addc_u32 s89,s31,0
v_mfma_f32_16x16x32_bf16 v[224:227],a[140:143],a[8:11],0
s_and_b32 s89,s89,0xffff
)"
R"(ds_read_b128 a[40:43],v0 offset:4096
v_mfma_f32_16x16x32_bf16 v[224:227],a[168:171],a[24:27],v[224:227]
s_mov_b32 s90,258048
ds_read_b64_tr_b16 a[84:85],v7 offset:0
v_mfma_f32_16x16x32_bf16 v[228:231],a[144:147],a[8:11],0
s_mov_b32 s91,0x20000
ds_read_b64_tr_b16 a[86:87],v7 offset:1024
v_mfma_f32_16x16x32_bf16 v[228:231],a[172:175],a[24:27],v[228:231]
s_lshl_b32 s80,s70,15
ds_read_b64_tr_b16 a[112:113],v7 offset:8192
v_mfma_f32_16x16x32_bf16 a[220:223],a[144:147],a[12:15],0
s_add_u32 s81,s80,8192
ds_read_b64_tr_b16 a[114:115],v7 offset:9216
v_mfma_f32_16x16x32_bf16 a[220:223],a[172:175],a[28:31],a[220:223]
s_add_u32 s82,s80,16384
ds_read_b128 a[56:59],v1 offset:4096
v_mfma_f32_16x16x32_bf16 a[216:219],a[140:143],a[12:15],0
s_add_u32 s83,s80,24576
ds_read_b64_tr_b16 a[88:89],v8 offset:0
v_mfma_f32_16x16x32_bf16 a[216:219],a[168:171],a[28:31],a[216:219]
s_mov_b32 s75,0
ds_read_b64_tr_b16 a[90:91],v8 offset:1024
v_mfma_f32_16x16x32_bf16 a[212:215],a[136:139],a[12:15],0
s_mov_b32 s24,8192
)"
R"(ds_read_b64_tr_b16 a[116:117],v8 offset:8192
v_mfma_f32_16x16x32_bf16 a[212:215],a[164:167],a[28:31],a[212:215]
s_mov_b32 s87,0
ds_read_b64_tr_b16 a[118:119],v8 offset:9216
v_mfma_f32_16x16x32_bf16 a[208:211],a[132:135],a[12:15],0
s_xor_b32 s56,s56,0x8000
ds_read_b128 a[44:47],v0 offset:6144
v_mfma_f32_16x16x32_bf16 a[208:211],a[160:163],a[28:31],a[208:211]
s_add_u32 s52,s52,128
ds_read_b128 a[60:63],v1 offset:6144
v_mfma_f32_16x16x32_bf16 a[204:207],a[128:131],a[12:15],0
s_cmp_eq_u32 s52,2048
v_mfma_f32_16x16x32_bf16 a[204:207],a[156:159],a[28:31],a[204:207]
s_cselect_b32 s52,0,s52
v_mfma_f32_16x16x32_bf16 a[200:203],a[124:127],a[12:15],0
v_mfma_f32_16x16x32_bf16 a[200:203],a[152:155],a[28:31],a[200:203]
v_mfma_f32_16x16x32_bf16 a[196:199],a[120:123],a[12:15],0
s_waitcnt lgkmcnt(0)
v_mfma_f32_16x16x32_bf16 a[196:199],a[148:151],a[28:31],a[196:199]
v_mfma_f32_16x16x32_bf16 v[64:67],a[64:67],a[32:35],v[64:67]
s_add_u32 m0,s57,0
s_add_u32 s63,s54,0
ds_read_b64_tr_b16 a[120:121],v2 offset:16384
v_mfma_f32_16x16x32_bf16 v[64:67],a[92:95],a[48:51],v[64:67]
)"
R"(buffer_load_dwordx4 v14,s[40:43],s63 offen lds
ds_read_b64_tr_b16 a[122:123],v2 offset:17408
v_mfma_f32_16x16x32_bf16 v[68:71],a[68:71],a[32:35],v[68:71]
s_add_u32 s66,s59,0x8000
ds_read_b64_tr_b16 a[148:149],v2 offset:24576
v_mfma_f32_16x16x32_bf16 v[68:71],a[96:99],a[48:51],v[68:71]
s_cmp_eq_u32 s66,0x18000
s_cselect_b32 s66,0,s66
ds_read_b64_tr_b16 a[150:151],v2 offset:25600
v_mfma_f32_16x16x32_bf16 v[72:75],a[72:75],a[32:35],v[72:75]
s_sub_u32 s58,s66,s59
ds_read_b64_tr_b16 a[124:125],v3 offset:16384
v_mfma_f32_16x16x32_bf16 v[72:75],a[100:103],a[48:51],v[72:75]
s_mov_b32 s59,s66
v_mfma_f32_16x16x32_bf16 v[76:79],a[76:79],a[32:35],v[76:79]
s_add_u32 m0,s57,1024
ds_read_b64_tr_b16 a[126:127],v3 offset:17408
v_mfma_f32_16x16x32_bf16 v[76:79],a[104:107],a[48:51],v[76:79]
s_add_u32 s64,s54,28672
buffer_load_dwordx4 v14,s[40:43],s64 offen lds
ds_read_b64_tr_b16 a[152:153],v3 offset:24576
v_mfma_f32_16x16x32_bf16 v[80:83],a[80:83],a[32:35],v[80:83]
s_add_u32 m0,s57,2048
ds_read_b64_tr_b16 a[154:155],v3 offset:25600
)"
R"(v_mfma_f32_16x16x32_bf16 v[80:83],a[108:111],a[48:51],v[80:83]
s_add_u32 s63,s54,57344
ds_read_b64_tr_b16 a[128:129],v4 offset:16384
v_mfma_f32_16x16x32_bf16 v[84:87],a[84:87],a[32:35],v[84:87]
buffer_load_dwordx4 v15,s[40:43],s63 offen lds
ds_read_b64_tr_b16 a[130:131],v4 offset:17408
v_mfma_f32_16x16x32_bf16 v[84:87],a[112:115],a[48:51],v[84:87]
s_add_u32 m0,s57,3072
s_add_u32 s64,s54,86016
v_mfma_f32_16x16x32_bf16 v[88:91],a[88:91],a[32:35],v[88:91]
buffer_load_dwordx4 v15,s[40:43],s64 offen lds
ds_read_b64_tr_b16 a[156:157],v4 offset:24576
v_mfma_f32_16x16x32_bf16 v[88:91],a[116:119],a[48:51],v[88:91]
s_add_u32 m0,s57,4096
ds_read_b64_tr_b16 a[158:159],v4 offset:25600
v_mfma_f32_16x16x32_bf16 v[144:147],a[88:91],a[36:39],v[144:147]
s_add_u32 s63,s54,114688
buffer_load_dwordx4 v14,s[40:43],s63 offen lds
ds_read_b64_tr_b16 a[132:133],v5 offset:16384
v_mfma_f32_16x16x32_bf16 v[144:147],a[116:119],a[52:55],v[144:147]
s_add_u32 m0,s57,5120
ds_read_b64_tr_b16 a[134:135],v5 offset:17408
v_mfma_f32_16x16x32_bf16 v[140:143],a[84:87],a[36:39],v[140:143]
s_add_u32 s64,s54,143360
)"
R"(ds_read_b64_tr_b16 a[160:161],v5 offset:24576
v_mfma_f32_16x16x32_bf16 v[140:143],a[112:115],a[52:55],v[140:143]
buffer_load_dwordx4 v14,s[40:43],s64 offen lds
s_add_u32 m0,s57,6144
v_mfma_f32_16x16x32_bf16 v[136:139],a[80:83],a[36:39],v[136:139]
s_add_u32 s63,s54,172032
ds_read_b64_tr_b16 a[162:163],v5 offset:25600
v_mfma_f32_16x16x32_bf16 v[136:139],a[108:111],a[52:55],v[136:139]
buffer_load_dwordx4 v15,s[40:43],s63 offen lds
ds_read_b64_tr_b16 a[136:137],v6 offset:16384
v_mfma_f32_16x16x32_bf16 v[132:135],a[76:79],a[36:39],v[132:135]
s_add_u32 m0,s57,7168
ds_read_b64_tr_b16 a[138:139],v6 offset:17408
v_mfma_f32_16x16x32_bf16 v[132:135],a[104:107],a[52:55],v[132:135]
s_add_u32 s64,s54,200704
buffer_load_dwordx4 v15,s[40:43],s64 offen lds
ds_read_b64_tr_b16 a[164:165],v6 offset:24576
v_mfma_f32_16x16x32_bf16 v[128:131],a[72:75],a[36:39],v[128:131]
s_add_u32 s57,s57,0x8000
ds_read_b64_tr_b16 a[166:167],v6 offset:25600
v_mfma_f32_16x16x32_bf16 v[128:131],a[100:103],a[52:55],v[128:131]
s_cmp_ge_u32 s57,0x28000
v_mfma_f32_16x16x32_bf16 v[124:127],a[68:71],a[36:39],v[124:127]
s_cselect_b32 s65,0x18000,0
)"
R"(ds_read_b64_tr_b16 a[140:141],v7 offset:16384
v_mfma_f32_16x16x32_bf16 v[124:127],a[96:99],a[52:55],v[124:127]
s_sub_u32 s57,s57,s65
s_add_u32 s54,s54,458752
ds_read_b64_tr_b16 a[142:143],v7 offset:17408
v_mfma_f32_16x16x32_bf16 v[120:123],a[64:67],a[36:39],v[120:123]
s_cmp_eq_u32 s54,7340032
ds_read_b64_tr_b16 a[168:169],v7 offset:24576
v_mfma_f32_16x16x32_bf16 v[120:123],a[92:95],a[52:55],v[120:123]
s_cselect_b32 s54,0,s54
ds_read_b64_tr_b16 a[170:171],v7 offset:25600
s_branch gm_KB_sk19
.p2align 12
gm_KB_pg19:
s_setpc_b64 s[100:101]
gm_KB_sk19:
v_mfma_f32_16x16x32_bf16 v[176:179],a[64:67],a[40:43],v[176:179]
s_mov_b32 s69,16384
buffer_store_dwordx4 v[40:43],v18,s[44:47],s69 offen sc1
ds_read_b64_tr_b16 a[144:145],v8 offset:16384
v_mfma_f32_16x16x32_bf16 v[176:179],a[92:95],a[56:59],v[176:179]
s_lshl_b32 s25,s70,12
v_mfma_f32_16x16x32_bf16 v[180:183],a[68:71],a[40:43],v[180:183]
s_add_u32 s25,s25,65536
)"
R"(ds_read_b64_tr_b16 a[146:147],v8 offset:17408
v_mfma_f32_16x16x32_bf16 v[180:183],a[96:99],a[56:59],v[180:183]
s_lshl_b32 s12,s26,3
ds_read_b64_tr_b16 a[172:173],v8 offset:24576
v_mfma_f32_16x16x32_bf16 v[184:187],a[72:75],a[40:43],v[184:187]
s_add_u32 s12,s12,s27
s_lshl_b32 s12,s12,18
ds_read_b64_tr_b16 a[174:175],v8 offset:25600
v_mfma_f32_16x16x32_bf16 v[184:187],a[100:103],a[56:59],v[184:187]
s_lshl_b32 s15,s70,14
v_mfma_f32_16x16x32_bf16 v[188:191],a[76:79],a[40:43],v[188:191]
s_add_u32 s12,s12,s15
v_mfma_f32_16x16x32_bf16 v[188:191],a[104:107],a[56:59],v[188:191]
s_add_u32 s12,s12,83886080
s_add_u32 s16,s30,s12
s_addc_u32 s17,s31,0
v_mfma_f32_16x16x32_bf16 v[192:195],a[80:83],a[40:43],v[192:195]
s_and_b32 s17,s17,0xffff
v_xor_b32 v0,0x8000,v0
v_mfma_f32_16x16x32_bf16 v[192:195],a[108:111],a[56:59],v[192:195]
s_mov_b32 s18,212992
v_xor_b32 v1,0x8000,v1
v_mfma_f32_16x16x32_bf16 v[196:199],a[84:87],a[40:43],v[196:199]
s_mov_b32 s19,0x20000
)"
R"(v_add_u32 v2,s58,v2
v_mfma_f32_16x16x32_bf16 v[196:199],a[112:115],a[56:59],v[196:199]
s_mov_b32 s69,16384
buffer_store_dwordx4 v[44:47],v58,s[44:47],s69 offen sc1
v_add_u32 v3,s58,v3
v_mfma_f32_16x16x32_bf16 v[200:203],a[88:91],a[40:43],v[200:203]
s_lshl_b32 s12,s26,2
v_add_u32 v4,s58,v4
v_mfma_f32_16x16x32_bf16 v[200:203],a[116:119],a[56:59],v[200:203]
s_add_u32 s12,s12,s70
v_add_u32 v5,s58,v5
v_mfma_f32_16x16x32_bf16 a[192:195],a[88:91],a[44:47],a[192:195]
s_lshl_b32 s12,s12,2
s_add_u32 s12,s12,s53
v_add_u32 v6,s58,v6
v_mfma_f32_16x16x32_bf16 a[192:195],a[116:119],a[60:63],a[192:195]
s_lshl_b32 s12,s12,15
v_add_u32 v7,s58,v7
v_mfma_f32_16x16x32_bf16 v[252:255],a[84:87],a[44:47],v[252:255]
s_lshl_b32 s15,s27,8
v_add_u32 v8,s58,v8
v_mfma_f32_16x16x32_bf16 v[252:255],a[112:115],a[60:63],v[252:255]
s_add_u32 s12,s12,s15
v_add_u32 v9,s58,v9
)"
R"(v_mfma_f32_16x16x32_bf16 v[248:251],a[80:83],a[44:47],v[248:251]
s_add_u32 s20,s96,s12
s_addc_u32 s21,s97,0
v_mfma_f32_16x16x32_bf16 v[248:251],a[108:111],a[60:63],v[248:251]
s_and_b32 s21,s21,0xffff
s_mov_b32 s22,30976
v_mfma_f32_16x16x32_bf16 v[244:247],a[76:79],a[44:47],v[244:247]
s_mov_b32 s23,0x20000
v_mfma_f32_16x16x32_bf16 v[244:247],a[104:107],a[60:63],v[244:247]
s_mov_b32 s74,24576
v_mfma_f32_16x16x32_bf16 v[240:243],a[72:75],a[44:47],v[240:243]
v_mfma_f32_16x16x32_bf16 v[240:243],a[100:103],a[60:63],v[240:243]
v_mfma_f32_16x16x32_bf16 v[236:239],a[68:71],a[44:47],v[236:239]
v_mfma_f32_16x16x32_bf16 v[236:239],a[96:99],a[60:63],v[236:239]
v_mfma_f32_16x16x32_bf16 v[232:235],a[64:67],a[44:47],v[232:235]
s_waitcnt vmcnt(12) lgkmcnt(0)
v_mfma_f32_16x16x32_bf16 v[232:235],a[92:95],a[60:63],v[232:235]
s_barrier
v_mfma_f32_16x16x32_bf16 v[92:95],a[120:123],a[32:35],v[92:95]
s_add_u32 m0,s56,0
s_add_u32 s61,s52,0
v_mfma_f32_16x16x32_bf16 v[92:95],a[148:151],a[48:51],v[92:95]
buffer_load_dwordx4 v10,s[32:35],s61 offen lds
v_mfma_f32_16x16x32_bf16 v[96:99],a[124:127],a[32:35],v[96:99]
)"
R"(s_add_u32 m0,s56,1024
ds_read_b64_tr_b16 a[64:65],v2 offset:0
v_mfma_f32_16x16x32_bf16 v[96:99],a[152:155],a[48:51],v[96:99]
s_add_u32 s62,s52,16384
ds_read_b64_tr_b16 a[66:67],v2 offset:1024
v_mfma_f32_16x16x32_bf16 v[100:103],a[128:131],a[32:35],v[100:103]
buffer_load_dwordx4 v11,s[32:35],s62 offen lds
v_mfma_f32_16x16x32_bf16 v[100:103],a[156:159],a[48:51],v[100:103]
s_add_u32 m0,s56,2048
ds_read_b64_tr_b16 a[92:93],v2 offset:8192
v_mfma_f32_16x16x32_bf16 v[104:107],a[132:135],a[32:35],v[104:107]
s_add_u32 s61,s52,32768
ds_read_b64_tr_b16 a[94:95],v2 offset:9216
v_mfma_f32_16x16x32_bf16 v[104:107],a[160:163],a[48:51],v[104:107]
buffer_load_dwordx4 v10,s[32:35],s61 offen lds
v_mfma_f32_16x16x32_bf16 v[108:111],a[136:139],a[32:35],v[108:111]
s_add_u32 m0,s56,3072
ds_read_b128 a[0:3],v0 offset:0
v_mfma_f32_16x16x32_bf16 v[108:111],a[164:167],a[48:51],v[108:111]
s_add_u32 s62,s52,49152
ds_read_b64_tr_b16 a[68:69],v3 offset:0
v_mfma_f32_16x16x32_bf16 v[112:115],a[140:143],a[32:35],v[112:115]
buffer_load_dwordx4 v11,s[32:35],s62 offen lds
v_mfma_f32_16x16x32_bf16 v[112:115],a[168:171],a[48:51],v[112:115]
)"
R"(s_add_u32 m0,s56,4096
ds_read_b64_tr_b16 a[70:71],v3 offset:1024
v_mfma_f32_16x16x32_bf16 v[116:119],a[144:147],a[32:35],v[116:119]
s_add_u32 s61,s52,65536
ds_read_b64_tr_b16 a[96:97],v3 offset:8192
v_mfma_f32_16x16x32_bf16 v[116:119],a[172:175],a[48:51],v[116:119]
buffer_load_dwordx4 v10,s[32:35],s61 offen lds
v_mfma_f32_16x16x32_bf16 v[172:175],a[144:147],a[36:39],v[172:175]
s_add_u32 m0,s56,5120
ds_read_b64_tr_b16 a[98:99],v3 offset:9216
v_mfma_f32_16x16x32_bf16 v[172:175],a[172:175],a[52:55],v[172:175]
s_add_u32 s62,s52,81920
ds_read_b128 a[16:19],v1 offset:0
v_mfma_f32_16x16x32_bf16 v[168:171],a[140:143],a[36:39],v[168:171]
buffer_load_dwordx4 v11,s[32:35],s62 offen lds
v_mfma_f32_16x16x32_bf16 v[168:171],a[168:171],a[52:55],v[168:171]
s_add_u32 m0,s56,6144
s_add_u32 s61,s52,98304
v_mfma_f32_16x16x32_bf16 v[164:167],a[136:139],a[36:39],v[164:167]
buffer_load_dwordx4 v10,s[32:35],s61 offen lds
v_mfma_f32_16x16x32_bf16 v[164:167],a[164:167],a[52:55],v[164:167]
s_add_u32 m0,s56,7168
ds_read_b64_tr_b16 a[72:73],v4 offset:0
v_mfma_f32_16x16x32_bf16 v[160:163],a[132:135],a[36:39],v[160:163]
)"
R"(s_add_u32 s62,s52,114688
ds_read_b64_tr_b16 a[74:75],v4 offset:1024
v_mfma_f32_16x16x32_bf16 v[160:163],a[160:163],a[52:55],v[160:163]
buffer_load_dwordx4 v11,s[32:35],s62 offen lds
v_mfma_f32_16x16x32_bf16 v[156:159],a[128:131],a[36:39],v[156:159]
s_mov_b32 s69,24576
ds_read_b64_tr_b16 a[100:101],v4 offset:8192
v_mfma_f32_16x16x32_bf16 v[156:159],a[156:159],a[52:55],v[156:159]
buffer_store_dwordx4 v[48:51],v18,s[44:47],s69 offen sc1
v_mfma_f32_16x16x32_bf16 v[152:155],a[124:127],a[36:39],v[152:155]
s_lshl_b32 s13,%1,2
ds_read_b64_tr_b16 a[102:103],v4 offset:9216
v_mfma_f32_16x16x32_bf16 v[152:155],a[152:155],a[52:55],v[152:155]
s_add_u32 s14,s13,28672
ds_read_b128 a[4:7],v0 offset:2048
v_mfma_f32_16x16x32_bf16 v[148:151],a[120:123],a[36:39],v[148:151]
s_add_u32 s13,s13,24576
ds_read_b64_tr_b16 a[76:77],v5 offset:0
v_mfma_f32_16x16x32_bf16 v[148:151],a[148:151],a[52:55],v[148:151]
s_lshr_b32 s67,s70,1
ds_read_b64_tr_b16 a[78:79],v5 offset:1024
v_mfma_f32_16x16x32_bf16 v[204:207],a[120:123],a[40:43],v[204:207]
s_lshl_b32 s68,s71,1
ds_read_b64_tr_b16 a[104:105],v5 offset:8192
)"
R"(v_mfma_f32_16x16x32_bf16 v[204:207],a[148:151],a[56:59],v[204:207]
s_add_u32 s67,s67,s68
ds_read_b64_tr_b16 a[106:107],v5 offset:9216
v_mfma_f32_16x16x32_bf16 v[208:211],a[124:127],a[40:43],v[208:211]
s_mul_i32 s67,s67,917504
ds_read_b128 a[20:23],v1 offset:2048
v_mfma_f32_16x16x32_bf16 v[208:211],a[152:155],a[56:59],v[208:211]
s_and_b32 s68,s70,1
ds_read_b64_tr_b16 a[80:81],v6 offset:0
v_mfma_f32_16x16x32_bf16 v[212:215],a[128:131],a[40:43],v[212:215]
s_lshl_b32 s68,s68,12
ds_read_b64_tr_b16 a[82:83],v6 offset:1024
v_mfma_f32_16x16x32_bf16 v[212:215],a[156:159],a[56:59],v[212:215]
s_add_u32 s67,s67,s68
ds_read_b64_tr_b16 a[108:109],v6 offset:8192
v_mfma_f32_16x16x32_bf16 v[216:219],a[132:135],a[40:43],v[216:219]
s_add_u32 s67,s67,50331648
s_add_u32 s48,s30,s67
s_addc_u32 s49,s31,0
v_mfma_f32_16x16x32_bf16 v[216:219],a[160:163],a[56:59],v[216:219]
s_mov_b32 s69,24576
ds_read_b64_tr_b16 a[110:111],v6 offset:9216
v_mfma_f32_16x16x32_bf16 v[220:223],a[136:139],a[40:43],v[220:223]
buffer_store_dwordx4 v[52:55],v58,s[44:47],s69 offen sc1
)"
R"(v_mfma_f32_16x16x32_bf16 v[220:223],a[164:167],a[56:59],v[220:223]
s_and_b32 s49,s49,0xffff
ds_read_b128 a[8:11],v0 offset:4096
v_mfma_f32_16x16x32_bf16 v[224:227],a[140:143],a[40:43],v[224:227]
s_mov_b32 s50,917504
ds_read_b64_tr_b16 a[84:85],v7 offset:0
v_mfma_f32_16x16x32_bf16 v[224:227],a[168:171],a[56:59],v[224:227]
s_mov_b32 s51,0x20000
ds_read_b64_tr_b16 a[86:87],v7 offset:1024
v_mfma_f32_16x16x32_bf16 v[228:231],a[144:147],a[40:43],v[228:231]
s_mul_i32 s67,s86,7
ds_read_b64_tr_b16 a[112:113],v7 offset:8192
v_mfma_f32_16x16x32_bf16 v[228:231],a[172:175],a[56:59],v[228:231]
s_add_u32 s68,s67,1
ds_read_b64_tr_b16 a[114:115],v7 offset:9216
v_mfma_f32_16x16x32_bf16 a[220:223],a[144:147],a[44:47],a[220:223]
s_lshr_b32 s68,s68,1
ds_read_b128 a[24:27],v1 offset:4096
v_mfma_f32_16x16x32_bf16 a[220:223],a[172:175],a[60:63],a[220:223]
s_lshl_b32 s84,s68,13
ds_read_b64_tr_b16 a[88:89],v8 offset:0
v_mfma_f32_16x16x32_bf16 a[216:219],a[140:143],a[44:47],a[216:219]
s_lshr_b32 s67,s67,1
ds_read_b64_tr_b16 a[90:91],v8 offset:1024
)"
R"(v_mfma_f32_16x16x32_bf16 a[216:219],a[168:171],a[60:63],a[216:219]
s_lshl_b32 s67,s67,13
ds_read_b64_tr_b16 a[116:117],v8 offset:8192
v_mfma_f32_16x16x32_bf16 a[212:215],a[136:139],a[44:47],a[212:215]
s_bitcmp1_b32 s86,0
s_cselect_b32 s68,8,24576
v_mfma_f32_16x16x32_bf16 a[212:215],a[164:167],a[60:63],a[212:215]
s_add_u32 s85,s67,s68
ds_read_b64_tr_b16 a[118:119],v8 offset:9216
v_mfma_f32_16x16x32_bf16 a[208:211],a[132:135],a[44:47],a[208:211]
s_xor_b32 s56,s56,0x8000
ds_read_b128 a[12:15],v0 offset:6144
v_mfma_f32_16x16x32_bf16 a[208:211],a[160:163],a[60:63],a[208:211]
s_add_u32 s52,s52,128
ds_read_b128 a[28:31],v1 offset:6144
v_mfma_f32_16x16x32_bf16 a[204:207],a[128:131],a[44:47],a[204:207]
s_cmp_eq_u32 s52,2048
v_mfma_f32_16x16x32_bf16 a[204:207],a[156:159],a[60:63],a[204:207]
s_cselect_b32 s52,0,s52
v_mfma_f32_16x16x32_bf16 a[200:203],a[124:127],a[44:47],a[200:203]
v_mfma_f32_16x16x32_bf16 a[200:203],a[152:155],a[60:63],a[200:203]
v_mfma_f32_16x16x32_bf16 a[196:199],a[120:123],a[44:47],a[196:199]
s_waitcnt lgkmcnt(0)
v_mfma_f32_16x16x32_bf16 a[196:199],a[148:151],a[60:63],a[196:199]
)"
R"(v_mfma_f32_16x16x32_bf16 v[64:67],a[64:67],a[0:3],v[64:67]
s_add_u32 m0,s57,0
s_add_u32 s63,s54,0
ds_read_b64_tr_b16 a[120:121],v2 offset:16384
v_mfma_f32_16x16x32_bf16 v[64:67],a[92:95],a[16:19],v[64:67]
buffer_load_dwordx4 v14,s[40:43],s63 offen lds
ds_read_b64_tr_b16 a[122:123],v2 offset:17408
v_mfma_f32_16x16x32_bf16 v[68:71],a[68:71],a[0:3],v[68:71]
s_add_u32 s66,s59,0x8000
s_cmp_eq_u32 s66,0x18000
ds_read_b64_tr_b16 a[148:149],v2 offset:24576
v_mfma_f32_16x16x32_bf16 v[68:71],a[96:99],a[16:19],v[68:71]
s_cselect_b32 s66,0,s66
ds_read_b64_tr_b16 a[150:151],v2 offset:25600
v_mfma_f32_16x16x32_bf16 v[72:75],a[72:75],a[0:3],v[72:75]
s_sub_u32 s58,s66,s59
s_mov_b32 s59,s66
ds_read_b64_tr_b16 a[124:125],v3 offset:16384
v_mfma_f32_16x16x32_bf16 v[72:75],a[100:103],a[16:19],v[72:75]
s_add_u32 m0,s57,1024
v_mfma_f32_16x16x32_bf16 v[76:79],a[76:79],a[0:3],v[76:79]
s_add_u32 s64,s54,28672
buffer_load_dwordx4 v14,s[40:43],s64 offen lds
ds_read_b64_tr_b16 a[126:127],v3 offset:17408
)"
R"(v_mfma_f32_16x16x32_bf16 v[76:79],a[104:107],a[16:19],v[76:79]
s_add_u32 m0,s57,2048
ds_read_b64_tr_b16 a[152:153],v3 offset:24576
v_mfma_f32_16x16x32_bf16 v[80:83],a[80:83],a[0:3],v[80:83]
s_add_u32 s63,s54,57344
buffer_load_dwordx4 v15,s[40:43],s63 offen lds
ds_read_b64_tr_b16 a[154:155],v3 offset:25600
v_mfma_f32_16x16x32_bf16 v[80:83],a[108:111],a[16:19],v[80:83]
s_add_u32 m0,s57,3072
ds_read_b64_tr_b16 a[128:129],v4 offset:16384
v_mfma_f32_16x16x32_bf16 v[84:87],a[84:87],a[0:3],v[84:87]
s_add_u32 s64,s54,86016
buffer_load_dwordx4 v15,s[40:43],s64 offen lds
ds_read_b64_tr_b16 a[130:131],v4 offset:17408
v_mfma_f32_16x16x32_bf16 v[84:87],a[112:115],a[16:19],v[84:87]
s_add_u32 m0,s57,4096
v_mfma_f32_16x16x32_bf16 v[88:91],a[88:91],a[0:3],v[88:91]
s_add_u32 s63,s54,114688
buffer_load_dwordx4 v14,s[40:43],s63 offen lds
ds_read_b64_tr_b16 a[156:157],v4 offset:24576
v_mfma_f32_16x16x32_bf16 v[88:91],a[116:119],a[16:19],v[88:91]
s_add_u32 m0,s57,5120
ds_read_b64_tr_b16 a[158:159],v4 offset:25600
v_mfma_f32_16x16x32_bf16 v[144:147],a[88:91],a[4:7],v[144:147]
)"
R"(s_add_u32 s64,s54,143360
buffer_load_dwordx4 v14,s[40:43],s64 offen lds
ds_read_b64_tr_b16 a[132:133],v5 offset:16384
v_mfma_f32_16x16x32_bf16 v[144:147],a[116:119],a[20:23],v[144:147]
s_add_u32 m0,s57,6144
ds_read_b64_tr_b16 a[134:135],v5 offset:17408
v_mfma_f32_16x16x32_bf16 v[140:143],a[84:87],a[4:7],v[140:143]
s_add_u32 s63,s54,172032
buffer_load_dwordx4 v15,s[40:43],s63 offen lds
ds_read_b64_tr_b16 a[160:161],v5 offset:24576
v_mfma_f32_16x16x32_bf16 v[140:143],a[112:115],a[20:23],v[140:143]
s_add_u32 m0,s57,7168
v_mfma_f32_16x16x32_bf16 v[136:139],a[80:83],a[4:7],v[136:139]
s_add_u32 s64,s54,200704
buffer_load_dwordx4 v15,s[40:43],s64 offen lds
ds_read_b64_tr_b16 a[162:163],v5 offset:25600
v_mfma_f32_16x16x32_bf16 v[136:139],a[108:111],a[20:23],v[136:139]
s_add_u32 s57,s57,0x8000
ds_read_b64_tr_b16 a[136:137],v6 offset:16384
v_mfma_f32_16x16x32_bf16 v[132:135],a[76:79],a[4:7],v[132:135]
s_cmp_ge_u32 s57,0x28000
s_cselect_b32 s65,0x18000,0
ds_read_b64_tr_b16 a[138:139],v6 offset:17408
v_mfma_f32_16x16x32_bf16 v[132:135],a[104:107],a[20:23],v[132:135]
)"
R"(s_sub_u32 s57,s57,s65
ds_read_b64_tr_b16 a[164:165],v6 offset:24576
v_mfma_f32_16x16x32_bf16 v[128:131],a[72:75],a[4:7],v[128:131]
s_add_u32 s54,s54,458752
s_cmp_eq_u32 s54,7340032
ds_read_b64_tr_b16 a[166:167],v6 offset:25600
v_mfma_f32_16x16x32_bf16 v[128:131],a[100:103],a[20:23],v[128:131]
s_cselect_b32 s54,0,s54
v_mfma_f32_16x16x32_bf16 v[124:127],a[68:71],a[4:7],v[124:127]
s_lshl_b32 s12,s53,21
s_lshl_b32 s15,s27,8
ds_read_b64_tr_b16 a[140:141],v7 offset:16384
v_mfma_f32_16x16x32_bf16 v[124:127],a[96:99],a[20:23],v[124:127]
s_add_u32 s12,s12,s15
ds_read_b64_tr_b16 a[142:143],v7 offset:17408
v_mfma_f32_16x16x32_bf16 v[120:123],a[64:67],a[4:7],v[120:123]
s_add_u32 s44,s8,s12
s_addc_u32 s45,s9,0
v_mfma_f32_16x16x32_bf16 v[120:123],a[92:95],a[20:23],v[120:123]
s_and_b32 s45,s45,0xffff
s_mov_b32 s46,0
ds_read_b64_tr_b16 a[168:169],v7 offset:24576
v_mfma_f32_16x16x32_bf16 v[176:179],a[64:67],a[8:11],v[176:179]
s_mov_b32 s47,0x20000
)"
R"(s_sub_u32 s46,8388608,s12
ds_read_b64_tr_b16 a[170:171],v7 offset:25600
v_mfma_f32_16x16x32_bf16 v[176:179],a[92:95],a[24:27],v[176:179]
s_lshl_b32 s12,s28,17
ds_read_b64_tr_b16 a[144:145],v8 offset:16384
v_mfma_f32_16x16x32_bf16 v[180:183],a[68:71],a[8:11],v[180:183]
s_lshl_b32 s15,s27,8
s_add_u32 s12,s12,s15
ds_read_b64_tr_b16 a[146:147],v8 offset:17408
v_mfma_f32_16x16x32_bf16 v[180:183],a[96:99],a[24:27],v[180:183]
s_add_u32 s92,s10,s12
s_addc_u32 s93,s11,0
v_mfma_f32_16x16x32_bf16 v[184:187],a[72:75],a[8:11],v[184:187]
s_and_b32 s93,s93,0xffff
s_mov_b32 s94,0
ds_read_b64_tr_b16 a[172:173],v8 offset:24576
v_mfma_f32_16x16x32_bf16 v[184:187],a[100:103],a[24:27],v[184:187]
s_mov_b32 s95,0x20000
ds_read_b64_tr_b16 a[174:175],v8 offset:25600
v_mfma_f32_16x16x32_bf16 v[188:191],a[76:79],a[8:11],v[188:191]
s_sub_u32 s94,7340032,s12
v_lshrrev_b32 v52,4,v19
v_mfma_f32_16x16x32_bf16 v[188:191],a[104:107],a[24:27],v[188:191]
v_or_b32 v52,0,v52
)"
R"(v_mfma_f32_16x16x32_bf16 v[192:195],a[80:83],a[8:11],v[192:195]
v_bfe_u32 v53,v19,1,3
v_xor_b32 v0,0x8000,v0
v_mfma_f32_16x16x32_bf16 v[192:195],a[108:111],a[24:27],v[192:195]
v_xor_b32 v52,v52,v53
v_xor_b32 v1,0x8000,v1
v_mfma_f32_16x16x32_bf16 v[196:199],a[84:87],a[8:11],v[196:199]
v_lshlrev_b32 v52,5,v52
v_add_u32 v2,s58,v2
v_mfma_f32_16x16x32_bf16 v[196:199],a[112:115],a[24:27],v[196:199]
v_and_b32 v53,1,v19
v_add_u32 v3,s58,v3
v_mfma_f32_16x16x32_bf16 v[200:203],a[88:91],a[8:11],v[200:203]
v_lshl_add_u32 v52,v53,4,v52
v_add_u32 v4,s58,v4
v_mfma_f32_16x16x32_bf16 v[200:203],a[116:119],a[24:27],v[200:203]
v_lshrrev_b32 v53,4,v19
v_add_u32 v5,s58,v5
v_mfma_f32_16x16x32_bf16 a[192:195],a[88:91],a[12:15],a[192:195]
v_lshl_add_u32 v39,v53,11,v52
v_add_u32 v6,s58,v6
v_mfma_f32_16x16x32_bf16 a[192:195],a[116:119],a[28:31],a[192:195]
v_lshrrev_b32 v52,4,v19
v_add_u32 v7,s58,v7
)"
R"(v_mfma_f32_16x16x32_bf16 v[252:255],a[84:87],a[12:15],v[252:255]
v_or_b32 v52,4,v52
v_add_u32 v8,s58,v8
v_mfma_f32_16x16x32_bf16 v[252:255],a[112:115],a[28:31],v[252:255]
v_bfe_u32 v53,v19,1,3
v_add_u32 v9,s58,v9
v_mfma_f32_16x16x32_bf16 v[248:251],a[80:83],a[12:15],v[248:251]
v_xor_b32 v52,v52,v53
v_lshlrev_b32 v52,5,v52
v_mfma_f32_16x16x32_bf16 v[248:251],a[108:111],a[28:31],v[248:251]
v_and_b32 v53,1,v19
v_lshl_add_u32 v52,v53,4,v52
v_mfma_f32_16x16x32_bf16 v[244:247],a[76:79],a[12:15],v[244:247]
v_lshrrev_b32 v53,4,v19
v_lshl_add_u32 v55,v53,11,v52
v_mfma_f32_16x16x32_bf16 v[244:247],a[104:107],a[28:31],v[244:247]
s_add_u32 s15,s53,0
s_and_b32 s15,s15,3
s_lshl_b32 s15,s15,10
v_mfma_f32_16x16x32_bf16 v[240:243],a[72:75],a[12:15],v[240:243]
v_add_u32 v40,s15,v18
s_add_u32 s15,s53,1
v_mfma_f32_16x16x32_bf16 v[240:243],a[100:103],a[28:31],v[240:243]
v_mfma_f32_16x16x32_bf16 v[236:239],a[68:71],a[12:15],v[236:239]
)"
R"(v_mfma_f32_16x16x32_bf16 v[236:239],a[96:99],a[28:31],v[236:239]
v_mfma_f32_16x16x32_bf16 v[232:235],a[64:67],a[12:15],v[232:235]
s_waitcnt vmcnt(10) lgkmcnt(0)
v_mfma_f32_16x16x32_bf16 v[232:235],a[92:95],a[28:31],v[232:235]
s_barrier
v_mfma_f32_16x16x32_bf16 v[92:95],a[120:123],a[0:3],v[92:95]
s_add_u32 m0,s56,0
s_add_u32 s61,s52,0
ds_read_b64_tr_b16 a[64:65],v2 offset:0
v_mfma_f32_16x16x32_bf16 v[92:95],a[148:151],a[16:19],v[92:95]
buffer_load_dwordx4 v10,s[32:35],s61 offen lds
ds_read_b64_tr_b16 a[66:67],v2 offset:1024
v_mfma_f32_16x16x32_bf16 v[96:99],a[124:127],a[0:3],v[96:99]
s_add_u32 m0,s56,1024
s_add_u32 s62,s52,16384
ds_read_b64_tr_b16 a[92:93],v2 offset:8192
v_mfma_f32_16x16x32_bf16 v[96:99],a[152:155],a[16:19],v[96:99]
buffer_load_dwordx4 v11,s[32:35],s62 offen lds
ds_read_b64_tr_b16 a[94:95],v2 offset:9216
v_mfma_f32_16x16x32_bf16 v[100:103],a[128:131],a[0:3],v[100:103]
s_add_u32 m0,s56,2048
s_add_u32 s61,s52,32768
ds_read_b128 a[32:35],v0 offset:0
v_mfma_f32_16x16x32_bf16 v[100:103],a[156:159],a[16:19],v[100:103]
)"
R"(buffer_load_dwordx4 v10,s[32:35],s61 offen lds
ds_read_b64_tr_b16 a[68:69],v3 offset:0
v_mfma_f32_16x16x32_bf16 v[104:107],a[132:135],a[0:3],v[104:107]
s_add_u32 m0,s56,3072
ds_read_b64_tr_b16 a[70:71],v3 offset:1024
v_mfma_f32_16x16x32_bf16 v[104:107],a[160:163],a[16:19],v[104:107]
s_add_u32 s62,s52,49152
buffer_load_dwordx4 v11,s[32:35],s62 offen lds
ds_read_b64_tr_b16 a[96:97],v3 offset:8192
v_mfma_f32_16x16x32_bf16 v[108:111],a[136:139],a[0:3],v[108:111]
s_add_u32 m0,s56,4096
ds_read_b64_tr_b16 a[98:99],v3 offset:9216
v_mfma_f32_16x16x32_bf16 v[108:111],a[164:167],a[16:19],v[108:111]
s_add_u32 s61,s52,65536
buffer_load_dwordx4 v10,s[32:35],s61 offen lds
ds_read_b128 a[48:51],v1 offset:0
v_mfma_f32_16x16x32_bf16 v[112:115],a[140:143],a[0:3],v[112:115]
s_add_u32 m0,s56,5120
ds_read_b64_tr_b16 a[72:73],v4 offset:0
v_mfma_f32_16x16x32_bf16 v[112:115],a[168:171],a[16:19],v[112:115]
s_add_u32 s62,s52,81920
buffer_load_dwordx4 v11,s[32:35],s62 offen lds
ds_read_b64_tr_b16 a[74:75],v4 offset:1024
v_mfma_f32_16x16x32_bf16 v[116:119],a[144:147],a[0:3],v[116:119]
)"
R"(s_add_u32 m0,s56,6144
ds_read_b64_tr_b16 a[100:101],v4 offset:8192
v_mfma_f32_16x16x32_bf16 v[116:119],a[172:175],a[16:19],v[116:119]
s_add_u32 s61,s52,98304
ds_read_b64_tr_b16 a[102:103],v4 offset:9216
v_mfma_f32_16x16x32_bf16 v[172:175],a[144:147],a[4:7],v[172:175]
buffer_load_dwordx4 v10,s[32:35],s61 offen lds
ds_read_b128 a[36:39],v0 offset:2048
v_mfma_f32_16x16x32_bf16 v[172:175],a[172:175],a[20:23],v[172:175]
s_add_u32 m0,s56,7168
s_add_u32 s62,s52,114688
ds_read_b64_tr_b16 a[76:77],v5 offset:0
v_mfma_f32_16x16x32_bf16 v[168:171],a[140:143],a[4:7],v[168:171]
buffer_load_dwordx4 v11,s[32:35],s62 offen lds
ds_read_b64_tr_b16 a[78:79],v5 offset:1024
v_mfma_f32_16x16x32_bf16 v[168:171],a[168:171],a[20:23],v[168:171]
s_and_b32 s15,s15,3
s_lshl_b32 s15,s15,10
ds_read_b64_tr_b16 a[104:105],v5 offset:8192
v_mfma_f32_16x16x32_bf16 v[164:167],a[136:139],a[4:7],v[164:167]
v_add_u32 v41,s15,v18
ds_read_b64_tr_b16 a[106:107],v5 offset:9216
v_mfma_f32_16x16x32_bf16 v[164:167],a[164:167],a[20:23],v[164:167]
s_add_u32 s15,s53,2
)"
R"(s_and_b32 s15,s15,3
ds_read_b128 a[52:55],v1 offset:2048
v_mfma_f32_16x16x32_bf16 v[160:163],a[132:135],a[4:7],v[160:163]
s_lshl_b32 s15,s15,10
ds_read_b64_tr_b16 a[80:81],v6 offset:0
v_mfma_f32_16x16x32_bf16 v[160:163],a[160:163],a[20:23],v[160:163]
v_add_u32 v42,s15,v18
ds_read_b64_tr_b16 a[82:83],v6 offset:1024
v_mfma_f32_16x16x32_bf16 v[156:159],a[128:131],a[4:7],v[156:159]
s_add_u32 s15,s53,3
s_and_b32 s15,s15,3
ds_read_b64_tr_b16 a[108:109],v6 offset:8192
v_mfma_f32_16x16x32_bf16 v[156:159],a[156:159],a[20:23],v[156:159]
s_lshl_b32 s15,s15,10
v_add_u32 v43,s15,v18
ds_read_b64_tr_b16 a[110:111],v6 offset:9216
v_mfma_f32_16x16x32_bf16 v[152:155],a[124:127],a[4:7],v[152:155]
v_bfe_u32 v53,v19,2,3
ds_read_b128 a[40:43],v0 offset:4096
v_mfma_f32_16x16x32_bf16 v[152:155],a[152:155],a[20:23],v[152:155]
v_lshrrev_b32 v52,2,v19
ds_read_b64_tr_b16 a[84:85],v7 offset:0
v_mfma_f32_16x16x32_bf16 v[148:151],a[120:123],a[4:7],v[148:151]
v_lshlrev_b32 v52,8,v52
)"
R"(ds_read_b64_tr_b16 a[86:87],v7 offset:1024
v_mfma_f32_16x16x32_bf16 v[148:151],a[148:151],a[20:23],v[148:151]
v_and_b32 v54,3,v19
ds_read_b64_tr_b16 a[112:113],v7 offset:8192
v_mfma_f32_16x16x32_bf16 v[204:207],a[120:123],a[8:11],v[204:207]
v_lshl_add_u32 v52,v54,3,v52
ds_read_b64_tr_b16 a[114:115],v7 offset:9216
v_mfma_f32_16x16x32_bf16 v[204:207],a[148:151],a[24:27],v[204:207]
v_add_u32 v52,65536,v52
ds_read_b128 a[56:59],v1 offset:4096
v_mfma_f32_16x16x32_bf16 v[208:211],a[124:127],a[8:11],v[208:211]
v_xor_b32 v54,0,v53
ds_read_b64_tr_b16 a[88:89],v8 offset:0
v_mfma_f32_16x16x32_bf16 v[208:211],a[152:155],a[24:27],v[208:211]
v_lshl_add_u32 v44,v54,5,v52
ds_read_b64_tr_b16 a[90:91],v8 offset:1024
v_mfma_f32_16x16x32_bf16 v[212:215],a[128:131],a[8:11],v[212:215]
v_xor_b32 v54,1,v53
ds_read_b64_tr_b16 a[116:117],v8 offset:8192
v_mfma_f32_16x16x32_bf16 v[212:215],a[156:159],a[24:27],v[212:215]
v_lshl_add_u32 v45,v54,5,v52
ds_read_b64_tr_b16 a[118:119],v8 offset:9216
v_mfma_f32_16x16x32_bf16 v[216:219],a[132:135],a[8:11],v[216:219]
v_xor_b32 v54,2,v53
)"
R"(ds_read_b128 a[44:47],v0 offset:6144
v_mfma_f32_16x16x32_bf16 v[216:219],a[160:163],a[24:27],v[216:219]
v_lshl_add_u32 v46,v54,5,v52
ds_read_b128 a[60:63],v1 offset:6144
v_mfma_f32_16x16x32_bf16 v[220:223],a[136:139],a[8:11],v[220:223]
v_xor_b32 v54,3,v53
v_lshl_add_u32 v47,v54,5,v52
v_mfma_f32_16x16x32_bf16 v[220:223],a[164:167],a[24:27],v[220:223]
v_xor_b32 v54,4,v53
v_lshl_add_u32 v48,v54,5,v52
v_mfma_f32_16x16x32_bf16 v[224:227],a[140:143],a[8:11],v[224:227]
v_xor_b32 v54,5,v53
v_lshl_add_u32 v49,v54,5,v52
v_mfma_f32_16x16x32_bf16 v[224:227],a[168:171],a[24:27],v[224:227]
v_xor_b32 v54,6,v53
v_lshl_add_u32 v50,v54,5,v52
v_mfma_f32_16x16x32_bf16 v[228:231],a[144:147],a[8:11],v[228:231]
v_xor_b32 v54,7,v53
v_lshl_add_u32 v51,v54,5,v52
v_mfma_f32_16x16x32_bf16 v[228:231],a[172:175],a[24:27],v[228:231]
v_and_b32 v54,15,v19
v_lshlrev_b32 v52,11,v54
v_mfma_f32_16x16x32_bf16 a[220:223],a[144:147],a[12:15],a[220:223]
v_bfe_u32 v53,v19,4,1
)"
R"(v_lshl_add_u32 v52,v53,5,v52
v_mfma_f32_16x16x32_bf16 a[220:223],a[172:175],a[28:31],a[220:223]
v_lshrrev_b32 v53,5,v19
v_lshl_add_u32 v60,v53,4,v52
v_mfma_f32_16x16x32_bf16 a[216:219],a[140:143],a[12:15],a[216:219]
v_mov_b32 v24,-1
v_mov_b32 v25,-1
v_mfma_f32_16x16x32_bf16 a[216:219],a[168:171],a[28:31],a[216:219]
v_mov_b32 v26,-1
v_mfma_f32_16x16x32_bf16 a[212:215],a[136:139],a[12:15],a[212:215]
v_mov_b32 v27,-1
v_mfma_f32_16x16x32_bf16 a[212:215],a[164:167],a[28:31],a[212:215]
s_lshl_b32 s55,s53,16
buffer_store_dwordx4 v[24:27],v18,s[16:19],s55 offen sc1
v_mfma_f32_16x16x32_bf16 a[208:211],a[132:135],a[12:15],a[208:211]
s_add_u32 s55,s55,1024
v_mfma_f32_16x16x32_bf16 a[208:211],a[160:163],a[28:31],a[208:211]
s_xor_b32 s56,s56,0x8000
s_add_u32 s52,s52,128
v_mfma_f32_16x16x32_bf16 a[204:207],a[128:131],a[12:15],a[204:207]
s_cmp_eq_u32 s52,2048
v_mfma_f32_16x16x32_bf16 a[204:207],a[156:159],a[28:31],a[204:207]
s_cselect_b32 s52,0,s52
v_mfma_f32_16x16x32_bf16 a[200:203],a[124:127],a[12:15],a[200:203]
)"
R"(v_mfma_f32_16x16x32_bf16 a[200:203],a[152:155],a[28:31],a[200:203]
v_mfma_f32_16x16x32_bf16 a[196:199],a[120:123],a[12:15],a[196:199]
s_waitcnt lgkmcnt(0)
v_mfma_f32_16x16x32_bf16 a[196:199],a[148:151],a[28:31],a[196:199]
v_mfma_f32_16x16x32_bf16 v[64:67],a[64:67],a[32:35],v[64:67]
s_add_u32 m0,s57,0
ds_read_b64_tr_b16 a[120:121],v2 offset:16384
v_mfma_f32_16x16x32_bf16 v[64:67],a[92:95],a[48:51],v[64:67]
s_add_u32 s63,s54,0
ds_read_b64_tr_b16 a[122:123],v2 offset:17408
v_mfma_f32_16x16x32_bf16 v[68:71],a[68:71],a[32:35],v[68:71]
buffer_load_dwordx4 v14,s[40:43],s63 offen lds
v_mfma_f32_16x16x32_bf16 v[68:71],a[96:99],a[48:51],v[68:71]
ds_read_b64_tr_b16 a[148:149],v2 offset:24576
v_mfma_f32_16x16x32_bf16 v[72:75],a[72:75],a[32:35],v[72:75]
s_branch gm_KB_sk20
.p2align 12
gm_KB_pg20:
s_setpc_b64 s[100:101]
gm_KB_sk20:
s_add_u32 s66,s59,0x8000
ds_read_b64_tr_b16 a[150:151],v2 offset:25600
v_mfma_f32_16x16x32_bf16 v[72:75],a[100:103],a[48:51],v[72:75]
s_cmp_eq_u32 s66,0x18000
)"
R"(ds_read_b64_tr_b16 a[124:125],v3 offset:16384
v_mfma_f32_16x16x32_bf16 v[76:79],a[76:79],a[32:35],v[76:79]
s_cselect_b32 s66,0,s66
ds_read_b64_tr_b16 a[126:127],v3 offset:17408
v_mfma_f32_16x16x32_bf16 v[76:79],a[104:107],a[48:51],v[76:79]
ds_read_b64_tr_b16 a[152:153],v3 offset:24576
v_mfma_f32_16x16x32_bf16 v[80:83],a[80:83],a[32:35],v[80:83]
s_sub_u32 s58,s66,s59
ds_read_b64_tr_b16 a[154:155],v3 offset:25600
v_mfma_f32_16x16x32_bf16 v[80:83],a[108:111],a[48:51],v[80:83]
s_mov_b32 s59,s66
ds_read_b64_tr_b16 a[128:129],v4 offset:16384
v_mfma_f32_16x16x32_bf16 v[84:87],a[84:87],a[32:35],v[84:87]
ds_read_b64_tr_b16 a[130:131],v4 offset:17408
v_mfma_f32_16x16x32_bf16 v[84:87],a[112:115],a[48:51],v[84:87]
s_add_u32 m0,s57,1024
v_mfma_f32_16x16x32_bf16 v[88:91],a[88:91],a[32:35],v[88:91]
s_add_u32 s64,s54,28672
ds_read_b64_tr_b16 a[156:157],v4 offset:24576
v_mfma_f32_16x16x32_bf16 v[88:91],a[116:119],a[48:51],v[88:91]
buffer_load_dwordx4 v14,s[40:43],s64 offen lds
v_mfma_f32_16x16x32_bf16 v[144:147],a[88:91],a[36:39],v[144:147]
ds_read_b64_tr_b16 a[158:159],v4 offset:25600
v_mfma_f32_16x16x32_bf16 v[144:147],a[116:119],a[52:55],v[144:147]
)"
R"(s_add_u32 m0,s57,2048
ds_read_b64_tr_b16 a[132:133],v5 offset:16384
v_mfma_f32_16x16x32_bf16 v[140:143],a[84:87],a[36:39],v[140:143]
s_add_u32 s63,s54,57344
ds_read_b64_tr_b16 a[134:135],v5 offset:17408
v_mfma_f32_16x16x32_bf16 v[140:143],a[112:115],a[52:55],v[140:143]
ds_read_b64_tr_b16 a[160:161],v5 offset:24576
v_mfma_f32_16x16x32_bf16 v[136:139],a[80:83],a[36:39],v[136:139]
buffer_load_dwordx4 v15,s[40:43],s63 offen lds
v_mfma_f32_16x16x32_bf16 v[136:139],a[108:111],a[52:55],v[136:139]
s_add_u32 m0,s57,3072
ds_read_b64_tr_b16 a[162:163],v5 offset:25600
v_mfma_f32_16x16x32_bf16 v[132:135],a[76:79],a[36:39],v[132:135]
s_add_u32 s64,s54,86016
ds_read_b64_tr_b16 a[136:137],v6 offset:16384
v_mfma_f32_16x16x32_bf16 v[132:135],a[104:107],a[52:55],v[132:135]
ds_read_b64_tr_b16 a[138:139],v6 offset:17408
v_mfma_f32_16x16x32_bf16 v[128:131],a[72:75],a[36:39],v[128:131]
buffer_load_dwordx4 v15,s[40:43],s64 offen lds
v_mfma_f32_16x16x32_bf16 v[128:131],a[100:103],a[52:55],v[128:131]
s_add_u32 m0,s57,4096
ds_read_b64_tr_b16 a[164:165],v6 offset:24576
v_mfma_f32_16x16x32_bf16 v[124:127],a[68:71],a[36:39],v[124:127]
ds_read_b64_tr_b16 a[166:167],v6 offset:25600
)"
R"(v_mfma_f32_16x16x32_bf16 v[124:127],a[96:99],a[52:55],v[124:127]
s_add_u32 s63,s54,114688
ds_read_b64_tr_b16 a[140:141],v7 offset:16384
v_mfma_f32_16x16x32_bf16 v[120:123],a[64:67],a[36:39],v[120:123]
buffer_load_dwordx4 v14,s[40:43],s63 offen lds
v_mfma_f32_16x16x32_bf16 v[120:123],a[92:95],a[52:55],v[120:123]
s_add_u32 m0,s57,5120
ds_read_b64_tr_b16 a[142:143],v7 offset:17408
v_mfma_f32_16x16x32_bf16 v[176:179],a[64:67],a[40:43],v[176:179]
ds_read_b64_tr_b16 a[168:169],v7 offset:24576
v_mfma_f32_16x16x32_bf16 v[176:179],a[92:95],a[56:59],v[176:179]
s_add_u32 s64,s54,143360
ds_read_b64_tr_b16 a[170:171],v7 offset:25600
v_mfma_f32_16x16x32_bf16 v[180:183],a[68:71],a[40:43],v[180:183]
buffer_load_dwordx4 v14,s[40:43],s64 offen lds
v_mfma_f32_16x16x32_bf16 v[180:183],a[96:99],a[56:59],v[180:183]
s_add_u32 m0,s57,6144
ds_read_b64_tr_b16 a[144:145],v8 offset:16384
v_mfma_f32_16x16x32_bf16 v[184:187],a[72:75],a[40:43],v[184:187]
ds_read_b64_tr_b16 a[146:147],v8 offset:17408
v_mfma_f32_16x16x32_bf16 v[184:187],a[100:103],a[56:59],v[184:187]
s_add_u32 s63,s54,172032
ds_read_b64_tr_b16 a[172:173],v8 offset:24576
v_mfma_f32_16x16x32_bf16 v[188:191],a[76:79],a[40:43],v[188:191]
)"
R"(buffer_load_dwordx4 v15,s[40:43],s63 offen lds
v_mfma_f32_16x16x32_bf16 v[188:191],a[104:107],a[56:59],v[188:191]
ds_read_b64_tr_b16 a[174:175],v8 offset:25600
v_mfma_f32_16x16x32_bf16 v[192:195],a[80:83],a[40:43],v[192:195]
s_add_u32 m0,s57,7168
v_xor_b32 v0,0x8000,v0
v_mfma_f32_16x16x32_bf16 v[192:195],a[108:111],a[56:59],v[192:195]
s_add_u32 s64,s54,200704
v_xor_b32 v1,0x8000,v1
v_mfma_f32_16x16x32_bf16 v[196:199],a[84:87],a[40:43],v[196:199]
buffer_load_dwordx4 v15,s[40:43],s64 offen lds
v_mfma_f32_16x16x32_bf16 v[196:199],a[112:115],a[56:59],v[196:199]
v_add_u32 v2,s58,v2
v_mfma_f32_16x16x32_bf16 v[200:203],a[88:91],a[40:43],v[200:203]
s_add_u32 s57,s57,0x8000
v_add_u32 v3,s58,v3
v_mfma_f32_16x16x32_bf16 v[200:203],a[116:119],a[56:59],v[200:203]
s_cmp_ge_u32 s57,0x28000
v_add_u32 v4,s58,v4
v_mfma_f32_16x16x32_bf16 a[192:195],a[88:91],a[44:47],a[192:195]
v_add_u32 v5,s58,v5
v_mfma_f32_16x16x32_bf16 a[192:195],a[116:119],a[60:63],a[192:195]
s_cselect_b32 s65,0x18000,0
v_add_u32 v6,s58,v6
)"
R"(v_mfma_f32_16x16x32_bf16 v[252:255],a[84:87],a[44:47],v[252:255]
s_sub_u32 s57,s57,s65
v_add_u32 v7,s58,v7
v_mfma_f32_16x16x32_bf16 v[252:255],a[112:115],a[60:63],v[252:255]
s_add_u32 s54,s54,458752
v_add_u32 v8,s58,v8
v_mfma_f32_16x16x32_bf16 v[248:251],a[80:83],a[44:47],v[248:251]
v_add_u32 v9,s58,v9
v_mfma_f32_16x16x32_bf16 v[248:251],a[108:111],a[60:63],v[248:251]
s_cmp_eq_u32 s54,7340032
v_mfma_f32_16x16x32_bf16 v[244:247],a[76:79],a[44:47],v[244:247]
s_cselect_b32 s54,0,s54
v_mfma_f32_16x16x32_bf16 v[244:247],a[104:107],a[60:63],v[244:247]
v_mfma_f32_16x16x32_bf16 v[240:243],a[72:75],a[44:47],v[240:243]
v_mfma_f32_16x16x32_bf16 v[240:243],a[100:103],a[60:63],v[240:243]
v_mfma_f32_16x16x32_bf16 v[236:239],a[68:71],a[44:47],v[236:239]
v_mfma_f32_16x16x32_bf16 v[236:239],a[96:99],a[60:63],v[236:239]
v_mfma_f32_16x16x32_bf16 v[232:235],a[64:67],a[44:47],v[232:235]
s_waitcnt vmcnt(9) lgkmcnt(0)
v_mfma_f32_16x16x32_bf16 v[232:235],a[92:95],a[60:63],v[232:235]
s_barrier
v_mfma_f32_16x16x32_bf16 v[92:95],a[120:123],a[32:35],v[92:95]
s_add_u32 m0,s56,0
ds_read_b64_tr_b16 a[64:65],v2 offset:0
)"
R"(v_mfma_f32_16x16x32_bf16 v[92:95],a[148:151],a[48:51],v[92:95]
s_add_u32 s61,s52,0
ds_read_b64_tr_b16 a[66:67],v2 offset:1024
v_mfma_f32_16x16x32_bf16 v[96:99],a[124:127],a[32:35],v[96:99]
buffer_load_dwordx4 v10,s[32:35],s61 offen lds
v_mfma_f32_16x16x32_bf16 v[96:99],a[152:155],a[48:51],v[96:99]
s_add_u32 m0,s56,1024
ds_read_b64_tr_b16 a[92:93],v2 offset:8192
v_mfma_f32_16x16x32_bf16 v[100:103],a[128:131],a[32:35],v[100:103]
ds_read_b64_tr_b16 a[94:95],v2 offset:9216
v_mfma_f32_16x16x32_bf16 v[100:103],a[156:159],a[48:51],v[100:103]
s_add_u32 s62,s52,16384
ds_read_b128 a[0:3],v0 offset:0
v_mfma_f32_16x16x32_bf16 v[104:107],a[132:135],a[32:35],v[104:107]
buffer_load_dwordx4 v11,s[32:35],s62 offen lds
v_mfma_f32_16x16x32_bf16 v[104:107],a[160:163],a[48:51],v[104:107]
s_add_u32 m0,s56,2048
ds_read_b64_tr_b16 a[68:69],v3 offset:0
v_mfma_f32_16x16x32_bf16 v[108:111],a[136:139],a[32:35],v[108:111]
ds_read_b64_tr_b16 a[70:71],v3 offset:1024
v_mfma_f32_16x16x32_bf16 v[108:111],a[164:167],a[48:51],v[108:111]
s_add_u32 s61,s52,32768
ds_read_b64_tr_b16 a[96:97],v3 offset:8192
v_mfma_f32_16x16x32_bf16 v[112:115],a[140:143],a[32:35],v[112:115]
)"
R"(buffer_load_dwordx4 v10,s[32:35],s61 offen lds
v_mfma_f32_16x16x32_bf16 v[112:115],a[168:171],a[48:51],v[112:115]
s_add_u32 m0,s56,3072
ds_read_b64_tr_b16 a[98:99],v3 offset:9216
v_mfma_f32_16x16x32_bf16 v[116:119],a[144:147],a[32:35],v[116:119]
ds_read_b128 a[16:19],v1 offset:0
v_mfma_f32_16x16x32_bf16 v[116:119],a[172:175],a[48:51],v[116:119]
s_add_u32 s62,s52,49152
ds_read_b64_tr_b16 a[72:73],v4 offset:0
v_mfma_f32_16x16x32_bf16 v[172:175],a[144:147],a[36:39],v[172:175]
buffer_load_dwordx4 v11,s[32:35],s62 offen lds
v_mfma_f32_16x16x32_bf16 v[172:175],a[172:175],a[52:55],v[172:175]
s_add_u32 m0,s56,4096
ds_read_b64_tr_b16 a[74:75],v4 offset:1024
v_mfma_f32_16x16x32_bf16 v[168:171],a[140:143],a[36:39],v[168:171]
s_add_u32 s61,s52,65536
ds_read_b64_tr_b16 a[100:101],v4 offset:8192
v_mfma_f32_16x16x32_bf16 v[168:171],a[168:171],a[52:55],v[168:171]
ds_read_b64_tr_b16 a[102:103],v4 offset:9216
v_mfma_f32_16x16x32_bf16 v[164:167],a[136:139],a[36:39],v[164:167]
buffer_load_dwordx4 v10,s[32:35],s61 offen lds
v_mfma_f32_16x16x32_bf16 v[164:167],a[164:167],a[52:55],v[164:167]
s_add_u32 m0,s56,5120
ds_read_b128 a[4:7],v0 offset:2048
)"
R"(v_mfma_f32_16x16x32_bf16 v[160:163],a[132:135],a[36:39],v[160:163]
s_add_u32 s62,s52,81920
ds_read_b64_tr_b16 a[76:77],v5 offset:0
v_mfma_f32_16x16x32_bf16 v[160:163],a[160:163],a[52:55],v[160:163]
ds_read_b64_tr_b16 a[78:79],v5 offset:1024
v_mfma_f32_16x16x32_bf16 v[156:159],a[128:131],a[36:39],v[156:159]
buffer_load_dwordx4 v11,s[32:35],s62 offen lds
v_mfma_f32_16x16x32_bf16 v[156:159],a[156:159],a[52:55],v[156:159]
s_add_u32 m0,s56,6144
ds_read_b64_tr_b16 a[104:105],v5 offset:8192
v_mfma_f32_16x16x32_bf16 v[152:155],a[124:127],a[36:39],v[152:155]
s_add_u32 s61,s52,98304
ds_read_b64_tr_b16 a[106:107],v5 offset:9216
v_mfma_f32_16x16x32_bf16 v[152:155],a[152:155],a[52:55],v[152:155]
ds_read_b128 a[20:23],v1 offset:2048
v_mfma_f32_16x16x32_bf16 v[148:151],a[120:123],a[36:39],v[148:151]
buffer_load_dwordx4 v10,s[32:35],s61 offen lds
v_mfma_f32_16x16x32_bf16 v[148:151],a[148:151],a[52:55],v[148:151]
s_add_u32 m0,s56,7168
ds_read_b64_tr_b16 a[80:81],v6 offset:0
v_mfma_f32_16x16x32_bf16 v[204:207],a[120:123],a[40:43],v[204:207]
s_add_u32 s62,s52,114688
ds_read_b64_tr_b16 a[82:83],v6 offset:1024
v_mfma_f32_16x16x32_bf16 v[204:207],a[148:151],a[56:59],v[204:207]
)"
R"(buffer_load_dwordx4 v11,s[32:35],s62 offen lds
v_mfma_f32_16x16x32_bf16 v[208:211],a[124:127],a[40:43],v[208:211]
ds_read_b64_tr_b16 a[108:109],v6 offset:8192
v_mfma_f32_16x16x32_bf16 v[208:211],a[152:155],a[56:59],v[208:211]
s_cmp_lg_u32 %2,0
s_cbranch_scc1 gm_KB_w33
v_mov_b32 v22,s72
v_mov_b32 v23,0
s_mov_b64 exec,1
buffer_store_dword v22,v23,s[4:7],s13 offen sc1
s_mov_b64 exec,-1
gm_KB_w33:
v_mfma_f32_16x16x32_bf16 v[212:215],a[128:131],a[40:43],v[212:215]
v_not_b32 v56,s72
v_mfma_f32_16x16x32_bf16 v[212:215],a[156:159],a[56:59],v[212:215]
v_not_b32 v57,s72
v_mfma_f32_16x16x32_bf16 v[216:219],a[132:135],a[40:43],v[216:219]
ds_read_b64_tr_b16 a[110:111],v6 offset:9216
v_mfma_f32_16x16x32_bf16 v[216:219],a[160:163],a[56:59],v[216:219]
v_not_b32 v58,s72
v_mfma_f32_16x16x32_bf16 v[220:223],a[136:139],a[40:43],v[220:223]
v_not_b32 v59,s72
v_mfma_f32_16x16x32_bf16 v[220:223],a[164:167],a[56:59],v[220:223]
buffer_store_dwordx4 v[24:27],v18,s[16:19],s55 offen sc1
)"
R"(v_mfma_f32_16x16x32_bf16 v[224:227],a[140:143],a[40:43],v[224:227]
ds_read_b128 a[8:11],v0 offset:4096
v_mfma_f32_16x16x32_bf16 v[224:227],a[168:171],a[56:59],v[224:227]
s_add_u32 s55,s55,1024
ds_read_b64_tr_b16 a[84:85],v7 offset:0
v_mfma_f32_16x16x32_bf16 v[228:231],a[144:147],a[40:43],v[228:231]
s_xor_b32 s56,s56,0x8000
ds_read_b64_tr_b16 a[86:87],v7 offset:1024
v_mfma_f32_16x16x32_bf16 v[228:231],a[172:175],a[56:59],v[228:231]
s_add_u32 s52,s52,128
ds_read_b64_tr_b16 a[112:113],v7 offset:8192
v_mfma_f32_16x16x32_bf16 a[220:223],a[144:147],a[44:47],a[220:223]
s_cmp_eq_u32 s52,2048
ds_read_b64_tr_b16 a[114:115],v7 offset:9216
v_mfma_f32_16x16x32_bf16 a[220:223],a[172:175],a[60:63],a[220:223]
ds_read_b128 a[24:27],v1 offset:4096
v_mfma_f32_16x16x32_bf16 a[216:219],a[140:143],a[44:47],a[216:219]
s_cselect_b32 s52,0,s52
ds_read_b64_tr_b16 a[88:89],v8 offset:0
v_mfma_f32_16x16x32_bf16 a[216:219],a[168:171],a[60:63],a[216:219]
ds_read_b64_tr_b16 a[90:91],v8 offset:1024
v_mfma_f32_16x16x32_bf16 a[212:215],a[136:139],a[44:47],a[212:215]
ds_read_b64_tr_b16 a[116:117],v8 offset:8192
v_mfma_f32_16x16x32_bf16 a[212:215],a[164:167],a[60:63],a[212:215]
)"
R"(ds_read_b64_tr_b16 a[118:119],v8 offset:9216
v_mfma_f32_16x16x32_bf16 a[208:211],a[132:135],a[44:47],a[208:211]
ds_read_b128 a[12:15],v0 offset:6144
v_mfma_f32_16x16x32_bf16 a[208:211],a[160:163],a[60:63],a[208:211]
ds_read_b128 a[28:31],v1 offset:6144
v_mfma_f32_16x16x32_bf16 a[204:207],a[128:131],a[44:47],a[204:207]
v_mfma_f32_16x16x32_bf16 a[204:207],a[156:159],a[60:63],a[204:207]
v_mfma_f32_16x16x32_bf16 a[200:203],a[124:127],a[44:47],a[200:203]
v_mfma_f32_16x16x32_bf16 a[200:203],a[152:155],a[60:63],a[200:203]
v_mfma_f32_16x16x32_bf16 a[196:199],a[120:123],a[44:47],a[196:199]
s_waitcnt lgkmcnt(0)
v_mfma_f32_16x16x32_bf16 a[196:199],a[148:151],a[60:63],a[196:199]
s_mov_b32 s60,5
s_branch gm_KB_sk21
.p2align 12
gm_KB_pg21:
s_setpc_b64 s[100:101]
s_nop 0
gm_KB_sk21:
gm_K1_GU_loop:
v_mfma_f32_16x16x32_bf16 v[64:67],a[64:67],a[0:3],v[64:67]
s_add_u32 m0,s57,0
ds_read_b64_tr_b16 a[120:121],v2 offset:16384
v_mfma_f32_16x16x32_bf16 v[64:67],a[92:95],a[16:19],v[64:67]
)"
R"(s_add_u32 s63,s54,0
ds_read_b64_tr_b16 a[122:123],v2 offset:17408
v_mfma_f32_16x16x32_bf16 v[68:71],a[68:71],a[0:3],v[68:71]
buffer_load_dwordx4 v14,s[40:43],s63 offen lds
v_mfma_f32_16x16x32_bf16 v[68:71],a[96:99],a[16:19],v[68:71]
ds_read_b64_tr_b16 a[148:149],v2 offset:24576
v_mfma_f32_16x16x32_bf16 v[72:75],a[72:75],a[0:3],v[72:75]
s_add_u32 s66,s59,0x8000
ds_read_b64_tr_b16 a[150:151],v2 offset:25600
v_mfma_f32_16x16x32_bf16 v[72:75],a[100:103],a[16:19],v[72:75]
s_cmp_eq_u32 s66,0x18000
ds_read_b64_tr_b16 a[124:125],v3 offset:16384
v_mfma_f32_16x16x32_bf16 v[76:79],a[76:79],a[0:3],v[76:79]
s_cselect_b32 s66,0,s66
ds_read_b64_tr_b16 a[126:127],v3 offset:17408
v_mfma_f32_16x16x32_bf16 v[76:79],a[104:107],a[16:19],v[76:79]
ds_read_b64_tr_b16 a[152:153],v3 offset:24576
v_mfma_f32_16x16x32_bf16 v[80:83],a[80:83],a[0:3],v[80:83]
s_sub_u32 s58,s66,s59
ds_read_b64_tr_b16 a[154:155],v3 offset:25600
v_mfma_f32_16x16x32_bf16 v[80:83],a[108:111],a[16:19],v[80:83]
s_mov_b32 s59,s66
ds_read_b64_tr_b16 a[128:129],v4 offset:16384
v_mfma_f32_16x16x32_bf16 v[84:87],a[84:87],a[0:3],v[84:87]
)"
R"(ds_read_b64_tr_b16 a[130:131],v4 offset:17408
v_mfma_f32_16x16x32_bf16 v[84:87],a[112:115],a[16:19],v[84:87]
s_add_u32 m0,s57,1024
v_mfma_f32_16x16x32_bf16 v[88:91],a[88:91],a[0:3],v[88:91]
s_add_u32 s64,s54,28672
ds_read_b64_tr_b16 a[156:157],v4 offset:24576
v_mfma_f32_16x16x32_bf16 v[88:91],a[116:119],a[16:19],v[88:91]
buffer_load_dwordx4 v14,s[40:43],s64 offen lds
v_mfma_f32_16x16x32_bf16 v[144:147],a[88:91],a[4:7],v[144:147]
ds_read_b64_tr_b16 a[158:159],v4 offset:25600
v_mfma_f32_16x16x32_bf16 v[144:147],a[116:119],a[20:23],v[144:147]
s_add_u32 m0,s57,2048
ds_read_b64_tr_b16 a[132:133],v5 offset:16384
v_mfma_f32_16x16x32_bf16 v[140:143],a[84:87],a[4:7],v[140:143]
s_add_u32 s63,s54,57344
ds_read_b64_tr_b16 a[134:135],v5 offset:17408
v_mfma_f32_16x16x32_bf16 v[140:143],a[112:115],a[20:23],v[140:143]
ds_read_b64_tr_b16 a[160:161],v5 offset:24576
v_mfma_f32_16x16x32_bf16 v[136:139],a[80:83],a[4:7],v[136:139]
buffer_load_dwordx4 v15,s[40:43],s63 offen lds
v_mfma_f32_16x16x32_bf16 v[136:139],a[108:111],a[20:23],v[136:139]
s_add_u32 m0,s57,3072
ds_read_b64_tr_b16 a[162:163],v5 offset:25600
v_mfma_f32_16x16x32_bf16 v[132:135],a[76:79],a[4:7],v[132:135]
)"
R"(s_add_u32 s64,s54,86016
ds_read_b64_tr_b16 a[136:137],v6 offset:16384
v_mfma_f32_16x16x32_bf16 v[132:135],a[104:107],a[20:23],v[132:135]
ds_read_b64_tr_b16 a[138:139],v6 offset:17408
v_mfma_f32_16x16x32_bf16 v[128:131],a[72:75],a[4:7],v[128:131]
buffer_load_dwordx4 v15,s[40:43],s64 offen lds
v_mfma_f32_16x16x32_bf16 v[128:131],a[100:103],a[20:23],v[128:131]
s_add_u32 m0,s57,4096
ds_read_b64_tr_b16 a[164:165],v6 offset:24576
v_mfma_f32_16x16x32_bf16 v[124:127],a[68:71],a[4:7],v[124:127]
ds_read_b64_tr_b16 a[166:167],v6 offset:25600
v_mfma_f32_16x16x32_bf16 v[124:127],a[96:99],a[20:23],v[124:127]
s_add_u32 s63,s54,114688
ds_read_b64_tr_b16 a[140:141],v7 offset:16384
v_mfma_f32_16x16x32_bf16 v[120:123],a[64:67],a[4:7],v[120:123]
buffer_load_dwordx4 v14,s[40:43],s63 offen lds
v_mfma_f32_16x16x32_bf16 v[120:123],a[92:95],a[20:23],v[120:123]
s_add_u32 m0,s57,5120
ds_read_b64_tr_b16 a[142:143],v7 offset:17408
v_mfma_f32_16x16x32_bf16 v[176:179],a[64:67],a[8:11],v[176:179]
ds_read_b64_tr_b16 a[168:169],v7 offset:24576
v_mfma_f32_16x16x32_bf16 v[176:179],a[92:95],a[24:27],v[176:179]
s_add_u32 s64,s54,143360
ds_read_b64_tr_b16 a[170:171],v7 offset:25600
)"
R"(v_mfma_f32_16x16x32_bf16 v[180:183],a[68:71],a[8:11],v[180:183]
buffer_load_dwordx4 v14,s[40:43],s64 offen lds
v_mfma_f32_16x16x32_bf16 v[180:183],a[96:99],a[24:27],v[180:183]
s_add_u32 m0,s57,6144
ds_read_b64_tr_b16 a[144:145],v8 offset:16384
v_mfma_f32_16x16x32_bf16 v[184:187],a[72:75],a[8:11],v[184:187]
ds_read_b64_tr_b16 a[146:147],v8 offset:17408
v_mfma_f32_16x16x32_bf16 v[184:187],a[100:103],a[24:27],v[184:187]
s_add_u32 s63,s54,172032
ds_read_b64_tr_b16 a[172:173],v8 offset:24576
v_mfma_f32_16x16x32_bf16 v[188:191],a[76:79],a[8:11],v[188:191]
buffer_load_dwordx4 v15,s[40:43],s63 offen lds
v_mfma_f32_16x16x32_bf16 v[188:191],a[104:107],a[24:27],v[188:191]
ds_read_b64_tr_b16 a[174:175],v8 offset:25600
v_mfma_f32_16x16x32_bf16 v[192:195],a[80:83],a[8:11],v[192:195]
s_add_u32 m0,s57,7168
v_xor_b32 v0,0x8000,v0
v_mfma_f32_16x16x32_bf16 v[192:195],a[108:111],a[24:27],v[192:195]
s_add_u32 s64,s54,200704
v_xor_b32 v1,0x8000,v1
v_mfma_f32_16x16x32_bf16 v[196:199],a[84:87],a[8:11],v[196:199]
buffer_load_dwordx4 v15,s[40:43],s64 offen lds
v_mfma_f32_16x16x32_bf16 v[196:199],a[112:115],a[24:27],v[196:199]
v_add_u32 v2,s58,v2
)"
R"(v_mfma_f32_16x16x32_bf16 v[200:203],a[88:91],a[8:11],v[200:203]
s_add_u32 s57,s57,0x8000
v_add_u32 v3,s58,v3
v_mfma_f32_16x16x32_bf16 v[200:203],a[116:119],a[24:27],v[200:203]
s_cmp_ge_u32 s57,0x28000
v_add_u32 v4,s58,v4
v_mfma_f32_16x16x32_bf16 a[192:195],a[88:91],a[12:15],a[192:195]
v_add_u32 v5,s58,v5
v_mfma_f32_16x16x32_bf16 a[192:195],a[116:119],a[28:31],a[192:195]
s_cselect_b32 s65,0x18000,0
v_add_u32 v6,s58,v6
v_mfma_f32_16x16x32_bf16 v[252:255],a[84:87],a[12:15],v[252:255]
s_sub_u32 s57,s57,s65
v_add_u32 v7,s58,v7
v_mfma_f32_16x16x32_bf16 v[252:255],a[112:115],a[28:31],v[252:255]
s_add_u32 s54,s54,458752
v_add_u32 v8,s58,v8
v_mfma_f32_16x16x32_bf16 v[248:251],a[80:83],a[12:15],v[248:251]
v_add_u32 v9,s58,v9
v_mfma_f32_16x16x32_bf16 v[248:251],a[108:111],a[28:31],v[248:251]
s_cmp_eq_u32 s54,7340032
v_mfma_f32_16x16x32_bf16 v[244:247],a[76:79],a[12:15],v[244:247]
s_cselect_b32 s54,0,s54
v_mfma_f32_16x16x32_bf16 v[244:247],a[104:107],a[28:31],v[244:247]
)"
R"(v_mfma_f32_16x16x32_bf16 v[240:243],a[72:75],a[12:15],v[240:243]
v_mfma_f32_16x16x32_bf16 v[240:243],a[100:103],a[28:31],v[240:243]
v_mfma_f32_16x16x32_bf16 v[236:239],a[68:71],a[12:15],v[236:239]
v_mfma_f32_16x16x32_bf16 v[236:239],a[96:99],a[28:31],v[236:239]
v_mfma_f32_16x16x32_bf16 v[232:235],a[64:67],a[12:15],v[232:235]
s_waitcnt vmcnt(9) lgkmcnt(0)
v_mfma_f32_16x16x32_bf16 v[232:235],a[92:95],a[28:31],v[232:235]
s_barrier
v_mfma_f32_16x16x32_bf16 v[92:95],a[120:123],a[0:3],v[92:95]
s_add_u32 m0,s56,0
ds_read_b64_tr_b16 a[64:65],v2 offset:0
v_mfma_f32_16x16x32_bf16 v[92:95],a[148:151],a[16:19],v[92:95]
s_add_u32 s61,s52,0
ds_read_b64_tr_b16 a[66:67],v2 offset:1024
v_mfma_f32_16x16x32_bf16 v[96:99],a[124:127],a[0:3],v[96:99]
buffer_load_dwordx4 v10,s[32:35],s61 offen lds
v_mfma_f32_16x16x32_bf16 v[96:99],a[152:155],a[16:19],v[96:99]
s_add_u32 m0,s56,1024
ds_read_b64_tr_b16 a[92:93],v2 offset:8192
v_mfma_f32_16x16x32_bf16 v[100:103],a[128:131],a[0:3],v[100:103]
ds_read_b64_tr_b16 a[94:95],v2 offset:9216
v_mfma_f32_16x16x32_bf16 v[100:103],a[156:159],a[16:19],v[100:103]
s_add_u32 s62,s52,16384
ds_read_b128 a[32:35],v0 offset:0
)"
R"(v_mfma_f32_16x16x32_bf16 v[104:107],a[132:135],a[0:3],v[104:107]
buffer_load_dwordx4 v11,s[32:35],s62 offen lds
v_mfma_f32_16x16x32_bf16 v[104:107],a[160:163],a[16:19],v[104:107]
s_add_u32 m0,s56,2048
ds_read_b64_tr_b16 a[68:69],v3 offset:0
v_mfma_f32_16x16x32_bf16 v[108:111],a[136:139],a[0:3],v[108:111]
ds_read_b64_tr_b16 a[70:71],v3 offset:1024
v_mfma_f32_16x16x32_bf16 v[108:111],a[164:167],a[16:19],v[108:111]
s_add_u32 s61,s52,32768
ds_read_b64_tr_b16 a[96:97],v3 offset:8192
v_mfma_f32_16x16x32_bf16 v[112:115],a[140:143],a[0:3],v[112:115]
buffer_load_dwordx4 v10,s[32:35],s61 offen lds
v_mfma_f32_16x16x32_bf16 v[112:115],a[168:171],a[16:19],v[112:115]
s_add_u32 m0,s56,3072
ds_read_b64_tr_b16 a[98:99],v3 offset:9216
v_mfma_f32_16x16x32_bf16 v[116:119],a[144:147],a[0:3],v[116:119]
ds_read_b128 a[48:51],v1 offset:0
v_mfma_f32_16x16x32_bf16 v[116:119],a[172:175],a[16:19],v[116:119]
s_add_u32 s62,s52,49152
ds_read_b64_tr_b16 a[72:73],v4 offset:0
v_mfma_f32_16x16x32_bf16 v[172:175],a[144:147],a[4:7],v[172:175]
buffer_load_dwordx4 v11,s[32:35],s62 offen lds
v_mfma_f32_16x16x32_bf16 v[172:175],a[172:175],a[20:23],v[172:175]
s_add_u32 m0,s56,4096
)"
R"(ds_read_b64_tr_b16 a[74:75],v4 offset:1024
v_mfma_f32_16x16x32_bf16 v[168:171],a[140:143],a[4:7],v[168:171]
s_add_u32 s61,s52,65536
ds_read_b64_tr_b16 a[100:101],v4 offset:8192
v_mfma_f32_16x16x32_bf16 v[168:171],a[168:171],a[20:23],v[168:171]
ds_read_b64_tr_b16 a[102:103],v4 offset:9216
v_mfma_f32_16x16x32_bf16 v[164:167],a[136:139],a[4:7],v[164:167]
buffer_load_dwordx4 v10,s[32:35],s61 offen lds
v_mfma_f32_16x16x32_bf16 v[164:167],a[164:167],a[20:23],v[164:167]
s_add_u32 m0,s56,5120
ds_read_b128 a[36:39],v0 offset:2048
v_mfma_f32_16x16x32_bf16 v[160:163],a[132:135],a[4:7],v[160:163]
s_add_u32 s62,s52,81920
ds_read_b64_tr_b16 a[76:77],v5 offset:0
v_mfma_f32_16x16x32_bf16 v[160:163],a[160:163],a[20:23],v[160:163]
ds_read_b64_tr_b16 a[78:79],v5 offset:1024
v_mfma_f32_16x16x32_bf16 v[156:159],a[128:131],a[4:7],v[156:159]
buffer_load_dwordx4 v11,s[32:35],s62 offen lds
v_mfma_f32_16x16x32_bf16 v[156:159],a[156:159],a[20:23],v[156:159]
s_add_u32 m0,s56,6144
ds_read_b64_tr_b16 a[104:105],v5 offset:8192
v_mfma_f32_16x16x32_bf16 v[152:155],a[124:127],a[4:7],v[152:155]
s_add_u32 s61,s52,98304
ds_read_b64_tr_b16 a[106:107],v5 offset:9216
)"
R"(v_mfma_f32_16x16x32_bf16 v[152:155],a[152:155],a[20:23],v[152:155]
ds_read_b128 a[52:55],v1 offset:2048
v_mfma_f32_16x16x32_bf16 v[148:151],a[120:123],a[4:7],v[148:151]
buffer_load_dwordx4 v10,s[32:35],s61 offen lds
v_mfma_f32_16x16x32_bf16 v[148:151],a[148:151],a[20:23],v[148:151]
s_add_u32 m0,s56,7168
ds_read_b64_tr_b16 a[80:81],v6 offset:0
v_mfma_f32_16x16x32_bf16 v[204:207],a[120:123],a[8:11],v[204:207]
s_add_u32 s62,s52,114688
ds_read_b64_tr_b16 a[82:83],v6 offset:1024
v_mfma_f32_16x16x32_bf16 v[204:207],a[148:151],a[24:27],v[204:207]
buffer_load_dwordx4 v11,s[32:35],s62 offen lds
v_mfma_f32_16x16x32_bf16 v[208:211],a[124:127],a[8:11],v[208:211]
ds_read_b64_tr_b16 a[108:109],v6 offset:8192
v_mfma_f32_16x16x32_bf16 v[208:211],a[152:155],a[24:27],v[208:211]
buffer_store_dwordx4 v[24:27],v18,s[16:19],s55 offen sc1
v_mfma_f32_16x16x32_bf16 v[212:215],a[128:131],a[8:11],v[212:215]
s_add_u32 s55,s55,1024
ds_read_b64_tr_b16 a[110:111],v6 offset:9216
v_mfma_f32_16x16x32_bf16 v[212:215],a[156:159],a[24:27],v[212:215]
s_xor_b32 s56,s56,0x8000
ds_read_b128 a[40:43],v0 offset:4096
v_mfma_f32_16x16x32_bf16 v[216:219],a[132:135],a[8:11],v[216:219]
ds_read_b64_tr_b16 a[84:85],v7 offset:0
)"
R"(v_mfma_f32_16x16x32_bf16 v[216:219],a[160:163],a[24:27],v[216:219]
s_add_u32 s52,s52,128
ds_read_b64_tr_b16 a[86:87],v7 offset:1024
v_mfma_f32_16x16x32_bf16 v[220:223],a[136:139],a[8:11],v[220:223]
s_cmp_eq_u32 s52,2048
ds_read_b64_tr_b16 a[112:113],v7 offset:8192
v_mfma_f32_16x16x32_bf16 v[220:223],a[164:167],a[24:27],v[220:223]
s_cselect_b32 s52,0,s52
ds_read_b64_tr_b16 a[114:115],v7 offset:9216
v_mfma_f32_16x16x32_bf16 v[224:227],a[140:143],a[8:11],v[224:227]
ds_read_b128 a[56:59],v1 offset:4096
v_mfma_f32_16x16x32_bf16 v[224:227],a[168:171],a[24:27],v[224:227]
ds_read_b64_tr_b16 a[88:89],v8 offset:0
v_mfma_f32_16x16x32_bf16 v[228:231],a[144:147],a[8:11],v[228:231]
ds_read_b64_tr_b16 a[90:91],v8 offset:1024
v_mfma_f32_16x16x32_bf16 v[228:231],a[172:175],a[24:27],v[228:231]
ds_read_b64_tr_b16 a[116:117],v8 offset:8192
v_mfma_f32_16x16x32_bf16 a[220:223],a[144:147],a[12:15],a[220:223]
ds_read_b64_tr_b16 a[118:119],v8 offset:9216
v_mfma_f32_16x16x32_bf16 a[220:223],a[172:175],a[28:31],a[220:223]
ds_read_b128 a[44:47],v0 offset:6144
v_mfma_f32_16x16x32_bf16 a[216:219],a[140:143],a[12:15],a[216:219]
ds_read_b128 a[60:63],v1 offset:6144
v_mfma_f32_16x16x32_bf16 a[216:219],a[168:171],a[28:31],a[216:219]
)"
R"(v_mfma_f32_16x16x32_bf16 a[212:215],a[136:139],a[12:15],a[212:215]
v_mfma_f32_16x16x32_bf16 a[212:215],a[164:167],a[28:31],a[212:215]
v_mfma_f32_16x16x32_bf16 a[208:211],a[132:135],a[12:15],a[208:211]
v_mfma_f32_16x16x32_bf16 a[208:211],a[160:163],a[28:31],a[208:211]
v_mfma_f32_16x16x32_bf16 a[204:207],a[128:131],a[12:15],a[204:207]
v_mfma_f32_16x16x32_bf16 a[204:207],a[156:159],a[28:31],a[204:207]
v_mfma_f32_16x16x32_bf16 a[200:203],a[124:127],a[12:15],a[200:203]
v_mfma_f32_16x16x32_bf16 a[200:203],a[152:155],a[28:31],a[200:203]
v_mfma_f32_16x16x32_bf16 a[196:199],a[120:123],a[12:15],a[196:199]
s_waitcnt lgkmcnt(0)
v_mfma_f32_16x16x32_bf16 a[196:199],a[148:151],a[28:31],a[196:199]
v_mfma_f32_16x16x32_bf16 v[64:67],a[64:67],a[32:35],v[64:67]
s_add_u32 m0,s57,0
ds_read_b64_tr_b16 a[120:121],v2 offset:16384
v_mfma_f32_16x16x32_bf16 v[64:67],a[92:95],a[48:51],v[64:67]
s_add_u32 s63,s54,0
ds_read_b64_tr_b16 a[122:123],v2 offset:17408
v_mfma_f32_16x16x32_bf16 v[68:71],a[68:71],a[32:35],v[68:71]
buffer_load_dwordx4 v14,s[40:43],s63 offen lds
v_mfma_f32_16x16x32_bf16 v[68:71],a[96:99],a[48:51],v[68:71]
ds_read_b64_tr_b16 a[148:149],v2 offset:24576
v_mfma_f32_16x16x32_bf16 v[72:75],a[72:75],a[32:35],v[72:75]
s_add_u32 s66,s59,0x8000
ds_read_b64_tr_b16 a[150:151],v2 offset:25600
)"
R"(v_mfma_f32_16x16x32_bf16 v[72:75],a[100:103],a[48:51],v[72:75]
s_cmp_eq_u32 s66,0x18000
ds_read_b64_tr_b16 a[124:125],v3 offset:16384
v_mfma_f32_16x16x32_bf16 v[76:79],a[76:79],a[32:35],v[76:79]
s_cselect_b32 s66,0,s66
ds_read_b64_tr_b16 a[126:127],v3 offset:17408
v_mfma_f32_16x16x32_bf16 v[76:79],a[104:107],a[48:51],v[76:79]
ds_read_b64_tr_b16 a[152:153],v3 offset:24576
v_mfma_f32_16x16x32_bf16 v[80:83],a[80:83],a[32:35],v[80:83]
s_sub_u32 s58,s66,s59
ds_read_b64_tr_b16 a[154:155],v3 offset:25600
v_mfma_f32_16x16x32_bf16 v[80:83],a[108:111],a[48:51],v[80:83]
s_mov_b32 s59,s66
ds_read_b64_tr_b16 a[128:129],v4 offset:16384
v_mfma_f32_16x16x32_bf16 v[84:87],a[84:87],a[32:35],v[84:87]
ds_read_b64_tr_b16 a[130:131],v4 offset:17408
v_mfma_f32_16x16x32_bf16 v[84:87],a[112:115],a[48:51],v[84:87]
s_add_u32 m0,s57,1024
v_mfma_f32_16x16x32_bf16 v[88:91],a[88:91],a[32:35],v[88:91]
s_add_u32 s64,s54,28672
ds_read_b64_tr_b16 a[156:157],v4 offset:24576
v_mfma_f32_16x16x32_bf16 v[88:91],a[116:119],a[48:51],v[88:91]
buffer_load_dwordx4 v14,s[40:43],s64 offen lds
v_mfma_f32_16x16x32_bf16 v[144:147],a[88:91],a[36:39],v[144:147]
)"
R"(ds_read_b64_tr_b16 a[158:159],v4 offset:25600
v_mfma_f32_16x16x32_bf16 v[144:147],a[116:119],a[52:55],v[144:147]
s_add_u32 m0,s57,2048
ds_read_b64_tr_b16 a[132:133],v5 offset:16384
v_mfma_f32_16x16x32_bf16 v[140:143],a[84:87],a[36:39],v[140:143]
s_add_u32 s63,s54,57344
ds_read_b64_tr_b16 a[134:135],v5 offset:17408
v_mfma_f32_16x16x32_bf16 v[140:143],a[112:115],a[52:55],v[140:143]
ds_read_b64_tr_b16 a[160:161],v5 offset:24576
v_mfma_f32_16x16x32_bf16 v[136:139],a[80:83],a[36:39],v[136:139]
buffer_load_dwordx4 v15,s[40:43],s63 offen lds
v_mfma_f32_16x16x32_bf16 v[136:139],a[108:111],a[52:55],v[136:139]
s_add_u32 m0,s57,3072
ds_read_b64_tr_b16 a[162:163],v5 offset:25600
v_mfma_f32_16x16x32_bf16 v[132:135],a[76:79],a[36:39],v[132:135]
s_add_u32 s64,s54,86016
ds_read_b64_tr_b16 a[136:137],v6 offset:16384
v_mfma_f32_16x16x32_bf16 v[132:135],a[104:107],a[52:55],v[132:135]
ds_read_b64_tr_b16 a[138:139],v6 offset:17408
v_mfma_f32_16x16x32_bf16 v[128:131],a[72:75],a[36:39],v[128:131]
buffer_load_dwordx4 v15,s[40:43],s64 offen lds
v_mfma_f32_16x16x32_bf16 v[128:131],a[100:103],a[52:55],v[128:131]
s_add_u32 m0,s57,4096
ds_read_b64_tr_b16 a[164:165],v6 offset:24576
)"
R"(v_mfma_f32_16x16x32_bf16 v[124:127],a[68:71],a[36:39],v[124:127]
ds_read_b64_tr_b16 a[166:167],v6 offset:25600
v_mfma_f32_16x16x32_bf16 v[124:127],a[96:99],a[52:55],v[124:127]
s_add_u32 s63,s54,114688
ds_read_b64_tr_b16 a[140:141],v7 offset:16384
v_mfma_f32_16x16x32_bf16 v[120:123],a[64:67],a[36:39],v[120:123]
buffer_load_dwordx4 v14,s[40:43],s63 offen lds
v_mfma_f32_16x16x32_bf16 v[120:123],a[92:95],a[52:55],v[120:123]
s_add_u32 m0,s57,5120
ds_read_b64_tr_b16 a[142:143],v7 offset:17408
v_mfma_f32_16x16x32_bf16 v[176:179],a[64:67],a[40:43],v[176:179]
ds_read_b64_tr_b16 a[168:169],v7 offset:24576
v_mfma_f32_16x16x32_bf16 v[176:179],a[92:95],a[56:59],v[176:179]
s_add_u32 s64,s54,143360
ds_read_b64_tr_b16 a[170:171],v7 offset:25600
v_mfma_f32_16x16x32_bf16 v[180:183],a[68:71],a[40:43],v[180:183]
buffer_load_dwordx4 v14,s[40:43],s64 offen lds
v_mfma_f32_16x16x32_bf16 v[180:183],a[96:99],a[56:59],v[180:183]
s_add_u32 m0,s57,6144
ds_read_b64_tr_b16 a[144:145],v8 offset:16384
v_mfma_f32_16x16x32_bf16 v[184:187],a[72:75],a[40:43],v[184:187]
ds_read_b64_tr_b16 a[146:147],v8 offset:17408
v_mfma_f32_16x16x32_bf16 v[184:187],a[100:103],a[56:59],v[184:187]
s_add_u32 s63,s54,172032
)"
R"(ds_read_b64_tr_b16 a[172:173],v8 offset:24576
v_mfma_f32_16x16x32_bf16 v[188:191],a[76:79],a[40:43],v[188:191]
buffer_load_dwordx4 v15,s[40:43],s63 offen lds
v_mfma_f32_16x16x32_bf16 v[188:191],a[104:107],a[56:59],v[188:191]
ds_read_b64_tr_b16 a[174:175],v8 offset:25600
v_mfma_f32_16x16x32_bf16 v[192:195],a[80:83],a[40:43],v[192:195]
s_add_u32 m0,s57,7168
v_xor_b32 v0,0x8000,v0
v_mfma_f32_16x16x32_bf16 v[192:195],a[108:111],a[56:59],v[192:195]
s_add_u32 s64,s54,200704
v_xor_b32 v1,0x8000,v1
v_mfma_f32_16x16x32_bf16 v[196:199],a[84:87],a[40:43],v[196:199]
buffer_load_dwordx4 v15,s[40:43],s64 offen lds
v_mfma_f32_16x16x32_bf16 v[196:199],a[112:115],a[56:59],v[196:199]
v_add_u32 v2,s58,v2
v_mfma_f32_16x16x32_bf16 v[200:203],a[88:91],a[40:43],v[200:203]
s_add_u32 s57,s57,0x8000
v_add_u32 v3,s58,v3
v_mfma_f32_16x16x32_bf16 v[200:203],a[116:119],a[56:59],v[200:203]
s_cmp_ge_u32 s57,0x28000
v_add_u32 v4,s58,v4
v_mfma_f32_16x16x32_bf16 a[192:195],a[88:91],a[44:47],a[192:195]
v_add_u32 v5,s58,v5
v_mfma_f32_16x16x32_bf16 a[192:195],a[116:119],a[60:63],a[192:195]
)"
R"(s_cselect_b32 s65,0x18000,0
v_add_u32 v6,s58,v6
v_mfma_f32_16x16x32_bf16 v[252:255],a[84:87],a[44:47],v[252:255]
s_sub_u32 s57,s57,s65
v_add_u32 v7,s58,v7
v_mfma_f32_16x16x32_bf16 v[252:255],a[112:115],a[60:63],v[252:255]
s_add_u32 s54,s54,458752
v_add_u32 v8,s58,v8
v_mfma_f32_16x16x32_bf16 v[248:251],a[80:83],a[44:47],v[248:251]
v_add_u32 v9,s58,v9
v_mfma_f32_16x16x32_bf16 v[248:251],a[108:111],a[60:63],v[248:251]
s_cmp_eq_u32 s54,7340032
v_mfma_f32_16x16x32_bf16 v[244:247],a[76:79],a[44:47],v[244:247]
s_cselect_b32 s54,0,s54
v_mfma_f32_16x16x32_bf16 v[244:247],a[104:107],a[60:63],v[244:247]
v_mfma_f32_16x16x32_bf16 v[240:243],a[72:75],a[44:47],v[240:243]
v_mfma_f32_16x16x32_bf16 v[240:243],a[100:103],a[60:63],v[240:243]
v_mfma_f32_16x16x32_bf16 v[236:239],a[68:71],a[44:47],v[236:239]
v_mfma_f32_16x16x32_bf16 v[236:239],a[96:99],a[60:63],v[236:239]
v_mfma_f32_16x16x32_bf16 v[232:235],a[64:67],a[44:47],v[232:235]
s_waitcnt vmcnt(9) lgkmcnt(0)
v_mfma_f32_16x16x32_bf16 v[232:235],a[92:95],a[60:63],v[232:235]
s_barrier
v_mfma_f32_16x16x32_bf16 v[92:95],a[120:123],a[32:35],v[92:95]
)"
R"(s_add_u32 m0,s56,0
ds_read_b64_tr_b16 a[64:65],v2 offset:0
v_mfma_f32_16x16x32_bf16 v[92:95],a[148:151],a[48:51],v[92:95]
s_add_u32 s61,s52,0
ds_read_b64_tr_b16 a[66:67],v2 offset:1024
v_mfma_f32_16x16x32_bf16 v[96:99],a[124:127],a[32:35],v[96:99]
buffer_load_dwordx4 v10,s[32:35],s61 offen lds
v_mfma_f32_16x16x32_bf16 v[96:99],a[152:155],a[48:51],v[96:99]
s_add_u32 m0,s56,1024
ds_read_b64_tr_b16 a[92:93],v2 offset:8192
v_mfma_f32_16x16x32_bf16 v[100:103],a[128:131],a[32:35],v[100:103]
ds_read_b64_tr_b16 a[94:95],v2 offset:9216
v_mfma_f32_16x16x32_bf16 v[100:103],a[156:159],a[48:51],v[100:103]
s_add_u32 s62,s52,16384
ds_read_b128 a[0:3],v0 offset:0
v_mfma_f32_16x16x32_bf16 v[104:107],a[132:135],a[32:35],v[104:107]
buffer_load_dwordx4 v11,s[32:35],s62 offen lds
v_mfma_f32_16x16x32_bf16 v[104:107],a[160:163],a[48:51],v[104:107]
s_add_u32 m0,s56,2048
ds_read_b64_tr_b16 a[68:69],v3 offset:0
v_mfma_f32_16x16x32_bf16 v[108:111],a[136:139],a[32:35],v[108:111]
ds_read_b64_tr_b16 a[70:71],v3 offset:1024
v_mfma_f32_16x16x32_bf16 v[108:111],a[164:167],a[48:51],v[108:111]
s_add_u32 s61,s52,32768
)"
R"(ds_read_b64_tr_b16 a[96:97],v3 offset:8192
v_mfma_f32_16x16x32_bf16 v[112:115],a[140:143],a[32:35],v[112:115]
buffer_load_dwordx4 v10,s[32:35],s61 offen lds
v_mfma_f32_16x16x32_bf16 v[112:115],a[168:171],a[48:51],v[112:115]
s_add_u32 m0,s56,3072
ds_read_b64_tr_b16 a[98:99],v3 offset:9216
v_mfma_f32_16x16x32_bf16 v[116:119],a[144:147],a[32:35],v[116:119]
ds_read_b128 a[16:19],v1 offset:0
v_mfma_f32_16x16x32_bf16 v[116:119],a[172:175],a[48:51],v[116:119]
s_add_u32 s62,s52,49152
ds_read_b64_tr_b16 a[72:73],v4 offset:0
v_mfma_f32_16x16x32_bf16 v[172:175],a[144:147],a[36:39],v[172:175]
buffer_load_dwordx4 v11,s[32:35],s62 offen lds
v_mfma_f32_16x16x32_bf16 v[172:175],a[172:175],a[52:55],v[172:175]
s_add_u32 m0,s56,4096
ds_read_b64_tr_b16 a[74:75],v4 offset:1024
v_mfma_f32_16x16x32_bf16 v[168:171],a[140:143],a[36:39],v[168:171]
s_add_u32 s61,s52,65536
ds_read_b64_tr_b16 a[100:101],v4 offset:8192
v_mfma_f32_16x16x32_bf16 v[168:171],a[168:171],a[52:55],v[168:171]
ds_read_b64_tr_b16 a[102:103],v4 offset:9216
v_mfma_f32_16x16x32_bf16 v[164:167],a[136:139],a[36:39],v[164:167]
buffer_load_dwordx4 v10,s[32:35],s61 offen lds
v_mfma_f32_16x16x32_bf16 v[164:167],a[164:167],a[52:55],v[164:167]
)"
R"(s_add_u32 m0,s56,5120
ds_read_b128 a[4:7],v0 offset:2048
v_mfma_f32_16x16x32_bf16 v[160:163],a[132:135],a[36:39],v[160:163]
s_add_u32 s62,s52,81920
ds_read_b64_tr_b16 a[76:77],v5 offset:0
v_mfma_f32_16x16x32_bf16 v[160:163],a[160:163],a[52:55],v[160:163]
ds_read_b64_tr_b16 a[78:79],v5 offset:1024
v_mfma_f32_16x16x32_bf16 v[156:159],a[128:131],a[36:39],v[156:159]
buffer_load_dwordx4 v11,s[32:35],s62 offen lds
v_mfma_f32_16x16x32_bf16 v[156:159],a[156:159],a[52:55],v[156:159]
s_add_u32 m0,s56,6144
ds_read_b64_tr_b16 a[104:105],v5 offset:8192
v_mfma_f32_16x16x32_bf16 v[152:155],a[124:127],a[36:39],v[152:155]
s_add_u32 s61,s52,98304
ds_read_b64_tr_b16 a[106:107],v5 offset:9216
v_mfma_f32_16x16x32_bf16 v[152:155],a[152:155],a[52:55],v[152:155]
ds_read_b128 a[20:23],v1 offset:2048
v_mfma_f32_16x16x32_bf16 v[148:151],a[120:123],a[36:39],v[148:151]
buffer_load_dwordx4 v10,s[32:35],s61 offen lds
v_mfma_f32_16x16x32_bf16 v[148:151],a[148:151],a[52:55],v[148:151]
s_add_u32 m0,s56,7168
ds_read_b64_tr_b16 a[80:81],v6 offset:0
v_mfma_f32_16x16x32_bf16 v[204:207],a[120:123],a[40:43],v[204:207]
s_add_u32 s62,s52,114688
)"
R"(ds_read_b64_tr_b16 a[82:83],v6 offset:1024
v_mfma_f32_16x16x32_bf16 v[204:207],a[148:151],a[56:59],v[204:207]
buffer_load_dwordx4 v11,s[32:35],s62 offen lds
v_mfma_f32_16x16x32_bf16 v[208:211],a[124:127],a[40:43],v[208:211]
ds_read_b64_tr_b16 a[108:109],v6 offset:8192
v_mfma_f32_16x16x32_bf16 v[208:211],a[152:155],a[56:59],v[208:211]
buffer_store_dwordx4 v[24:27],v18,s[16:19],s55 offen sc1
v_mfma_f32_16x16x32_bf16 v[212:215],a[128:131],a[40:43],v[212:215]
s_add_u32 s55,s55,1024
ds_read_b64_tr_b16 a[110:111],v6 offset:9216
v_mfma_f32_16x16x32_bf16 v[212:215],a[156:159],a[56:59],v[212:215]
s_xor_b32 s56,s56,0x8000
ds_read_b128 a[8:11],v0 offset:4096
v_mfma_f32_16x16x32_bf16 v[216:219],a[132:135],a[40:43],v[216:219]
ds_read_b64_tr_b16 a[84:85],v7 offset:0
v_mfma_f32_16x16x32_bf16 v[216:219],a[160:163],a[56:59],v[216:219]
s_add_u32 s52,s52,128
ds_read_b64_tr_b16 a[86:87],v7 offset:1024
v_mfma_f32_16x16x32_bf16 v[220:223],a[136:139],a[40:43],v[220:223]
s_cmp_eq_u32 s52,2048
ds_read_b64_tr_b16 a[112:113],v7 offset:8192
v_mfma_f32_16x16x32_bf16 v[220:223],a[164:167],a[56:59],v[220:223]
s_cselect_b32 s52,0,s52
ds_read_b64_tr_b16 a[114:115],v7 offset:9216
)"
R"(v_mfma_f32_16x16x32_bf16 v[224:227],a[140:143],a[40:43],v[224:227]
ds_read_b128 a[24:27],v1 offset:4096
v_mfma_f32_16x16x32_bf16 v[224:227],a[168:171],a[56:59],v[224:227]
s_sub_u32 s60,s60,1
ds_read_b64_tr_b16 a[88:89],v8 offset:0
v_mfma_f32_16x16x32_bf16 v[228:231],a[144:147],a[40:43],v[228:231]
s_cmp_lg_u32 s60,0
ds_read_b64_tr_b16 a[90:91],v8 offset:1024
v_mfma_f32_16x16x32_bf16 v[228:231],a[172:175],a[56:59],v[228:231]
ds_read_b64_tr_b16 a[116:117],v8 offset:8192
v_mfma_f32_16x16x32_bf16 a[220:223],a[144:147],a[44:47],a[220:223]
ds_read_b64_tr_b16 a[118:119],v8 offset:9216
v_mfma_f32_16x16x32_bf16 a[220:223],a[172:175],a[60:63],a[220:223]
ds_read_b128 a[12:15],v0 offset:6144
v_mfma_f32_16x16x32_bf16 a[216:219],a[140:143],a[44:47],a[216:219]
ds_read_b128 a[28:31],v1 offset:6144
v_mfma_f32_16x16x32_bf16 a[216:219],a[168:171],a[60:63],a[216:219]
v_mfma_f32_16x16x32_bf16 a[212:215],a[136:139],a[44:47],a[212:215]
v_mfma_f32_16x16x32_bf16 a[212:215],a[164:167],a[60:63],a[212:215]
v_mfma_f32_16x16x32_bf16 a[208:211],a[132:135],a[44:47],a[208:211]
v_mfma_f32_16x16x32_bf16 a[208:211],a[160:163],a[60:63],a[208:211]
v_mfma_f32_16x16x32_bf16 a[204:207],a[128:131],a[44:47],a[204:207]
v_mfma_f32_16x16x32_bf16 a[204:207],a[156:159],a[60:63],a[204:207]
v_mfma_f32_16x16x32_bf16 a[200:203],a[124:127],a[44:47],a[200:203]
)"
R"(v_mfma_f32_16x16x32_bf16 a[200:203],a[152:155],a[60:63],a[200:203]
v_mfma_f32_16x16x32_bf16 a[196:199],a[120:123],a[44:47],a[196:199]
s_waitcnt lgkmcnt(0)
v_mfma_f32_16x16x32_bf16 a[196:199],a[148:151],a[60:63],a[196:199]
s_cbranch_scc1 gm_K1_GU_loop
v_mfma_f32_16x16x32_bf16 v[64:67],a[64:67],a[0:3],v[64:67]
s_add_u32 s66,s59,0x8000
s_cmp_eq_u32 s66,0x18000
ds_read_b64_tr_b16 a[120:121],v2 offset:16384
v_mfma_f32_16x16x32_bf16 v[64:67],a[92:95],a[16:19],v[64:67]
s_cselect_b32 s66,0,s66
s_sub_u32 s58,s66,s59
ds_read_b64_tr_b16 a[122:123],v2 offset:17408
v_mfma_f32_16x16x32_bf16 v[68:71],a[68:71],a[0:3],v[68:71]
s_mov_b32 s59,s66
s_cmp_lg_u32 %2,0
s_cbranch_scc1 gm_KB_w34
buffer_load_dwordx4 v[56:59],v18,s[4:7],s74 offen sc1
gm_KB_w34:
v_mfma_f32_16x16x32_bf16 v[68:71],a[96:99],a[16:19],v[68:71]
s_add_u32 m0,s25,0
s_add_u32 s12,s87,s80
ds_read_b64_tr_b16 a[148:149],v2 offset:24576
v_mfma_f32_16x16x32_bf16 v[72:75],a[72:75],a[0:3],v[72:75]
)"
R"(ds_read_b64_tr_b16 a[150:151],v2 offset:25600
ds_read_b64_tr_b16 a[124:125],v3 offset:16384
v_mfma_f32_16x16x32_bf16 v[72:75],a[100:103],a[16:19],v[72:75]
v_mfma_f32_16x16x32_bf16 v[76:79],a[76:79],a[0:3],v[76:79]
s_branch gm_KB_sk22
.p2align 12
gm_KB_pg22:
s_setpc_b64 s[100:101]
gm_KB_sk22:
buffer_load_dwordx4 v39,s[44:47],s12 offen lds
ds_read_b64_tr_b16 a[126:127],v3 offset:17408
v_mfma_f32_16x16x32_bf16 v[76:79],a[104:107],a[16:19],v[76:79]
ds_read_b64_tr_b16 a[152:153],v3 offset:24576
v_mfma_f32_16x16x32_bf16 v[80:83],a[80:83],a[0:3],v[80:83]
ds_read_b64_tr_b16 a[154:155],v3 offset:25600
v_mfma_f32_16x16x32_bf16 v[80:83],a[108:111],a[16:19],v[80:83]
ds_read_b64_tr_b16 a[128:129],v4 offset:16384
v_mfma_f32_16x16x32_bf16 v[84:87],a[84:87],a[0:3],v[84:87]
ds_read_b64_tr_b16 a[130:131],v4 offset:17408
v_mfma_f32_16x16x32_bf16 v[84:87],a[112:115],a[16:19],v[84:87]
v_mfma_f32_16x16x32_bf16 v[88:91],a[88:91],a[0:3],v[88:91]
ds_read_b64_tr_b16 a[156:157],v4 offset:24576
v_mfma_f32_16x16x32_bf16 v[88:91],a[116:119],a[16:19],v[88:91]
ds_read_b64_tr_b16 a[158:159],v4 offset:25600
)"
R"(v_mfma_f32_16x16x32_bf16 v[144:147],a[88:91],a[4:7],v[144:147]
s_add_u32 m0,s25,1024
s_add_u32 s15,s87,s81
ds_read_b64_tr_b16 a[132:133],v5 offset:16384
v_mfma_f32_16x16x32_bf16 v[144:147],a[116:119],a[20:23],v[144:147]
ds_read_b64_tr_b16 a[134:135],v5 offset:17408
v_mfma_f32_16x16x32_bf16 v[140:143],a[84:87],a[4:7],v[140:143]
ds_read_b64_tr_b16 a[160:161],v5 offset:24576
v_mfma_f32_16x16x32_bf16 v[140:143],a[112:115],a[20:23],v[140:143]
v_mfma_f32_16x16x32_bf16 v[136:139],a[80:83],a[4:7],v[136:139]
ds_read_b64_tr_b16 a[162:163],v5 offset:25600
v_mfma_f32_16x16x32_bf16 v[136:139],a[108:111],a[20:23],v[136:139]
ds_read_b64_tr_b16 a[136:137],v6 offset:16384
v_mfma_f32_16x16x32_bf16 v[132:135],a[76:79],a[4:7],v[132:135]
buffer_load_dwordx4 v55,s[44:47],s15 offen lds
ds_read_b64_tr_b16 a[138:139],v6 offset:17408
v_mfma_f32_16x16x32_bf16 v[132:135],a[104:107],a[20:23],v[132:135]
ds_read_b64_tr_b16 a[164:165],v6 offset:24576
v_mfma_f32_16x16x32_bf16 v[128:131],a[72:75],a[4:7],v[128:131]
ds_read_b64_tr_b16 a[166:167],v6 offset:25600
v_mfma_f32_16x16x32_bf16 v[128:131],a[100:103],a[20:23],v[128:131]
v_mfma_f32_16x16x32_bf16 v[124:127],a[68:71],a[4:7],v[124:127]
ds_read_b64_tr_b16 a[140:141],v7 offset:16384
v_mfma_f32_16x16x32_bf16 v[124:127],a[96:99],a[20:23],v[124:127]
)"
R"(ds_read_b64_tr_b16 a[142:143],v7 offset:17408
v_mfma_f32_16x16x32_bf16 v[120:123],a[64:67],a[4:7],v[120:123]
ds_read_b64_tr_b16 a[168:169],v7 offset:24576
v_mfma_f32_16x16x32_bf16 v[120:123],a[92:95],a[20:23],v[120:123]
ds_read_b64_tr_b16 a[170:171],v7 offset:25600
v_mfma_f32_16x16x32_bf16 v[176:179],a[64:67],a[8:11],v[176:179]
ds_read_b64_tr_b16 a[144:145],v8 offset:16384
v_mfma_f32_16x16x32_bf16 v[176:179],a[92:95],a[24:27],v[176:179]
s_add_u32 m0,s25,2048
s_add_u32 s12,s87,s82
v_mfma_f32_16x16x32_bf16 v[180:183],a[68:71],a[8:11],v[180:183]
ds_read_b64_tr_b16 a[146:147],v8 offset:17408
v_mfma_f32_16x16x32_bf16 v[180:183],a[96:99],a[24:27],v[180:183]
ds_read_b64_tr_b16 a[172:173],v8 offset:24576
v_mfma_f32_16x16x32_bf16 v[184:187],a[72:75],a[8:11],v[184:187]
ds_read_b64_tr_b16 a[174:175],v8 offset:25600
v_mfma_f32_16x16x32_bf16 v[184:187],a[100:103],a[24:27],v[184:187]
v_mfma_f32_16x16x32_bf16 v[188:191],a[76:79],a[8:11],v[188:191]
v_mfma_f32_16x16x32_bf16 v[188:191],a[104:107],a[24:27],v[188:191]
buffer_load_dwordx4 v39,s[44:47],s12 offen lds
v_mfma_f32_16x16x32_bf16 v[192:195],a[80:83],a[8:11],v[192:195]
v_xor_b32 v0,0x8000,v0
v_mfma_f32_16x16x32_bf16 v[192:195],a[108:111],a[24:27],v[192:195]
v_xor_b32 v1,0x8000,v1
)"
R"(v_mfma_f32_16x16x32_bf16 v[196:199],a[84:87],a[8:11],v[196:199]
v_add_u32 v2,s58,v2
v_mfma_f32_16x16x32_bf16 v[196:199],a[112:115],a[24:27],v[196:199]
v_add_u32 v3,s58,v3
v_mfma_f32_16x16x32_bf16 v[200:203],a[88:91],a[8:11],v[200:203]
v_add_u32 v4,s58,v4
v_mfma_f32_16x16x32_bf16 v[200:203],a[116:119],a[24:27],v[200:203]
v_add_u32 v5,s58,v5
v_mfma_f32_16x16x32_bf16 a[192:195],a[88:91],a[12:15],a[192:195]
v_add_u32 v6,s58,v6
v_mfma_f32_16x16x32_bf16 a[192:195],a[116:119],a[28:31],a[192:195]
v_add_u32 v7,s58,v7
v_mfma_f32_16x16x32_bf16 v[252:255],a[84:87],a[12:15],v[252:255]
s_add_u32 m0,s25,3072
s_add_u32 s15,s87,s83
v_add_u32 v8,s58,v8
v_mfma_f32_16x16x32_bf16 v[252:255],a[112:115],a[28:31],v[252:255]
v_add_u32 v9,s58,v9
v_mfma_f32_16x16x32_bf16 v[248:251],a[80:83],a[12:15],v[248:251]
v_mfma_f32_16x16x32_bf16 v[248:251],a[108:111],a[28:31],v[248:251]
v_mfma_f32_16x16x32_bf16 v[244:247],a[76:79],a[12:15],v[244:247]
v_mfma_f32_16x16x32_bf16 v[244:247],a[104:107],a[28:31],v[244:247]
v_mfma_f32_16x16x32_bf16 v[240:243],a[72:75],a[12:15],v[240:243]
buffer_load_dwordx4 v55,s[44:47],s15 offen lds
)"
R"(s_add_u32 s87,s87,131072
v_mfma_f32_16x16x32_bf16 v[240:243],a[100:103],a[28:31],v[240:243]
v_mfma_f32_16x16x32_bf16 v[236:239],a[68:71],a[12:15],v[236:239]
v_mfma_f32_16x16x32_bf16 v[236:239],a[96:99],a[28:31],v[236:239]
v_mfma_f32_16x16x32_bf16 v[232:235],a[64:67],a[12:15],v[232:235]
s_cmp_lg_u32 %2,0
s_cbranch_scc1 gm_KB_gotc
s_mov_b32 s98,0x10000
gm_KB_pollc:
v_xor_b32 v53,s72,v56
v_xor_b32 v54,s72,v57
v_or_b32 v53,v53,v54
v_xor_b32 v54,s72,v58
v_or_b32 v53,v53,v54
v_xor_b32 v54,s72,v59
v_or_b32 v53,v53,v54
v_cmp_ne_u32 vcc,0,v53
s_nop 1
s_cmp_eq_u64 vcc,0
s_cbranch_scc1 gm_KB_gotc
s_sub_u32 s98,s98,1
s_cmp_eq_u32 s98,0
s_cbranch_scc1 gm_KB_gotc
buffer_load_dwordx4 v[56:59],v18,s[4:7],s74 offen sc1
)"
R"(s_waitcnt vmcnt(0)
s_branch gm_KB_pollc
gm_KB_gotc:
s_waitcnt vmcnt(5) lgkmcnt(0)
v_mfma_f32_16x16x32_bf16 v[232:235],a[92:95],a[28:31],v[232:235]
s_barrier
v_mfma_f32_16x16x32_bf16 v[92:95],a[120:123],a[0:3],v[92:95]
ds_read_b64_tr_b16 a[64:65],v2 offset:0
v_mfma_f32_16x16x32_bf16 v[92:95],a[148:151],a[16:19],v[92:95]
buffer_load_dwordx4 a[224:227],v40,s[36:39],s75 offen sc1
v_mfma_f32_16x16x32_bf16 v[96:99],a[124:127],a[0:3],v[96:99]
ds_read_b64_tr_b16 a[66:67],v2 offset:1024
v_mfma_f32_16x16x32_bf16 v[96:99],a[152:155],a[16:19],v[96:99]
ds_read_b64_tr_b16 a[92:93],v2 offset:8192
v_mfma_f32_16x16x32_bf16 v[100:103],a[128:131],a[0:3],v[100:103]
ds_read_b64_tr_b16 a[94:95],v2 offset:9216
v_mfma_f32_16x16x32_bf16 v[100:103],a[156:159],a[16:19],v[100:103]
buffer_load_dwordx4 a[228:231],v41,s[36:39],s75 offen sc1
v_mfma_f32_16x16x32_bf16 v[104:107],a[132:135],a[0:3],v[104:107]
ds_read_b128 a[32:35],v0 offset:0
v_mfma_f32_16x16x32_bf16 v[104:107],a[160:163],a[16:19],v[104:107]
ds_read_b64_tr_b16 a[68:69],v3 offset:0
v_mfma_f32_16x16x32_bf16 v[108:111],a[136:139],a[0:3],v[108:111]
ds_read_b64_tr_b16 a[70:71],v3 offset:1024
)"
R"(v_mfma_f32_16x16x32_bf16 v[108:111],a[164:167],a[16:19],v[108:111]
buffer_load_dwordx4 a[232:235],v42,s[36:39],s75 offen sc1
v_mfma_f32_16x16x32_bf16 v[112:115],a[140:143],a[0:3],v[112:115]
ds_read_b64_tr_b16 a[96:97],v3 offset:8192
v_mfma_f32_16x16x32_bf16 v[112:115],a[168:171],a[16:19],v[112:115]
ds_read_b64_tr_b16 a[98:99],v3 offset:9216
v_mfma_f32_16x16x32_bf16 v[116:119],a[144:147],a[0:3],v[116:119]
ds_read_b128 a[48:51],v1 offset:0
v_mfma_f32_16x16x32_bf16 v[116:119],a[172:175],a[16:19],v[116:119]
ds_read_b64_tr_b16 a[72:73],v4 offset:0
v_mfma_f32_16x16x32_bf16 v[172:175],a[144:147],a[4:7],v[172:175]
buffer_load_dwordx4 a[236:239],v43,s[36:39],s75 offen sc1
v_mfma_f32_16x16x32_bf16 v[172:175],a[172:175],a[20:23],v[172:175]
ds_read_b64_tr_b16 a[74:75],v4 offset:1024
v_mfma_f32_16x16x32_bf16 v[168:171],a[140:143],a[4:7],v[168:171]
ds_read_b64_tr_b16 a[100:101],v4 offset:8192
v_mfma_f32_16x16x32_bf16 v[168:171],a[168:171],a[20:23],v[168:171]
ds_read_b64_tr_b16 a[102:103],v4 offset:9216
v_mfma_f32_16x16x32_bf16 v[164:167],a[136:139],a[4:7],v[164:167]
buffer_load_dwordx4 a[240:243],v40,s[36:39],s24 offen sc1
v_mfma_f32_16x16x32_bf16 v[164:167],a[164:167],a[20:23],v[164:167]
ds_read_b128 a[36:39],v0 offset:2048
v_mfma_f32_16x16x32_bf16 v[160:163],a[132:135],a[4:7],v[160:163]
ds_read_b64_tr_b16 a[76:77],v5 offset:0
)"
R"(v_mfma_f32_16x16x32_bf16 v[160:163],a[160:163],a[20:23],v[160:163]
ds_read_b64_tr_b16 a[78:79],v5 offset:1024
v_mfma_f32_16x16x32_bf16 v[156:159],a[128:131],a[4:7],v[156:159]
ds_read_b64_tr_b16 a[104:105],v5 offset:8192
v_mfma_f32_16x16x32_bf16 v[156:159],a[156:159],a[20:23],v[156:159]
buffer_load_dwordx4 a[244:247],v41,s[36:39],s24 offen sc1
v_mfma_f32_16x16x32_bf16 v[152:155],a[124:127],a[4:7],v[152:155]
ds_read_b64_tr_b16 a[106:107],v5 offset:9216
v_mfma_f32_16x16x32_bf16 v[152:155],a[152:155],a[20:23],v[152:155]
ds_read_b128 a[52:55],v1 offset:2048
v_mfma_f32_16x16x32_bf16 v[148:151],a[120:123],a[4:7],v[148:151]
ds_read_b64_tr_b16 a[80:81],v6 offset:0
v_mfma_f32_16x16x32_bf16 v[148:151],a[148:151],a[20:23],v[148:151]
buffer_load_dwordx4 a[248:251],v42,s[36:39],s24 offen sc1
v_mfma_f32_16x16x32_bf16 v[204:207],a[120:123],a[8:11],v[204:207]
ds_read_b64_tr_b16 a[82:83],v6 offset:1024
v_mfma_f32_16x16x32_bf16 v[204:207],a[148:151],a[24:27],v[204:207]
ds_read_b64_tr_b16 a[108:109],v6 offset:8192
v_mfma_f32_16x16x32_bf16 v[208:211],a[124:127],a[8:11],v[208:211]
ds_read_b64_tr_b16 a[110:111],v6 offset:9216
v_mfma_f32_16x16x32_bf16 v[208:211],a[152:155],a[24:27],v[208:211]
ds_read_b128 a[40:43],v0 offset:4096
v_mfma_f32_16x16x32_bf16 v[212:215],a[128:131],a[8:11],v[212:215]
buffer_load_dwordx4 a[252:255],v43,s[36:39],s24 offen sc1
)"
R"(v_mfma_f32_16x16x32_bf16 v[212:215],a[156:159],a[24:27],v[212:215]
s_add_u32 s75,s75,16384
s_add_u32 s24,s24,16384
v_mfma_f32_16x16x32_bf16 v[216:219],a[132:135],a[8:11],v[216:219]
s_lshl_b32 s67,s71,2
s_add_u32 s67,s67,s70
v_mfma_f32_16x16x32_bf16 v[216:219],a[160:163],a[24:27],v[216:219]
s_lshl_b32 s67,s67,8
s_add_u32 s67,s67,8192
v_mfma_f32_16x16x32_bf16 v[220:223],a[136:139],a[8:11],v[220:223]
v_and_b32 v38,15,v19
v_mfma_f32_16x16x32_bf16 v[220:223],a[164:167],a[24:27],v[220:223]
v_lshlrev_b32 v38,2,v38
v_mfma_f32_16x16x32_bf16 v[224:227],a[140:143],a[8:11],v[224:227]
buffer_load_dword v34,v38,s[4:7],s67 offen offset:0 sc1
v_mfma_f32_16x16x32_bf16 v[224:227],a[168:171],a[24:27],v[224:227]
ds_read_b64_tr_b16 a[84:85],v7 offset:0
v_mfma_f32_16x16x32_bf16 v[228:231],a[144:147],a[8:11],v[228:231]
ds_read_b64_tr_b16 a[86:87],v7 offset:1024
v_mfma_f32_16x16x32_bf16 v[228:231],a[172:175],a[24:27],v[228:231]
buffer_load_dword v35,v38,s[4:7],s67 offen offset:64 sc1
v_mfma_f32_16x16x32_bf16 a[220:223],a[144:147],a[12:15],a[220:223]
ds_read_b64_tr_b16 a[112:113],v7 offset:8192
v_mfma_f32_16x16x32_bf16 a[220:223],a[172:175],a[28:31],a[220:223]
)"
R"(ds_read_b64_tr_b16 a[114:115],v7 offset:9216
v_mfma_f32_16x16x32_bf16 a[216:219],a[140:143],a[12:15],a[216:219]
ds_read_b128 a[56:59],v1 offset:4096
v_mfma_f32_16x16x32_bf16 a[216:219],a[168:171],a[28:31],a[216:219]
buffer_load_dword v36,v38,s[4:7],s67 offen offset:128 sc1
v_mfma_f32_16x16x32_bf16 a[212:215],a[136:139],a[12:15],a[212:215]
ds_read_b64_tr_b16 a[88:89],v8 offset:0
v_mfma_f32_16x16x32_bf16 a[212:215],a[164:167],a[28:31],a[212:215]
ds_read_b64_tr_b16 a[90:91],v8 offset:1024
v_mfma_f32_16x16x32_bf16 a[208:211],a[132:135],a[12:15],a[208:211]
ds_read_b64_tr_b16 a[116:117],v8 offset:8192
v_mfma_f32_16x16x32_bf16 a[208:211],a[160:163],a[28:31],a[208:211]
ds_read_b64_tr_b16 a[118:119],v8 offset:9216
v_mfma_f32_16x16x32_bf16 a[204:207],a[128:131],a[12:15],a[204:207]
buffer_load_dword v37,v38,s[4:7],s67 offen offset:192 sc1
v_mfma_f32_16x16x32_bf16 a[204:207],a[156:159],a[28:31],a[204:207]
ds_read_b128 a[44:47],v0 offset:6144
v_mfma_f32_16x16x32_bf16 a[200:203],a[124:127],a[12:15],a[200:203]
ds_read_b128 a[60:63],v1 offset:6144
v_mfma_f32_16x16x32_bf16 a[200:203],a[152:155],a[28:31],a[200:203]
v_mfma_f32_16x16x32_bf16 a[196:199],a[120:123],a[12:15],a[196:199]
s_waitcnt lgkmcnt(0)
v_mfma_f32_16x16x32_bf16 a[196:199],a[148:151],a[28:31],a[196:199]
v_mfma_f32_16x16x32_bf16 v[64:67],a[64:67],a[32:35],v[64:67]
)"
R"(s_add_u32 s66,s59,0x8000
s_cmp_eq_u32 s66,0x18000
ds_read_b64_tr_b16 a[120:121],v2 offset:16384
v_mfma_f32_16x16x32_bf16 v[64:67],a[92:95],a[48:51],v[64:67]
s_cselect_b32 s66,0,s66
s_sub_u32 s58,s66,s59
ds_read_b64_tr_b16 a[122:123],v2 offset:17408
v_mfma_f32_16x16x32_bf16 v[68:71],a[68:71],a[32:35],v[68:71]
s_mov_b32 s59,s66
s_add_u32 m0,s25,16384
ds_read_b64_tr_b16 a[148:149],v2 offset:24576
v_mfma_f32_16x16x32_bf16 v[68:71],a[96:99],a[48:51],v[68:71]
s_add_u32 s12,s87,s80
ds_read_b64_tr_b16 a[150:151],v2 offset:25600
v_mfma_f32_16x16x32_bf16 v[72:75],a[72:75],a[32:35],v[72:75]
ds_read_b64_tr_b16 a[124:125],v3 offset:16384
v_mfma_f32_16x16x32_bf16 v[72:75],a[100:103],a[48:51],v[72:75]
v_mfma_f32_16x16x32_bf16 v[76:79],a[76:79],a[32:35],v[76:79]
buffer_load_dwordx4 v39,s[44:47],s12 offen lds
ds_read_b64_tr_b16 a[126:127],v3 offset:17408
v_mfma_f32_16x16x32_bf16 v[76:79],a[104:107],a[48:51],v[76:79]
s_add_u32 m0,s25,17408
s_add_u32 s15,s87,s81
ds_read_b64_tr_b16 a[152:153],v3 offset:24576
)"
R"(v_mfma_f32_16x16x32_bf16 v[80:83],a[80:83],a[32:35],v[80:83]
ds_read_b64_tr_b16 a[154:155],v3 offset:25600
v_mfma_f32_16x16x32_bf16 v[80:83],a[108:111],a[48:51],v[80:83]
ds_read_b64_tr_b16 a[128:129],v4 offset:16384
v_mfma_f32_16x16x32_bf16 v[84:87],a[84:87],a[32:35],v[84:87]
buffer_load_dwordx4 v55,s[44:47],s15 offen lds
ds_read_b64_tr_b16 a[130:131],v4 offset:17408
v_mfma_f32_16x16x32_bf16 v[84:87],a[112:115],a[48:51],v[84:87]
s_add_u32 m0,s25,18432
s_add_u32 s12,s87,s82
v_mfma_f32_16x16x32_bf16 v[88:91],a[88:91],a[32:35],v[88:91]
ds_read_b64_tr_b16 a[156:157],v4 offset:24576
v_mfma_f32_16x16x32_bf16 v[88:91],a[116:119],a[48:51],v[88:91]
ds_read_b64_tr_b16 a[158:159],v4 offset:25600
v_mfma_f32_16x16x32_bf16 v[144:147],a[88:91],a[36:39],v[144:147]
buffer_load_dwordx4 v39,s[44:47],s12 offen lds
ds_read_b64_tr_b16 a[132:133],v5 offset:16384
v_mfma_f32_16x16x32_bf16 v[144:147],a[116:119],a[52:55],v[144:147]
s_add_u32 m0,s25,19456
s_add_u32 s15,s87,s83
ds_read_b64_tr_b16 a[134:135],v5 offset:17408
v_mfma_f32_16x16x32_bf16 v[140:143],a[84:87],a[36:39],v[140:143]
ds_read_b64_tr_b16 a[160:161],v5 offset:24576
v_mfma_f32_16x16x32_bf16 v[140:143],a[112:115],a[52:55],v[140:143]
)"
R"(v_mfma_f32_16x16x32_bf16 v[136:139],a[80:83],a[36:39],v[136:139]
buffer_load_dwordx4 v55,s[44:47],s15 offen lds
ds_read_b64_tr_b16 a[162:163],v5 offset:25600
v_mfma_f32_16x16x32_bf16 v[136:139],a[108:111],a[52:55],v[136:139]
s_add_u32 s87,s87,131072
ds_read_b64_tr_b16 a[136:137],v6 offset:16384
v_mfma_f32_16x16x32_bf16 v[132:135],a[76:79],a[36:39],v[132:135]
ds_read_b64_tr_b16 a[138:139],v6 offset:17408
v_mfma_f32_16x16x32_bf16 v[132:135],a[104:107],a[52:55],v[132:135]
ds_read_b64_tr_b16 a[164:165],v6 offset:24576
v_mfma_f32_16x16x32_bf16 v[128:131],a[72:75],a[36:39],v[128:131]
buffer_load_dwordx4 a[0:3],v40,s[36:39],s75 offen sc1
ds_read_b64_tr_b16 a[166:167],v6 offset:25600
v_mfma_f32_16x16x32_bf16 v[128:131],a[100:103],a[52:55],v[128:131]
v_mfma_f32_16x16x32_bf16 v[124:127],a[68:71],a[36:39],v[124:127]
ds_read_b64_tr_b16 a[140:141],v7 offset:16384
v_mfma_f32_16x16x32_bf16 v[124:127],a[96:99],a[52:55],v[124:127]
ds_read_b64_tr_b16 a[142:143],v7 offset:17408
v_mfma_f32_16x16x32_bf16 v[120:123],a[64:67],a[36:39],v[120:123]
buffer_load_dwordx4 a[4:7],v41,s[36:39],s75 offen sc1
ds_read_b64_tr_b16 a[168:169],v7 offset:24576
v_mfma_f32_16x16x32_bf16 v[120:123],a[92:95],a[52:55],v[120:123]
ds_read_b64_tr_b16 a[170:171],v7 offset:25600
v_mfma_f32_16x16x32_bf16 v[176:179],a[64:67],a[40:43],v[176:179]
)"
R"(ds_read_b64_tr_b16 a[144:145],v8 offset:16384
v_mfma_f32_16x16x32_bf16 v[176:179],a[92:95],a[56:59],v[176:179]
v_mfma_f32_16x16x32_bf16 v[180:183],a[68:71],a[40:43],v[180:183]
buffer_load_dwordx4 a[8:11],v42,s[36:39],s75 offen sc1
ds_read_b64_tr_b16 a[146:147],v8 offset:17408
v_mfma_f32_16x16x32_bf16 v[180:183],a[96:99],a[56:59],v[180:183]
ds_read_b64_tr_b16 a[172:173],v8 offset:24576
v_mfma_f32_16x16x32_bf16 v[184:187],a[72:75],a[40:43],v[184:187]
ds_read_b64_tr_b16 a[174:175],v8 offset:25600
v_mfma_f32_16x16x32_bf16 v[184:187],a[100:103],a[56:59],v[184:187]
v_mfma_f32_16x16x32_bf16 v[188:191],a[76:79],a[40:43],v[188:191]
buffer_load_dwordx4 a[12:15],v43,s[36:39],s75 offen sc1
v_mfma_f32_16x16x32_bf16 v[188:191],a[104:107],a[56:59],v[188:191]
v_mfma_f32_16x16x32_bf16 v[192:195],a[80:83],a[40:43],v[192:195]
v_xor_b32 v0,0x8000,v0
v_mfma_f32_16x16x32_bf16 v[192:195],a[108:111],a[56:59],v[192:195]
v_xor_b32 v1,0x8000,v1
v_mfma_f32_16x16x32_bf16 v[196:199],a[84:87],a[40:43],v[196:199]
buffer_load_dwordx4 a[16:19],v40,s[36:39],s24 offen sc1
v_add_u32 v2,s58,v2
v_mfma_f32_16x16x32_bf16 v[196:199],a[112:115],a[56:59],v[196:199]
v_add_u32 v3,s58,v3
v_mfma_f32_16x16x32_bf16 v[200:203],a[88:91],a[40:43],v[200:203]
v_add_u32 v4,s58,v4
)"
R"(v_mfma_f32_16x16x32_bf16 v[200:203],a[116:119],a[56:59],v[200:203]
v_add_u32 v5,s58,v5
v_mfma_f32_16x16x32_bf16 a[192:195],a[88:91],a[44:47],a[192:195]
buffer_load_dwordx4 a[20:23],v41,s[36:39],s24 offen sc1
v_add_u32 v6,s58,v6
v_mfma_f32_16x16x32_bf16 a[192:195],a[116:119],a[60:63],a[192:195]
v_add_u32 v7,s58,v7
v_mfma_f32_16x16x32_bf16 v[252:255],a[84:87],a[44:47],v[252:255]
v_add_u32 v8,s58,v8
v_mfma_f32_16x16x32_bf16 v[252:255],a[112:115],a[60:63],v[252:255]
v_add_u32 v9,s58,v9
v_mfma_f32_16x16x32_bf16 v[248:251],a[80:83],a[44:47],v[248:251]
buffer_load_dwordx4 a[24:27],v42,s[36:39],s24 offen sc1
v_mfma_f32_16x16x32_bf16 v[248:251],a[108:111],a[60:63],v[248:251]
v_mfma_f32_16x16x32_bf16 v[244:247],a[76:79],a[44:47],v[244:247]
v_mfma_f32_16x16x32_bf16 v[244:247],a[104:107],a[60:63],v[244:247]
v_mfma_f32_16x16x32_bf16 v[240:243],a[72:75],a[44:47],v[240:243]
buffer_load_dwordx4 a[28:31],v43,s[36:39],s24 offen sc1
s_add_u32 s75,s75,16384
v_mfma_f32_16x16x32_bf16 v[240:243],a[100:103],a[60:63],v[240:243]
s_add_u32 s24,s24,16384
v_mfma_f32_16x16x32_bf16 v[236:239],a[68:71],a[44:47],v[236:239]
v_mfma_f32_16x16x32_bf16 v[236:239],a[96:99],a[60:63],v[236:239]
v_mfma_f32_16x16x32_bf16 v[232:235],a[64:67],a[44:47],v[232:235]
)"
R"(s_waitcnt lgkmcnt(0)
v_mfma_f32_16x16x32_bf16 v[232:235],a[92:95],a[60:63],v[232:235]
s_barrier
v_mfma_f32_16x16x32_bf16 v[92:95],a[120:123],a[32:35],v[92:95]
s_add_u32 m0,s25,32768
s_add_u32 s12,s87,s80
v_mfma_f32_16x16x32_bf16 v[92:95],a[148:151],a[48:51],v[92:95]
buffer_load_dwordx4 v39,s[44:47],s12 offen lds
v_mfma_f32_16x16x32_bf16 v[96:99],a[124:127],a[32:35],v[96:99]
s_add_u32 m0,s25,33792
s_add_u32 s15,s87,s81
v_mfma_f32_16x16x32_bf16 v[96:99],a[152:155],a[48:51],v[96:99]
v_mfma_f32_16x16x32_bf16 v[100:103],a[128:131],a[32:35],v[100:103]
v_mfma_f32_16x16x32_bf16 v[100:103],a[156:159],a[48:51],v[100:103]
buffer_load_dwordx4 v55,s[44:47],s15 offen lds
v_mfma_f32_16x16x32_bf16 v[104:107],a[132:135],a[32:35],v[104:107]
s_add_u32 m0,s25,34816
s_add_u32 s12,s87,s82
v_mfma_f32_16x16x32_bf16 v[104:107],a[160:163],a[48:51],v[104:107]
v_mfma_f32_16x16x32_bf16 v[108:111],a[136:139],a[32:35],v[108:111]
v_mfma_f32_16x16x32_bf16 v[108:111],a[164:167],a[48:51],v[108:111]
buffer_load_dwordx4 v39,s[44:47],s12 offen lds
v_mfma_f32_16x16x32_bf16 v[112:115],a[140:143],a[32:35],v[112:115]
v_mfma_f32_16x16x32_bf16 v[112:115],a[168:171],a[48:51],v[112:115]
)"
R"(s_add_u32 m0,s25,35840
s_add_u32 s15,s87,s83
v_mfma_f32_16x16x32_bf16 v[116:119],a[144:147],a[32:35],v[116:119]
v_mfma_f32_16x16x32_bf16 v[116:119],a[172:175],a[48:51],v[116:119]
v_mfma_f32_16x16x32_bf16 v[172:175],a[144:147],a[36:39],v[172:175]
buffer_load_dwordx4 v55,s[44:47],s15 offen lds
v_mfma_f32_16x16x32_bf16 v[172:175],a[172:175],a[52:55],v[172:175]
s_add_u32 s87,s87,131072
v_mfma_f32_16x16x32_bf16 v[168:171],a[140:143],a[36:39],v[168:171]
v_mfma_f32_16x16x32_bf16 v[168:171],a[168:171],a[52:55],v[168:171]
v_mfma_f32_16x16x32_bf16 v[164:167],a[136:139],a[36:39],v[164:167]
buffer_load_dwordx4 a[64:67],v40,s[36:39],s75 offen sc1
v_mfma_f32_16x16x32_bf16 v[164:167],a[164:167],a[52:55],v[164:167]
v_mfma_f32_16x16x32_bf16 v[160:163],a[132:135],a[36:39],v[160:163]
v_mfma_f32_16x16x32_bf16 v[160:163],a[160:163],a[52:55],v[160:163]
v_mfma_f32_16x16x32_bf16 v[156:159],a[128:131],a[36:39],v[156:159]
v_mfma_f32_16x16x32_bf16 v[156:159],a[156:159],a[52:55],v[156:159]
buffer_load_dwordx4 a[68:71],v41,s[36:39],s75 offen sc1
v_mfma_f32_16x16x32_bf16 v[152:155],a[124:127],a[36:39],v[152:155]
v_mfma_f32_16x16x32_bf16 v[152:155],a[152:155],a[52:55],v[152:155]
v_mfma_f32_16x16x32_bf16 v[148:151],a[120:123],a[36:39],v[148:151]
v_mfma_f32_16x16x32_bf16 v[148:151],a[148:151],a[52:55],v[148:151]
buffer_load_dwordx4 a[72:75],v42,s[36:39],s75 offen sc1
v_mfma_f32_16x16x32_bf16 v[204:207],a[120:123],a[40:43],v[204:207]
)"
R"(v_mfma_f32_16x16x32_bf16 v[204:207],a[148:151],a[56:59],v[204:207]
v_mfma_f32_16x16x32_bf16 v[208:211],a[124:127],a[40:43],v[208:211]
v_mfma_f32_16x16x32_bf16 v[208:211],a[152:155],a[56:59],v[208:211]
v_mfma_f32_16x16x32_bf16 v[212:215],a[128:131],a[40:43],v[212:215]
buffer_load_dwordx4 a[76:79],v43,s[36:39],s75 offen sc1
v_mfma_f32_16x16x32_bf16 v[212:215],a[156:159],a[56:59],v[212:215]
v_mfma_f32_16x16x32_bf16 v[216:219],a[132:135],a[40:43],v[216:219]
v_mfma_f32_16x16x32_bf16 v[216:219],a[160:163],a[56:59],v[216:219]
v_mfma_f32_16x16x32_bf16 v[220:223],a[136:139],a[40:43],v[220:223]
buffer_load_dwordx4 a[80:83],v40,s[36:39],s24 offen sc1
v_mfma_f32_16x16x32_bf16 v[220:223],a[164:167],a[56:59],v[220:223]
v_mfma_f32_16x16x32_bf16 v[224:227],a[140:143],a[40:43],v[224:227]
v_mfma_f32_16x16x32_bf16 v[224:227],a[168:171],a[56:59],v[224:227]
v_mfma_f32_16x16x32_bf16 v[228:231],a[144:147],a[40:43],v[228:231]
v_mfma_f32_16x16x32_bf16 v[228:231],a[172:175],a[56:59],v[228:231]
buffer_load_dwordx4 a[84:87],v41,s[36:39],s24 offen sc1
v_mfma_f32_16x16x32_bf16 a[220:223],a[144:147],a[44:47],a[220:223]
v_mfma_f32_16x16x32_bf16 a[220:223],a[172:175],a[60:63],a[220:223]
v_mfma_f32_16x16x32_bf16 a[216:219],a[140:143],a[44:47],a[216:219]
v_mfma_f32_16x16x32_bf16 a[216:219],a[168:171],a[60:63],a[216:219]
buffer_load_dwordx4 a[88:91],v42,s[36:39],s24 offen sc1
v_mfma_f32_16x16x32_bf16 a[212:215],a[136:139],a[44:47],a[212:215]
v_mfma_f32_16x16x32_bf16 a[212:215],a[164:167],a[60:63],a[212:215]
v_mfma_f32_16x16x32_bf16 a[208:211],a[132:135],a[44:47],a[208:211]
)"
R"(v_mfma_f32_16x16x32_bf16 a[208:211],a[160:163],a[60:63],a[208:211]
v_mfma_f32_16x16x32_bf16 a[204:207],a[128:131],a[44:47],a[204:207]
buffer_load_dwordx4 a[92:95],v43,s[36:39],s24 offen sc1
v_mfma_f32_16x16x32_bf16 a[204:207],a[156:159],a[60:63],a[204:207]
s_add_u32 s75,s75,16384
s_add_u32 s24,s24,16384
v_mfma_f32_16x16x32_bf16 a[200:203],a[124:127],a[44:47],a[200:203]
v_mfma_f32_16x16x32_bf16 a[200:203],a[152:155],a[60:63],a[200:203]
v_mfma_f32_16x16x32_bf16 a[196:199],a[120:123],a[44:47],a[196:199]
s_waitcnt lgkmcnt(0)
v_mfma_f32_16x16x32_bf16 a[196:199],a[148:151],a[60:63],a[196:199]
s_waitcnt vmcnt(24)
s_barrier
ds_read_b64_tr_b16 a[96:97],v44 offset:0
ds_read_b64_tr_b16 a[98:99],v44 offset:4096
ds_read_b64_tr_b16 a[100:101],v45 offset:0
ds_read_b64_tr_b16 a[102:103],v45 offset:4096
ds_read_b64_tr_b16 a[104:105],v46 offset:0
ds_read_b64_tr_b16 a[106:107],v46 offset:4096
ds_read_b64_tr_b16 a[108:109],v47 offset:0
ds_read_b64_tr_b16 a[110:111],v47 offset:4096
ds_read_b64_tr_b16 a[112:113],v48 offset:0
ds_read_b64_tr_b16 a[114:115],v48 offset:4096
ds_read_b64_tr_b16 a[116:117],v49 offset:0
)"
R"(ds_read_b64_tr_b16 a[118:119],v49 offset:4096
ds_read_b64_tr_b16 a[120:121],v50 offset:0
ds_read_b64_tr_b16 a[122:123],v50 offset:4096
ds_read_b64_tr_b16 a[124:125],v51 offset:0
ds_read_b64_tr_b16 a[126:127],v51 offset:4096
ds_read_b64_tr_b16 a[128:129],v44 offset:8192
ds_read_b64_tr_b16 a[130:131],v44 offset:12288
ds_read_b64_tr_b16 a[132:133],v45 offset:8192
ds_read_b64_tr_b16 a[134:135],v45 offset:12288
ds_read_b64_tr_b16 a[136:137],v46 offset:8192
ds_read_b64_tr_b16 a[138:139],v46 offset:12288
ds_read_b64_tr_b16 a[140:141],v47 offset:8192
ds_read_b64_tr_b16 a[142:143],v47 offset:12288
ds_read_b64_tr_b16 a[144:145],v48 offset:8192
ds_read_b64_tr_b16 a[146:147],v48 offset:12288
ds_read_b64_tr_b16 a[148:149],v49 offset:8192
ds_read_b64_tr_b16 a[150:151],v49 offset:12288
ds_read_b64_tr_b16 a[152:153],v50 offset:8192
ds_read_b64_tr_b16 a[154:155],v50 offset:12288
ds_read_b64_tr_b16 a[156:157],v51 offset:8192
ds_read_b64_tr_b16 a[158:159],v51 offset:12288
v_mov_b32 v32,s76
v_mov_b32 v33,s76
s_add_u32 s99,s84,0
)"
R"(v_pk_mul_f32 v[0:1],v[64:65],v[64:65]
v_pk_mul_f32 v[2:3],v[66:67],v[66:67]
v_pk_fma_f32 v[0:1],v[0:1],s[78:79],v[32:33]
v_pk_fma_f32 v[2:3],v[2:3],s[78:79],v[32:33]
v_pk_mul_f32 v[0:1],v[0:1],v[64:65]
v_pk_mul_f32 v[2:3],v[2:3],v[66:67]
v_exp_f32 v0,v0
v_exp_f32 v1,v1
v_exp_f32 v2,v2
v_exp_f32 v3,v3
v_pk_mul_f32 v[92:93],v[92:93],v[64:65]
v_pk_mul_f32 v[94:95],v[94:95],v[66:67]
v_pk_fma_f32 v[0:1],v[0:1],v[34:35],v[34:35] op_sel:[0,0,0] op_sel_hi:[1,0,0]
v_pk_fma_f32 v[2:3],v[2:3],v[34:35],v[34:35] op_sel:[0,0,0] op_sel_hi:[1,0,0]
v_rcp_f32 v0,v0
v_rcp_f32 v1,v1
v_rcp_f32 v2,v2
v_rcp_f32 v3,v3
v_pk_mul_f32 v[92:93],v[92:93],v[0:1]
v_pk_mul_f32 v[94:95],v[94:95],v[2:3]
v_cvt_pk_bf16_f32 v24,v92,v93
v_cvt_pk_bf16_f32 v25,v94,v95
v_pk_mul_f32 v[4:5],v[68:69],v[68:69]
v_pk_mul_f32 v[6:7],v[70:71],v[70:71]
)"
R"(v_pk_fma_f32 v[4:5],v[4:5],s[78:79],v[32:33]
v_pk_fma_f32 v[6:7],v[6:7],s[78:79],v[32:33]
v_pk_mul_f32 v[4:5],v[4:5],v[68:69]
v_pk_mul_f32 v[6:7],v[6:7],v[70:71]
v_exp_f32 v4,v4
v_exp_f32 v5,v5
v_exp_f32 v6,v6
v_exp_f32 v7,v7
v_pk_mul_f32 v[96:97],v[96:97],v[68:69]
v_pk_mul_f32 v[98:99],v[98:99],v[70:71]
v_pk_fma_f32 v[4:5],v[4:5],v[34:35],v[34:35] op_sel:[0,0,0] op_sel_hi:[1,0,0]
v_pk_fma_f32 v[6:7],v[6:7],v[34:35],v[34:35] op_sel:[0,0,0] op_sel_hi:[1,0,0]
v_rcp_f32 v4,v4
v_rcp_f32 v5,v5
v_rcp_f32 v6,v6
v_rcp_f32 v7,v7
v_pk_mul_f32 v[96:97],v[96:97],v[4:5]
v_pk_mul_f32 v[98:99],v[98:99],v[6:7]
v_cvt_pk_bf16_f32 v26,v96,v97
v_cvt_pk_bf16_f32 v27,v98,v99
buffer_store_dwordx4 v[24:27],v18,s[48:51],s99 offen offset:0 sc1
v_pk_mul_f32 v[0:1],v[120:121],v[120:121]
v_pk_mul_f32 v[2:3],v[122:123],v[122:123]
v_pk_fma_f32 v[0:1],v[0:1],s[78:79],v[32:33]
)"
R"(v_pk_fma_f32 v[2:3],v[2:3],s[78:79],v[32:33]
v_pk_mul_f32 v[0:1],v[0:1],v[120:121]
v_pk_mul_f32 v[2:3],v[2:3],v[122:123]
v_exp_f32 v0,v0
v_exp_f32 v1,v1
v_exp_f32 v2,v2
v_exp_f32 v3,v3
v_pk_mul_f32 v[148:149],v[148:149],v[120:121]
v_pk_mul_f32 v[150:151],v[150:151],v[122:123]
v_pk_fma_f32 v[0:1],v[0:1],v[34:35],v[34:35] op_sel:[0,1,1] op_sel_hi:[1,1,1]
v_pk_fma_f32 v[2:3],v[2:3],v[34:35],v[34:35] op_sel:[0,1,1] op_sel_hi:[1,1,1]
v_rcp_f32 v0,v0
v_rcp_f32 v1,v1
v_rcp_f32 v2,v2
v_rcp_f32 v3,v3
v_pk_mul_f32 v[148:149],v[148:149],v[0:1]
v_pk_mul_f32 v[150:151],v[150:151],v[2:3]
v_cvt_pk_bf16_f32 v28,v148,v149
v_cvt_pk_bf16_f32 v29,v150,v151
v_pk_mul_f32 v[4:5],v[124:125],v[124:125]
v_pk_mul_f32 v[6:7],v[126:127],v[126:127]
v_pk_fma_f32 v[4:5],v[4:5],s[78:79],v[32:33]
v_pk_fma_f32 v[6:7],v[6:7],s[78:79],v[32:33]
v_pk_mul_f32 v[4:5],v[4:5],v[124:125]
)"
R"(v_pk_mul_f32 v[6:7],v[6:7],v[126:127]
v_exp_f32 v4,v4
v_exp_f32 v5,v5
v_exp_f32 v6,v6
v_exp_f32 v7,v7
v_pk_mul_f32 v[152:153],v[152:153],v[124:125]
v_pk_mul_f32 v[154:155],v[154:155],v[126:127]
v_pk_fma_f32 v[4:5],v[4:5],v[34:35],v[34:35] op_sel:[0,1,1] op_sel_hi:[1,1,1]
v_pk_fma_f32 v[6:7],v[6:7],v[34:35],v[34:35] op_sel:[0,1,1] op_sel_hi:[1,1,1]
v_rcp_f32 v4,v4
v_rcp_f32 v5,v5
v_rcp_f32 v6,v6
v_rcp_f32 v7,v7
v_pk_mul_f32 v[152:153],v[152:153],v[4:5]
v_pk_mul_f32 v[154:155],v[154:155],v[6:7]
v_cvt_pk_bf16_f32 v30,v152,v153
v_cvt_pk_bf16_f32 v31,v154,v155
s_branch gm_KB_sk23
.p2align 12
gm_KB_pg23:
s_setpc_b64 s[100:101]
gm_KB_sk23:
buffer_store_dwordx4 v[28:31],v18,s[48:51],s99 offen offset:1024 sc1
v_pk_mul_f32 v[0:1],v[176:177],v[176:177]
)"
R"(v_pk_mul_f32 v[2:3],v[178:179],v[178:179]
v_pk_fma_f32 v[0:1],v[0:1],s[78:79],v[32:33]
v_pk_fma_f32 v[2:3],v[2:3],s[78:79],v[32:33]
v_pk_mul_f32 v[0:1],v[0:1],v[176:177]
v_pk_mul_f32 v[2:3],v[2:3],v[178:179]
v_exp_f32 v0,v0
v_exp_f32 v1,v1
v_exp_f32 v2,v2
v_exp_f32 v3,v3
v_pk_mul_f32 v[204:205],v[204:205],v[176:177]
v_pk_mul_f32 v[206:207],v[206:207],v[178:179]
v_pk_fma_f32 v[0:1],v[0:1],v[36:37],v[36:37] op_sel:[0,0,0] op_sel_hi:[1,0,0]
v_pk_fma_f32 v[2:3],v[2:3],v[36:37],v[36:37] op_sel:[0,0,0] op_sel_hi:[1,0,0]
v_rcp_f32 v0,v0
v_rcp_f32 v1,v1
v_rcp_f32 v2,v2
v_rcp_f32 v3,v3
v_pk_mul_f32 v[204:205],v[204:205],v[0:1]
v_pk_mul_f32 v[206:207],v[206:207],v[2:3]
v_cvt_pk_bf16_f32 v24,v204,v205
v_cvt_pk_bf16_f32 v25,v206,v207
v_pk_mul_f32 v[4:5],v[180:181],v[180:181]
v_pk_mul_f32 v[6:7],v[182:183],v[182:183]
v_pk_fma_f32 v[4:5],v[4:5],s[78:79],v[32:33]
)"
R"(v_pk_fma_f32 v[6:7],v[6:7],s[78:79],v[32:33]
v_pk_mul_f32 v[4:5],v[4:5],v[180:181]
v_pk_mul_f32 v[6:7],v[6:7],v[182:183]
v_exp_f32 v4,v4
v_exp_f32 v5,v5
v_exp_f32 v6,v6
v_exp_f32 v7,v7
v_pk_mul_f32 v[208:209],v[208:209],v[180:181]
v_pk_mul_f32 v[210:211],v[210:211],v[182:183]
v_pk_fma_f32 v[4:5],v[4:5],v[36:37],v[36:37] op_sel:[0,0,0] op_sel_hi:[1,0,0]
v_pk_fma_f32 v[6:7],v[6:7],v[36:37],v[36:37] op_sel:[0,0,0] op_sel_hi:[1,0,0]
v_rcp_f32 v4,v4
v_rcp_f32 v5,v5
v_rcp_f32 v6,v6
v_rcp_f32 v7,v7
v_pk_mul_f32 v[208:209],v[208:209],v[4:5]
v_pk_mul_f32 v[210:211],v[210:211],v[6:7]
v_cvt_pk_bf16_f32 v26,v208,v209
v_cvt_pk_bf16_f32 v27,v210,v211
buffer_store_dwordx4 v[24:27],v18,s[48:51],s99 offen offset:2048 sc1
v_accvgpr_read_b32 v8,a196
v_accvgpr_read_b32 v9,a197
v_accvgpr_read_b32 v10,a198
v_accvgpr_read_b32 v11,a199
)"
R"(v_pk_mul_f32 v[0:1],v[232:233],v[232:233]
v_pk_mul_f32 v[2:3],v[234:235],v[234:235]
v_pk_fma_f32 v[0:1],v[0:1],s[78:79],v[32:33]
v_pk_fma_f32 v[2:3],v[2:3],s[78:79],v[32:33]
v_pk_mul_f32 v[0:1],v[0:1],v[232:233]
v_pk_mul_f32 v[2:3],v[2:3],v[234:235]
v_exp_f32 v0,v0
v_exp_f32 v1,v1
v_exp_f32 v2,v2
v_exp_f32 v3,v3
v_pk_mul_f32 v[8:9],v[8:9],v[232:233]
v_pk_mul_f32 v[10:11],v[10:11],v[234:235]
v_pk_fma_f32 v[0:1],v[0:1],v[36:37],v[36:37] op_sel:[0,1,1] op_sel_hi:[1,1,1]
v_pk_fma_f32 v[2:3],v[2:3],v[36:37],v[36:37] op_sel:[0,1,1] op_sel_hi:[1,1,1]
v_rcp_f32 v0,v0
v_rcp_f32 v1,v1
v_rcp_f32 v2,v2
v_rcp_f32 v3,v3
v_pk_mul_f32 v[8:9],v[8:9],v[0:1]
v_pk_mul_f32 v[10:11],v[10:11],v[2:3]
v_cvt_pk_bf16_f32 v28,v8,v9
v_cvt_pk_bf16_f32 v29,v10,v11
v_accvgpr_read_b32 v12,a200
v_accvgpr_read_b32 v13,a201
)"
R"(v_accvgpr_read_b32 v14,a202
v_accvgpr_read_b32 v15,a203
v_pk_mul_f32 v[4:5],v[236:237],v[236:237]
v_pk_mul_f32 v[6:7],v[238:239],v[238:239]
v_pk_fma_f32 v[4:5],v[4:5],s[78:79],v[32:33]
v_pk_fma_f32 v[6:7],v[6:7],s[78:79],v[32:33]
v_pk_mul_f32 v[4:5],v[4:5],v[236:237]
v_pk_mul_f32 v[6:7],v[6:7],v[238:239]
v_exp_f32 v4,v4
v_exp_f32 v5,v5
v_exp_f32 v6,v6
v_exp_f32 v7,v7
v_pk_mul_f32 v[12:13],v[12:13],v[236:237]
v_pk_mul_f32 v[14:15],v[14:15],v[238:239]
v_pk_fma_f32 v[4:5],v[4:5],v[36:37],v[36:37] op_sel:[0,1,1] op_sel_hi:[1,1,1]
v_pk_fma_f32 v[6:7],v[6:7],v[36:37],v[36:37] op_sel:[0,1,1] op_sel_hi:[1,1,1]
v_rcp_f32 v4,v4
v_rcp_f32 v5,v5
v_rcp_f32 v6,v6
v_rcp_f32 v7,v7
v_pk_mul_f32 v[12:13],v[12:13],v[4:5]
v_pk_mul_f32 v[14:15],v[14:15],v[6:7]
v_cvt_pk_bf16_f32 v30,v12,v13
v_cvt_pk_bf16_f32 v31,v14,v15
)"
R"(buffer_store_dwordx4 v[28:31],v18,s[48:51],s99 offen offset:3072 sc1
s_add_u32 s99,s84,8192
v_pk_mul_f32 v[0:1],v[72:73],v[72:73]
v_pk_mul_f32 v[2:3],v[74:75],v[74:75]
v_pk_fma_f32 v[0:1],v[0:1],s[78:79],v[32:33]
v_pk_fma_f32 v[2:3],v[2:3],s[78:79],v[32:33]
v_pk_mul_f32 v[0:1],v[0:1],v[72:73]
v_pk_mul_f32 v[2:3],v[2:3],v[74:75]
v_exp_f32 v0,v0
v_exp_f32 v1,v1
v_exp_f32 v2,v2
v_exp_f32 v3,v3
v_pk_mul_f32 v[100:101],v[100:101],v[72:73]
v_pk_mul_f32 v[102:103],v[102:103],v[74:75]
v_pk_fma_f32 v[0:1],v[0:1],v[34:35],v[34:35] op_sel:[0,0,0] op_sel_hi:[1,0,0]
v_pk_fma_f32 v[2:3],v[2:3],v[34:35],v[34:35] op_sel:[0,0,0] op_sel_hi:[1,0,0]
v_rcp_f32 v0,v0
v_rcp_f32 v1,v1
v_rcp_f32 v2,v2
v_rcp_f32 v3,v3
v_pk_mul_f32 v[100:101],v[100:101],v[0:1]
v_pk_mul_f32 v[102:103],v[102:103],v[2:3]
v_cvt_pk_bf16_f32 v24,v100,v101
v_cvt_pk_bf16_f32 v25,v102,v103
)"
R"(v_pk_mul_f32 v[4:5],v[76:77],v[76:77]
v_pk_mul_f32 v[6:7],v[78:79],v[78:79]
v_pk_fma_f32 v[4:5],v[4:5],s[78:79],v[32:33]
v_pk_fma_f32 v[6:7],v[6:7],s[78:79],v[32:33]
v_pk_mul_f32 v[4:5],v[4:5],v[76:77]
v_pk_mul_f32 v[6:7],v[6:7],v[78:79]
v_exp_f32 v4,v4
v_exp_f32 v5,v5
v_exp_f32 v6,v6
v_exp_f32 v7,v7
v_pk_mul_f32 v[104:105],v[104:105],v[76:77]
v_pk_mul_f32 v[106:107],v[106:107],v[78:79]
v_pk_fma_f32 v[4:5],v[4:5],v[34:35],v[34:35] op_sel:[0,0,0] op_sel_hi:[1,0,0]
v_pk_fma_f32 v[6:7],v[6:7],v[34:35],v[34:35] op_sel:[0,0,0] op_sel_hi:[1,0,0]
v_rcp_f32 v4,v4
v_rcp_f32 v5,v5
v_rcp_f32 v6,v6
v_rcp_f32 v7,v7
v_pk_mul_f32 v[104:105],v[104:105],v[4:5]
v_pk_mul_f32 v[106:107],v[106:107],v[6:7]
v_cvt_pk_bf16_f32 v26,v104,v105
v_cvt_pk_bf16_f32 v27,v106,v107
buffer_store_dwordx4 v[24:27],v18,s[48:51],s99 offen offset:0 sc1
v_pk_mul_f32 v[0:1],v[128:129],v[128:129]
)"
R"(v_pk_mul_f32 v[2:3],v[130:131],v[130:131]
v_pk_fma_f32 v[0:1],v[0:1],s[78:79],v[32:33]
v_pk_fma_f32 v[2:3],v[2:3],s[78:79],v[32:33]
v_pk_mul_f32 v[0:1],v[0:1],v[128:129]
v_pk_mul_f32 v[2:3],v[2:3],v[130:131]
v_exp_f32 v0,v0
v_exp_f32 v1,v1
v_exp_f32 v2,v2
v_exp_f32 v3,v3
v_pk_mul_f32 v[156:157],v[156:157],v[128:129]
v_pk_mul_f32 v[158:159],v[158:159],v[130:131]
v_pk_fma_f32 v[0:1],v[0:1],v[34:35],v[34:35] op_sel:[0,1,1] op_sel_hi:[1,1,1]
v_pk_fma_f32 v[2:3],v[2:3],v[34:35],v[34:35] op_sel:[0,1,1] op_sel_hi:[1,1,1]
v_rcp_f32 v0,v0
v_rcp_f32 v1,v1
v_rcp_f32 v2,v2
v_rcp_f32 v3,v3
v_pk_mul_f32 v[156:157],v[156:157],v[0:1]
v_pk_mul_f32 v[158:159],v[158:159],v[2:3]
v_cvt_pk_bf16_f32 v28,v156,v157
v_cvt_pk_bf16_f32 v29,v158,v159
v_pk_mul_f32 v[4:5],v[132:133],v[132:133]
v_pk_mul_f32 v[6:7],v[134:135],v[134:135]
v_pk_fma_f32 v[4:5],v[4:5],s[78:79],v[32:33]
)"
R"(v_pk_fma_f32 v[6:7],v[6:7],s[78:79],v[32:33]
v_pk_mul_f32 v[4:5],v[4:5],v[132:133]
v_pk_mul_f32 v[6:7],v[6:7],v[134:135]
v_exp_f32 v4,v4
v_exp_f32 v5,v5
v_exp_f32 v6,v6
v_exp_f32 v7,v7
v_pk_mul_f32 v[160:161],v[160:161],v[132:133]
v_pk_mul_f32 v[162:163],v[162:163],v[134:135]
v_pk_fma_f32 v[4:5],v[4:5],v[34:35],v[34:35] op_sel:[0,1,1] op_sel_hi:[1,1,1]
v_pk_fma_f32 v[6:7],v[6:7],v[34:35],v[34:35] op_sel:[0,1,1] op_sel_hi:[1,1,1]
v_rcp_f32 v4,v4
v_rcp_f32 v5,v5
v_rcp_f32 v6,v6
v_rcp_f32 v7,v7
v_pk_mul_f32 v[160:161],v[160:161],v[4:5]
v_pk_mul_f32 v[162:163],v[162:163],v[6:7]
v_cvt_pk_bf16_f32 v30,v160,v161
v_cvt_pk_bf16_f32 v31,v162,v163
buffer_store_dwordx4 v[28:31],v18,s[48:51],s99 offen offset:1024 sc1
v_pk_mul_f32 v[0:1],v[184:185],v[184:185]
v_pk_mul_f32 v[2:3],v[186:187],v[186:187]
v_pk_fma_f32 v[0:1],v[0:1],s[78:79],v[32:33]
v_pk_fma_f32 v[2:3],v[2:3],s[78:79],v[32:33]
)"
R"(v_pk_mul_f32 v[0:1],v[0:1],v[184:185]
v_pk_mul_f32 v[2:3],v[2:3],v[186:187]
v_exp_f32 v0,v0
v_exp_f32 v1,v1
v_exp_f32 v2,v2
v_exp_f32 v3,v3
v_pk_mul_f32 v[212:213],v[212:213],v[184:185]
v_pk_mul_f32 v[214:215],v[214:215],v[186:187]
v_pk_fma_f32 v[0:1],v[0:1],v[36:37],v[36:37] op_sel:[0,0,0] op_sel_hi:[1,0,0]
v_pk_fma_f32 v[2:3],v[2:3],v[36:37],v[36:37] op_sel:[0,0,0] op_sel_hi:[1,0,0]
v_rcp_f32 v0,v0
v_rcp_f32 v1,v1
v_rcp_f32 v2,v2
v_rcp_f32 v3,v3
v_pk_mul_f32 v[212:213],v[212:213],v[0:1]
v_pk_mul_f32 v[214:215],v[214:215],v[2:3]
v_cvt_pk_bf16_f32 v24,v212,v213
v_cvt_pk_bf16_f32 v25,v214,v215
v_pk_mul_f32 v[4:5],v[188:189],v[188:189]
v_pk_mul_f32 v[6:7],v[190:191],v[190:191]
v_pk_fma_f32 v[4:5],v[4:5],s[78:79],v[32:33]
v_pk_fma_f32 v[6:7],v[6:7],s[78:79],v[32:33]
v_pk_mul_f32 v[4:5],v[4:5],v[188:189]
v_pk_mul_f32 v[6:7],v[6:7],v[190:191]
)"
R"(v_exp_f32 v4,v4
v_exp_f32 v5,v5
v_exp_f32 v6,v6
v_exp_f32 v7,v7
v_pk_mul_f32 v[216:217],v[216:217],v[188:189]
v_pk_mul_f32 v[218:219],v[218:219],v[190:191]
v_pk_fma_f32 v[4:5],v[4:5],v[36:37],v[36:37] op_sel:[0,0,0] op_sel_hi:[1,0,0]
v_pk_fma_f32 v[6:7],v[6:7],v[36:37],v[36:37] op_sel:[0,0,0] op_sel_hi:[1,0,0]
v_rcp_f32 v4,v4
v_rcp_f32 v5,v5
v_rcp_f32 v6,v6
v_rcp_f32 v7,v7
v_pk_mul_f32 v[216:217],v[216:217],v[4:5]
v_pk_mul_f32 v[218:219],v[218:219],v[6:7]
v_cvt_pk_bf16_f32 v26,v216,v217
v_cvt_pk_bf16_f32 v27,v218,v219
buffer_store_dwordx4 v[24:27],v18,s[48:51],s99 offen offset:2048 sc1
v_accvgpr_read_b32 v8,a204
v_accvgpr_read_b32 v9,a205
v_accvgpr_read_b32 v10,a206
v_accvgpr_read_b32 v11,a207
v_pk_mul_f32 v[0:1],v[240:241],v[240:241]
v_pk_mul_f32 v[2:3],v[242:243],v[242:243]
v_pk_fma_f32 v[0:1],v[0:1],s[78:79],v[32:33]
)"
R"(v_pk_fma_f32 v[2:3],v[2:3],s[78:79],v[32:33]
v_pk_mul_f32 v[0:1],v[0:1],v[240:241]
v_pk_mul_f32 v[2:3],v[2:3],v[242:243]
v_exp_f32 v0,v0
v_exp_f32 v1,v1
v_exp_f32 v2,v2
v_exp_f32 v3,v3
v_pk_mul_f32 v[8:9],v[8:9],v[240:241]
v_pk_mul_f32 v[10:11],v[10:11],v[242:243]
v_pk_fma_f32 v[0:1],v[0:1],v[36:37],v[36:37] op_sel:[0,1,1] op_sel_hi:[1,1,1]
v_pk_fma_f32 v[2:3],v[2:3],v[36:37],v[36:37] op_sel:[0,1,1] op_sel_hi:[1,1,1]
v_rcp_f32 v0,v0
v_rcp_f32 v1,v1
v_rcp_f32 v2,v2
v_rcp_f32 v3,v3
v_pk_mul_f32 v[8:9],v[8:9],v[0:1]
v_pk_mul_f32 v[10:11],v[10:11],v[2:3]
v_cvt_pk_bf16_f32 v28,v8,v9
v_cvt_pk_bf16_f32 v29,v10,v11
v_accvgpr_read_b32 v12,a208
v_accvgpr_read_b32 v13,a209
v_accvgpr_read_b32 v14,a210
v_accvgpr_read_b32 v15,a211
v_pk_mul_f32 v[4:5],v[244:245],v[244:245]
)"
R"(v_pk_mul_f32 v[6:7],v[246:247],v[246:247]
v_pk_fma_f32 v[4:5],v[4:5],s[78:79],v[32:33]
v_pk_fma_f32 v[6:7],v[6:7],s[78:79],v[32:33]
v_pk_mul_f32 v[4:5],v[4:5],v[244:245]
v_pk_mul_f32 v[6:7],v[6:7],v[246:247]
v_exp_f32 v4,v4
v_exp_f32 v5,v5
v_exp_f32 v6,v6
v_exp_f32 v7,v7
v_pk_mul_f32 v[12:13],v[12:13],v[244:245]
v_pk_mul_f32 v[14:15],v[14:15],v[246:247]
v_pk_fma_f32 v[4:5],v[4:5],v[36:37],v[36:37] op_sel:[0,1,1] op_sel_hi:[1,1,1]
v_pk_fma_f32 v[6:7],v[6:7],v[36:37],v[36:37] op_sel:[0,1,1] op_sel_hi:[1,1,1]
v_rcp_f32 v4,v4
v_rcp_f32 v5,v5
v_rcp_f32 v6,v6
v_rcp_f32 v7,v7
v_pk_mul_f32 v[12:13],v[12:13],v[4:5]
v_pk_mul_f32 v[14:15],v[14:15],v[6:7]
v_cvt_pk_bf16_f32 v30,v12,v13
v_cvt_pk_bf16_f32 v31,v14,v15
buffer_store_dwordx4 v[28:31],v18,s[48:51],s99 offen offset:3072 sc1
s_add_u32 s99,s84,16384
v_pk_mul_f32 v[0:1],v[80:81],v[80:81]
)"
R"(v_pk_mul_f32 v[2:3],v[82:83],v[82:83]
v_pk_fma_f32 v[0:1],v[0:1],s[78:79],v[32:33]
v_pk_fma_f32 v[2:3],v[2:3],s[78:79],v[32:33]
v_pk_mul_f32 v[0:1],v[0:1],v[80:81]
v_pk_mul_f32 v[2:3],v[2:3],v[82:83]
v_exp_f32 v0,v0
v_exp_f32 v1,v1
v_exp_f32 v2,v2
v_exp_f32 v3,v3
v_pk_mul_f32 v[108:109],v[108:109],v[80:81]
v_pk_mul_f32 v[110:111],v[110:111],v[82:83]
v_pk_fma_f32 v[0:1],v[0:1],v[34:35],v[34:35] op_sel:[0,0,0] op_sel_hi:[1,0,0]
v_pk_fma_f32 v[2:3],v[2:3],v[34:35],v[34:35] op_sel:[0,0,0] op_sel_hi:[1,0,0]
v_rcp_f32 v0,v0
v_rcp_f32 v1,v1
v_rcp_f32 v2,v2
v_rcp_f32 v3,v3
v_pk_mul_f32 v[108:109],v[108:109],v[0:1]
v_pk_mul_f32 v[110:111],v[110:111],v[2:3]
v_cvt_pk_bf16_f32 v24,v108,v109
v_cvt_pk_bf16_f32 v25,v110,v111
v_pk_mul_f32 v[4:5],v[84:85],v[84:85]
v_pk_mul_f32 v[6:7],v[86:87],v[86:87]
v_pk_fma_f32 v[4:5],v[4:5],s[78:79],v[32:33]
)"
R"(v_pk_fma_f32 v[6:7],v[6:7],s[78:79],v[32:33]
v_pk_mul_f32 v[4:5],v[4:5],v[84:85]
v_pk_mul_f32 v[6:7],v[6:7],v[86:87]
v_exp_f32 v4,v4
v_exp_f32 v5,v5
v_exp_f32 v6,v6
v_exp_f32 v7,v7
v_pk_mul_f32 v[112:113],v[112:113],v[84:85]
v_pk_mul_f32 v[114:115],v[114:115],v[86:87]
v_pk_fma_f32 v[4:5],v[4:5],v[34:35],v[34:35] op_sel:[0,0,0] op_sel_hi:[1,0,0]
v_pk_fma_f32 v[6:7],v[6:7],v[34:35],v[34:35] op_sel:[0,0,0] op_sel_hi:[1,0,0]
v_rcp_f32 v4,v4
v_rcp_f32 v5,v5
v_rcp_f32 v6,v6
v_rcp_f32 v7,v7
v_pk_mul_f32 v[112:113],v[112:113],v[4:5]
v_pk_mul_f32 v[114:115],v[114:115],v[6:7]
v_cvt_pk_bf16_f32 v26,v112,v113
v_cvt_pk_bf16_f32 v27,v114,v115
buffer_store_dwordx4 v[24:27],v18,s[48:51],s99 offen offset:0 sc1
v_pk_mul_f32 v[0:1],v[136:137],v[136:137]
v_pk_mul_f32 v[2:3],v[138:139],v[138:139]
v_pk_fma_f32 v[0:1],v[0:1],s[78:79],v[32:33]
v_pk_fma_f32 v[2:3],v[2:3],s[78:79],v[32:33]
)"
R"(v_pk_mul_f32 v[0:1],v[0:1],v[136:137]
v_pk_mul_f32 v[2:3],v[2:3],v[138:139]
v_exp_f32 v0,v0
v_exp_f32 v1,v1
v_exp_f32 v2,v2
v_exp_f32 v3,v3
v_pk_mul_f32 v[164:165],v[164:165],v[136:137]
v_pk_mul_f32 v[166:167],v[166:167],v[138:139]
v_pk_fma_f32 v[0:1],v[0:1],v[34:35],v[34:35] op_sel:[0,1,1] op_sel_hi:[1,1,1]
v_pk_fma_f32 v[2:3],v[2:3],v[34:35],v[34:35] op_sel:[0,1,1] op_sel_hi:[1,1,1]
v_rcp_f32 v0,v0
v_rcp_f32 v1,v1
v_rcp_f32 v2,v2
v_rcp_f32 v3,v3
v_pk_mul_f32 v[164:165],v[164:165],v[0:1]
v_pk_mul_f32 v[166:167],v[166:167],v[2:3]
v_cvt_pk_bf16_f32 v28,v164,v165
v_cvt_pk_bf16_f32 v29,v166,v167
v_pk_mul_f32 v[4:5],v[140:141],v[140:141]
v_pk_mul_f32 v[6:7],v[142:143],v[142:143]
v_pk_fma_f32 v[4:5],v[4:5],s[78:79],v[32:33]
v_pk_fma_f32 v[6:7],v[6:7],s[78:79],v[32:33]
v_pk_mul_f32 v[4:5],v[4:5],v[140:141]
v_pk_mul_f32 v[6:7],v[6:7],v[142:143]
)"
R"(v_exp_f32 v4,v4
v_exp_f32 v5,v5
v_exp_f32 v6,v6
v_exp_f32 v7,v7
v_pk_mul_f32 v[168:169],v[168:169],v[140:141]
v_pk_mul_f32 v[170:171],v[170:171],v[142:143]
v_pk_fma_f32 v[4:5],v[4:5],v[34:35],v[34:35] op_sel:[0,1,1] op_sel_hi:[1,1,1]
v_pk_fma_f32 v[6:7],v[6:7],v[34:35],v[34:35] op_sel:[0,1,1] op_sel_hi:[1,1,1]
v_rcp_f32 v4,v4
v_rcp_f32 v5,v5
v_rcp_f32 v6,v6
v_rcp_f32 v7,v7
v_pk_mul_f32 v[168:169],v[168:169],v[4:5]
v_pk_mul_f32 v[170:171],v[170:171],v[6:7]
v_cvt_pk_bf16_f32 v30,v168,v169
v_cvt_pk_bf16_f32 v31,v170,v171
buffer_store_dwordx4 v[28:31],v18,s[48:51],s99 offen offset:1024 sc1
v_pk_mul_f32 v[0:1],v[192:193],v[192:193]
v_pk_mul_f32 v[2:3],v[194:195],v[194:195]
v_pk_fma_f32 v[0:1],v[0:1],s[78:79],v[32:33]
v_pk_fma_f32 v[2:3],v[2:3],s[78:79],v[32:33]
v_pk_mul_f32 v[0:1],v[0:1],v[192:193]
v_pk_mul_f32 v[2:3],v[2:3],v[194:195]
v_exp_f32 v0,v0
)"
R"(v_exp_f32 v1,v1
v_exp_f32 v2,v2
v_exp_f32 v3,v3
v_pk_mul_f32 v[220:221],v[220:221],v[192:193]
v_pk_mul_f32 v[222:223],v[222:223],v[194:195]
v_pk_fma_f32 v[0:1],v[0:1],v[36:37],v[36:37] op_sel:[0,0,0] op_sel_hi:[1,0,0]
v_pk_fma_f32 v[2:3],v[2:3],v[36:37],v[36:37] op_sel:[0,0,0] op_sel_hi:[1,0,0]
v_rcp_f32 v0,v0
v_rcp_f32 v1,v1
v_rcp_f32 v2,v2
v_rcp_f32 v3,v3
v_pk_mul_f32 v[220:221],v[220:221],v[0:1]
v_pk_mul_f32 v[222:223],v[222:223],v[2:3]
v_cvt_pk_bf16_f32 v24,v220,v221
v_cvt_pk_bf16_f32 v25,v222,v223
v_pk_mul_f32 v[4:5],v[196:197],v[196:197]
v_pk_mul_f32 v[6:7],v[198:199],v[198:199]
v_pk_fma_f32 v[4:5],v[4:5],s[78:79],v[32:33]
v_pk_fma_f32 v[6:7],v[6:7],s[78:79],v[32:33]
v_pk_mul_f32 v[4:5],v[4:5],v[196:197]
v_pk_mul_f32 v[6:7],v[6:7],v[198:199]
v_exp_f32 v4,v4
v_exp_f32 v5,v5
v_exp_f32 v6,v6
)"
R"(v_exp_f32 v7,v7
v_pk_mul_f32 v[224:225],v[224:225],v[196:197]
v_pk_mul_f32 v[226:227],v[226:227],v[198:199]
v_pk_fma_f32 v[4:5],v[4:5],v[36:37],v[36:37] op_sel:[0,0,0] op_sel_hi:[1,0,0]
v_pk_fma_f32 v[6:7],v[6:7],v[36:37],v[36:37] op_sel:[0,0,0] op_sel_hi:[1,0,0]
v_rcp_f32 v4,v4
v_rcp_f32 v5,v5
v_rcp_f32 v6,v6
v_rcp_f32 v7,v7
v_pk_mul_f32 v[224:225],v[224:225],v[4:5]
v_pk_mul_f32 v[226:227],v[226:227],v[6:7]
v_cvt_pk_bf16_f32 v26,v224,v225
v_cvt_pk_bf16_f32 v27,v226,v227
buffer_store_dwordx4 v[24:27],v18,s[48:51],s99 offen offset:2048 sc1
v_accvgpr_read_b32 v8,a212
v_accvgpr_read_b32 v9,a213
v_accvgpr_read_b32 v10,a214
v_accvgpr_read_b32 v11,a215
v_pk_mul_f32 v[0:1],v[248:249],v[248:249]
v_pk_mul_f32 v[2:3],v[250:251],v[250:251]
v_pk_fma_f32 v[0:1],v[0:1],s[78:79],v[32:33]
v_pk_fma_f32 v[2:3],v[2:3],s[78:79],v[32:33]
v_pk_mul_f32 v[0:1],v[0:1],v[248:249]
v_pk_mul_f32 v[2:3],v[2:3],v[250:251]
)"
R"(v_exp_f32 v0,v0
v_exp_f32 v1,v1
v_exp_f32 v2,v2
v_exp_f32 v3,v3
v_pk_mul_f32 v[8:9],v[8:9],v[248:249]
v_pk_mul_f32 v[10:11],v[10:11],v[250:251]
v_pk_fma_f32 v[0:1],v[0:1],v[36:37],v[36:37] op_sel:[0,1,1] op_sel_hi:[1,1,1]
v_pk_fma_f32 v[2:3],v[2:3],v[36:37],v[36:37] op_sel:[0,1,1] op_sel_hi:[1,1,1]
v_rcp_f32 v0,v0
v_rcp_f32 v1,v1
v_rcp_f32 v2,v2
v_rcp_f32 v3,v3
v_pk_mul_f32 v[8:9],v[8:9],v[0:1]
v_pk_mul_f32 v[10:11],v[10:11],v[2:3]
v_cvt_pk_bf16_f32 v28,v8,v9
v_cvt_pk_bf16_f32 v29,v10,v11
v_accvgpr_read_b32 v12,a216
v_accvgpr_read_b32 v13,a217
v_accvgpr_read_b32 v14,a218
v_accvgpr_read_b32 v15,a219
v_pk_mul_f32 v[4:5],v[252:253],v[252:253]
v_pk_mul_f32 v[6:7],v[254:255],v[254:255]
v_pk_fma_f32 v[4:5],v[4:5],s[78:79],v[32:33]
v_pk_fma_f32 v[6:7],v[6:7],s[78:79],v[32:33]
)"
R"(v_pk_mul_f32 v[4:5],v[4:5],v[252:253]
v_pk_mul_f32 v[6:7],v[6:7],v[254:255]
v_exp_f32 v4,v4
v_exp_f32 v5,v5
v_exp_f32 v6,v6
v_exp_f32 v7,v7
v_pk_mul_f32 v[12:13],v[12:13],v[252:253]
v_pk_mul_f32 v[14:15],v[14:15],v[254:255]
v_pk_fma_f32 v[4:5],v[4:5],v[36:37],v[36:37] op_sel:[0,1,1] op_sel_hi:[1,1,1]
v_pk_fma_f32 v[6:7],v[6:7],v[36:37],v[36:37] op_sel:[0,1,1] op_sel_hi:[1,1,1]
v_rcp_f32 v4,v4
v_rcp_f32 v5,v5
v_rcp_f32 v6,v6
v_rcp_f32 v7,v7
v_pk_mul_f32 v[12:13],v[12:13],v[4:5]
v_pk_mul_f32 v[14:15],v[14:15],v[6:7]
v_cvt_pk_bf16_f32 v30,v12,v13
v_cvt_pk_bf16_f32 v31,v14,v15
buffer_store_dwordx4 v[28:31],v18,s[48:51],s99 offen offset:3072 sc1
v_pk_mul_f32 v[0:1],v[88:89],v[88:89]
v_pk_mul_f32 v[2:3],v[90:91],v[90:91]
v_pk_fma_f32 v[0:1],v[0:1],s[78:79],v[32:33]
v_pk_fma_f32 v[2:3],v[2:3],s[78:79],v[32:33]
v_pk_mul_f32 v[0:1],v[0:1],v[88:89]
)"
R"(v_pk_mul_f32 v[2:3],v[2:3],v[90:91]
v_exp_f32 v0,v0
v_exp_f32 v1,v1
v_exp_f32 v2,v2
v_exp_f32 v3,v3
v_pk_mul_f32 v[116:117],v[116:117],v[88:89]
v_pk_mul_f32 v[118:119],v[118:119],v[90:91]
v_pk_fma_f32 v[0:1],v[0:1],v[34:35],v[34:35] op_sel:[0,0,0] op_sel_hi:[1,0,0]
v_pk_fma_f32 v[2:3],v[2:3],v[34:35],v[34:35] op_sel:[0,0,0] op_sel_hi:[1,0,0]
v_rcp_f32 v0,v0
v_rcp_f32 v1,v1
v_rcp_f32 v2,v2
v_rcp_f32 v3,v3
v_pk_mul_f32 v[116:117],v[116:117],v[0:1]
v_pk_mul_f32 v[118:119],v[118:119],v[2:3]
v_cvt_pk_bf16_f32 v24,v116,v117
v_cvt_pk_bf16_f32 v25,v118,v119
buffer_store_dwordx2 v[24:25],v18,s[48:51],s85 offen offset:0 sc1
v_pk_mul_f32 v[4:5],v[144:145],v[144:145]
v_pk_mul_f32 v[6:7],v[146:147],v[146:147]
v_pk_fma_f32 v[4:5],v[4:5],s[78:79],v[32:33]
v_pk_fma_f32 v[6:7],v[6:7],s[78:79],v[32:33]
v_pk_mul_f32 v[4:5],v[4:5],v[144:145]
v_pk_mul_f32 v[6:7],v[6:7],v[146:147]
)"
R"(v_exp_f32 v4,v4
v_exp_f32 v5,v5
v_exp_f32 v6,v6
v_exp_f32 v7,v7
v_pk_mul_f32 v[172:173],v[172:173],v[144:145]
v_pk_mul_f32 v[174:175],v[174:175],v[146:147]
v_pk_fma_f32 v[4:5],v[4:5],v[34:35],v[34:35] op_sel:[0,1,1] op_sel_hi:[1,1,1]
v_pk_fma_f32 v[6:7],v[6:7],v[34:35],v[34:35] op_sel:[0,1,1] op_sel_hi:[1,1,1]
v_rcp_f32 v4,v4
v_rcp_f32 v5,v5
v_rcp_f32 v6,v6
v_rcp_f32 v7,v7
v_pk_mul_f32 v[172:173],v[172:173],v[4:5]
v_pk_mul_f32 v[174:175],v[174:175],v[6:7]
v_cvt_pk_bf16_f32 v28,v172,v173
v_cvt_pk_bf16_f32 v29,v174,v175
buffer_store_dwordx2 v[28:29],v18,s[48:51],s85 offen offset:1024 sc1
v_pk_mul_f32 v[0:1],v[200:201],v[200:201]
v_pk_mul_f32 v[2:3],v[202:203],v[202:203]
v_pk_fma_f32 v[0:1],v[0:1],s[78:79],v[32:33]
v_pk_fma_f32 v[2:3],v[2:3],s[78:79],v[32:33]
v_pk_mul_f32 v[0:1],v[0:1],v[200:201]
v_pk_mul_f32 v[2:3],v[2:3],v[202:203]
v_exp_f32 v0,v0
)"
R"(v_exp_f32 v1,v1
v_exp_f32 v2,v2
v_exp_f32 v3,v3
v_pk_mul_f32 v[228:229],v[228:229],v[200:201]
v_pk_mul_f32 v[230:231],v[230:231],v[202:203]
v_pk_fma_f32 v[0:1],v[0:1],v[36:37],v[36:37] op_sel:[0,0,0] op_sel_hi:[1,0,0]
v_pk_fma_f32 v[2:3],v[2:3],v[36:37],v[36:37] op_sel:[0,0,0] op_sel_hi:[1,0,0]
v_rcp_f32 v0,v0
v_rcp_f32 v1,v1
v_rcp_f32 v2,v2
v_rcp_f32 v3,v3
v_pk_mul_f32 v[228:229],v[228:229],v[0:1]
v_pk_mul_f32 v[230:231],v[230:231],v[2:3]
v_cvt_pk_bf16_f32 v24,v228,v229
v_cvt_pk_bf16_f32 v25,v230,v231
buffer_store_dwordx2 v[24:25],v18,s[48:51],s85 offen offset:2048 sc1
v_accvgpr_read_b32 v8,a192
v_accvgpr_read_b32 v9,a193
v_accvgpr_read_b32 v10,a194
v_accvgpr_read_b32 v11,a195
v_accvgpr_read_b32 v12,a220
v_accvgpr_read_b32 v13,a221
v_accvgpr_read_b32 v14,a222
v_accvgpr_read_b32 v15,a223
)"
R"(v_pk_mul_f32 v[4:5],v[8:9],v[8:9]
v_pk_mul_f32 v[6:7],v[10:11],v[10:11]
v_pk_fma_f32 v[4:5],v[4:5],s[78:79],v[32:33]
v_pk_fma_f32 v[6:7],v[6:7],s[78:79],v[32:33]
v_pk_mul_f32 v[4:5],v[4:5],v[8:9]
v_pk_mul_f32 v[6:7],v[6:7],v[10:11]
v_exp_f32 v4,v4
v_exp_f32 v5,v5
v_exp_f32 v6,v6
v_exp_f32 v7,v7
v_pk_mul_f32 v[12:13],v[12:13],v[8:9]
v_pk_mul_f32 v[14:15],v[14:15],v[10:11]
v_pk_fma_f32 v[4:5],v[4:5],v[36:37],v[36:37] op_sel:[0,1,1] op_sel_hi:[1,1,1]
v_pk_fma_f32 v[6:7],v[6:7],v[36:37],v[36:37] op_sel:[0,1,1] op_sel_hi:[1,1,1]
v_rcp_f32 v4,v4
v_rcp_f32 v5,v5
v_rcp_f32 v6,v6
v_rcp_f32 v7,v7
v_pk_mul_f32 v[12:13],v[12:13],v[4:5]
v_pk_mul_f32 v[14:15],v[14:15],v[6:7]
v_cvt_pk_bf16_f32 v28,v12,v13
v_cvt_pk_bf16_f32 v29,v14,v15
buffer_store_dwordx2 v[28:29],v18,s[48:51],s85 offen offset:3072 sc1
s_mov_b32 s74,28672
)"
R"(v_not_b32 v56,s72
v_not_b32 v57,s72
v_not_b32 v58,s72
v_not_b32 v59,s72
s_waitcnt lgkmcnt(0)
v_mfma_f32_16x16x32_bf16 v[64:67],a[96:99],a[224:227],0
s_waitcnt vmcnt(36)
s_barrier
v_mfma_f32_16x16x32_bf16 v[64:67],a[128:131],a[240:243],v[64:67]
s_add_u32 m0,s25,49152
s_add_u32 s12,s87,s80
ds_read_b64_tr_b16 a[160:161],v44 offset:16384
v_mfma_f32_16x16x32_bf16 v[96:99],a[96:99],a[228:231],0
ds_read_b64_tr_b16 a[162:163],v44 offset:20480
v_mfma_f32_16x16x32_bf16 v[96:99],a[128:131],a[244:247],v[96:99]
ds_read_b64_tr_b16 a[164:165],v45 offset:16384
v_mfma_f32_16x16x32_bf16 v[128:131],a[96:99],a[232:235],0
buffer_load_dwordx4 v39,s[44:47],s12 offen lds
v_mfma_f32_16x16x32_bf16 v[128:131],a[128:131],a[248:251],v[128:131]
ds_read_b64_tr_b16 a[166:167],v45 offset:20480
v_mfma_f32_16x16x32_bf16 v[160:163],a[96:99],a[236:239],0
s_add_u32 m0,s25,50176
s_add_u32 s15,s87,s81
ds_read_b64_tr_b16 a[168:169],v46 offset:16384
)"
R"(v_mfma_f32_16x16x32_bf16 v[160:163],a[128:131],a[252:255],v[160:163]
ds_read_b64_tr_b16 a[170:171],v46 offset:20480
v_mfma_f32_16x16x32_bf16 v[164:167],a[100:103],a[236:239],0
ds_read_b64_tr_b16 a[172:173],v47 offset:16384
v_mfma_f32_16x16x32_bf16 v[164:167],a[132:135],a[252:255],v[164:167]
buffer_load_dwordx4 v55,s[44:47],s15 offen lds
v_mfma_f32_16x16x32_bf16 v[132:135],a[100:103],a[232:235],0
ds_read_b64_tr_b16 a[174:175],v47 offset:20480
s_branch gm_KB_sk24
.p2align 12
gm_KB_pg24:
s_setpc_b64 s[100:101]
gm_KB_sk24:
v_mfma_f32_16x16x32_bf16 v[132:135],a[132:135],a[248:251],v[132:135]
s_add_u32 m0,s25,51200
s_add_u32 s12,s87,s82
ds_read_b64_tr_b16 a[176:177],v48 offset:16384
v_mfma_f32_16x16x32_bf16 v[100:103],a[100:103],a[228:231],0
ds_read_b64_tr_b16 a[178:179],v48 offset:20480
v_mfma_f32_16x16x32_bf16 v[100:103],a[132:135],a[244:247],v[100:103]
v_mfma_f32_16x16x32_bf16 v[68:71],a[100:103],a[224:227],0
buffer_load_dwordx4 v39,s[44:47],s12 offen lds
v_mfma_f32_16x16x32_bf16 v[68:71],a[132:135],a[240:243],v[68:71]
ds_read_b64_tr_b16 a[180:181],v49 offset:16384
)"
R"(v_mfma_f32_16x16x32_bf16 v[72:75],a[104:107],a[224:227],0
s_add_u32 m0,s25,52224
s_add_u32 s15,s87,s83
ds_read_b64_tr_b16 a[182:183],v49 offset:20480
v_mfma_f32_16x16x32_bf16 v[72:75],a[136:139],a[240:243],v[72:75]
ds_read_b64_tr_b16 a[184:185],v50 offset:16384
v_mfma_f32_16x16x32_bf16 v[104:107],a[104:107],a[228:231],0
ds_read_b64_tr_b16 a[186:187],v50 offset:20480
v_mfma_f32_16x16x32_bf16 v[104:107],a[136:139],a[244:247],v[104:107]
buffer_load_dwordx4 v55,s[44:47],s15 offen lds
v_mfma_f32_16x16x32_bf16 v[136:139],a[104:107],a[232:235],0
ds_read_b64_tr_b16 a[188:189],v51 offset:16384
v_mfma_f32_16x16x32_bf16 v[136:139],a[136:139],a[248:251],v[136:139]
s_add_u32 s87,s87,131072
ds_read_b64_tr_b16 a[190:191],v51 offset:20480
v_mfma_f32_16x16x32_bf16 v[168:171],a[104:107],a[236:239],0
ds_read_b64_tr_b16 a[192:193],v44 offset:24576
v_mfma_f32_16x16x32_bf16 v[168:171],a[136:139],a[252:255],v[168:171]
ds_read_b64_tr_b16 a[194:195],v44 offset:28672
v_mfma_f32_16x16x32_bf16 v[172:175],a[108:111],a[236:239],0
buffer_load_dwordx4 a[32:35],v40,s[36:39],s75 offen sc1
v_mfma_f32_16x16x32_bf16 v[172:175],a[140:143],a[252:255],v[172:175]
ds_read_b64_tr_b16 a[196:197],v45 offset:24576
v_mfma_f32_16x16x32_bf16 v[140:143],a[108:111],a[232:235],0
)"
R"(ds_read_b64_tr_b16 a[198:199],v45 offset:28672
v_mfma_f32_16x16x32_bf16 v[140:143],a[140:143],a[248:251],v[140:143]
ds_read_b64_tr_b16 a[200:201],v46 offset:24576
v_mfma_f32_16x16x32_bf16 v[108:111],a[108:111],a[228:231],0
ds_read_b64_tr_b16 a[202:203],v46 offset:28672
v_mfma_f32_16x16x32_bf16 v[108:111],a[140:143],a[244:247],v[108:111]
buffer_load_dwordx4 a[36:39],v41,s[36:39],s75 offen sc1
v_mfma_f32_16x16x32_bf16 v[76:79],a[108:111],a[224:227],0
ds_read_b64_tr_b16 a[204:205],v47 offset:24576
v_mfma_f32_16x16x32_bf16 v[76:79],a[140:143],a[240:243],v[76:79]
ds_read_b64_tr_b16 a[206:207],v47 offset:28672
v_mfma_f32_16x16x32_bf16 v[80:83],a[112:115],a[224:227],0
ds_read_b64_tr_b16 a[208:209],v48 offset:24576
v_mfma_f32_16x16x32_bf16 v[80:83],a[144:147],a[240:243],v[80:83]
ds_read_b64_tr_b16 a[210:211],v48 offset:28672
v_mfma_f32_16x16x32_bf16 v[112:115],a[112:115],a[228:231],0
buffer_load_dwordx4 a[40:43],v42,s[36:39],s75 offen sc1
v_mfma_f32_16x16x32_bf16 v[112:115],a[144:147],a[244:247],v[112:115]
ds_read_b64_tr_b16 a[212:213],v49 offset:24576
v_mfma_f32_16x16x32_bf16 v[144:147],a[112:115],a[232:235],0
ds_read_b64_tr_b16 a[214:215],v49 offset:28672
v_mfma_f32_16x16x32_bf16 v[144:147],a[144:147],a[248:251],v[144:147]
ds_read_b64_tr_b16 a[216:217],v50 offset:24576
v_mfma_f32_16x16x32_bf16 v[176:179],a[112:115],a[236:239],0
)"
R"(ds_read_b64_tr_b16 a[218:219],v50 offset:28672
v_mfma_f32_16x16x32_bf16 v[176:179],a[144:147],a[252:255],v[176:179]
buffer_load_dwordx4 a[44:47],v43,s[36:39],s75 offen sc1
v_mfma_f32_16x16x32_bf16 v[180:183],a[116:119],a[236:239],0
ds_read_b64_tr_b16 a[220:221],v51 offset:24576
v_mfma_f32_16x16x32_bf16 v[180:183],a[148:151],a[252:255],v[180:183]
ds_read_b64_tr_b16 a[222:223],v51 offset:28672
v_mfma_f32_16x16x32_bf16 v[148:151],a[116:119],a[232:235],0
v_mfma_f32_16x16x32_bf16 v[148:151],a[148:151],a[248:251],v[148:151]
v_mfma_f32_16x16x32_bf16 v[116:119],a[116:119],a[228:231],0
buffer_load_dwordx4 a[48:51],v40,s[36:39],s24 offen sc1
v_mfma_f32_16x16x32_bf16 v[116:119],a[148:151],a[244:247],v[116:119]
v_mfma_f32_16x16x32_bf16 v[84:87],a[116:119],a[224:227],0
v_mfma_f32_16x16x32_bf16 v[84:87],a[148:151],a[240:243],v[84:87]
v_mfma_f32_16x16x32_bf16 v[88:91],a[120:123],a[224:227],0
v_mfma_f32_16x16x32_bf16 v[88:91],a[152:155],a[240:243],v[88:91]
buffer_load_dwordx4 a[52:55],v41,s[36:39],s24 offen sc1
v_mfma_f32_16x16x32_bf16 v[120:123],a[120:123],a[228:231],0
v_mfma_f32_16x16x32_bf16 v[120:123],a[152:155],a[244:247],v[120:123]
v_mfma_f32_16x16x32_bf16 v[152:155],a[120:123],a[232:235],0
v_mfma_f32_16x16x32_bf16 v[152:155],a[152:155],a[248:251],v[152:155]
v_mfma_f32_16x16x32_bf16 v[184:187],a[120:123],a[236:239],0
buffer_load_dwordx4 a[56:59],v42,s[36:39],s24 offen sc1
v_mfma_f32_16x16x32_bf16 v[184:187],a[152:155],a[252:255],v[184:187]
)"
R"(v_mfma_f32_16x16x32_bf16 v[188:191],a[124:127],a[236:239],0
v_mfma_f32_16x16x32_bf16 v[188:191],a[156:159],a[252:255],v[188:191]
v_mfma_f32_16x16x32_bf16 v[156:159],a[124:127],a[232:235],0
v_mfma_f32_16x16x32_bf16 v[156:159],a[156:159],a[248:251],v[156:159]
buffer_load_dwordx4 a[60:63],v43,s[36:39],s24 offen sc1
s_add_u32 s75,s75,16384
v_mfma_f32_16x16x32_bf16 v[124:127],a[124:127],a[228:231],0
s_add_u32 s24,s24,16384
v_mfma_f32_16x16x32_bf16 v[124:127],a[156:159],a[244:247],v[124:127]
v_mfma_f32_16x16x32_bf16 v[92:95],a[124:127],a[224:227],0
s_waitcnt vmcnt(40) lgkmcnt(0)
v_mfma_f32_16x16x32_bf16 v[92:95],a[156:159],a[240:243],v[92:95]
s_mov_b32 s32,0
s_mov_b32 s33,3
s_cmp_eq_u32 %2,0
s_cselect_b32 s34,0,-1
s_cselect_b32 s35,2,-1
s_branch gm_KB_sk25
.p2align 12
gm_KB_pg25:
s_setpc_b64 s[100:101]
s_nop 0
gm_KB_sk25:
gm_K3_loop:
)"
R"(v_mfma_f32_16x16x32_bf16 v[64:67],a[160:163],a[0:3],v[64:67]
s_waitcnt vmcnt(20)
s_barrier
v_mfma_f32_16x16x32_bf16 v[64:67],a[192:195],a[16:19],v[64:67]
s_add_u32 m0,s25,0
s_add_u32 s12,s87,s80
ds_read_b64_tr_b16 a[96:97],v44 offset:32768
v_mfma_f32_16x16x32_bf16 v[96:99],a[160:163],a[4:7],v[96:99]
ds_read_b64_tr_b16 a[98:99],v44 offset:36864
v_mfma_f32_16x16x32_bf16 v[96:99],a[192:195],a[20:23],v[96:99]
ds_read_b64_tr_b16 a[100:101],v45 offset:32768
v_mfma_f32_16x16x32_bf16 v[128:131],a[160:163],a[8:11],v[128:131]
buffer_load_dwordx4 v39,s[44:47],s12 offen lds
v_mfma_f32_16x16x32_bf16 v[128:131],a[192:195],a[24:27],v[128:131]
ds_read_b64_tr_b16 a[102:103],v45 offset:36864
v_mfma_f32_16x16x32_bf16 v[160:163],a[160:163],a[12:15],v[160:163]
s_add_u32 m0,s25,1024
s_add_u32 s15,s87,s81
ds_read_b64_tr_b16 a[104:105],v46 offset:32768
v_mfma_f32_16x16x32_bf16 v[160:163],a[192:195],a[28:31],v[160:163]
ds_read_b64_tr_b16 a[106:107],v46 offset:36864
v_mfma_f32_16x16x32_bf16 v[164:167],a[164:167],a[12:15],v[164:167]
ds_read_b64_tr_b16 a[108:109],v47 offset:32768
v_mfma_f32_16x16x32_bf16 v[164:167],a[196:199],a[28:31],v[164:167]
)"
R"(buffer_load_dwordx4 v55,s[44:47],s15 offen lds
v_mfma_f32_16x16x32_bf16 v[132:135],a[164:167],a[8:11],v[132:135]
ds_read_b64_tr_b16 a[110:111],v47 offset:36864
v_mfma_f32_16x16x32_bf16 v[132:135],a[196:199],a[24:27],v[132:135]
s_add_u32 m0,s25,2048
s_add_u32 s12,s87,s82
ds_read_b64_tr_b16 a[112:113],v48 offset:32768
v_mfma_f32_16x16x32_bf16 v[100:103],a[164:167],a[4:7],v[100:103]
ds_read_b64_tr_b16 a[114:115],v48 offset:36864
v_mfma_f32_16x16x32_bf16 v[100:103],a[196:199],a[20:23],v[100:103]
v_mfma_f32_16x16x32_bf16 v[68:71],a[164:167],a[0:3],v[68:71]
buffer_load_dwordx4 v39,s[44:47],s12 offen lds
v_mfma_f32_16x16x32_bf16 v[68:71],a[196:199],a[16:19],v[68:71]
ds_read_b64_tr_b16 a[116:117],v49 offset:32768
v_mfma_f32_16x16x32_bf16 v[72:75],a[168:171],a[0:3],v[72:75]
s_add_u32 m0,s25,3072
s_add_u32 s15,s87,s83
ds_read_b64_tr_b16 a[118:119],v49 offset:36864
v_mfma_f32_16x16x32_bf16 v[72:75],a[200:203],a[16:19],v[72:75]
ds_read_b64_tr_b16 a[120:121],v50 offset:32768
v_mfma_f32_16x16x32_bf16 v[104:107],a[168:171],a[4:7],v[104:107]
ds_read_b64_tr_b16 a[122:123],v50 offset:36864
v_mfma_f32_16x16x32_bf16 v[104:107],a[200:203],a[20:23],v[104:107]
buffer_load_dwordx4 v55,s[44:47],s15 offen lds
)"
R"(v_mfma_f32_16x16x32_bf16 v[136:139],a[168:171],a[8:11],v[136:139]
ds_read_b64_tr_b16 a[124:125],v51 offset:32768
v_mfma_f32_16x16x32_bf16 v[136:139],a[200:203],a[24:27],v[136:139]
s_add_u32 s87,s87,131072
ds_read_b64_tr_b16 a[126:127],v51 offset:36864
v_mfma_f32_16x16x32_bf16 v[168:171],a[168:171],a[12:15],v[168:171]
ds_read_b64_tr_b16 a[128:129],v44 offset:40960
v_mfma_f32_16x16x32_bf16 v[168:171],a[200:203],a[28:31],v[168:171]
ds_read_b64_tr_b16 a[130:131],v44 offset:45056
v_mfma_f32_16x16x32_bf16 v[172:175],a[172:175],a[12:15],v[172:175]
buffer_load_dwordx4 a[224:227],v40,s[36:39],s75 offen sc1
v_mfma_f32_16x16x32_bf16 v[172:175],a[204:207],a[28:31],v[172:175]
ds_read_b64_tr_b16 a[132:133],v45 offset:40960
v_mfma_f32_16x16x32_bf16 v[140:143],a[172:175],a[8:11],v[140:143]
ds_read_b64_tr_b16 a[134:135],v45 offset:45056
v_mfma_f32_16x16x32_bf16 v[140:143],a[204:207],a[24:27],v[140:143]
ds_read_b64_tr_b16 a[136:137],v46 offset:40960
v_mfma_f32_16x16x32_bf16 v[108:111],a[172:175],a[4:7],v[108:111]
ds_read_b64_tr_b16 a[138:139],v46 offset:45056
v_mfma_f32_16x16x32_bf16 v[108:111],a[204:207],a[20:23],v[108:111]
buffer_load_dwordx4 a[228:231],v41,s[36:39],s75 offen sc1
v_mfma_f32_16x16x32_bf16 v[76:79],a[172:175],a[0:3],v[76:79]
ds_read_b64_tr_b16 a[140:141],v47 offset:40960
v_mfma_f32_16x16x32_bf16 v[76:79],a[204:207],a[16:19],v[76:79]
)"
R"(ds_read_b64_tr_b16 a[142:143],v47 offset:45056
v_mfma_f32_16x16x32_bf16 v[80:83],a[176:179],a[0:3],v[80:83]
ds_read_b64_tr_b16 a[144:145],v48 offset:40960
v_mfma_f32_16x16x32_bf16 v[80:83],a[208:211],a[16:19],v[80:83]
ds_read_b64_tr_b16 a[146:147],v48 offset:45056
v_mfma_f32_16x16x32_bf16 v[112:115],a[176:179],a[4:7],v[112:115]
buffer_load_dwordx4 a[232:235],v42,s[36:39],s75 offen sc1
v_mfma_f32_16x16x32_bf16 v[112:115],a[208:211],a[20:23],v[112:115]
ds_read_b64_tr_b16 a[148:149],v49 offset:40960
v_mfma_f32_16x16x32_bf16 v[144:147],a[176:179],a[8:11],v[144:147]
ds_read_b64_tr_b16 a[150:151],v49 offset:45056
v_mfma_f32_16x16x32_bf16 v[144:147],a[208:211],a[24:27],v[144:147]
ds_read_b64_tr_b16 a[152:153],v50 offset:40960
v_mfma_f32_16x16x32_bf16 v[176:179],a[176:179],a[12:15],v[176:179]
ds_read_b64_tr_b16 a[154:155],v50 offset:45056
v_mfma_f32_16x16x32_bf16 v[176:179],a[208:211],a[28:31],v[176:179]
buffer_load_dwordx4 a[236:239],v43,s[36:39],s75 offen sc1
v_mfma_f32_16x16x32_bf16 v[180:183],a[180:183],a[12:15],v[180:183]
ds_read_b64_tr_b16 a[156:157],v51 offset:40960
v_mfma_f32_16x16x32_bf16 v[180:183],a[212:215],a[28:31],v[180:183]
ds_read_b64_tr_b16 a[158:159],v51 offset:45056
v_mfma_f32_16x16x32_bf16 v[148:151],a[180:183],a[8:11],v[148:151]
v_mfma_f32_16x16x32_bf16 v[148:151],a[212:215],a[24:27],v[148:151]
v_mfma_f32_16x16x32_bf16 v[116:119],a[180:183],a[4:7],v[116:119]
)"
R"(buffer_load_dwordx4 a[240:243],v40,s[36:39],s24 offen sc1
v_mfma_f32_16x16x32_bf16 v[116:119],a[212:215],a[20:23],v[116:119]
v_mfma_f32_16x16x32_bf16 v[84:87],a[180:183],a[0:3],v[84:87]
v_mfma_f32_16x16x32_bf16 v[84:87],a[212:215],a[16:19],v[84:87]
v_mfma_f32_16x16x32_bf16 v[88:91],a[184:187],a[0:3],v[88:91]
v_mfma_f32_16x16x32_bf16 v[88:91],a[216:219],a[16:19],v[88:91]
buffer_load_dwordx4 a[244:247],v41,s[36:39],s24 offen sc1
v_mfma_f32_16x16x32_bf16 v[120:123],a[184:187],a[4:7],v[120:123]
v_mfma_f32_16x16x32_bf16 v[120:123],a[216:219],a[20:23],v[120:123]
v_mfma_f32_16x16x32_bf16 v[152:155],a[184:187],a[8:11],v[152:155]
v_mfma_f32_16x16x32_bf16 v[152:155],a[216:219],a[24:27],v[152:155]
v_mfma_f32_16x16x32_bf16 v[184:187],a[184:187],a[12:15],v[184:187]
buffer_load_dwordx4 a[248:251],v42,s[36:39],s24 offen sc1
v_mfma_f32_16x16x32_bf16 v[184:187],a[216:219],a[28:31],v[184:187]
v_mfma_f32_16x16x32_bf16 v[188:191],a[188:191],a[12:15],v[188:191]
v_mfma_f32_16x16x32_bf16 v[188:191],a[220:223],a[28:31],v[188:191]
v_mfma_f32_16x16x32_bf16 v[156:159],a[188:191],a[8:11],v[156:159]
v_mfma_f32_16x16x32_bf16 v[156:159],a[220:223],a[24:27],v[156:159]
buffer_load_dwordx4 a[252:255],v43,s[36:39],s24 offen sc1
s_add_u32 s75,s75,16384
v_mfma_f32_16x16x32_bf16 v[124:127],a[188:191],a[4:7],v[124:127]
s_add_u32 s24,s24,16384
v_mfma_f32_16x16x32_bf16 v[124:127],a[220:223],a[20:23],v[124:127]
v_mfma_f32_16x16x32_bf16 v[92:95],a[188:191],a[0:3],v[92:95]
)"
R"(s_waitcnt vmcnt(24) lgkmcnt(0)
v_mfma_f32_16x16x32_bf16 v[92:95],a[220:223],a[16:19],v[92:95]
v_mfma_f32_16x16x32_bf16 v[64:67],a[96:99],a[64:67],v[64:67]
s_waitcnt vmcnt(20)
s_barrier
v_mfma_f32_16x16x32_bf16 v[64:67],a[128:131],a[80:83],v[64:67]
s_cmp_eq_u32 s32,s34
s_cbranch_scc1 gm_K3_o0
gm_K3_r0:
v_mfma_f32_16x16x32_bf16 v[96:99],a[96:99],a[68:71],v[96:99]
s_add_u32 m0,s25,16384
s_add_u32 s12,s87,s80
ds_read_b64_tr_b16 a[160:161],v44 offset:49152
v_mfma_f32_16x16x32_bf16 v[96:99],a[128:131],a[84:87],v[96:99]
ds_read_b64_tr_b16 a[162:163],v44 offset:53248
v_mfma_f32_16x16x32_bf16 v[128:131],a[96:99],a[72:75],v[128:131]
buffer_load_dwordx4 v39,s[44:47],s12 offen lds
v_mfma_f32_16x16x32_bf16 v[128:131],a[128:131],a[88:91],v[128:131]
ds_read_b64_tr_b16 a[164:165],v45 offset:49152
v_mfma_f32_16x16x32_bf16 v[160:163],a[96:99],a[76:79],v[160:163]
s_add_u32 m0,s25,17408
s_add_u32 s15,s87,s81
ds_read_b64_tr_b16 a[166:167],v45 offset:53248
v_mfma_f32_16x16x32_bf16 v[160:163],a[128:131],a[92:95],v[160:163]
)"
R"(ds_read_b64_tr_b16 a[168:169],v46 offset:49152
v_mfma_f32_16x16x32_bf16 v[164:167],a[100:103],a[76:79],v[164:167]
ds_read_b64_tr_b16 a[170:171],v46 offset:53248
v_mfma_f32_16x16x32_bf16 v[164:167],a[132:135],a[92:95],v[164:167]
buffer_load_dwordx4 v55,s[44:47],s15 offen lds
v_mfma_f32_16x16x32_bf16 v[132:135],a[100:103],a[72:75],v[132:135]
ds_read_b64_tr_b16 a[172:173],v47 offset:49152
v_mfma_f32_16x16x32_bf16 v[132:135],a[132:135],a[88:91],v[132:135]
s_add_u32 m0,s25,18432
s_add_u32 s12,s87,s82
ds_read_b64_tr_b16 a[174:175],v47 offset:53248
v_mfma_f32_16x16x32_bf16 v[100:103],a[100:103],a[68:71],v[100:103]
ds_read_b64_tr_b16 a[176:177],v48 offset:49152
v_mfma_f32_16x16x32_bf16 v[100:103],a[132:135],a[84:87],v[100:103]
ds_read_b64_tr_b16 a[178:179],v48 offset:53248
v_mfma_f32_16x16x32_bf16 v[68:71],a[100:103],a[64:67],v[68:71]
buffer_load_dwordx4 v39,s[44:47],s12 offen lds
v_mfma_f32_16x16x32_bf16 v[68:71],a[132:135],a[80:83],v[68:71]
ds_read_b64_tr_b16 a[180:181],v49 offset:49152
v_mfma_f32_16x16x32_bf16 v[72:75],a[104:107],a[64:67],v[72:75]
s_add_u32 m0,s25,19456
s_add_u32 s15,s87,s83
ds_read_b64_tr_b16 a[182:183],v49 offset:53248
v_mfma_f32_16x16x32_bf16 v[72:75],a[136:139],a[80:83],v[72:75]
)"
R"(ds_read_b64_tr_b16 a[184:185],v50 offset:49152
v_mfma_f32_16x16x32_bf16 v[104:107],a[104:107],a[68:71],v[104:107]
ds_read_b64_tr_b16 a[186:187],v50 offset:53248
v_mfma_f32_16x16x32_bf16 v[104:107],a[136:139],a[84:87],v[104:107]
buffer_load_dwordx4 v55,s[44:47],s15 offen lds
v_mfma_f32_16x16x32_bf16 v[136:139],a[104:107],a[72:75],v[136:139]
ds_read_b64_tr_b16 a[188:189],v51 offset:49152
v_mfma_f32_16x16x32_bf16 v[136:139],a[136:139],a[88:91],v[136:139]
s_add_u32 s87,s87,131072
ds_read_b64_tr_b16 a[190:191],v51 offset:53248
v_mfma_f32_16x16x32_bf16 v[168:171],a[104:107],a[76:79],v[168:171]
ds_read_b64_tr_b16 a[192:193],v44 offset:57344
v_mfma_f32_16x16x32_bf16 v[168:171],a[136:139],a[92:95],v[168:171]
ds_read_b64_tr_b16 a[194:195],v44 offset:61440
v_mfma_f32_16x16x32_bf16 v[172:175],a[108:111],a[76:79],v[172:175]
buffer_load_dwordx4 a[0:3],v40,s[36:39],s75 offen sc1
v_mfma_f32_16x16x32_bf16 v[172:175],a[140:143],a[92:95],v[172:175]
ds_read_b64_tr_b16 a[196:197],v45 offset:57344
v_mfma_f32_16x16x32_bf16 v[140:143],a[108:111],a[72:75],v[140:143]
ds_read_b64_tr_b16 a[198:199],v45 offset:61440
v_mfma_f32_16x16x32_bf16 v[140:143],a[140:143],a[88:91],v[140:143]
ds_read_b64_tr_b16 a[200:201],v46 offset:57344
v_mfma_f32_16x16x32_bf16 v[108:111],a[108:111],a[68:71],v[108:111]
ds_read_b64_tr_b16 a[202:203],v46 offset:61440
)"
R"(v_mfma_f32_16x16x32_bf16 v[108:111],a[140:143],a[84:87],v[108:111]
buffer_load_dwordx4 a[4:7],v41,s[36:39],s75 offen sc1
v_mfma_f32_16x16x32_bf16 v[76:79],a[108:111],a[64:67],v[76:79]
ds_read_b64_tr_b16 a[204:205],v47 offset:57344
v_mfma_f32_16x16x32_bf16 v[76:79],a[140:143],a[80:83],v[76:79]
ds_read_b64_tr_b16 a[206:207],v47 offset:61440
v_mfma_f32_16x16x32_bf16 v[80:83],a[112:115],a[64:67],v[80:83]
ds_read_b64_tr_b16 a[208:209],v48 offset:57344
v_mfma_f32_16x16x32_bf16 v[80:83],a[144:147],a[80:83],v[80:83]
ds_read_b64_tr_b16 a[210:211],v48 offset:61440
v_mfma_f32_16x16x32_bf16 v[112:115],a[112:115],a[68:71],v[112:115]
buffer_load_dwordx4 a[8:11],v42,s[36:39],s75 offen sc1
v_mfma_f32_16x16x32_bf16 v[112:115],a[144:147],a[84:87],v[112:115]
ds_read_b64_tr_b16 a[212:213],v49 offset:57344
v_mfma_f32_16x16x32_bf16 v[144:147],a[112:115],a[72:75],v[144:147]
ds_read_b64_tr_b16 a[214:215],v49 offset:61440
v_mfma_f32_16x16x32_bf16 v[144:147],a[144:147],a[88:91],v[144:147]
ds_read_b64_tr_b16 a[216:217],v50 offset:57344
v_mfma_f32_16x16x32_bf16 v[176:179],a[112:115],a[76:79],v[176:179]
ds_read_b64_tr_b16 a[218:219],v50 offset:61440
v_mfma_f32_16x16x32_bf16 v[176:179],a[144:147],a[92:95],v[176:179]
buffer_load_dwordx4 a[12:15],v43,s[36:39],s75 offen sc1
v_mfma_f32_16x16x32_bf16 v[180:183],a[116:119],a[76:79],v[180:183]
ds_read_b64_tr_b16 a[220:221],v51 offset:57344
)"
R"(v_mfma_f32_16x16x32_bf16 v[180:183],a[148:151],a[92:95],v[180:183]
ds_read_b64_tr_b16 a[222:223],v51 offset:61440
v_mfma_f32_16x16x32_bf16 v[148:151],a[116:119],a[72:75],v[148:151]
v_mfma_f32_16x16x32_bf16 v[148:151],a[148:151],a[88:91],v[148:151]
v_mfma_f32_16x16x32_bf16 v[116:119],a[116:119],a[68:71],v[116:119]
buffer_load_dwordx4 a[16:19],v40,s[36:39],s24 offen sc1
v_mfma_f32_16x16x32_bf16 v[116:119],a[148:151],a[84:87],v[116:119]
v_mfma_f32_16x16x32_bf16 v[84:87],a[116:119],a[64:67],v[84:87]
v_mfma_f32_16x16x32_bf16 v[84:87],a[148:151],a[80:83],v[84:87]
v_mfma_f32_16x16x32_bf16 v[88:91],a[120:123],a[64:67],v[88:91]
v_mfma_f32_16x16x32_bf16 v[88:91],a[152:155],a[80:83],v[88:91]
buffer_load_dwordx4 a[20:23],v41,s[36:39],s24 offen sc1
v_mfma_f32_16x16x32_bf16 v[120:123],a[120:123],a[68:71],v[120:123]
v_mfma_f32_16x16x32_bf16 v[120:123],a[152:155],a[84:87],v[120:123]
v_mfma_f32_16x16x32_bf16 v[152:155],a[120:123],a[72:75],v[152:155]
v_mfma_f32_16x16x32_bf16 v[152:155],a[152:155],a[88:91],v[152:155]
v_mfma_f32_16x16x32_bf16 v[184:187],a[120:123],a[76:79],v[184:187]
buffer_load_dwordx4 a[24:27],v42,s[36:39],s24 offen sc1
v_mfma_f32_16x16x32_bf16 v[184:187],a[152:155],a[92:95],v[184:187]
v_mfma_f32_16x16x32_bf16 v[188:191],a[124:127],a[76:79],v[188:191]
v_mfma_f32_16x16x32_bf16 v[188:191],a[156:159],a[92:95],v[188:191]
v_mfma_f32_16x16x32_bf16 v[156:159],a[124:127],a[72:75],v[156:159]
v_mfma_f32_16x16x32_bf16 v[156:159],a[156:159],a[88:91],v[156:159]
buffer_load_dwordx4 a[28:31],v43,s[36:39],s24 offen sc1
)"
R"(s_add_u32 s75,s75,16384
v_mfma_f32_16x16x32_bf16 v[124:127],a[124:127],a[68:71],v[124:127]
s_add_u32 s24,s24,16384
v_mfma_f32_16x16x32_bf16 v[124:127],a[156:159],a[84:87],v[124:127]
v_mfma_f32_16x16x32_bf16 v[92:95],a[124:127],a[64:67],v[92:95]
s_waitcnt vmcnt(24) lgkmcnt(0)
v_mfma_f32_16x16x32_bf16 v[92:95],a[156:159],a[80:83],v[92:95]
v_mfma_f32_16x16x32_bf16 v[64:67],a[160:163],a[32:35],v[64:67]
s_waitcnt vmcnt(20)
s_barrier
v_mfma_f32_16x16x32_bf16 v[64:67],a[192:195],a[48:51],v[64:67]
s_cmp_eq_u32 s32,s35
s_cbranch_scc1 gm_K3_o2
gm_K3_r2:
v_mfma_f32_16x16x32_bf16 v[96:99],a[160:163],a[36:39],v[96:99]
s_add_u32 m0,s25,32768
s_add_u32 s12,s87,s80
ds_read_b64_tr_b16 a[96:97],v44 offset:0
v_mfma_f32_16x16x32_bf16 v[96:99],a[192:195],a[52:55],v[96:99]
ds_read_b64_tr_b16 a[98:99],v44 offset:4096
v_mfma_f32_16x16x32_bf16 v[128:131],a[160:163],a[40:43],v[128:131]
buffer_load_dwordx4 v39,s[44:47],s12 offen lds
v_mfma_f32_16x16x32_bf16 v[128:131],a[192:195],a[56:59],v[128:131]
ds_read_b64_tr_b16 a[100:101],v45 offset:0
)"
R"(v_mfma_f32_16x16x32_bf16 v[160:163],a[160:163],a[44:47],v[160:163]
s_add_u32 m0,s25,33792
s_add_u32 s15,s87,s81
ds_read_b64_tr_b16 a[102:103],v45 offset:4096
v_mfma_f32_16x16x32_bf16 v[160:163],a[192:195],a[60:63],v[160:163]
ds_read_b64_tr_b16 a[104:105],v46 offset:0
v_mfma_f32_16x16x32_bf16 v[164:167],a[164:167],a[44:47],v[164:167]
ds_read_b64_tr_b16 a[106:107],v46 offset:4096
v_mfma_f32_16x16x32_bf16 v[164:167],a[196:199],a[60:63],v[164:167]
buffer_load_dwordx4 v55,s[44:47],s15 offen lds
v_mfma_f32_16x16x32_bf16 v[132:135],a[164:167],a[40:43],v[132:135]
ds_read_b64_tr_b16 a[108:109],v47 offset:0
v_mfma_f32_16x16x32_bf16 v[132:135],a[196:199],a[56:59],v[132:135]
s_add_u32 m0,s25,34816
s_add_u32 s12,s87,s82
ds_read_b64_tr_b16 a[110:111],v47 offset:4096
v_mfma_f32_16x16x32_bf16 v[100:103],a[164:167],a[36:39],v[100:103]
ds_read_b64_tr_b16 a[112:113],v48 offset:0
v_mfma_f32_16x16x32_bf16 v[100:103],a[196:199],a[52:55],v[100:103]
ds_read_b64_tr_b16 a[114:115],v48 offset:4096
v_mfma_f32_16x16x32_bf16 v[68:71],a[164:167],a[32:35],v[68:71]
buffer_load_dwordx4 v39,s[44:47],s12 offen lds
v_mfma_f32_16x16x32_bf16 v[68:71],a[196:199],a[48:51],v[68:71]
ds_read_b64_tr_b16 a[116:117],v49 offset:0
)"
R"(v_mfma_f32_16x16x32_bf16 v[72:75],a[168:171],a[32:35],v[72:75]
s_add_u32 m0,s25,35840
s_add_u32 s15,s87,s83
ds_read_b64_tr_b16 a[118:119],v49 offset:4096
v_mfma_f32_16x16x32_bf16 v[72:75],a[200:203],a[48:51],v[72:75]
ds_read_b64_tr_b16 a[120:121],v50 offset:0
v_mfma_f32_16x16x32_bf16 v[104:107],a[168:171],a[36:39],v[104:107]
ds_read_b64_tr_b16 a[122:123],v50 offset:4096
v_mfma_f32_16x16x32_bf16 v[104:107],a[200:203],a[52:55],v[104:107]
buffer_load_dwordx4 v55,s[44:47],s15 offen lds
v_mfma_f32_16x16x32_bf16 v[136:139],a[168:171],a[40:43],v[136:139]
ds_read_b64_tr_b16 a[124:125],v51 offset:0
v_mfma_f32_16x16x32_bf16 v[136:139],a[200:203],a[56:59],v[136:139]
s_add_u32 s87,s87,131072
ds_read_b64_tr_b16 a[126:127],v51 offset:4096
v_mfma_f32_16x16x32_bf16 v[168:171],a[168:171],a[44:47],v[168:171]
ds_read_b64_tr_b16 a[128:129],v44 offset:8192
v_mfma_f32_16x16x32_bf16 v[168:171],a[200:203],a[60:63],v[168:171]
ds_read_b64_tr_b16 a[130:131],v44 offset:12288
v_mfma_f32_16x16x32_bf16 v[172:175],a[172:175],a[44:47],v[172:175]
buffer_load_dwordx4 a[64:67],v40,s[36:39],s75 offen sc1
v_mfma_f32_16x16x32_bf16 v[172:175],a[204:207],a[60:63],v[172:175]
ds_read_b64_tr_b16 a[132:133],v45 offset:8192
v_mfma_f32_16x16x32_bf16 v[140:143],a[172:175],a[40:43],v[140:143]
)"
R"(ds_read_b64_tr_b16 a[134:135],v45 offset:12288
v_mfma_f32_16x16x32_bf16 v[140:143],a[204:207],a[56:59],v[140:143]
ds_read_b64_tr_b16 a[136:137],v46 offset:8192
v_mfma_f32_16x16x32_bf16 v[108:111],a[172:175],a[36:39],v[108:111]
ds_read_b64_tr_b16 a[138:139],v46 offset:12288
v_mfma_f32_16x16x32_bf16 v[108:111],a[204:207],a[52:55],v[108:111]
buffer_load_dwordx4 a[68:71],v41,s[36:39],s75 offen sc1
v_mfma_f32_16x16x32_bf16 v[76:79],a[172:175],a[32:35],v[76:79]
ds_read_b64_tr_b16 a[140:141],v47 offset:8192
v_mfma_f32_16x16x32_bf16 v[76:79],a[204:207],a[48:51],v[76:79]
ds_read_b64_tr_b16 a[142:143],v47 offset:12288
v_mfma_f32_16x16x32_bf16 v[80:83],a[176:179],a[32:35],v[80:83]
ds_read_b64_tr_b16 a[144:145],v48 offset:8192
v_mfma_f32_16x16x32_bf16 v[80:83],a[208:211],a[48:51],v[80:83]
ds_read_b64_tr_b16 a[146:147],v48 offset:12288
v_mfma_f32_16x16x32_bf16 v[112:115],a[176:179],a[36:39],v[112:115]
buffer_load_dwordx4 a[72:75],v42,s[36:39],s75 offen sc1
v_mfma_f32_16x16x32_bf16 v[112:115],a[208:211],a[52:55],v[112:115]
ds_read_b64_tr_b16 a[148:149],v49 offset:8192
v_mfma_f32_16x16x32_bf16 v[144:147],a[176:179],a[40:43],v[144:147]
ds_read_b64_tr_b16 a[150:151],v49 offset:12288
v_mfma_f32_16x16x32_bf16 v[144:147],a[208:211],a[56:59],v[144:147]
ds_read_b64_tr_b16 a[152:153],v50 offset:8192
v_mfma_f32_16x16x32_bf16 v[176:179],a[176:179],a[44:47],v[176:179]
)"
R"(ds_read_b64_tr_b16 a[154:155],v50 offset:12288
v_mfma_f32_16x16x32_bf16 v[176:179],a[208:211],a[60:63],v[176:179]
buffer_load_dwordx4 a[76:79],v43,s[36:39],s75 offen sc1
v_mfma_f32_16x16x32_bf16 v[180:183],a[180:183],a[44:47],v[180:183]
ds_read_b64_tr_b16 a[156:157],v51 offset:8192
v_mfma_f32_16x16x32_bf16 v[180:183],a[212:215],a[60:63],v[180:183]
ds_read_b64_tr_b16 a[158:159],v51 offset:12288
v_mfma_f32_16x16x32_bf16 v[148:151],a[180:183],a[40:43],v[148:151]
v_mfma_f32_16x16x32_bf16 v[148:151],a[212:215],a[56:59],v[148:151]
v_mfma_f32_16x16x32_bf16 v[116:119],a[180:183],a[36:39],v[116:119]
buffer_load_dwordx4 a[80:83],v40,s[36:39],s24 offen sc1
v_mfma_f32_16x16x32_bf16 v[116:119],a[212:215],a[52:55],v[116:119]
v_mfma_f32_16x16x32_bf16 v[84:87],a[180:183],a[32:35],v[84:87]
v_mfma_f32_16x16x32_bf16 v[84:87],a[212:215],a[48:51],v[84:87]
v_mfma_f32_16x16x32_bf16 v[88:91],a[184:187],a[32:35],v[88:91]
v_mfma_f32_16x16x32_bf16 v[88:91],a[216:219],a[48:51],v[88:91]
buffer_load_dwordx4 a[84:87],v41,s[36:39],s24 offen sc1
v_mfma_f32_16x16x32_bf16 v[120:123],a[184:187],a[36:39],v[120:123]
v_mfma_f32_16x16x32_bf16 v[120:123],a[216:219],a[52:55],v[120:123]
v_mfma_f32_16x16x32_bf16 v[152:155],a[184:187],a[40:43],v[152:155]
v_mfma_f32_16x16x32_bf16 v[152:155],a[216:219],a[56:59],v[152:155]
v_mfma_f32_16x16x32_bf16 v[184:187],a[184:187],a[44:47],v[184:187]
buffer_load_dwordx4 a[88:91],v42,s[36:39],s24 offen sc1
v_mfma_f32_16x16x32_bf16 v[184:187],a[216:219],a[60:63],v[184:187]
)"
R"(v_mfma_f32_16x16x32_bf16 v[188:191],a[188:191],a[44:47],v[188:191]
v_mfma_f32_16x16x32_bf16 v[188:191],a[220:223],a[60:63],v[188:191]
v_mfma_f32_16x16x32_bf16 v[156:159],a[188:191],a[40:43],v[156:159]
v_mfma_f32_16x16x32_bf16 v[156:159],a[220:223],a[56:59],v[156:159]
buffer_load_dwordx4 a[92:95],v43,s[36:39],s24 offen sc1
s_add_u32 s75,s75,16384
v_mfma_f32_16x16x32_bf16 v[124:127],a[188:191],a[36:39],v[124:127]
s_add_u32 s24,s24,16384
v_mfma_f32_16x16x32_bf16 v[124:127],a[220:223],a[52:55],v[124:127]
v_mfma_f32_16x16x32_bf16 v[92:95],a[188:191],a[32:35],v[92:95]
s_waitcnt vmcnt(24) lgkmcnt(0)
v_mfma_f32_16x16x32_bf16 v[92:95],a[220:223],a[48:51],v[92:95]
v_mfma_f32_16x16x32_bf16 v[64:67],a[96:99],a[224:227],v[64:67]
s_waitcnt vmcnt(20)
s_barrier
v_mfma_f32_16x16x32_bf16 v[64:67],a[128:131],a[240:243],v[64:67]
s_add_u32 s32,s32,1
s_add_u32 m0,s25,49152
ds_read_b64_tr_b16 a[160:161],v44 offset:16384
v_mfma_f32_16x16x32_bf16 v[96:99],a[96:99],a[228:231],v[96:99]
s_add_u32 s12,s87,s80
ds_read_b64_tr_b16 a[162:163],v44 offset:20480
v_mfma_f32_16x16x32_bf16 v[96:99],a[128:131],a[244:247],v[96:99]
ds_read_b64_tr_b16 a[164:165],v45 offset:16384
)"
R"(v_mfma_f32_16x16x32_bf16 v[128:131],a[96:99],a[232:235],v[128:131]
buffer_load_dwordx4 v39,s[44:47],s12 offen lds
v_mfma_f32_16x16x32_bf16 v[128:131],a[128:131],a[248:251],v[128:131]
ds_read_b64_tr_b16 a[166:167],v45 offset:20480
v_mfma_f32_16x16x32_bf16 v[160:163],a[96:99],a[236:239],v[160:163]
s_add_u32 m0,s25,50176
s_add_u32 s15,s87,s81
ds_read_b64_tr_b16 a[168:169],v46 offset:16384
v_mfma_f32_16x16x32_bf16 v[160:163],a[128:131],a[252:255],v[160:163]
ds_read_b64_tr_b16 a[170:171],v46 offset:20480
v_mfma_f32_16x16x32_bf16 v[164:167],a[100:103],a[236:239],v[164:167]
ds_read_b64_tr_b16 a[172:173],v47 offset:16384
v_mfma_f32_16x16x32_bf16 v[164:167],a[132:135],a[252:255],v[164:167]
buffer_load_dwordx4 v55,s[44:47],s15 offen lds
v_mfma_f32_16x16x32_bf16 v[132:135],a[100:103],a[232:235],v[132:135]
ds_read_b64_tr_b16 a[174:175],v47 offset:20480
v_mfma_f32_16x16x32_bf16 v[132:135],a[132:135],a[248:251],v[132:135]
s_add_u32 m0,s25,51200
s_add_u32 s12,s87,s82
ds_read_b64_tr_b16 a[176:177],v48 offset:16384
v_mfma_f32_16x16x32_bf16 v[100:103],a[100:103],a[228:231],v[100:103]
ds_read_b64_tr_b16 a[178:179],v48 offset:20480
v_mfma_f32_16x16x32_bf16 v[100:103],a[132:135],a[244:247],v[100:103]
v_mfma_f32_16x16x32_bf16 v[68:71],a[100:103],a[224:227],v[68:71]
)"
R"(buffer_load_dwordx4 v39,s[44:47],s12 offen lds
v_mfma_f32_16x16x32_bf16 v[68:71],a[132:135],a[240:243],v[68:71]
ds_read_b64_tr_b16 a[180:181],v49 offset:16384
v_mfma_f32_16x16x32_bf16 v[72:75],a[104:107],a[224:227],v[72:75]
s_add_u32 m0,s25,52224
s_add_u32 s15,s87,s83
ds_read_b64_tr_b16 a[182:183],v49 offset:20480
v_mfma_f32_16x16x32_bf16 v[72:75],a[136:139],a[240:243],v[72:75]
ds_read_b64_tr_b16 a[184:185],v50 offset:16384
v_mfma_f32_16x16x32_bf16 v[104:107],a[104:107],a[228:231],v[104:107]
ds_read_b64_tr_b16 a[186:187],v50 offset:20480
v_mfma_f32_16x16x32_bf16 v[104:107],a[136:139],a[244:247],v[104:107]
buffer_load_dwordx4 v55,s[44:47],s15 offen lds
v_mfma_f32_16x16x32_bf16 v[136:139],a[104:107],a[232:235],v[136:139]
ds_read_b64_tr_b16 a[188:189],v51 offset:16384
v_mfma_f32_16x16x32_bf16 v[136:139],a[136:139],a[248:251],v[136:139]
s_add_u32 s87,s87,131072
ds_read_b64_tr_b16 a[190:191],v51 offset:20480
v_mfma_f32_16x16x32_bf16 v[168:171],a[104:107],a[236:239],v[168:171]
ds_read_b64_tr_b16 a[192:193],v44 offset:24576
v_mfma_f32_16x16x32_bf16 v[168:171],a[136:139],a[252:255],v[168:171]
ds_read_b64_tr_b16 a[194:195],v44 offset:28672
v_mfma_f32_16x16x32_bf16 v[172:175],a[108:111],a[236:239],v[172:175]
buffer_load_dwordx4 a[32:35],v40,s[36:39],s75 offen sc1
)"
R"(v_mfma_f32_16x16x32_bf16 v[172:175],a[140:143],a[252:255],v[172:175]
ds_read_b64_tr_b16 a[196:197],v45 offset:24576
v_mfma_f32_16x16x32_bf16 v[140:143],a[108:111],a[232:235],v[140:143]
ds_read_b64_tr_b16 a[198:199],v45 offset:28672
v_mfma_f32_16x16x32_bf16 v[140:143],a[140:143],a[248:251],v[140:143]
ds_read_b64_tr_b16 a[200:201],v46 offset:24576
v_mfma_f32_16x16x32_bf16 v[108:111],a[108:111],a[228:231],v[108:111]
ds_read_b64_tr_b16 a[202:203],v46 offset:28672
v_mfma_f32_16x16x32_bf16 v[108:111],a[140:143],a[244:247],v[108:111]
buffer_load_dwordx4 a[36:39],v41,s[36:39],s75 offen sc1
v_mfma_f32_16x16x32_bf16 v[76:79],a[108:111],a[224:227],v[76:79]
ds_read_b64_tr_b16 a[204:205],v47 offset:24576
v_mfma_f32_16x16x32_bf16 v[76:79],a[140:143],a[240:243],v[76:79]
ds_read_b64_tr_b16 a[206:207],v47 offset:28672
v_mfma_f32_16x16x32_bf16 v[80:83],a[112:115],a[224:227],v[80:83]
ds_read_b64_tr_b16 a[208:209],v48 offset:24576
v_mfma_f32_16x16x32_bf16 v[80:83],a[144:147],a[240:243],v[80:83]
ds_read_b64_tr_b16 a[210:211],v48 offset:28672
v_mfma_f32_16x16x32_bf16 v[112:115],a[112:115],a[228:231],v[112:115]
buffer_load_dwordx4 a[40:43],v42,s[36:39],s75 offen sc1
v_mfma_f32_16x16x32_bf16 v[112:115],a[144:147],a[244:247],v[112:115]
ds_read_b64_tr_b16 a[212:213],v49 offset:24576
v_mfma_f32_16x16x32_bf16 v[144:147],a[112:115],a[232:235],v[144:147]
ds_read_b64_tr_b16 a[214:215],v49 offset:28672
)"
R"(v_mfma_f32_16x16x32_bf16 v[144:147],a[144:147],a[248:251],v[144:147]
ds_read_b64_tr_b16 a[216:217],v50 offset:24576
v_mfma_f32_16x16x32_bf16 v[176:179],a[112:115],a[236:239],v[176:179]
ds_read_b64_tr_b16 a[218:219],v50 offset:28672
v_mfma_f32_16x16x32_bf16 v[176:179],a[144:147],a[252:255],v[176:179]
buffer_load_dwordx4 a[44:47],v43,s[36:39],s75 offen sc1
v_mfma_f32_16x16x32_bf16 v[180:183],a[116:119],a[236:239],v[180:183]
ds_read_b64_tr_b16 a[220:221],v51 offset:24576
v_mfma_f32_16x16x32_bf16 v[180:183],a[148:151],a[252:255],v[180:183]
ds_read_b64_tr_b16 a[222:223],v51 offset:28672
v_mfma_f32_16x16x32_bf16 v[148:151],a[116:119],a[232:235],v[148:151]
v_mfma_f32_16x16x32_bf16 v[148:151],a[148:151],a[248:251],v[148:151]
v_mfma_f32_16x16x32_bf16 v[116:119],a[116:119],a[228:231],v[116:119]
buffer_load_dwordx4 a[48:51],v40,s[36:39],s24 offen sc1
v_mfma_f32_16x16x32_bf16 v[116:119],a[148:151],a[244:247],v[116:119]
v_mfma_f32_16x16x32_bf16 v[84:87],a[116:119],a[224:227],v[84:87]
v_mfma_f32_16x16x32_bf16 v[84:87],a[148:151],a[240:243],v[84:87]
v_mfma_f32_16x16x32_bf16 v[88:91],a[120:123],a[224:227],v[88:91]
v_mfma_f32_16x16x32_bf16 v[88:91],a[152:155],a[240:243],v[88:91]
buffer_load_dwordx4 a[52:55],v41,s[36:39],s24 offen sc1
v_mfma_f32_16x16x32_bf16 v[120:123],a[120:123],a[228:231],v[120:123]
v_mfma_f32_16x16x32_bf16 v[120:123],a[152:155],a[244:247],v[120:123]
v_mfma_f32_16x16x32_bf16 v[152:155],a[120:123],a[232:235],v[152:155]
v_mfma_f32_16x16x32_bf16 v[152:155],a[152:155],a[248:251],v[152:155]
)"
R"(v_mfma_f32_16x16x32_bf16 v[184:187],a[120:123],a[236:239],v[184:187]
buffer_load_dwordx4 a[56:59],v42,s[36:39],s24 offen sc1
v_mfma_f32_16x16x32_bf16 v[184:187],a[152:155],a[252:255],v[184:187]
v_mfma_f32_16x16x32_bf16 v[188:191],a[124:127],a[236:239],v[188:191]
v_mfma_f32_16x16x32_bf16 v[188:191],a[156:159],a[252:255],v[188:191]
v_mfma_f32_16x16x32_bf16 v[156:159],a[124:127],a[232:235],v[156:159]
v_mfma_f32_16x16x32_bf16 v[156:159],a[156:159],a[248:251],v[156:159]
buffer_load_dwordx4 a[60:63],v43,s[36:39],s24 offen sc1
s_add_u32 s75,s75,16384
v_mfma_f32_16x16x32_bf16 v[124:127],a[124:127],a[228:231],v[124:127]
s_add_u32 s24,s24,16384
s_cmp_lg_u32 s32,s33
v_mfma_f32_16x16x32_bf16 v[124:127],a[156:159],a[244:247],v[124:127]
v_mfma_f32_16x16x32_bf16 v[92:95],a[124:127],a[224:227],v[92:95]
s_waitcnt vmcnt(24) lgkmcnt(0)
v_mfma_f32_16x16x32_bf16 v[92:95],a[156:159],a[240:243],v[92:95]
s_cbranch_scc1 gm_K3_loop
s_cmp_lg_u32 s32,3
s_cbranch_scc1 gm_K3_tail
s_mov_b64 s[36:37],s[88:89]
s_mov_b64 s[38:39],s[90:91]
s_mov_b64 s[44:45],s[92:93]
s_mov_b64 s[46:47],s[94:95]
s_mov_b32 s75,0
)"
R"(s_mov_b32 s24,8192
s_mov_b32 s87,0
s_mov_b32 s33,s29
s_cmp_lg_u32 %2,0
s_cbranch_scc1 gm_KB_goth
s_mov_b32 s98,0x10000
gm_KB_pollh:
v_xor_b32 v53,s72,v56
v_xor_b32 v54,s72,v57
v_or_b32 v53,v53,v54
v_xor_b32 v54,s72,v58
v_or_b32 v53,v53,v54
v_xor_b32 v54,s72,v59
v_or_b32 v53,v53,v54
v_cmp_ne_u32 vcc,0,v53
s_nop 1
s_cmp_eq_u64 vcc,0
s_cbranch_scc1 gm_KB_goth
s_sub_u32 s98,s98,1
s_cmp_eq_u32 s98,0
s_cbranch_scc1 gm_KB_goth
buffer_load_dwordx4 v[56:59],v18,s[4:7],s74 offen sc1
s_waitcnt vmcnt(0)
s_branch gm_KB_pollh
)"
R"(gm_KB_goth:
s_branch gm_K3_loop
gm_K3_o0:
v_mov_b32 v22,s72
v_mov_b32 v23,0
s_mov_b64 exec,1
buffer_store_dword v22,v23,s[4:7],s14 offen sc1
s_mov_b64 exec,-1
s_branch gm_K3_r0
gm_K3_o2:
buffer_load_dwordx4 v[56:59],v18,s[4:7],s74 offen sc1
s_branch gm_K3_r2
gm_K3_tail:
v_mfma_f32_16x16x32_bf16 v[64:67],a[160:163],a[0:3],v[64:67]
s_waitcnt vmcnt(20)
s_barrier
v_mfma_f32_16x16x32_bf16 v[64:67],a[192:195],a[16:19],v[64:67]
s_lshl_b32 s33,s53,16
s_add_u32 s34,s33,4096
ds_read_b64_tr_b16 a[96:97],v44 offset:32768
v_mfma_f32_16x16x32_bf16 v[96:99],a[160:163],a[4:7],v[96:99]
s_add_u32 s35,s33,8192
ds_read_b64_tr_b16 a[98:99],v44 offset:36864
v_mfma_f32_16x16x32_bf16 v[96:99],a[192:195],a[20:23],v[96:99]
)"
R"(ds_read_b64_tr_b16 a[100:101],v45 offset:32768
s_branch gm_KB_sk26
.p2align 12
gm_KB_pg26:
s_setpc_b64 s[100:101]
gm_KB_sk26:
v_mfma_f32_16x16x32_bf16 v[128:131],a[160:163],a[8:11],v[128:131]
ds_read_b64_tr_b16 a[102:103],v45 offset:36864
v_mfma_f32_16x16x32_bf16 v[128:131],a[192:195],a[24:27],v[128:131]
v_mfma_f32_16x16x32_bf16 v[160:163],a[160:163],a[12:15],v[160:163]
ds_read_b64_tr_b16 a[104:105],v46 offset:32768
v_mfma_f32_16x16x32_bf16 v[160:163],a[192:195],a[28:31],v[160:163]
ds_read_b64_tr_b16 a[106:107],v46 offset:36864
v_mfma_f32_16x16x32_bf16 v[164:167],a[164:167],a[12:15],v[164:167]
ds_read_b64_tr_b16 a[108:109],v47 offset:32768
v_mfma_f32_16x16x32_bf16 v[164:167],a[196:199],a[28:31],v[164:167]
v_mfma_f32_16x16x32_bf16 v[132:135],a[164:167],a[8:11],v[132:135]
ds_read_b64_tr_b16 a[110:111],v47 offset:36864
v_mfma_f32_16x16x32_bf16 v[132:135],a[196:199],a[24:27],v[132:135]
ds_read_b64_tr_b16 a[112:113],v48 offset:32768
v_mfma_f32_16x16x32_bf16 v[100:103],a[164:167],a[4:7],v[100:103]
ds_read_b64_tr_b16 a[114:115],v48 offset:36864
v_mfma_f32_16x16x32_bf16 v[100:103],a[196:199],a[20:23],v[100:103]
v_mfma_f32_16x16x32_bf16 v[68:71],a[164:167],a[0:3],v[68:71]
)"
R"(ds_read_b64_tr_b16 a[116:117],v49 offset:32768
v_mfma_f32_16x16x32_bf16 v[68:71],a[196:199],a[16:19],v[68:71]
ds_read_b64_tr_b16 a[118:119],v49 offset:36864
v_mfma_f32_16x16x32_bf16 v[72:75],a[168:171],a[0:3],v[72:75]
ds_read_b64_tr_b16 a[120:121],v50 offset:32768
v_mfma_f32_16x16x32_bf16 v[72:75],a[200:203],a[16:19],v[72:75]
ds_read_b64_tr_b16 a[122:123],v50 offset:36864
v_mfma_f32_16x16x32_bf16 v[104:107],a[168:171],a[4:7],v[104:107]
v_mfma_f32_16x16x32_bf16 v[104:107],a[200:203],a[20:23],v[104:107]
ds_read_b64_tr_b16 a[124:125],v51 offset:32768
v_mfma_f32_16x16x32_bf16 v[136:139],a[168:171],a[8:11],v[136:139]
ds_read_b64_tr_b16 a[126:127],v51 offset:36864
v_mfma_f32_16x16x32_bf16 v[136:139],a[200:203],a[24:27],v[136:139]
ds_read_b64_tr_b16 a[128:129],v44 offset:40960
v_mfma_f32_16x16x32_bf16 v[168:171],a[168:171],a[12:15],v[168:171]
v_mfma_f32_16x16x32_bf16 v[168:171],a[200:203],a[28:31],v[168:171]
ds_read_b64_tr_b16 a[130:131],v44 offset:45056
v_mfma_f32_16x16x32_bf16 v[172:175],a[172:175],a[12:15],v[172:175]
ds_read_b64_tr_b16 a[132:133],v45 offset:40960
v_mfma_f32_16x16x32_bf16 v[172:175],a[204:207],a[28:31],v[172:175]
ds_read_b64_tr_b16 a[134:135],v45 offset:45056
v_mfma_f32_16x16x32_bf16 v[140:143],a[172:175],a[8:11],v[140:143]
v_mfma_f32_16x16x32_bf16 v[140:143],a[204:207],a[24:27],v[140:143]
ds_read_b64_tr_b16 a[136:137],v46 offset:40960
)"
R"(v_mfma_f32_16x16x32_bf16 v[108:111],a[172:175],a[4:7],v[108:111]
ds_read_b64_tr_b16 a[138:139],v46 offset:45056
v_mfma_f32_16x16x32_bf16 v[108:111],a[204:207],a[20:23],v[108:111]
ds_read_b64_tr_b16 a[140:141],v47 offset:40960
v_mfma_f32_16x16x32_bf16 v[76:79],a[172:175],a[0:3],v[76:79]
ds_read_b64_tr_b16 a[142:143],v47 offset:45056
v_mfma_f32_16x16x32_bf16 v[76:79],a[204:207],a[16:19],v[76:79]
v_mfma_f32_16x16x32_bf16 v[80:83],a[176:179],a[0:3],v[80:83]
ds_read_b64_tr_b16 a[144:145],v48 offset:40960
v_mfma_f32_16x16x32_bf16 v[80:83],a[208:211],a[16:19],v[80:83]
ds_read_b64_tr_b16 a[146:147],v48 offset:45056
v_mfma_f32_16x16x32_bf16 v[112:115],a[176:179],a[4:7],v[112:115]
ds_read_b64_tr_b16 a[148:149],v49 offset:40960
v_mfma_f32_16x16x32_bf16 v[112:115],a[208:211],a[20:23],v[112:115]
v_mfma_f32_16x16x32_bf16 v[144:147],a[176:179],a[8:11],v[144:147]
ds_read_b64_tr_b16 a[150:151],v49 offset:45056
v_mfma_f32_16x16x32_bf16 v[144:147],a[208:211],a[24:27],v[144:147]
ds_read_b64_tr_b16 a[152:153],v50 offset:40960
v_mfma_f32_16x16x32_bf16 v[176:179],a[176:179],a[12:15],v[176:179]
ds_read_b64_tr_b16 a[154:155],v50 offset:45056
v_mfma_f32_16x16x32_bf16 v[176:179],a[208:211],a[28:31],v[176:179]
v_mfma_f32_16x16x32_bf16 v[180:183],a[180:183],a[12:15],v[180:183]
ds_read_b64_tr_b16 a[156:157],v51 offset:40960
v_mfma_f32_16x16x32_bf16 v[180:183],a[212:215],a[28:31],v[180:183]
)"
R"(ds_read_b64_tr_b16 a[158:159],v51 offset:45056
v_mfma_f32_16x16x32_bf16 v[148:151],a[180:183],a[8:11],v[148:151]
v_mfma_f32_16x16x32_bf16 v[148:151],a[212:215],a[24:27],v[148:151]
v_mfma_f32_16x16x32_bf16 v[116:119],a[180:183],a[4:7],v[116:119]
v_mfma_f32_16x16x32_bf16 v[116:119],a[212:215],a[20:23],v[116:119]
v_mfma_f32_16x16x32_bf16 v[84:87],a[180:183],a[0:3],v[84:87]
v_mfma_f32_16x16x32_bf16 v[84:87],a[212:215],a[16:19],v[84:87]
v_mfma_f32_16x16x32_bf16 v[88:91],a[184:187],a[0:3],v[88:91]
v_mfma_f32_16x16x32_bf16 v[88:91],a[216:219],a[16:19],v[88:91]
v_mfma_f32_16x16x32_bf16 v[120:123],a[184:187],a[4:7],v[120:123]
v_mfma_f32_16x16x32_bf16 v[120:123],a[216:219],a[20:23],v[120:123]
v_mfma_f32_16x16x32_bf16 v[152:155],a[184:187],a[8:11],v[152:155]
v_mfma_f32_16x16x32_bf16 v[152:155],a[216:219],a[24:27],v[152:155]
v_mfma_f32_16x16x32_bf16 v[184:187],a[184:187],a[12:15],v[184:187]
v_mfma_f32_16x16x32_bf16 v[184:187],a[216:219],a[28:31],v[184:187]
v_mfma_f32_16x16x32_bf16 v[188:191],a[188:191],a[12:15],v[188:191]
v_mfma_f32_16x16x32_bf16 v[188:191],a[220:223],a[28:31],v[188:191]
v_mfma_f32_16x16x32_bf16 v[156:159],a[188:191],a[8:11],v[156:159]
v_mfma_f32_16x16x32_bf16 v[156:159],a[220:223],a[24:27],v[156:159]
v_mfma_f32_16x16x32_bf16 v[124:127],a[188:191],a[4:7],v[124:127]
v_mfma_f32_16x16x32_bf16 v[124:127],a[220:223],a[20:23],v[124:127]
v_mfma_f32_16x16x32_bf16 v[92:95],a[188:191],a[0:3],v[92:95]
s_waitcnt vmcnt(12) lgkmcnt(0)
v_mfma_f32_16x16x32_bf16 v[92:95],a[220:223],a[16:19],v[92:95]
)"
R"(v_mfma_f32_16x16x32_bf16 v[64:67],a[96:99],a[64:67],v[64:67]
s_waitcnt vmcnt(8)
s_barrier
v_mfma_f32_16x16x32_bf16 v[64:67],a[128:131],a[80:83],v[64:67]
ds_read_b64_tr_b16 a[160:161],v44 offset:49152
v_mfma_f32_16x16x32_bf16 v[96:99],a[96:99],a[68:71],v[96:99]
ds_read_b64_tr_b16 a[162:163],v44 offset:53248
v_mfma_f32_16x16x32_bf16 v[96:99],a[128:131],a[84:87],v[96:99]
ds_read_b64_tr_b16 a[164:165],v45 offset:49152
v_mfma_f32_16x16x32_bf16 v[128:131],a[96:99],a[72:75],v[128:131]
ds_read_b64_tr_b16 a[166:167],v45 offset:53248
v_mfma_f32_16x16x32_bf16 v[128:131],a[128:131],a[88:91],v[128:131]
v_mfma_f32_16x16x32_bf16 v[160:163],a[96:99],a[76:79],v[160:163]
ds_read_b64_tr_b16 a[168:169],v46 offset:49152
v_mfma_f32_16x16x32_bf16 v[160:163],a[128:131],a[92:95],v[160:163]
ds_read_b64_tr_b16 a[170:171],v46 offset:53248
v_mfma_f32_16x16x32_bf16 v[164:167],a[100:103],a[76:79],v[164:167]
ds_read_b64_tr_b16 a[172:173],v47 offset:49152
v_mfma_f32_16x16x32_bf16 v[164:167],a[132:135],a[92:95],v[164:167]
v_mfma_f32_16x16x32_bf16 v[132:135],a[100:103],a[72:75],v[132:135]
ds_read_b64_tr_b16 a[174:175],v47 offset:53248
v_mfma_f32_16x16x32_bf16 v[132:135],a[132:135],a[88:91],v[132:135]
ds_read_b64_tr_b16 a[176:177],v48 offset:49152
v_mfma_f32_16x16x32_bf16 v[100:103],a[100:103],a[68:71],v[100:103]
)"
R"(ds_read_b64_tr_b16 a[178:179],v48 offset:53248
v_mfma_f32_16x16x32_bf16 v[100:103],a[132:135],a[84:87],v[100:103]
v_mfma_f32_16x16x32_bf16 v[68:71],a[100:103],a[64:67],v[68:71]
ds_read_b64_tr_b16 a[180:181],v49 offset:49152
v_mfma_f32_16x16x32_bf16 v[68:71],a[132:135],a[80:83],v[68:71]
ds_read_b64_tr_b16 a[182:183],v49 offset:53248
v_mfma_f32_16x16x32_bf16 v[72:75],a[104:107],a[64:67],v[72:75]
ds_read_b64_tr_b16 a[184:185],v50 offset:49152
v_mfma_f32_16x16x32_bf16 v[72:75],a[136:139],a[80:83],v[72:75]
ds_read_b64_tr_b16 a[186:187],v50 offset:53248
v_mfma_f32_16x16x32_bf16 v[104:107],a[104:107],a[68:71],v[104:107]
v_mfma_f32_16x16x32_bf16 v[104:107],a[136:139],a[84:87],v[104:107]
ds_read_b64_tr_b16 a[188:189],v51 offset:49152
v_mfma_f32_16x16x32_bf16 v[136:139],a[104:107],a[72:75],v[136:139]
ds_read_b64_tr_b16 a[190:191],v51 offset:53248
v_mfma_f32_16x16x32_bf16 v[136:139],a[136:139],a[88:91],v[136:139]
ds_read_b64_tr_b16 a[192:193],v44 offset:57344
v_mfma_f32_16x16x32_bf16 v[168:171],a[104:107],a[76:79],v[168:171]
v_mfma_f32_16x16x32_bf16 v[168:171],a[136:139],a[92:95],v[168:171]
ds_read_b64_tr_b16 a[194:195],v44 offset:61440
v_mfma_f32_16x16x32_bf16 v[172:175],a[108:111],a[76:79],v[172:175]
ds_read_b64_tr_b16 a[196:197],v45 offset:57344
v_mfma_f32_16x16x32_bf16 v[172:175],a[140:143],a[92:95],v[172:175]
ds_read_b64_tr_b16 a[198:199],v45 offset:61440
)"
R"(v_mfma_f32_16x16x32_bf16 v[140:143],a[108:111],a[72:75],v[140:143]
v_mfma_f32_16x16x32_bf16 v[140:143],a[140:143],a[88:91],v[140:143]
ds_read_b64_tr_b16 a[200:201],v46 offset:57344
v_mfma_f32_16x16x32_bf16 v[108:111],a[108:111],a[68:71],v[108:111]
ds_read_b64_tr_b16 a[202:203],v46 offset:61440
v_mfma_f32_16x16x32_bf16 v[108:111],a[140:143],a[84:87],v[108:111]
ds_read_b64_tr_b16 a[204:205],v47 offset:57344
v_mfma_f32_16x16x32_bf16 v[76:79],a[108:111],a[64:67],v[76:79]
ds_read_b64_tr_b16 a[206:207],v47 offset:61440
v_mfma_f32_16x16x32_bf16 v[76:79],a[140:143],a[80:83],v[76:79]
v_mfma_f32_16x16x32_bf16 v[80:83],a[112:115],a[64:67],v[80:83]
ds_read_b64_tr_b16 a[208:209],v48 offset:57344
v_mfma_f32_16x16x32_bf16 v[80:83],a[144:147],a[80:83],v[80:83]
ds_read_b64_tr_b16 a[210:211],v48 offset:61440
v_mfma_f32_16x16x32_bf16 v[112:115],a[112:115],a[68:71],v[112:115]
ds_read_b64_tr_b16 a[212:213],v49 offset:57344
v_mfma_f32_16x16x32_bf16 v[112:115],a[144:147],a[84:87],v[112:115]
v_mfma_f32_16x16x32_bf16 v[144:147],a[112:115],a[72:75],v[144:147]
ds_read_b64_tr_b16 a[214:215],v49 offset:61440
v_mfma_f32_16x16x32_bf16 v[144:147],a[144:147],a[88:91],v[144:147]
ds_read_b64_tr_b16 a[216:217],v50 offset:57344
v_mfma_f32_16x16x32_bf16 v[176:179],a[112:115],a[76:79],v[176:179]
ds_read_b64_tr_b16 a[218:219],v50 offset:61440
v_mfma_f32_16x16x32_bf16 v[176:179],a[144:147],a[92:95],v[176:179]
)"
R"(v_mfma_f32_16x16x32_bf16 v[180:183],a[116:119],a[76:79],v[180:183]
ds_read_b64_tr_b16 a[220:221],v51 offset:57344
v_mfma_f32_16x16x32_bf16 v[180:183],a[148:151],a[92:95],v[180:183]
ds_read_b64_tr_b16 a[222:223],v51 offset:61440
v_mfma_f32_16x16x32_bf16 v[148:151],a[116:119],a[72:75],v[148:151]
v_mfma_f32_16x16x32_bf16 v[148:151],a[148:151],a[88:91],v[148:151]
v_mfma_f32_16x16x32_bf16 v[116:119],a[116:119],a[68:71],v[116:119]
v_mfma_f32_16x16x32_bf16 v[116:119],a[148:151],a[84:87],v[116:119]
v_mfma_f32_16x16x32_bf16 v[84:87],a[116:119],a[64:67],v[84:87]
v_mfma_f32_16x16x32_bf16 v[84:87],a[148:151],a[80:83],v[84:87]
v_mfma_f32_16x16x32_bf16 v[88:91],a[120:123],a[64:67],v[88:91]
v_mfma_f32_16x16x32_bf16 v[88:91],a[152:155],a[80:83],v[88:91]
v_mfma_f32_16x16x32_bf16 v[120:123],a[120:123],a[68:71],v[120:123]
v_mfma_f32_16x16x32_bf16 v[120:123],a[152:155],a[84:87],v[120:123]
v_mfma_f32_16x16x32_bf16 v[152:155],a[120:123],a[72:75],v[152:155]
v_mfma_f32_16x16x32_bf16 v[152:155],a[152:155],a[88:91],v[152:155]
v_mfma_f32_16x16x32_bf16 v[184:187],a[120:123],a[76:79],v[184:187]
v_mfma_f32_16x16x32_bf16 v[184:187],a[152:155],a[92:95],v[184:187]
v_mfma_f32_16x16x32_bf16 v[188:191],a[124:127],a[76:79],v[188:191]
v_mfma_f32_16x16x32_bf16 v[188:191],a[156:159],a[92:95],v[188:191]
v_mfma_f32_16x16x32_bf16 v[156:159],a[124:127],a[72:75],v[156:159]
v_mfma_f32_16x16x32_bf16 v[156:159],a[156:159],a[88:91],v[156:159]
v_mfma_f32_16x16x32_bf16 v[124:127],a[124:127],a[68:71],v[124:127]
v_mfma_f32_16x16x32_bf16 v[124:127],a[156:159],a[84:87],v[124:127]
)"
R"(v_mfma_f32_16x16x32_bf16 v[92:95],a[124:127],a[64:67],v[92:95]
s_waitcnt vmcnt(0) lgkmcnt(0)
v_mfma_f32_16x16x32_bf16 v[92:95],a[156:159],a[80:83],v[92:95]
v_mfma_f32_16x16x32_bf16 v[96:99],a[160:163],a[36:39],v[96:99]
v_mfma_f32_16x16x32_bf16 v[96:99],a[192:195],a[52:55],v[96:99]
v_mfma_f32_16x16x32_bf16 v[100:103],a[164:167],a[36:39],v[100:103]
v_mfma_f32_16x16x32_bf16 v[100:103],a[196:199],a[52:55],v[100:103]
v_mfma_f32_16x16x32_bf16 v[104:107],a[168:171],a[36:39],v[104:107]
v_mfma_f32_16x16x32_bf16 v[104:107],a[200:203],a[52:55],v[104:107]
v_mfma_f32_16x16x32_bf16 v[108:111],a[172:175],a[36:39],v[108:111]
v_mfma_f32_16x16x32_bf16 v[108:111],a[204:207],a[52:55],v[108:111]
v_mfma_f32_16x16x32_bf16 v[112:115],a[176:179],a[36:39],v[112:115]
v_mfma_f32_16x16x32_bf16 v[112:115],a[208:211],a[52:55],v[112:115]
v_mfma_f32_16x16x32_bf16 v[116:119],a[180:183],a[36:39],v[116:119]
v_mfma_f32_16x16x32_bf16 v[116:119],a[212:215],a[52:55],v[116:119]
v_mfma_f32_16x16x32_bf16 v[120:123],a[184:187],a[36:39],v[120:123]
v_mfma_f32_16x16x32_bf16 v[120:123],a[216:219],a[52:55],v[120:123]
v_mfma_f32_16x16x32_bf16 v[124:127],a[188:191],a[36:39],v[124:127]
v_mfma_f32_16x16x32_bf16 v[124:127],a[220:223],a[52:55],v[124:127]
v_mfma_f32_16x16x32_bf16 v[128:131],a[160:163],a[40:43],v[128:131]
s_add_u32 s12,s53,1
s_and_b32 s12,s12,3
v_mfma_f32_16x16x32_bf16 v[128:131],a[192:195],a[56:59],v[128:131]
s_lshl_b32 s12,s12,16
)"
R"(s_add_u32 s12,s12,0
v_mfma_f32_16x16x32_bf16 v[132:135],a[164:167],a[40:43],v[132:135]
v_cvt_pk_bf16_f32 v96,v96,v97
v_cvt_pk_bf16_f32 v97,v98,v99
v_mfma_f32_16x16x32_bf16 v[132:135],a[196:199],a[56:59],v[132:135]
v_cvt_pk_bf16_f32 v98,v100,v101
v_cvt_pk_bf16_f32 v99,v102,v103
v_mfma_f32_16x16x32_bf16 v[136:139],a[168:171],a[40:43],v[136:139]
buffer_store_dwordx4 v[96:99],v18,s[16:19],s12 offen offset:0 sc1
v_cvt_pk_bf16_f32 v104,v104,v105
v_mfma_f32_16x16x32_bf16 v[136:139],a[200:203],a[56:59],v[136:139]
v_cvt_pk_bf16_f32 v105,v106,v107
v_mfma_f32_16x16x32_bf16 v[140:143],a[172:175],a[40:43],v[140:143]
v_cvt_pk_bf16_f32 v106,v108,v109
v_cvt_pk_bf16_f32 v107,v110,v111
v_mfma_f32_16x16x32_bf16 v[140:143],a[204:207],a[56:59],v[140:143]
buffer_store_dwordx4 v[104:107],v18,s[16:19],s12 offen offset:1024 sc1
v_cvt_pk_bf16_f32 v112,v112,v113
v_mfma_f32_16x16x32_bf16 v[144:147],a[176:179],a[40:43],v[144:147]
v_cvt_pk_bf16_f32 v113,v114,v115
v_cvt_pk_bf16_f32 v114,v116,v117
v_mfma_f32_16x16x32_bf16 v[144:147],a[208:211],a[56:59],v[144:147]
v_cvt_pk_bf16_f32 v115,v118,v119
buffer_store_dwordx4 v[112:115],v18,s[16:19],s12 offen offset:2048 sc1
)"
R"(v_mfma_f32_16x16x32_bf16 v[148:151],a[180:183],a[40:43],v[148:151]
v_cvt_pk_bf16_f32 v120,v120,v121
v_mfma_f32_16x16x32_bf16 v[148:151],a[212:215],a[56:59],v[148:151]
v_cvt_pk_bf16_f32 v121,v122,v123
v_cvt_pk_bf16_f32 v122,v124,v125
v_mfma_f32_16x16x32_bf16 v[152:155],a[184:187],a[40:43],v[152:155]
v_cvt_pk_bf16_f32 v123,v126,v127
buffer_store_dwordx4 v[120:123],v18,s[16:19],s12 offen offset:3072 sc1
v_mfma_f32_16x16x32_bf16 v[152:155],a[216:219],a[56:59],v[152:155]
v_mfma_f32_16x16x32_bf16 v[156:159],a[188:191],a[40:43],v[156:159]
v_mfma_f32_16x16x32_bf16 v[156:159],a[220:223],a[56:59],v[156:159]
v_mfma_f32_16x16x32_bf16 v[160:163],a[160:163],a[44:47],v[160:163]
s_add_u32 s12,s53,2
s_and_b32 s12,s12,3
v_mfma_f32_16x16x32_bf16 v[160:163],a[192:195],a[60:63],v[160:163]
s_lshl_b32 s12,s12,16
s_add_u32 s12,s12,4096
v_mfma_f32_16x16x32_bf16 v[164:167],a[164:167],a[44:47],v[164:167]
v_cvt_pk_bf16_f32 v128,v128,v129
v_cvt_pk_bf16_f32 v129,v130,v131
v_mfma_f32_16x16x32_bf16 v[164:167],a[196:199],a[60:63],v[164:167]
v_cvt_pk_bf16_f32 v130,v132,v133
v_cvt_pk_bf16_f32 v131,v134,v135
v_mfma_f32_16x16x32_bf16 v[168:171],a[168:171],a[44:47],v[168:171]
)"
R"(buffer_store_dwordx4 v[128:131],v18,s[16:19],s12 offen offset:0 sc1
v_cvt_pk_bf16_f32 v136,v136,v137
v_mfma_f32_16x16x32_bf16 v[168:171],a[200:203],a[60:63],v[168:171]
v_cvt_pk_bf16_f32 v137,v138,v139
v_mfma_f32_16x16x32_bf16 v[172:175],a[172:175],a[44:47],v[172:175]
v_cvt_pk_bf16_f32 v138,v140,v141
v_cvt_pk_bf16_f32 v139,v142,v143
v_mfma_f32_16x16x32_bf16 v[172:175],a[204:207],a[60:63],v[172:175]
buffer_store_dwordx4 v[136:139],v18,s[16:19],s12 offen offset:1024 sc1
v_cvt_pk_bf16_f32 v144,v144,v145
v_mfma_f32_16x16x32_bf16 v[176:179],a[176:179],a[44:47],v[176:179]
v_cvt_pk_bf16_f32 v145,v146,v147
v_cvt_pk_bf16_f32 v146,v148,v149
v_mfma_f32_16x16x32_bf16 v[176:179],a[208:211],a[60:63],v[176:179]
v_cvt_pk_bf16_f32 v147,v150,v151
buffer_store_dwordx4 v[144:147],v18,s[16:19],s12 offen offset:2048 sc1
v_mfma_f32_16x16x32_bf16 v[180:183],a[180:183],a[44:47],v[180:183]
v_cvt_pk_bf16_f32 v152,v152,v153
v_mfma_f32_16x16x32_bf16 v[180:183],a[212:215],a[60:63],v[180:183]
v_cvt_pk_bf16_f32 v153,v154,v155
v_cvt_pk_bf16_f32 v154,v156,v157
v_mfma_f32_16x16x32_bf16 v[184:187],a[184:187],a[44:47],v[184:187]
v_cvt_pk_bf16_f32 v155,v158,v159
buffer_store_dwordx4 v[152:155],v18,s[16:19],s12 offen offset:3072 sc1
)"
R"(v_mfma_f32_16x16x32_bf16 v[184:187],a[216:219],a[60:63],v[184:187]
v_mfma_f32_16x16x32_bf16 v[188:191],a[188:191],a[44:47],v[188:191]
v_mfma_f32_16x16x32_bf16 v[188:191],a[220:223],a[60:63],v[188:191]
v_mfma_f32_16x16x32_bf16 v[64:67],a[160:163],a[32:35],v[64:67]
s_add_u32 s12,s53,3
s_and_b32 s12,s12,3
v_mfma_f32_16x16x32_bf16 v[64:67],a[192:195],a[48:51],v[64:67]
s_lshl_b32 s12,s12,16
s_add_u32 s12,s12,8192
v_mfma_f32_16x16x32_bf16 v[68:71],a[164:167],a[32:35],v[68:71]
v_cvt_pk_bf16_f32 v160,v160,v161
v_cvt_pk_bf16_f32 v161,v162,v163
v_mfma_f32_16x16x32_bf16 v[68:71],a[196:199],a[48:51],v[68:71]
v_cvt_pk_bf16_f32 v162,v164,v165
v_cvt_pk_bf16_f32 v163,v166,v167
v_mfma_f32_16x16x32_bf16 v[72:75],a[168:171],a[32:35],v[72:75]
buffer_store_dwordx4 v[160:163],v18,s[16:19],s12 offen offset:0 sc1
v_cvt_pk_bf16_f32 v168,v168,v169
v_mfma_f32_16x16x32_bf16 v[72:75],a[200:203],a[48:51],v[72:75]
v_cvt_pk_bf16_f32 v169,v170,v171
v_mfma_f32_16x16x32_bf16 v[76:79],a[172:175],a[32:35],v[76:79]
v_cvt_pk_bf16_f32 v170,v172,v173
v_cvt_pk_bf16_f32 v171,v174,v175
v_mfma_f32_16x16x32_bf16 v[76:79],a[204:207],a[48:51],v[76:79]
)"
R"(buffer_store_dwordx4 v[168:171],v18,s[16:19],s12 offen offset:1024 sc1
v_cvt_pk_bf16_f32 v176,v176,v177
v_mfma_f32_16x16x32_bf16 v[80:83],a[176:179],a[32:35],v[80:83]
v_cvt_pk_bf16_f32 v177,v178,v179
v_cvt_pk_bf16_f32 v178,v180,v181
v_mfma_f32_16x16x32_bf16 v[80:83],a[208:211],a[48:51],v[80:83]
v_cvt_pk_bf16_f32 v179,v182,v183
buffer_store_dwordx4 v[176:179],v18,s[16:19],s12 offen offset:2048 sc1
v_mfma_f32_16x16x32_bf16 v[84:87],a[180:183],a[32:35],v[84:87]
v_cvt_pk_bf16_f32 v184,v184,v185
v_mfma_f32_16x16x32_bf16 v[84:87],a[212:215],a[48:51],v[84:87]
v_cvt_pk_bf16_f32 v185,v186,v187
v_cvt_pk_bf16_f32 v186,v188,v189
v_mfma_f32_16x16x32_bf16 v[88:91],a[184:187],a[32:35],v[88:91]
v_cvt_pk_bf16_f32 v187,v190,v191
buffer_store_dwordx4 v[184:187],v18,s[16:19],s12 offen offset:3072 sc1
v_mfma_f32_16x16x32_bf16 v[88:91],a[216:219],a[48:51],v[88:91]
v_mfma_f32_16x16x32_bf16 v[92:95],a[188:191],a[32:35],v[92:95]
v_mfma_f32_16x16x32_bf16 v[92:95],a[220:223],a[48:51],v[92:95]
buffer_load_dwordx4 v[192:195],v18,s[16:19],s33 offen offset:0 sc1
buffer_load_dwordx4 v[196:199],v18,s[16:19],s33 offen offset:1024 sc1
buffer_load_dwordx4 v[200:203],v18,s[16:19],s33 offen offset:2048 sc1
buffer_load_dwordx4 v[204:207],v18,s[16:19],s33 offen offset:3072 sc1
buffer_load_dwordx4 v[208:211],v18,s[16:19],s34 offen offset:0 sc1
)"
R"(buffer_load_dwordx4 v[212:215],v18,s[16:19],s34 offen offset:1024 sc1
buffer_load_dwordx4 v[216:219],v18,s[16:19],s34 offen offset:2048 sc1
buffer_load_dwordx4 v[220:223],v18,s[16:19],s34 offen offset:3072 sc1
buffer_load_dwordx4 v[224:227],v18,s[16:19],s35 offen offset:0 sc1
buffer_load_dwordx4 v[228:231],v18,s[16:19],s35 offen offset:1024 sc1
buffer_load_dwordx4 v[232:235],v18,s[16:19],s35 offen offset:2048 sc1
buffer_load_dwordx4 v[236:239],v18,s[16:19],s35 offen offset:3072 sc1
s_waitcnt vmcnt(8)
s_mov_b32 s98,0x10000
gm_K3_pp0:
v_max3_u32 v53,v192,v193,v194
v_max3_u32 v54,v195,v196,v197
v_max3_u32 v53,v53,v198,v199
v_max3_u32 v54,v54,v200,v201
v_max3_u32 v53,v53,v202,v203
v_max3_u32 v54,v54,v204,v205
v_max3_u32 v53,v53,v206,v207
v_max_u32 v53,v53,v54
v_cmp_eq_u32 vcc,-1,v53
s_nop 1
s_cmp_eq_u64 vcc,0
s_cbranch_scc1 gm_K3_pg0
s_sub_u32 s98,s98,1
s_cmp_eq_u32 s98,0
)"
R"(s_cbranch_scc1 gm_K3_pg0
buffer_load_dwordx4 v[192:195],v18,s[16:19],s33 offen offset:0 sc1
buffer_load_dwordx4 v[196:199],v18,s[16:19],s33 offen offset:1024 sc1
buffer_load_dwordx4 v[200:203],v18,s[16:19],s33 offen offset:2048 sc1
buffer_load_dwordx4 v[204:207],v18,s[16:19],s33 offen offset:3072 sc1
buffer_load_dwordx4 v[208:211],v18,s[16:19],s34 offen offset:0 sc1
buffer_load_dwordx4 v[212:215],v18,s[16:19],s34 offen offset:1024 sc1
buffer_load_dwordx4 v[216:219],v18,s[16:19],s34 offen offset:2048 sc1
buffer_load_dwordx4 v[220:223],v18,s[16:19],s34 offen offset:3072 sc1
buffer_load_dwordx4 v[224:227],v18,s[16:19],s35 offen offset:0 sc1
buffer_load_dwordx4 v[228:231],v18,s[16:19],s35 offen offset:1024 sc1
buffer_load_dwordx4 v[232:235],v18,s[16:19],s35 offen offset:2048 sc1
buffer_load_dwordx4 v[236:239],v18,s[16:19],s35 offen offset:3072 sc1
s_waitcnt vmcnt(0)
s_branch gm_K3_pp0
gm_K3_pg0:
v_lshlrev_b32 v53,16,v192
v_and_b32 v54,0xffff0000,v192
v_add_f32 v64,v64,v53
v_add_f32 v65,v65,v54
v_lshlrev_b32 v53,16,v193
v_and_b32 v54,0xffff0000,v193
v_add_f32 v66,v66,v53
v_add_f32 v67,v67,v54
)"
R"(v_lshlrev_b32 v53,16,v194
v_and_b32 v54,0xffff0000,v194
v_add_f32 v68,v68,v53
v_add_f32 v69,v69,v54
v_lshlrev_b32 v53,16,v195
v_and_b32 v54,0xffff0000,v195
v_add_f32 v70,v70,v53
v_add_f32 v71,v71,v54
v_lshlrev_b32 v53,16,v196
v_and_b32 v54,0xffff0000,v196
v_add_f32 v72,v72,v53
v_add_f32 v73,v73,v54
v_lshlrev_b32 v53,16,v197
v_and_b32 v54,0xffff0000,v197
v_add_f32 v74,v74,v53
v_add_f32 v75,v75,v54
v_lshlrev_b32 v53,16,v198
v_and_b32 v54,0xffff0000,v198
v_add_f32 v76,v76,v53
v_add_f32 v77,v77,v54
v_lshlrev_b32 v53,16,v199
v_and_b32 v54,0xffff0000,v199
v_add_f32 v78,v78,v53
v_add_f32 v79,v79,v54
)"
R"(v_lshlrev_b32 v53,16,v200
v_and_b32 v54,0xffff0000,v200
v_add_f32 v80,v80,v53
v_add_f32 v81,v81,v54
v_lshlrev_b32 v53,16,v201
v_and_b32 v54,0xffff0000,v201
v_add_f32 v82,v82,v53
v_add_f32 v83,v83,v54
v_lshlrev_b32 v53,16,v202
v_and_b32 v54,0xffff0000,v202
v_add_f32 v84,v84,v53
v_add_f32 v85,v85,v54
v_lshlrev_b32 v53,16,v203
v_and_b32 v54,0xffff0000,v203
v_add_f32 v86,v86,v53
v_add_f32 v87,v87,v54
v_lshlrev_b32 v53,16,v204
v_and_b32 v54,0xffff0000,v204
v_add_f32 v88,v88,v53
v_add_f32 v89,v89,v54
v_lshlrev_b32 v53,16,v205
v_and_b32 v54,0xffff0000,v205
v_add_f32 v90,v90,v53
v_add_f32 v91,v91,v54
)"
R"(v_lshlrev_b32 v53,16,v206
v_and_b32 v54,0xffff0000,v206
v_add_f32 v92,v92,v53
v_add_f32 v93,v93,v54
v_lshlrev_b32 v53,16,v207
v_and_b32 v54,0xffff0000,v207
v_add_f32 v94,v94,v53
v_add_f32 v95,v95,v54
s_waitcnt vmcnt(4)
s_mov_b32 s98,0x10000
gm_K3_pp1:
v_max3_u32 v53,v208,v209,v210
v_max3_u32 v54,v211,v212,v213
v_max3_u32 v53,v53,v214,v215
v_max3_u32 v54,v54,v216,v217
v_max3_u32 v53,v53,v218,v219
v_max3_u32 v54,v54,v220,v221
v_max3_u32 v53,v53,v222,v223
v_max_u32 v53,v53,v54
v_cmp_eq_u32 vcc,-1,v53
s_nop 1
s_cmp_eq_u64 vcc,0
s_cbranch_scc1 gm_K3_pg1
s_sub_u32 s98,s98,1
)"
R"(s_cmp_eq_u32 s98,0
s_cbranch_scc1 gm_K3_pg1
buffer_load_dwordx4 v[208:211],v18,s[16:19],s34 offen offset:0 sc1
buffer_load_dwordx4 v[212:215],v18,s[16:19],s34 offen offset:1024 sc1
buffer_load_dwordx4 v[216:219],v18,s[16:19],s34 offen offset:2048 sc1
buffer_load_dwordx4 v[220:223],v18,s[16:19],s34 offen offset:3072 sc1
buffer_load_dwordx4 v[224:227],v18,s[16:19],s35 offen offset:0 sc1
buffer_load_dwordx4 v[228:231],v18,s[16:19],s35 offen offset:1024 sc1
buffer_load_dwordx4 v[232:235],v18,s[16:19],s35 offen offset:2048 sc1
buffer_load_dwordx4 v[236:239],v18,s[16:19],s35 offen offset:3072 sc1
s_waitcnt vmcnt(0)
s_branch gm_K3_pp1
gm_K3_pg1:
v_lshlrev_b32 v53,16,v208
v_and_b32 v54,0xffff0000,v208
v_add_f32 v64,v64,v53
v_add_f32 v65,v65,v54
v_lshlrev_b32 v53,16,v209
v_and_b32 v54,0xffff0000,v209
v_add_f32 v66,v66,v53
v_add_f32 v67,v67,v54
v_lshlrev_b32 v53,16,v210
v_and_b32 v54,0xffff0000,v210
v_add_f32 v68,v68,v53
)"
R"(v_add_f32 v69,v69,v54
v_lshlrev_b32 v53,16,v211
v_and_b32 v54,0xffff0000,v211
v_add_f32 v70,v70,v53
v_add_f32 v71,v71,v54
v_lshlrev_b32 v53,16,v212
v_and_b32 v54,0xffff0000,v212
v_add_f32 v72,v72,v53
v_add_f32 v73,v73,v54
v_lshlrev_b32 v53,16,v213
v_and_b32 v54,0xffff0000,v213
v_add_f32 v74,v74,v53
v_add_f32 v75,v75,v54
v_lshlrev_b32 v53,16,v214
v_and_b32 v54,0xffff0000,v214
v_add_f32 v76,v76,v53
v_add_f32 v77,v77,v54
v_lshlrev_b32 v53,16,v215
v_and_b32 v54,0xffff0000,v215
v_add_f32 v78,v78,v53
v_add_f32 v79,v79,v54
v_lshlrev_b32 v53,16,v216
v_and_b32 v54,0xffff0000,v216
v_add_f32 v80,v80,v53
)"
R"(v_add_f32 v81,v81,v54
v_lshlrev_b32 v53,16,v217
v_and_b32 v54,0xffff0000,v217
v_add_f32 v82,v82,v53
v_add_f32 v83,v83,v54
v_lshlrev_b32 v53,16,v218
v_and_b32 v54,0xffff0000,v218
v_add_f32 v84,v84,v53
v_add_f32 v85,v85,v54
v_lshlrev_b32 v53,16,v219
v_and_b32 v54,0xffff0000,v219
v_add_f32 v86,v86,v53
v_add_f32 v87,v87,v54
v_lshlrev_b32 v53,16,v220
v_and_b32 v54,0xffff0000,v220
v_add_f32 v88,v88,v53
v_add_f32 v89,v89,v54
v_lshlrev_b32 v53,16,v221
v_and_b32 v54,0xffff0000,v221
v_add_f32 v90,v90,v53
v_add_f32 v91,v91,v54
v_lshlrev_b32 v53,16,v222
v_and_b32 v54,0xffff0000,v222
v_add_f32 v92,v92,v53
)"
R"(v_add_f32 v93,v93,v54
v_lshlrev_b32 v53,16,v223
v_and_b32 v54,0xffff0000,v223
v_add_f32 v94,v94,v53
v_add_f32 v95,v95,v54
s_waitcnt vmcnt(0)
s_mov_b32 s98,0x10000
gm_K3_pp2:
v_max3_u32 v53,v224,v225,v226
v_max3_u32 v54,v227,v228,v229
v_max3_u32 v53,v53,v230,v231
v_max3_u32 v54,v54,v232,v233
v_max3_u32 v53,v53,v234,v235
v_max3_u32 v54,v54,v236,v237
v_max3_u32 v53,v53,v238,v239
v_max_u32 v53,v53,v54
v_cmp_eq_u32 vcc,-1,v53
s_nop 1
s_cmp_eq_u64 vcc,0
s_cbranch_scc1 gm_K3_pg2
s_sub_u32 s98,s98,1
s_cmp_eq_u32 s98,0
s_cbranch_scc1 gm_K3_pg2
buffer_load_dwordx4 v[224:227],v18,s[16:19],s35 offen offset:0 sc1
)"
R"(buffer_load_dwordx4 v[228:231],v18,s[16:19],s35 offen offset:1024 sc1
buffer_load_dwordx4 v[232:235],v18,s[16:19],s35 offen offset:2048 sc1
buffer_load_dwordx4 v[236:239],v18,s[16:19],s35 offen offset:3072 sc1
s_waitcnt vmcnt(0)
s_branch gm_K3_pp2
gm_K3_pg2:
v_lshlrev_b32 v53,16,v224
v_and_b32 v54,0xffff0000,v224
v_add_f32 v64,v64,v53
v_add_f32 v65,v65,v54
v_lshlrev_b32 v53,16,v225
v_and_b32 v54,0xffff0000,v225
v_add_f32 v66,v66,v53
v_add_f32 v67,v67,v54
v_lshlrev_b32 v53,16,v226
v_and_b32 v54,0xffff0000,v226
v_add_f32 v68,v68,v53
v_add_f32 v69,v69,v54
v_lshlrev_b32 v53,16,v227
v_and_b32 v54,0xffff0000,v227
v_add_f32 v70,v70,v53
v_add_f32 v71,v71,v54
v_cvt_pk_bf16_f32 v64,v64,v65
v_cvt_pk_bf16_f32 v65,v66,v67
)"
R"(v_cvt_pk_bf16_f32 v66,v68,v69
v_cvt_pk_bf16_f32 v67,v70,v71
s_nop 0
v_permlane16_swap_b32 v64,v66
v_permlane16_swap_b32 v65,v67
buffer_store_dwordx4 v[64:67],v60,s[20:23],0 offen offset:0 sc0 sc1
v_lshlrev_b32 v53,16,v228
v_and_b32 v54,0xffff0000,v228
v_add_f32 v72,v72,v53
v_add_f32 v73,v73,v54
v_lshlrev_b32 v53,16,v229
v_and_b32 v54,0xffff0000,v229
v_add_f32 v74,v74,v53
v_add_f32 v75,v75,v54
v_lshlrev_b32 v53,16,v230
v_and_b32 v54,0xffff0000,v230
v_add_f32 v76,v76,v53
v_add_f32 v77,v77,v54
v_lshlrev_b32 v53,16,v231
v_and_b32 v54,0xffff0000,v231
v_add_f32 v78,v78,v53
v_add_f32 v79,v79,v54
v_cvt_pk_bf16_f32 v72,v72,v73
v_cvt_pk_bf16_f32 v73,v74,v75
)"
R"(v_cvt_pk_bf16_f32 v74,v76,v77
v_cvt_pk_bf16_f32 v75,v78,v79
s_nop 0
v_permlane16_swap_b32 v72,v74
v_permlane16_swap_b32 v73,v75
buffer_store_dwordx4 v[72:75],v60,s[20:23],0 offen offset:64 sc0 sc1
v_lshlrev_b32 v53,16,v232
v_and_b32 v54,0xffff0000,v232
v_add_f32 v80,v80,v53
v_add_f32 v81,v81,v54
s_branch gm_KB_sk27
.p2align 12
gm_KB_pg27:
s_setpc_b64 s[100:101]
gm_KB_sk27:
v_lshlrev_b32 v53,16,v233
v_and_b32 v54,0xffff0000,v233
v_add_f32 v82,v82,v53
v_add_f32 v83,v83,v54
v_lshlrev_b32 v53,16,v234
v_and_b32 v54,0xffff0000,v234
v_add_f32 v84,v84,v53
v_add_f32 v85,v85,v54
v_lshlrev_b32 v53,16,v235
)"
R"(v_and_b32 v54,0xffff0000,v235
v_add_f32 v86,v86,v53
v_add_f32 v87,v87,v54
v_cvt_pk_bf16_f32 v80,v80,v81
v_cvt_pk_bf16_f32 v81,v82,v83
v_cvt_pk_bf16_f32 v82,v84,v85
v_cvt_pk_bf16_f32 v83,v86,v87
s_nop 0
v_permlane16_swap_b32 v80,v82
v_permlane16_swap_b32 v81,v83
buffer_store_dwordx4 v[80:83],v60,s[20:23],0 offen offset:128 sc0 sc1
v_lshlrev_b32 v53,16,v236
v_and_b32 v54,0xffff0000,v236
v_add_f32 v88,v88,v53
v_add_f32 v89,v89,v54
v_lshlrev_b32 v53,16,v237
v_and_b32 v54,0xffff0000,v237
v_add_f32 v90,v90,v53
v_add_f32 v91,v91,v54
v_lshlrev_b32 v53,16,v238
v_and_b32 v54,0xffff0000,v238
v_add_f32 v92,v92,v53
v_add_f32 v93,v93,v54
v_lshlrev_b32 v53,16,v239
)"
R"(v_and_b32 v54,0xffff0000,v239
v_add_f32 v94,v94,v53
v_add_f32 v95,v95,v54
v_cvt_pk_bf16_f32 v88,v88,v89
v_cvt_pk_bf16_f32 v89,v90,v91
v_cvt_pk_bf16_f32 v90,v92,v93
v_cvt_pk_bf16_f32 v91,v94,v95
s_nop 0
v_permlane16_swap_b32 v88,v90
v_permlane16_swap_b32 v89,v91
buffer_store_dwordx4 v[88:91],v60,s[20:23],0 offen offset:192 sc0 sc1
s_or_b32 s13,%1,%2
s_cmp_lg_u32 s13,0
s_cbranch_scc1 gm_KB_fin
s_xor_b32 s72,s72,0x9e3779b9
s_add_u32 s72,s72,1
s_mov_b32 s13,16384
v_mov_b32 v22,s72
v_mov_b32 v23,0
s_mov_b64 exec,1
buffer_store_dword v22,v23,s[4:7],s13 offen sc1
s_mov_b64 exec,-1
s_waitcnt vmcnt(0)
gm_KB_fin:
)"
R"(s_branch gm_KB_end
gm_K2_seam0:
v_accvgpr_mov_b32 a0,a32
v_accvgpr_mov_b32 a1,a33
v_accvgpr_mov_b32 a2,a34
v_accvgpr_mov_b32 a3,a35
v_accvgpr_mov_b32 a4,a36
v_accvgpr_mov_b32 a5,a37
v_accvgpr_mov_b32 a6,a38
v_accvgpr_mov_b32 a7,a39
v_accvgpr_mov_b32 a8,a40
v_accvgpr_mov_b32 a9,a41
v_accvgpr_mov_b32 a10,a42
v_accvgpr_mov_b32 a11,a43
v_accvgpr_mov_b32 a12,a44
v_accvgpr_mov_b32 a13,a45
v_accvgpr_mov_b32 a14,a46
v_accvgpr_mov_b32 a15,a47
v_accvgpr_mov_b32 a16,a48
v_accvgpr_mov_b32 a17,a49
v_accvgpr_mov_b32 a18,a50
v_accvgpr_mov_b32 a19,a51
v_accvgpr_mov_b32 a20,a52
v_accvgpr_mov_b32 a21,a53
)"
R"(v_accvgpr_mov_b32 a22,a54
v_accvgpr_mov_b32 a23,a55
v_accvgpr_mov_b32 a24,a56
v_accvgpr_mov_b32 a25,a57
v_accvgpr_mov_b32 a26,a58
v_accvgpr_mov_b32 a27,a59
v_accvgpr_mov_b32 a28,a60
v_accvgpr_mov_b32 a29,a61
v_accvgpr_mov_b32 a30,a62
v_accvgpr_mov_b32 a31,a63
s_nop 1
s_branch gm_K2_seam
gm_K2_stub0:
s_mov_b32 s84,0
s_getpc_b64 s[98:99]
s_branch gm_K2_rare1
s_branch gm_K2_ret0
gm_K2_stub1:
s_mov_b32 s84,0
s_getpc_b64 s[98:99]
s_branch gm_K2_rare2
s_branch gm_K2_ret1
gm_K2_stub2:
s_mov_b32 s84,0
)"
R"(s_getpc_b64 s[98:99]
s_branch gm_K2_rare3
s_branch gm_K2_ret2
gm_K2_stub3:
s_mov_b32 s84,0
s_getpc_b64 s[98:99]
s_branch gm_K2_rare0
s_branch gm_K2_ret3
gm_K2_stub4:
s_mov_b32 s84,0
s_getpc_b64 s[98:99]
s_branch gm_K2_rare1
s_branch gm_K2_ret4
gm_K2_stub5:
s_mov_b32 s84,0
s_getpc_b64 s[98:99]
s_branch gm_K2_rare2
s_branch gm_K2_ret5
gm_K2_stub6:
s_mov_b32 s84,0
s_getpc_b64 s[98:99]
s_branch gm_K2_rare3
s_branch gm_K2_ret6
gm_K2_stub7:
)"
R"(s_mov_b32 s84,0
s_getpc_b64 s[98:99]
s_branch gm_K2_rare0
s_branch gm_K2_ret7
gm_K2_stub8:
s_mov_b32 s84,0
s_getpc_b64 s[98:99]
s_branch gm_K2_rare1
s_branch gm_K2_ret8
gm_K2_stub9:
s_mov_b32 s84,2
s_getpc_b64 s[98:99]
s_branch gm_K2_rare2
s_branch gm_K2_ret9
gm_K2_stub10:
s_mov_b32 s84,2
s_getpc_b64 s[98:99]
s_branch gm_K2_rare3
s_branch gm_K2_ret10
gm_K2_stub11:
s_mov_b32 s84,1
s_getpc_b64 s[98:99]
s_branch gm_K2_rare0
s_branch gm_K2_ret11
)"
R"(gm_K2_stub12:
s_mov_b32 s84,0
s_getpc_b64 s[98:99]
s_branch gm_K2_rare1
s_branch gm_K2_ret12
gm_K2_stub13:
s_mov_b32 s84,0
s_getpc_b64 s[98:99]
s_branch gm_K2_rare2
s_branch gm_K2_ret13
gm_K2_stub14:
s_mov_b32 s84,0
s_getpc_b64 s[98:99]
s_branch gm_K2_rare3
s_branch gm_K2_ret14
gm_K2_stub15:
s_mov_b32 s84,0
s_getpc_b64 s[98:99]
s_branch gm_K2_rare0
s_branch gm_K2_ret15
gm_K2_stub16:
s_mov_b32 s84,0
s_getpc_b64 s[98:99]
s_branch gm_K2_rare1
)"
R"(s_branch gm_K2_ret16
gm_K2_stub17:
s_mov_b32 s84,2
s_getpc_b64 s[98:99]
s_branch gm_K2_rare2
s_branch gm_K2_ret17
gm_K2_stub18:
s_mov_b32 s84,2
s_getpc_b64 s[98:99]
s_branch gm_K2_rare3
s_branch gm_K2_ret18
gm_K2_stub19:
s_mov_b32 s84,1
s_getpc_b64 s[98:99]
s_branch gm_K2_rare0
s_branch gm_K2_ret19
gm_K2_rare0:
s_nop 15
s_cmp_eq_u32 s87,0
s_cbranch_scc1 gm_K2_rareb0
v_mul_f32 v64,v64,v238
v_mul_f32 v65,v65,v238
v_mul_f32 v66,v66,v238
v_mul_f32 v67,v67,v238
)"
R"(v_mul_f32 v68,v68,v238
v_mul_f32 v69,v69,v238
v_mul_f32 v70,v70,v238
v_mul_f32 v71,v71,v238
v_mul_f32 v72,v72,v238
v_mul_f32 v73,v73,v238
v_mul_f32 v74,v74,v238
v_mul_f32 v75,v75,v238
v_mul_f32 v76,v76,v238
v_mul_f32 v77,v77,v238
v_mul_f32 v78,v78,v238
v_mul_f32 v79,v79,v238
v_mul_f32 v80,v80,v238
v_mul_f32 v81,v81,v238
v_mul_f32 v82,v82,v238
v_mul_f32 v83,v83,v238
v_mul_f32 v84,v84,v238
v_mul_f32 v85,v85,v238
v_mul_f32 v86,v86,v238
v_mul_f32 v87,v87,v238
v_mul_f32 v88,v88,v238
v_mul_f32 v89,v89,v238
v_mul_f32 v90,v90,v238
v_mul_f32 v91,v91,v238
)"
R"(v_mul_f32 v92,v92,v238
v_mul_f32 v93,v93,v238
v_mul_f32 v94,v94,v238
v_mul_f32 v95,v95,v238
v_mul_f32 v96,v96,v239
v_mul_f32 v97,v97,v239
v_mul_f32 v98,v98,v239
v_mul_f32 v99,v99,v239
v_mul_f32 v100,v100,v239
v_mul_f32 v101,v101,v239
v_mul_f32 v102,v102,v239
v_mul_f32 v103,v103,v239
v_mul_f32 v104,v104,v239
v_mul_f32 v105,v105,v239
v_mul_f32 v106,v106,v239
v_mul_f32 v107,v107,v239
v_mul_f32 v108,v108,v239
v_mul_f32 v109,v109,v239
v_mul_f32 v110,v110,v239
v_mul_f32 v111,v111,v239
v_mul_f32 v112,v112,v239
v_mul_f32 v113,v113,v239
v_mul_f32 v114,v114,v239
v_mul_f32 v115,v115,v239
)"
R"(v_mul_f32 v116,v116,v239
v_mul_f32 v117,v117,v239
v_mul_f32 v118,v118,v239
v_mul_f32 v119,v119,v239
v_mul_f32 v120,v120,v239
v_mul_f32 v121,v121,v239
v_mul_f32 v122,v122,v239
v_mul_f32 v123,v123,v239
v_mul_f32 v124,v124,v239
v_mul_f32 v125,v125,v239
v_mul_f32 v126,v126,v239
v_mul_f32 v127,v127,v239
v_mul_f32 v200,v200,v238
v_mul_f32 v201,v201,v238
v_mul_f32 v202,v202,v238
v_mul_f32 v203,v203,v238
v_mul_f32 v204,v204,v239
v_mul_f32 v205,v205,v239
v_mul_f32 v206,v206,v239
v_mul_f32 v207,v207,v239
s_mov_b32 s87,0
s_mov_b32 s12,0x41000000
gm_K2_rareb0:
s_cmp_eq_u32 s84,1
)"
R"(s_cbranch_scc1 gm_K2_rarex0
v_cmp_lt_f32 vcc,s12,v248
s_nop 4
s_cmp_eq_u64 vcc,0
s_cbranch_scc1 gm_K2_rarex0
v_max3_f32 v242,v128,v129,0
v_max3_f32 v242,v242,v130,v131
v_max3_f32 v242,v242,v132,v133
v_max3_f32 v242,v242,v134,v135
v_max3_f32 v243,v136,v137,0
v_max3_f32 v243,v243,v138,v139
v_max3_f32 v243,v243,v140,v141
v_max3_f32 v243,v243,v142,v143
v_mov_b32 v244,v242
v_mov_b32 v253,v243
s_nop 0
v_permlane16_swap_b32 v242,v244
v_permlane16_swap_b32 v243,v253
v_max_f32 v242,v242,v244
v_max_f32 v243,v243,v253
v_mov_b32 v244,v242
v_mov_b32 v253,v243
s_nop 0
v_permlane32_swap_b32 v242,v244
)"
R"(v_permlane32_swap_b32 v243,v253
v_max_f32 v242,v242,v244
v_max_f32 v243,v243,v253
s_cmp_eq_u32 s84,2
s_cselect_b32 s100,0,-1
v_and_b32 v242,s100,v242
v_sub_f32 v208,v208,v242
v_sub_f32 v209,v209,v242
v_sub_f32 v210,v210,v242
v_sub_f32 v211,v211,v242
v_sub_f32 v212,v212,v242
v_sub_f32 v213,v213,v242
v_sub_f32 v214,v214,v242
v_sub_f32 v215,v215,v242
v_sub_f32 v216,v216,v243
v_sub_f32 v217,v217,v243
v_sub_f32 v218,v218,v243
v_sub_f32 v219,v219,v243
v_sub_f32 v220,v220,v243
v_sub_f32 v221,v221,v243
v_sub_f32 v222,v222,v243
v_sub_f32 v223,v223,v243
v_sub_f32 v128,v128,v242
v_sub_f32 v129,v129,v242
)"
R"(v_sub_f32 v130,v130,v242
v_sub_f32 v131,v131,v242
v_sub_f32 v132,v132,v242
v_sub_f32 v133,v133,v242
v_sub_f32 v134,v134,v242
v_sub_f32 v135,v135,v242
v_sub_f32 v136,v136,v243
v_sub_f32 v137,v137,v243
v_sub_f32 v138,v138,v243
v_sub_f32 v139,v139,v243
v_sub_f32 v140,v140,v243
v_sub_f32 v141,v141,v243
v_sub_f32 v142,v142,v243
v_sub_f32 v143,v143,v243
v_exp_f32 v238,-v242
v_exp_f32 v239,-v243
s_mov_b32 s87,1
s_mov_b32 s12,0xff800000
gm_K2_rarex0:
s_add_u32 s98,s98,4
s_addc_u32 s99,s99,0
s_nop 7
s_setpc_b64 s[98:99]
gm_K2_rare1:
)"
R"(s_nop 15
s_cmp_eq_u32 s87,0
s_cbranch_scc1 gm_K2_rareb1
v_mul_f32 v64,v64,v238
v_mul_f32 v65,v65,v238
v_mul_f32 v66,v66,v238
v_mul_f32 v67,v67,v238
v_mul_f32 v68,v68,v238
v_mul_f32 v69,v69,v238
v_mul_f32 v70,v70,v238
v_mul_f32 v71,v71,v238
v_mul_f32 v72,v72,v238
v_mul_f32 v73,v73,v238
v_mul_f32 v74,v74,v238
v_mul_f32 v75,v75,v238
v_mul_f32 v76,v76,v238
v_mul_f32 v77,v77,v238
v_mul_f32 v78,v78,v238
v_mul_f32 v79,v79,v238
v_mul_f32 v80,v80,v238
v_mul_f32 v81,v81,v238
v_mul_f32 v82,v82,v238
v_mul_f32 v83,v83,v238
v_mul_f32 v84,v84,v238
)"
R"(v_mul_f32 v85,v85,v238
v_mul_f32 v86,v86,v238
v_mul_f32 v87,v87,v238
v_mul_f32 v88,v88,v238
v_mul_f32 v89,v89,v238
v_mul_f32 v90,v90,v238
v_mul_f32 v91,v91,v238
v_mul_f32 v92,v92,v238
v_mul_f32 v93,v93,v238
v_mul_f32 v94,v94,v238
v_mul_f32 v95,v95,v238
v_mul_f32 v96,v96,v239
v_mul_f32 v97,v97,v239
v_mul_f32 v98,v98,v239
v_mul_f32 v99,v99,v239
v_mul_f32 v100,v100,v239
v_mul_f32 v101,v101,v239
v_mul_f32 v102,v102,v239
v_mul_f32 v103,v103,v239
v_mul_f32 v104,v104,v239
v_mul_f32 v105,v105,v239
v_mul_f32 v106,v106,v239
v_mul_f32 v107,v107,v239
v_mul_f32 v108,v108,v239
)"
R"(v_mul_f32 v109,v109,v239
v_mul_f32 v110,v110,v239
v_mul_f32 v111,v111,v239
v_mul_f32 v112,v112,v239
v_mul_f32 v113,v113,v239
v_mul_f32 v114,v114,v239
v_mul_f32 v115,v115,v239
v_mul_f32 v116,v116,v239
v_mul_f32 v117,v117,v239
v_mul_f32 v118,v118,v239
v_mul_f32 v119,v119,v239
v_mul_f32 v120,v120,v239
v_mul_f32 v121,v121,v239
v_mul_f32 v122,v122,v239
v_mul_f32 v123,v123,v239
v_mul_f32 v124,v124,v239
v_mul_f32 v125,v125,v239
v_mul_f32 v126,v126,v239
v_mul_f32 v127,v127,v239
v_mul_f32 v200,v200,v238
v_mul_f32 v201,v201,v238
v_mul_f32 v202,v202,v238
v_mul_f32 v203,v203,v238
v_mul_f32 v204,v204,v239
)"
R"(v_mul_f32 v205,v205,v239
v_mul_f32 v206,v206,v239
v_mul_f32 v207,v207,v239
s_mov_b32 s87,0
s_mov_b32 s12,0x41000000
gm_K2_rareb1:
s_cmp_eq_u32 s84,1
s_cbranch_scc1 gm_K2_rarex1
v_cmp_lt_f32 vcc,s12,v248
s_nop 4
s_cmp_eq_u64 vcc,0
s_cbranch_scc1 gm_K2_rarex1
v_max3_f32 v242,v144,v145,0
v_max3_f32 v242,v242,v146,v147
v_max3_f32 v242,v242,v148,v149
v_max3_f32 v242,v242,v150,v151
v_max3_f32 v243,v152,v153,0
v_max3_f32 v243,v243,v154,v155
v_max3_f32 v243,v243,v156,v157
v_max3_f32 v243,v243,v158,v159
v_mov_b32 v244,v242
v_mov_b32 v253,v243
s_nop 0
v_permlane16_swap_b32 v242,v244
)"
R"(v_permlane16_swap_b32 v243,v253
v_max_f32 v242,v242,v244
v_max_f32 v243,v243,v253
v_mov_b32 v244,v242
v_mov_b32 v253,v243
s_nop 0
v_permlane32_swap_b32 v242,v244
v_permlane32_swap_b32 v243,v253
v_max_f32 v242,v242,v244
v_max_f32 v243,v243,v253
s_cmp_eq_u32 s84,2
s_cselect_b32 s100,0,-1
v_and_b32 v242,s100,v242
v_sub_f32 v208,v208,v242
v_sub_f32 v209,v209,v242
v_sub_f32 v210,v210,v242
v_sub_f32 v211,v211,v242
v_sub_f32 v212,v212,v242
v_sub_f32 v213,v213,v242
v_sub_f32 v214,v214,v242
v_sub_f32 v215,v215,v242
v_sub_f32 v216,v216,v243
v_sub_f32 v217,v217,v243
v_sub_f32 v218,v218,v243
)"
R"(v_sub_f32 v219,v219,v243
v_sub_f32 v220,v220,v243
v_sub_f32 v221,v221,v243
v_sub_f32 v222,v222,v243
v_sub_f32 v223,v223,v243
v_sub_f32 v144,v144,v242
v_sub_f32 v145,v145,v242
v_sub_f32 v146,v146,v242
v_sub_f32 v147,v147,v242
v_sub_f32 v148,v148,v242
v_sub_f32 v149,v149,v242
v_sub_f32 v150,v150,v242
v_sub_f32 v151,v151,v242
v_sub_f32 v152,v152,v243
v_sub_f32 v153,v153,v243
v_sub_f32 v154,v154,v243
v_sub_f32 v155,v155,v243
v_sub_f32 v156,v156,v243
v_sub_f32 v157,v157,v243
v_sub_f32 v158,v158,v243
v_sub_f32 v159,v159,v243
v_exp_f32 v238,-v242
v_exp_f32 v239,-v243
s_mov_b32 s87,1
)"
R"(s_mov_b32 s12,0xff800000
gm_K2_rarex1:
s_add_u32 s98,s98,4
s_addc_u32 s99,s99,0
s_nop 7
s_setpc_b64 s[98:99]
gm_K2_rare2:
s_nop 15
s_cmp_eq_u32 s87,0
s_cbranch_scc1 gm_K2_rareb2
v_mul_f32 v64,v64,v238
v_mul_f32 v65,v65,v238
v_mul_f32 v66,v66,v238
v_mul_f32 v67,v67,v238
v_mul_f32 v68,v68,v238
v_mul_f32 v69,v69,v238
v_mul_f32 v70,v70,v238
v_mul_f32 v71,v71,v238
v_mul_f32 v72,v72,v238
v_mul_f32 v73,v73,v238
v_mul_f32 v74,v74,v238
v_mul_f32 v75,v75,v238
v_mul_f32 v76,v76,v238
v_mul_f32 v77,v77,v238
)"
R"(v_mul_f32 v78,v78,v238
v_mul_f32 v79,v79,v238
v_mul_f32 v80,v80,v238
v_mul_f32 v81,v81,v238
v_mul_f32 v82,v82,v238
v_mul_f32 v83,v83,v238
v_mul_f32 v84,v84,v238
v_mul_f32 v85,v85,v238
v_mul_f32 v86,v86,v238
v_mul_f32 v87,v87,v238
v_mul_f32 v88,v88,v238
v_mul_f32 v89,v89,v238
v_mul_f32 v90,v90,v238
v_mul_f32 v91,v91,v238
v_mul_f32 v92,v92,v238
v_mul_f32 v93,v93,v238
v_mul_f32 v94,v94,v238
v_mul_f32 v95,v95,v238
v_mul_f32 v96,v96,v239
v_mul_f32 v97,v97,v239
v_mul_f32 v98,v98,v239
v_mul_f32 v99,v99,v239
v_mul_f32 v100,v100,v239
v_mul_f32 v101,v101,v239
)"
R"(v_mul_f32 v102,v102,v239
v_mul_f32 v103,v103,v239
v_mul_f32 v104,v104,v239
v_mul_f32 v105,v105,v239
v_mul_f32 v106,v106,v239
v_mul_f32 v107,v107,v239
v_mul_f32 v108,v108,v239
v_mul_f32 v109,v109,v239
v_mul_f32 v110,v110,v239
v_mul_f32 v111,v111,v239
v_mul_f32 v112,v112,v239
v_mul_f32 v113,v113,v239
v_mul_f32 v114,v114,v239
v_mul_f32 v115,v115,v239
v_mul_f32 v116,v116,v239
v_mul_f32 v117,v117,v239
v_mul_f32 v118,v118,v239
v_mul_f32 v119,v119,v239
v_mul_f32 v120,v120,v239
v_mul_f32 v121,v121,v239
v_mul_f32 v122,v122,v239
v_mul_f32 v123,v123,v239
v_mul_f32 v124,v124,v239
v_mul_f32 v125,v125,v239
)"
R"(v_mul_f32 v126,v126,v239
v_mul_f32 v127,v127,v239
v_mul_f32 v200,v200,v238
v_mul_f32 v201,v201,v238
v_mul_f32 v202,v202,v238
v_mul_f32 v203,v203,v238
v_mul_f32 v204,v204,v239
v_mul_f32 v205,v205,v239
v_mul_f32 v206,v206,v239
v_mul_f32 v207,v207,v239
s_mov_b32 s87,0
s_mov_b32 s12,0x41000000
gm_K2_rareb2:
s_cmp_eq_u32 s84,1
s_cbranch_scc1 gm_K2_rarex2
v_cmp_lt_f32 vcc,s12,v248
s_nop 4
s_cmp_eq_u64 vcc,0
s_cbranch_scc1 gm_K2_rarex2
v_max3_f32 v242,v160,v161,0
v_max3_f32 v242,v242,v162,v163
v_max3_f32 v242,v242,v164,v165
v_max3_f32 v242,v242,v166,v167
v_max3_f32 v243,v168,v169,0
)"
R"(v_max3_f32 v243,v243,v170,v171
v_max3_f32 v243,v243,v172,v173
v_max3_f32 v243,v243,v174,v175
v_mov_b32 v244,v242
v_mov_b32 v253,v243
s_nop 0
v_permlane16_swap_b32 v242,v244
v_permlane16_swap_b32 v243,v253
v_max_f32 v242,v242,v244
v_max_f32 v243,v243,v253
v_mov_b32 v244,v242
v_mov_b32 v253,v243
s_nop 0
v_permlane32_swap_b32 v242,v244
v_permlane32_swap_b32 v243,v253
v_max_f32 v242,v242,v244
v_max_f32 v243,v243,v253
s_cmp_eq_u32 s84,2
s_cselect_b32 s100,0,-1
v_and_b32 v242,s100,v242
v_sub_f32 v208,v208,v242
v_sub_f32 v209,v209,v242
v_sub_f32 v210,v210,v242
v_sub_f32 v211,v211,v242
)"
R"(v_sub_f32 v212,v212,v242
v_sub_f32 v213,v213,v242
v_sub_f32 v214,v214,v242
v_sub_f32 v215,v215,v242
v_sub_f32 v216,v216,v243
v_sub_f32 v217,v217,v243
v_sub_f32 v218,v218,v243
v_sub_f32 v219,v219,v243
v_sub_f32 v220,v220,v243
v_sub_f32 v221,v221,v243
v_sub_f32 v222,v222,v243
v_sub_f32 v223,v223,v243
v_sub_f32 v160,v160,v242
v_sub_f32 v161,v161,v242
v_sub_f32 v162,v162,v242
v_sub_f32 v163,v163,v242
v_sub_f32 v164,v164,v242
v_sub_f32 v165,v165,v242
v_sub_f32 v166,v166,v242
v_sub_f32 v167,v167,v242
v_sub_f32 v168,v168,v243
v_sub_f32 v169,v169,v243
v_sub_f32 v170,v170,v243
v_sub_f32 v171,v171,v243
)"
R"(v_sub_f32 v172,v172,v243
v_sub_f32 v173,v173,v243
v_sub_f32 v174,v174,v243
v_sub_f32 v175,v175,v243
v_exp_f32 v238,-v242
v_exp_f32 v239,-v243
s_mov_b32 s87,1
s_mov_b32 s12,0xff800000
gm_K2_rarex2:
s_add_u32 s98,s98,4
s_addc_u32 s99,s99,0
s_nop 7
s_setpc_b64 s[98:99]
gm_K2_rare3:
s_nop 15
s_cmp_eq_u32 s87,0
s_cbranch_scc1 gm_K2_rareb3
v_mul_f32 v64,v64,v238
v_mul_f32 v65,v65,v238
v_mul_f32 v66,v66,v238
v_mul_f32 v67,v67,v238
v_mul_f32 v68,v68,v238
v_mul_f32 v69,v69,v238
v_mul_f32 v70,v70,v238
)"
R"(v_mul_f32 v71,v71,v238
v_mul_f32 v72,v72,v238
v_mul_f32 v73,v73,v238
v_mul_f32 v74,v74,v238
v_mul_f32 v75,v75,v238
v_mul_f32 v76,v76,v238
v_mul_f32 v77,v77,v238
v_mul_f32 v78,v78,v238
v_mul_f32 v79,v79,v238
v_mul_f32 v80,v80,v238
v_mul_f32 v81,v81,v238
v_mul_f32 v82,v82,v238
v_mul_f32 v83,v83,v238
v_mul_f32 v84,v84,v238
v_mul_f32 v85,v85,v238
v_mul_f32 v86,v86,v238
v_mul_f32 v87,v87,v238
v_mul_f32 v88,v88,v238
v_mul_f32 v89,v89,v238
v_mul_f32 v90,v90,v238
v_mul_f32 v91,v91,v238
v_mul_f32 v92,v92,v238
v_mul_f32 v93,v93,v238
v_mul_f32 v94,v94,v238
)"
R"(v_mul_f32 v95,v95,v238
v_mul_f32 v96,v96,v239
v_mul_f32 v97,v97,v239
v_mul_f32 v98,v98,v239
v_mul_f32 v99,v99,v239
v_mul_f32 v100,v100,v239
v_mul_f32 v101,v101,v239
v_mul_f32 v102,v102,v239
v_mul_f32 v103,v103,v239
v_mul_f32 v104,v104,v239
v_mul_f32 v105,v105,v239
v_mul_f32 v106,v106,v239
v_mul_f32 v107,v107,v239
v_mul_f32 v108,v108,v239
v_mul_f32 v109,v109,v239
v_mul_f32 v110,v110,v239
v_mul_f32 v111,v111,v239
v_mul_f32 v112,v112,v239
v_mul_f32 v113,v113,v239
v_mul_f32 v114,v114,v239
v_mul_f32 v115,v115,v239
v_mul_f32 v116,v116,v239
v_mul_f32 v117,v117,v239
v_mul_f32 v118,v118,v239
)"
R"(v_mul_f32 v119,v119,v239
v_mul_f32 v120,v120,v239
v_mul_f32 v121,v121,v239
v_mul_f32 v122,v122,v239
v_mul_f32 v123,v123,v239
v_mul_f32 v124,v124,v239
v_mul_f32 v125,v125,v239
v_mul_f32 v126,v126,v239
v_mul_f32 v127,v127,v239
v_mul_f32 v200,v200,v238
v_mul_f32 v201,v201,v238
v_mul_f32 v202,v202,v238
v_mul_f32 v203,v203,v238
v_mul_f32 v204,v204,v239
v_mul_f32 v205,v205,v239
v_mul_f32 v206,v206,v239
v_mul_f32 v207,v207,v239
s_mov_b32 s87,0
s_mov_b32 s12,0x41000000
gm_K2_rareb3:
s_cmp_eq_u32 s84,1
s_cbranch_scc1 gm_K2_rarex3
v_cmp_lt_f32 vcc,s12,v248
s_nop 4
)"
R"(s_cmp_eq_u64 vcc,0
s_cbranch_scc1 gm_K2_rarex3
v_max3_f32 v242,v176,v177,0
v_max3_f32 v242,v242,v178,v179
v_max3_f32 v242,v242,v180,v181
v_max3_f32 v242,v242,v182,v183
v_max3_f32 v243,v184,v185,0
v_max3_f32 v243,v243,v186,v187
v_max3_f32 v243,v243,v188,v189
v_max3_f32 v243,v243,v190,v191
v_mov_b32 v244,v242
v_mov_b32 v253,v243
s_nop 0
v_permlane16_swap_b32 v242,v244
v_permlane16_swap_b32 v243,v253
v_max_f32 v242,v242,v244
v_max_f32 v243,v243,v253
v_mov_b32 v244,v242
v_mov_b32 v253,v243
s_nop 0
v_permlane32_swap_b32 v242,v244
v_permlane32_swap_b32 v243,v253
v_max_f32 v242,v242,v244
v_max_f32 v243,v243,v253
)"
R"(s_cmp_eq_u32 s84,2
s_cselect_b32 s100,0,-1
v_and_b32 v242,s100,v242
v_sub_f32 v208,v208,v242
v_sub_f32 v209,v209,v242
v_sub_f32 v210,v210,v242
v_sub_f32 v211,v211,v242
v_sub_f32 v212,v212,v242
v_sub_f32 v213,v213,v242
v_sub_f32 v214,v214,v242
v_sub_f32 v215,v215,v242
v_sub_f32 v216,v216,v243
v_sub_f32 v217,v217,v243
v_sub_f32 v218,v218,v243
v_sub_f32 v219,v219,v243
v_sub_f32 v220,v220,v243
v_sub_f32 v221,v221,v243
v_sub_f32 v222,v222,v243
v_sub_f32 v223,v223,v243
v_sub_f32 v176,v176,v242
v_sub_f32 v177,v177,v242
v_sub_f32 v178,v178,v242
v_sub_f32 v179,v179,v242
v_sub_f32 v180,v180,v242
)"
R"(v_sub_f32 v181,v181,v242
v_sub_f32 v182,v182,v242
v_sub_f32 v183,v183,v242
v_sub_f32 v184,v184,v243
v_sub_f32 v185,v185,v243
v_sub_f32 v186,v186,v243
v_sub_f32 v187,v187,v243
v_sub_f32 v188,v188,v243
v_sub_f32 v189,v189,v243
v_sub_f32 v190,v190,v243
v_sub_f32 v191,v191,v243
v_exp_f32 v238,-v242
v_exp_f32 v239,-v243
s_mov_b32 s87,1
s_mov_b32 s12,0xff800000
gm_K2_rarex3:
s_add_u32 s98,s98,4
s_addc_u32 s99,s99,0
s_nop 7
s_setpc_b64 s[98:99]
gm_KB_pg28:
gm_KB_pg29:
gm_KB_pg30:
gm_KB_pg31:
)"
R"(gm_KB_pg32:
gm_KB_fn0:
gm_KB_fn1:
gm_KB_fn2:
gm_KB_fn3:
gm_KB_fn4:
gm_KB_fn5:
gm_KB_fn6:
gm_KB_fn7:
gm_KB_fn8:
gm_KB_fn9:
gm_KB_fn10:
gm_KB_fn11:
gm_KB_fn12:
gm_KB_fn13:
gm_KB_fn14:
gm_KB_fn15:
gm_KB_fn16:
gm_KB_fn17:
gm_KB_fn18:
gm_KB_fn19:
gm_KB_fn20:
gm_KB_fn21:
gm_KB_fn22:
)"
R"(gm_KB_fn23:
gm_KB_fn24:
gm_KB_fn25:
gm_KB_fn26:
gm_KB_fn27:
gm_KB_fn28:
gm_KB_fn29:
gm_KB_fn30:
gm_KB_fn31:
gm_KB_fn32:
gm_KB_fn33:
gm_KB_fn34:
gm_KB_fn35:
gm_KB_fn36:
gm_KB_fn37:
gm_KB_fn38:
gm_KB_fn39:
gm_KB_fn40:
gm_KB_fn41:
gm_KB_fn42:
gm_KB_fn43:
gm_KB_fn44:
gm_KB_fn45:
gm_KB_fn46:
)"
R"(gm_KB_fn47:
gm_KB_fn48:
gm_KB_fn49:
gm_KB_fn50:
gm_KB_fn51:
gm_KB_fn52:
gm_KB_fn53:
gm_KB_fn54:
gm_KB_fn55:
gm_KB_fn56:
gm_KB_fn57:
gm_KB_fn58:
gm_KB_fn59:
gm_KB_fn60:
gm_KB_fn61:
gm_KB_fn62:
gm_KB_fn63:
s_setpc_b64 s[100:101]
gm_KB_end:
)"
    :: "s"(__builtin_amdgcn_kernarg_segment_ptr()), "s"(blockIdx.x), "s"(wid)
    : "v0", "v1", "v2", "v3", "v4", "v5", "v6", "v7", "v8", "v9", "v10", "v11", "v12", "v13", "v14", "v15", "v16", "v17", "v18", "v19", "v20", "v21", "v22", "v23", "v24", "v25", "v26", "v27", "v28", "v29", "v30", "v31", "v32", "v33", "v34", "v35", "v36", "v37", "v38", "v39", "v40", "v41", "v42", "v43", "v44", "v45", "v46", "v47", "v48", "v49", "v50", "v51", "v52", "v53", "v54", "v55", "v56", "v57", "v58", "v59", "v60", "v61", "v62", "v63", "v64", "v65", "v66", "v67", "v68", "v69", "v70", "v71", "v72", "v73", "v74", "v75", "v76", "v77", "v78", "v79", "v80", "v81", "v82", "v83", "v84", "v85", "v86", "v87", "v88", "v89", "v90", "v91", "v92", "v93", "v94", "v95", "v96", "v97", "v98", "v99", "v100", "v101", "v102", "v103", "v104", "v105", "v106", "v107", "v108", "v109", "v110", "v111", "v112", "v113", "v114", "v115", "v116", "v117", "v118", "v119", "v120", "v121", "v122", "v123", "v124", "v125", "v126", "v127", "v128", "v129", "v130", "v131", "v132", "v133", "v134", "v135", "v136", "v137", "v138", "v139", "v140", "v141", "v142", "v143", "v144", "v145", "v146", "v147", "v148", "v149", "v150", "v151", "v152", "v153", "v154", "v155", "v156", "v157", "v158", "v159", "v160", "v161", "v162", "v163", "v164", "v165", "v166", "v167", "v168", "v169", "v170", "v171", "v172", "v173", "v174", "v175", "v176", "v177", "v178", "v179", "v180", "v181", "v182", "v183", "v184", "v185", "v186", "v187", "v188", "v189", "v190", "v191", "v192", "v193", "v194", "v195", "v196", "v197", "v198", "v199", "v200", "v201", "v202", "v203", "v204", "v205", "v206", "v207", "v208", "v209", "v210", "v211", "v212", "v213", "v214", "v215", "v216", "v217", "v218", "v219", "v220", "v221", "v222", "v223", "v224", "v225", "v226", "v227", "v228", "v229", "v230", "v231", "v232", "v233", "v234", "v235", "v236", "v237", "v238", "v239", "v240", "v241", "v242", "v243", "v244", "v245", "v246", "v247", "v248", "v249", "v250", "v251", "v252", "v253", "v254", "v255", "a0", "a1", "a2", "a3", "a4", "a5", "a6", "a7", "a8", "a9", "a10", "a11", "a12", "a13", "a14", "a15", "a16", "a17", "a18", "a19", "a20", "a21", "a22", "a23", "a24", "a25", "a26", "a27", "a28", "a29", "a30", "a31", "a32", "a33", "a34", "a35", "a36", "a37", "a38", "a39", "a40", "a41", "a42", "a43", "a44", "a45", "a46", "a47", "a48", "a49", "a50", "a51", "a52", "a53", "a54", "a55", "a56", "a57", "a58", "a59", "a60", "a61", "a62", "a63", "a64", "a65", "a66", "a67", "a68", "a69", "a70", "a71", "a72", "a73", "a74", "a75", "a76", "a77", "a78", "a79", "a80", "a81", "a82", "a83", "a84", "a85", "a86", "a87", "a88", "a89", "a90", "a91", "a92", "a93", "a94", "a95", "a96", "a97", "a98", "a99", "a100", "a101", "a102", "a103", "a104", "a105", "a106", "a107", "a108", "a109", "a110", "a111", "a112", "a113", "a114", "a115", "a116", "a117", "a118", "a119", "a120", "a121", "a122", "a123", "a124", "a125", "a126", "a127", "a128", "a129", "a130", "a131", "a132", "a133", "a134", "a135", "a136", "a137", "a138", "a139", "a140", "a141", "a142", "a143", "a144", "a145", "a146", "a147", "a148", "a149", "a150", "a151", "a152", "a153", "a154", "a155", "a156", "a157", "a158", "a159", "a160", "a161", "a162", "a163", "a164", "a165", "a166", "a167", "a168", "a169", "a170", "a171", "a172", "a173", "a174", "a175", "a176", "a177", "a178", "a179", "a180", "a181", "a182", "a183", "a184", "a185", "a186", "a187", "a188", "a189", "a190", "a191", "a192", "a193", "a194", "a195", "a196", "a197", "a198", "a199", "a200", "a201", "a202", "a203", "a204", "a205", "a206", "a207", "a208", "a209", "a210", "a211", "a212", "a213", "a214", "a215", "a216", "a217", "a218", "a219", "a220", "a221", "a222", "a223", "a224", "a225", "a226", "a227", "a228", "a229", "a230", "a231", "a232", "a233", "a234", "a235", "a236", "a237", "a238", "a239", "a240", "a241", "a242", "a243", "a244", "a245", "a246", "a247", "a248", "a249", "a250", "a251", "a252", "a253", "a254", "a255", "s4", "s5", "s6", "s7", "s8", "s9", "s10", "s11", "s12", "s13", "s14", "s15", "s16", "s17", "s18", "s19", "s20", "s21", "s22", "s23", "s24", "s25", "s26", "s27", "s28", "s29", "s30", "s31", "s32", "s33", "s34", "s35", "s36", "s37", "s38", "s39", "s40", "s41", "s42", "s43", "s44", "s45", "s46", "s47", "s48", "s49", "s50", "s51", "s52", "s53", "s54", "s55", "s56", "s57", "s58", "s59", "s60", "s61", "s62", "s63", "s64", "s65", "s66", "s67", "s68", "s69", "s70", "s71", "s72", "s73", "s74", "s75", "s76", "s77", "s78", "s79", "s80", "s81", "s82", "s83", "s84", "s85", "s86", "s87", "s88", "s89", "s90", "s91", "s92", "s93", "s94", "s95", "s96", "s97", "s98", "s99", "s100", "s101", "vcc", "scc", "memory");
}
extern "C" void kernel_launch(void* const* d_in, const int* in_sizes, int n_in, void* d_out, int out_size, void* d_ws, size_t ws_size, hipStream_t stream) {
  (void)in_sizes; (void)n_in; (void)out_size; (void)ws_size;
  void* ws = d_ws;
  (void)hipFuncSetAttribute((const void*)gm_k, hipFuncAttributeMaxDynamicSharedMemorySize, 163840);
  gm_k<<<256, 256, 163840, stream>>>(d_in[0], d_in[1], d_in[2], d_in[3], d_in[4], d_in[7], d_in[8], ws, d_in[10], d_in[5], d_in[9], d_out);
}
```
